```python
import torch

def _softmax(x, axis=-1):
    return torch.softmax(x, dim=axis)
def _gelu(x):
    return torch.nn.functional.gelu(x, approximate="tanh")
def _silu(x):
    return torch.nn.functional.silu(x)
def _sigmoid(x):
    return torch.sigmoid(x)
def _layernorm(x, axis=-1, eps=1e-5):
    m = x.mean(dim=axis, keepdim=True)
    v = x.var(dim=axis, keepdim=True, unbiased=False)
    return (x - m) / torch.sqrt(v + eps)
def _scatter_add(tgt, idx, src, axis=0):
    return tgt.clone().index_add_(axis, idx, src)
def _segment_sum(src, seg, n):
    out = torch.zeros((n,) + src.shape[1:], dtype=src.dtype)
    return out.index_add_(0, seg, src)
def _pad(x, pw):
    flat = tuple(v for pair in reversed(pw) for v in pair)
    return torch.nn.functional.pad(x, flat)

kernel_name = 'gated_mix__mha_block__compressed_kv_cla__prompt'

def setup_inputs():

    _w = (lambda a: (lambda t: t.to(torch.bfloat16) if t.is_floating_point() else t)(torch.as_tensor(a)))
    def _A_setup():
        B, S, D, H = 1, 2048, 448, 16
        return {
            "x": _w((lambda *s: torch.randn(*s, dtype=torch.bfloat16) * 0.05)(B, S, D)),
            "wqkv": _w((lambda *s: torch.randn(*s, dtype=torch.bfloat16) * 1.6)(D, 3 * D)),
            "wo": _w((lambda *s: torch.randn(*s, dtype=torch.bfloat16) * 0.05)(D, D)),
        }
    _A_in = _A_setup()

    def _B_setup():
        B, H, Sq, Sk, D, M = 1, 16, 2048, 2048, 256, 448
        Dc = 4 * D
        return {
            "x": _w((lambda *s: torch.randn(*s, dtype=torch.bfloat16) * 0.05)(B, Sq, M)),
            "c_kv": _w((lambda *s: torch.randn(*s, dtype=torch.bfloat16) * 1.6)(B, Sk, Dc)),
            "w_q": _w((lambda *s: torch.randn(*s, dtype=torch.bfloat16) * 6.4)(M, H, D) * (M ** -0.5)),
            "w_uk": _w((lambda *s: torch.randn(*s, dtype=torch.bfloat16) * 6.4)(Dc, H, D) * (Dc ** -0.5)),
            "w_uv": _w((lambda *s: torch.randn(*s, dtype=torch.bfloat16) * 1.6)(Dc, H, D) * (Dc ** -0.5)),
            "w_o": _w((lambda *s: torch.randn(*s, dtype=torch.bfloat16) * 1.6)(H, D, M) * ((H * D) ** -0.5)),
        }
    _B_in = _B_setup()

    return {
        "A_x": _A_in["x"],
        "A_wqkv": _A_in["wqkv"],
        "A_wo": _A_in["wo"],
        "B_x": _B_in["x"],
        "B_c_kv": _B_in["c_kv"],
        "B_w_q": _B_in["w_q"],
        "B_w_uk": _B_in["w_uk"],
        "B_w_uv": _B_in["w_uv"],
        "B_w_o": _B_in["w_o"],
        "gate_w": _w((lambda *s: torch.randn(*s, dtype=torch.bfloat16) * 0.05)(448) * 16.0),
    }

def reference(A_x, A_wqkv, A_wo, B_x, B_c_kv, B_w_q, B_w_uk, B_w_uv, B_w_o, gate_w):
    def _A_ref(x, wqkv, wo):
        B, S, D = x.shape
        H = 16
        Dh = D // H
        qkv = (lambda x, *d: x.permute(*d))((x @ wqkv).reshape(B, S, 3, H, Dh), 2, 0, 3, 1, 4)
        q, k, v = qkv[0], qkv[1], qkv[2]
        scores = torch.einsum("bhqd,bhkd->bhqk", q, k) * (Dh ** -0.5)
        p = _softmax(scores, axis=-1)
        o = torch.einsum("bhqk,bhkd->bhqd", p, v)
        return (lambda x, *d: x.permute(*d))(o, 0, 2, 1, 3).reshape(B, S, D) @ wo

    def _B_ref(x, c_kv, w_q, w_uk, w_uv, w_o):
        B, Sq, M = x.shape[0], x.shape[1], x.shape[2]
        Sk = c_kv.shape[1]
        H, D = w_q.shape[1], w_q.shape[2]
        q = torch.einsum("bqm,mhd->bhqd", x, w_q)
        k = torch.einsum("bkc,chd->bhkd", c_kv, w_uk)
        v = torch.einsum("bkc,chd->bhkd", c_kv, w_uv)
        scores = torch.einsum("bhqd,bhkd->bhqk", q, k) * (D ** -0.5)
        off = Sk - Sq
        qi = torch.arange(Sq)[:, None]
        kj = torch.arange(Sk)[None, :]
        cmask = kj <= (qi + off)
        scores = torch.where(cmask[None, None], scores, -1e9)
        p = _softmax(scores, axis=-1)
        ctx = torch.einsum("bhqk,bhkd->bhqd", p, v)
        return torch.einsum("bhqd,hdm->bqm", ctx, w_o)

    x = A_x
    g = _sigmoid((x * gate_w).sum(-1, keepdims=True))
    a = _A_ref(x, A_wqkv, A_wo)
    b = _B_ref(x, B_c_kv, B_w_q, B_w_uk, B_w_uv, B_w_o)
    return g * a + (1 - g) * b
```

```cpp
#include <hip/hip_runtime.h>
#include <hip/hip_bf16.h>
#include <stdint.h>

#define DI __device__ __forceinline__
typedef unsigned u32x4 __attribute__((ext_vector_type(4)));
typedef unsigned u32x8 __attribute__((ext_vector_type(8)));
typedef unsigned u32x16 __attribute__((ext_vector_type(16)));

#define WS_FLAG  ((size_t)0)
#define WS_FLAG3 (WS_FLAG + 8192)
#define WS_F1    (WS_FLAG + 10240)
#define WS_EPOCH (WS_F1 + 1024)
#define WS_F2    (WS_FLAG + 11520)
#define WS_F3    (WS_FLAG + 12544)
#define WS_G    ((size_t)65536)
#define WS_LA   ((size_t)1 << 20)
#define WS_KV   ((size_t)2 << 20)
#define WS_QB   ((size_t)34 << 20)
#define WS_QKA  ((size_t)50 << 20)
#define WS_VA   ((size_t)54 << 20)
#define WS_PA   ((size_t)56 << 20)
#define WS_PART ((size_t)60 << 20)
#define WS_OP   ((size_t)70 << 20)
#define K2_DELTA0 6
#define K2_DELTA 6
#define TOUCH_A 1
#define TLBT 1

DI unsigned rfl(unsigned x) { return __builtin_amdgcn_readfirstlane(x); }
constexpr unsigned K1_WRING = 3 * 32768, F_LDS = 163840;
__global__ __launch_bounds__(256, 1) void fused_kernel(const unsigned short* __restrict__ x, const unsigned short* __restrict__ wqkv, const unsigned short* __restrict__ ckv,
                                                       const unsigned short* __restrict__ wq, const unsigned short* __restrict__ wuk, const unsigned short* __restrict__ wuv,
                                                       const unsigned short* __restrict__ gw, const unsigned short* __restrict__ wo_a, const unsigned short* __restrict__ wo_b,
                                                       char* __restrict__ ws, unsigned short* __restrict__ out) {
  extern __shared__ __attribute__((aligned(16))) char lds[];
  const unsigned tid = threadIdx.x, w = rfl(tid >> 6), wr = w >> 1, wc = w & 1u;
  unsigned lane = tid & 63u;
  const unsigned b = blockIdx.x, xcd = b & 7u, idx = b >> 3;
  const unsigned lds0 = rfl((unsigned)(uintptr_t)lds);
  const bool hasA = idx < 31;
  const unsigned n2 = xcd * 31u + (hasA ? idx : 0u), ct = n2 >> 3, mt2 = n2 & 7u;
  const unsigned kind = ct < 22 ? 0u : (ct < 28 ? 1u : 2u), cl = kind == 0 ? ct : (kind == 1 ? ct - 22u : ct - 28u);
  const unsigned ldw = kind == 0 ? 8192u : 2688u, cbs = kind == 0 ? 64u : 56u;
  const unsigned mt = idx & 7u, nt = idx >> 3, headB = 2u * xcd + (nt & 1u), tyB = nt >> 1;
  u32x16 sa; u32x4 vo;
  { const unsigned sl = lane & 31u, hi = lane >> 5;
    const unsigned c0 = sl ^ ((hi | ((w & 1u) << 2)) << 1), c1 = sl ^ (((2u + hi) | ((w & 1u) << 2)) << 1);
    vo[0] = (lane >> 3) * 896u + (((lane & 7u) ^ (lane >> 4)) << 4); vo[1] = (lane >> 3) * 896u + (((lane & 7u) ^ (4u + (lane >> 4))) << 4);
    const unsigned lim = cl == (kind == 0 ? 21u : (kind == 1 ? 5u : 2u)) ? (kind == 2 ? 16u : 8u) : 24u;
    vo[2] = c0 < lim ? hi * ldw + (c0 >> 2) * cbs + (c0 & 3u) * 16u : 0x80000000u;
    vo[3] = c1 < lim ? hi * ldw + (c1 >> 2) * cbs + (c1 & 3u) * 16u : 0x80000000u;
    const uint64_t px = (uint64_t)x, pw = (uint64_t)(kind == 0 ? wq : wqkv);
    sa[0] = (unsigned)px; sa[1] = (unsigned)(px >> 32) & 0xffffu; sa[2] = hasA ? 2048u * 896u : 0u; sa[3] = 0x00020000u;
    sa[4] = (unsigned)pw; sa[5] = (unsigned)(pw >> 32) & 0xffffu; sa[6] = hasA ? 448u * ldw : 0u; sa[7] = 0x00020000u;
    sa[8] = (256u * mt2 + 32u * w) * 896u; sa[9] = 8u * w * ldw + cl * 6u * cbs + (kind == 2 ? 1792u : 0u); sa[10] = 8u * 896u; sa[11] = 2u * ldw;
    sa[12] = lds0 + w * 4096u; sa[13] = lds0 + K1_WRING + w * 4096u; sa[14] = tyB; sa[15] = kind == 2 ? 2u : 0u;
#pragma unroll
    for (int i = 0; i < 16; ++i) sa[i] = rfl(sa[i]); }
  asm volatile("global_load_dword v201, %0, %1" :: "v"(0u), "s"((uint64_t)(ws + WS_EPOCH)) : "v201", "memory");
  asm volatile(R"ASM(
.macro dm v,r,s
buffer_load_dwordx4 v\v, s[\r:\r+3], s\s offen lds
.endm
s_nop 4
s_lshl_b32 s64, s30, 4
s_lshl_b32 s65, s31, 4
s_add_u32 m0, s32, 0
s_mov_b32 s61, s28
dm 12,20,61
s_add_u32 m0, s32, 1024
s_add_u32 s61, s61, s30
dm 13,20,61
s_add_u32 m0, s32, 2048
s_add_u32 s61, s61, s30
dm 12,20,61
s_add_u32 m0, s32, 3072
s_add_u32 s61, s61, s30
dm 13,20,61
s_add_u32 m0, s33, 0
s_mov_b32 s67, s29
dm 14,24,67
s_add_u32 m0, s33, 1024
s_add_u32 s67, s67, s31
dm 15,24,67
s_add_u32 m0, s33, 2048
s_add_u32 s67, s67, s31
dm 14,24,67
s_add_u32 m0, s33, 3072
s_add_u32 s67, s67, s31
dm 15,24,67
s_add_u32 s29, s29, s65
s_add_u32 m0, s32, 16384
s_add_u32 s61, s28, s64
dm 12,20,61
s_add_u32 m0, s32, 17408
s_add_u32 s61, s61, s30
dm 13,20,61
s_add_u32 m0, s32, 18432
s_add_u32 s61, s61, s30
dm 12,20,61
s_add_u32 m0, s32, 19456
s_add_u32 s61, s61, s30
dm 13,20,61
s_add_u32 s28, s28, 128
s_add_u32 m0, s33, 16384
s_mov_b32 s67, s29
dm 14,24,67
s_add_u32 m0, s33, 17408
s_add_u32 s67, s67, s31
dm 15,24,67
s_add_u32 m0, s33, 18432
s_add_u32 s67, s67, s31
dm 14,24,67
s_add_u32 m0, s33, 19456
s_add_u32 s67, s67, s31
dm 15,24,67
s_add_u32 s29, s29, s65
s_add_u32 m0, s32, 32768
s_mov_b32 s61, s28
dm 12,20,61
s_add_u32 m0, s32, 33792
s_add_u32 s61, s61, s30
dm 13,20,61
s_add_u32 m0, s32, 34816
s_add_u32 s61, s61, s30
dm 12,20,61
s_add_u32 m0, s32, 35840
s_add_u32 s61, s61, s30
dm 13,20,61
s_add_u32 m0, s33, 32768
s_mov_b32 s67, s29
dm 14,24,67
s_add_u32 m0, s33, 33792
s_add_u32 s67, s67, s31
dm 15,24,67
s_add_u32 m0, s33, 34816
s_add_u32 s67, s67, s31
dm 14,24,67
s_add_u32 m0, s33, 35840
s_add_u32 s67, s67, s31
dm 15,24,67
s_add_u32 s29, s29, s65
s_add_u32 m0, s32, 49152
s_add_u32 s61, s28, s64
dm 12,20,61
s_add_u32 m0, s32, 50176
s_add_u32 s61, s61, s30
dm 13,20,61
s_add_u32 m0, s32, 51200
s_add_u32 s61, s61, s30
dm 12,20,61
s_add_u32 m0, s32, 52224
s_add_u32 s61, s61, s30
dm 13,20,61
s_add_u32 s28, s28, 128
s_add_u32 m0, s33, 49152
s_mov_b32 s67, s29
dm 14,24,67
s_add_u32 m0, s33, 50176
s_add_u32 s67, s67, s31
dm 15,24,67
s_add_u32 m0, s33, 51200
s_add_u32 s67, s67, s31
dm 14,24,67
s_add_u32 m0, s33, 52224
s_add_u32 s67, s67, s31
dm 15,24,67
s_add_u32 s29, s29, s65
s_add_u32 m0, s32, 65536
s_mov_b32 s61, s28
dm 12,20,61
s_add_u32 m0, s32, 66560
s_add_u32 s61, s61, s30
dm 13,20,61
s_add_u32 m0, s32, 67584
s_add_u32 s61, s61, s30
dm 12,20,61
s_add_u32 m0, s32, 68608
s_add_u32 s61, s61, s30
dm 13,20,61
.purgem dm
)ASM" : "+{s[20:35]}"(sa), "+v"(lane) : "{v[12:15]}"(vo) : "s61", "s64", "s65", "s67", "scc", "memory");
  {
    const unsigned sl = b * 4u + w, o = lane * 128u;
    asm volatile("global_load_dword v200, %0, %3\n\tglobal_load_dword v200, %1, %4\n\tglobal_load_dword v200, %2, %5"
                 :: "v"(o < 1664u ? o : 1664u), "v"(o < 3456u ? o : 3456u), "v"(o < 1152u ? o : 1152u),
                    "s"((uint64_t)x + sl * 1792u), "s"((uint64_t)wq + sl * 3584u), "s"((uint64_t)wqkv + sl * 1176u) : "v200", "memory"); }
  {
    const unsigned sl = idx * 4u + w, e = sl * 64u + lane;
    const uint64_t p = sl < 25u ? (uint64_t)ws + (uint64_t)(e < 1568u ? e : 1567u) * 65536u
                     : sl == 25u ? (uint64_t)wuk + (4u << 20) + lane * 65536u : sl == 26u ? (uint64_t)wuv + (4u << 20) + lane * 65536u
                     : sl == 27u ? (uint64_t)wo_b + (lane < 56u ? lane : 55u) * 65536u : (uint64_t)out + (lane < 28u ? lane : 27u) * 65536u;
    asm volatile("global_load_dword v200, %0, off sc1" :: "v"(p) : "v200", "memory"); }
  u32x4 vin0, vin0b, vin0c; u32x16 vin2, vin4; u32x8 vin1; unsigned t0 = 0u;
#define W0(i, v) asm volatile("v_writelane_b32 %0, %1, %2" : "+v"(t0) : "s"(rfl(v)), "i"(i))
  const unsigned c = lane & 15u, g = lane >> 4, q = (lane >> 2) & 3u;
  { const unsigned p = lane & 3u, phi = q | ((g & 1u) << 2);
#pragma unroll
    for (int s = 0; s < 2; ++s) { vin0[s] = lds0 + (128u * wr + c) * 128u + (((4u * s + g) ^ ((c >> 1) & 7u)) << 4); vin0[2 + s] = 0u; }
#pragma unroll
    for (int j = 0; j < 8; ++j) { const unsigned a = lds0 + K1_WRING + (8u * g + q) * 512u + ((6u * wc + j) ^ phi) * 32u + p * 8u; if (j < 4) vin0b[j] = a; else vin0c[j - 4] = a;
                                  vin2[8 + j] = lds0 + K1_WRING + (8u * g + q) * 512u + ((8u * wc + j) ^ phi) * 32u + p * 8u; }
    const unsigned sl = lane & 31u, hi = lane >> 5, c0 = sl ^ ((hi | ((w & 1u) << 2)) << 1), c1 = sl ^ (((2u + hi) | ((w & 1u) << 2)) << 1);
    vin2[4] = (lane >> 3) * 2048u + (((lane & 7u) ^ (lane >> 4)) << 4); vin2[5] = (lane >> 3) * 2048u + (((lane & 7u) ^ (4u + (lane >> 4))) << 4);
    vin2[6] = hi * 8192u + c0 * 16u; vin2[7] = hi * 8192u + c1 * 16u; }
  { const uint64_t px = (uint64_t)x, pg = (uint64_t)gw;
    W0(32, (unsigned)px); W0(33, (unsigned)(px >> 32) & 0xffffu); W0(34, 2048u * 896u); W0(35, 0x00020000u);
    W0(36, (unsigned)pg); W0(37, (unsigned)(pg >> 32) & 0xffffu); W0(38, 896u); W0(39, 0x00020000u); }
  { const uint64_t px = (uint64_t)ckv, pw = (uint64_t)(tyB ? wuv : wuk), po = (uint64_t)(ws + WS_KV);
    W0(0, (unsigned)px); W0(1, (unsigned)(px >> 32) & 0xffffu); W0(2, 2048u * 2048u); W0(3, 0x00020000u);
    W0(4, (unsigned)pw); W0(5, (unsigned)(pw >> 32) & 0xffffu); W0(6, 1024u * 8192u); W0(7, 0x00020000u);
    W0(8, (256u * mt + 32u * w) * 2048u); W0(9, 8u * w * 8192u + headB * 512u); W0(10, 8u * 2048u); W0(11, 2u * 8192u);
    W0(12, __float_as_uint(kind == 0 ? 1.4426950408889634f / 16.f : (kind == 1 ? 0.52215298f : 1.f)));
    W0(13, w); W0(14, 1024u); W0(15, kind == 2 ? 65536u : 131072u);
    W0(16, (unsigned)po); W0(17, (unsigned)(po >> 32) & 0xffffu); W0(18, 32u << 20); W0(19, 0x00020000u);
    W0(20, (headB * 64u + 8u * mt + 4u * wr) * 32768u + (tyB ? 16384u + wc * 8192u : wc * 4096u)); W0(21, 32768u); W0(22, tyB ? 4096u : 8192u); W0(23, 4u * b); }
  { const uint64_t po = (uint64_t)(ws + (kind == 0 ? WS_QB : (kind == 1 ? WS_QKA : WS_VA)));
    W0(24, (unsigned)po); W0(25, (unsigned)(po >> 32) & 0xffffu); W0(26, !hasA ? 0u : (kind == 0 ? (16u << 20) : (kind == 1 ? (4u << 20) : (2u << 20)))); W0(27, 0x00020000u);
    W0(28, kind == 2 ? (12u * cl + 6u * wc) * 65536u + (8u * mt2 + 4u * wr) * 1024u : (6u * cl + 3u * wc) * 131072u + (16u * mt2 + 8u * wr) * 1024u); W0(29, (((xcd * 2u + (nt & 1u)) * 8u + mt) * 4u + w) * 8192u);
  }
  const unsigned hA = 2u * xcd + (idx & 1u), kh = (idx >> 1) & 1u, qr = idx >> 2;
  { const uint64_t pk = (uint64_t)(ws + WS_QKA), pq = pk + (uint64_t)hA * 131072u + (16u * qr + 4u * w) * 1024u;
    W0(40, (unsigned)pk); W0(41, (unsigned)(pk >> 32) & 0xffffu); W0(42, 6u << 20); W0(43, 0x00020000u);
    W0(44, w < 2u ? (16u + hA) * 131072u + kh * 65536u + w * 1024u : (4u << 20) + (2u * hA + (w - 2u)) * 65536u + kh * 32768u); W0(45, w < 2u ? 2048u : 1024u);
    W0(46, (unsigned)pq); W0(47, (unsigned)(pq >> 32));
    const uint64_t pn = (uint64_t)(ws + WS_PA) + (uint64_t)(kh * 16u + hA) * 131072u + (16u * qr + 4u * w) * 1024u;
    const uint64_t pl = (uint64_t)(ws + WS_LA) + (uint64_t)(kh * 16u + hA) * 8192u + (256u * qr + 64u * w) * 4u;
    W0(48, (unsigned)pn); W0(49, (unsigned)(pn >> 32)); W0(50, (unsigned)pl); W0(51, (unsigned)(pl >> 32));
    const uint64_t pf = (uint64_t)(ws + WS_F1), pgo = (uint64_t)(ws + WS_G) + (8u * b + 2u * w) * 4u;
    W0(52, (unsigned)pf); W0(53, (unsigned)(pf >> 32)); W0(54, (unsigned)pgo); W0(55, (unsigned)(pgo >> 32));
    W0(56, (8u * b + 2u * w) * 896u); W0(57, lds0 + w * 1024u); W0(58, lds0); W0(59, (b * 4u + w) * 4096u);
    { const uint64_t pt = (uint64_t)wo_b + (b * 4u + w) * 3584u; W0(60, (unsigned)pt); W0(61, (unsigned)(pt >> 32)); } }
  vin1[0] = lane * 16u; vin1[1] = (kind == 1 || (kind == 2 && c == 12u)) ? 0x3f803f80u : 0u; vin1[2] = lane < 56u ? lane * 16u : 0x80000000u;
  vin1[3] = kind == 0 ? 0u : (kind == 1 ? (g == 3u ? 1u : 0u) : (c >= 12u ? 1u : 0u));
#pragma unroll
  for (int i = 4; i < 8; ++i) vin1[i] = i == 4 ? 0x3f80u : 0u;
  vin2[0] = 0u; vin2[1] = 0u; vin2[2] = 0u;
  { const unsigned head = hA, r = idx >> 1, roleA = r >> 3, p = r & 7u;
    const unsigned Jb = 15u - p; const int nb_ = 30 - 4 * (int)p - (p == 0u ? K2_DELTA0 : K2_DELTA); const unsigned nB = nb_ < 1 ? 1u : (unsigned)nb_;
    const unsigned e = c - 4u * g;
    vin4[1] = e; vin4[2] = c * 4u; vin4[3] = e == 0u ? 0x3f80u : (e == 1u ? 0x3f800000u : 0u); vin4[4] = e == 2u ? 0x3f80u : (e == 3u ? 0x3f800000u : 0u);
#pragma unroll
    for (int i = 0; i < 7; ++i) { const unsigned n = 64u * i + lane, row = n / 14u, c16 = n - 14u * row; vin4[5 + i] = row * 896u + c16 * 16u; }
    vin4[12] = lds0 + (4u * g + q) * 224u + (lane & 3u) * 8u; vin4[13] = c * 896u + g * 8u;
    const unsigned Jf = roleA ? Jb : p, nf = roleA ? 4u * Jb + 4u - nB : 4u * p + 4u;
    const uint64_t pkv = (uint64_t)(ws + WS_KV) + (uint64_t)head * (2u << 20), pq = (uint64_t)(ws + WS_QB) + (uint64_t)head * (1u << 20);
    const uint64_t pg = (uint64_t)(ws + WS_G) + (128u * Jf + 32u * w) * 4u;
    const uint64_t pp = (uint64_t)(ws + WS_PART) + (uint64_t)((head * 8u + p) * 4u + w) * 17408u, pf = (uint64_t)(ws + WS_FLAG) + (head * 8u + p) * 64u;
    const uint64_t pq1 = pq + (8u * Jb + 2u * w) * 1024u, pq2 = pq + (8u * Jf + 2u * w) * 1024u;
    const uint64_t pop = (uint64_t)(ws + WS_OP) + (uint64_t)(Jf * 16u) * 114688u, pown = pop + head * 114688u + w * 7168u;
    const unsigned rec = roleA ? (4u * Jb + 4u) * 32768u : (2u << 20), tb = roleA ? nB * 32768u : 0u;
    const unsigned slot = 4u * head + w, ua = slot, ub = slot + 48u;
    const uint64_t pf2c = (uint64_t)(ws + WS_F2) + head * 64u, pf2p = (uint64_t)(ws + WS_F2) + headB * 64u + (tyB * 8u + mt) * 4u;
    unsigned t1 = 0u, t2 = 0u;
#define W1(i, v) asm volatile("v_writelane_b32 %0, %1, %2" : "+v"(t1) : "s"(rfl(v)), "i"(i))
#define W2(i, v) asm volatile("v_writelane_b32 %0, %1, %2" : "+v"(t2) : "s"(rfl(v)), "i"(i))
#define W1P(i, v) W1(i, (unsigned)(v)); W1(i + 1, (unsigned)((v) >> 32))
#define W2P(i, v) W2(i, (unsigned)(v)); W2(i + 1, (unsigned)((v) >> 32))
    W1(0, (unsigned)pkv); W1(1, (unsigned)(pkv >> 32) & 0xffffu); W1(2, rec); W1(3, 0x00020000u); W1(4, (unsigned)pkv); W1(5, (unsigned)(pkv >> 32) & 0xffffu); W1(6, rec); W1(7, 0x00020000u);
    W1P(8, pp); W1P(10, pf); W1P(12, pg); W1P(14, pown); W1P(16, pq1); W1P(18, pq2);
    W1(20, lds0 + w * 4096u); W1(21, lds0); W1(22, tb + w * 4096u); W1(23, tb + 16384u + w * 4096u);
    W1(24, roleA ? 0xffffffffu : nB * 32768u + w * 4096u); W1(25, roleA ? 0xffffffffu : nB * 32768u + 16384u + w * 4096u); W1(26, w * 4096u); W1(27, 16384u + w * 4096u);
    W1(28, nf * 32768u); W1(29, nB - 1u); W1(30, nf - 4u + w); W1(31, 3u - w);
    W1(32, lds0 + w * 7168u); W1(33, 0u); asm volatile("s_nop 1\n\tv_writelane_b32 %0, %1, 34" : "+v"(t1) : "s"(rfl(4u * head + ((slot - 56u) >> 31) + ((7u - slot) >> 31) * 2u))); W1(35, lds0 + w * 4096u);
    W1P(36, pf2c); W1P(38, pf2p); W1(40, roleA);
#pragma unroll
    for (int k = 0; k < 2; ++k) { const unsigned u = k ? ub : ua, qq = u / 28u, v = u - 28u * qq, ws_ = v / 7u, n = v - 7u * ws_;
      const uint64_t t = (uint64_t)out + (128u * Jf + 32u * ws_) * 896u + qq * 224u + n * 32u; W2P(2 * k, t); }
#pragma unroll
    for (int k = 0; k < 9; ++k) { const unsigned i = 4u * k + w, qq = i < 18u ? (i & 1u) : i / 9u, cc = i < 18u ? i >> 1 : i - 9u * qq; W2(4 + k, (cc < 8u ? cc * 28672u : 0u) + qq * 224u); }
    W2(13, w < 2u ? 4u : 2u * w + 2u); W2P(14, pop + ub * 1024u);
    const unsigned rbA = 8u * Jf + 2u * w;
    W2(16, nf - 5u + w); W2(17, roleA); W2P(18, (uint64_t)(ws + WS_PA) + (uint64_t)(head * 128u + rbA) * 1024u); W2P(20, (uint64_t)(ws + WS_LA) + (head * 2048u + 16u * rbA) * 4u);
    W2P(22, (uint64_t)(ws + WS_FLAG3) + Jf * 128u);
    W2P(26, pop + ua * 1024u);
    const uint64_t pb = (uint64_t)wo_b + head * 229376u, pa = (uint64_t)wo_a + head * 25088u;
    W2(28, (unsigned)pb); W2(29, (unsigned)(pb >> 32) & 0xffffu); W2(30, 229376u); W2(31, 0x00020000u);
    W2(32, (unsigned)pa); W2(33, (unsigned)(pa >> 32) & 0xffffu); W2(34, 401408u - head * 25088u); W2(35, 0x00020000u);
    W2P(36, (uint64_t)(ws + WS_F3) + ((hA * 8u + qr) * 2u + kh) * 4u); W2P(38, (uint64_t)(ws + WS_F3) + (head * 8u + (Jf >> 1)) * 8u);
    vin4[14] = t1; vin4[15] = t2; }
  vin2[3] = t0; vin4[0] = lane * 64u;
  asm volatile(R"ASM(
.macro rt d,a,o
ds_read_b64_tr_b16 v[\d:\d+1], v\a offset:\o
.endm
.macro rqv d,a,o
ds_read_b128 v[\d:\d+3], v\a offset:\o
.endm
.macro zavv d,a,b
v_mfma_f32_16x16x32_bf16 a[\d:\d+3], v[\a:\a+3], v[\b:\b+3], 0
.endm
.macro dm v,r,s
buffer_load_dwordx4 v\v, s[\r:\r+3], s\s offen lds
.endm
.macro mavv d,a,b
v_mfma_f32_16x16x32_bf16 a[\d:\d+3], v[\a:\a+3], v[\b:\b+3], a[\d:\d+3]
.endm
.macro ar d,a
v_accvgpr_read_b32 v\d, a\a
.endm
.macro cv d,a,b
v_cvt_pk_bf16_f32 v\d, v\a, v\b
.endm
.macro ex d
v_exp_f32 v\d, v\d
.endm
.macro m3 d,a,b,c
v_max3_f32 v\d, v\a, v\b, v\c
.endm
.macro rqa d,a,o
ds_read_b128 a[\d:\d+3], v\a offset:\o
.endm
.macro zvaa d,a,b
v_mfma_f32_16x16x32_bf16 v[\d:\d+3], a[\a:\a+3], a[\b:\b+3], 0
.endm
.macro mvaa d,a,b
v_mfma_f32_16x16x32_bf16 v[\d:\d+3], a[\a:\a+3], a[\b:\b+3], v[\d:\d+3]
.endm
.macro mvav d,a,b
v_mfma_f32_16x16x32_bf16 v[\d:\d+3], a[\a:\a+3], v[\b:\b+3], v[\d:\d+3]
.endm
.macro mvvv d,a,b
v_mfma_f32_16x16x32_bf16 v[\d:\d+3], v[\a:\a+3], v[\b:\b+3], v[\d:\d+3]
.endm
.macro mvva d,a,b
v_mfma_f32_16x16x32_bf16 v[\d:\d+3], v[\a:\a+3], a[\b:\b+3], v[\d:\d+3]
.endm
.macro zvva d,a,b
v_mfma_f32_16x16x32_bf16 v[\d:\d+3], v[\a:\a+3], a[\b:\b+3], 0
.endm
s_nop 4
v_readlane_b32 s36, v223, 0
v_readlane_b32 s37, v223, 1
v_readlane_b32 s38, v223, 2
v_readlane_b32 s39, v223, 3
v_readlane_b32 s40, v223, 4
v_readlane_b32 s41, v223, 5
v_readlane_b32 s42, v223, 6
v_readlane_b32 s43, v223, 7
v_readlane_b32 s44, v223, 8
v_readlane_b32 s45, v223, 9
v_readlane_b32 s46, v223, 10
v_readlane_b32 s47, v223, 11
v_readlane_b32 s48, v223, 12
v_readlane_b32 s49, v223, 13
v_readlane_b32 s50, v223, 14
v_readlane_b32 s51, v223, 15
v_readlane_b32 s52, v223, 16
v_readlane_b32 s53, v223, 17
v_readlane_b32 s54, v223, 18
v_readlane_b32 s55, v223, 19
v_readlane_b32 s56, v223, 20
v_readlane_b32 s57, v223, 21
v_readlane_b32 s58, v223, 22
v_readlane_b32 s59, v223, 23
v_readlane_b32 s84, v223, 24
v_readlane_b32 s85, v223, 25
v_readlane_b32 s86, v223, 26
v_readlane_b32 s87, v223, 27
v_readlane_b32 s88, v223, 28
v_readlane_b32 s89, v223, 29
v_readlane_b32 s90, v223, 30
v_readlane_b32 s91, v223, 31
v_readlane_b32 s8, v223, 32
v_readlane_b32 s9, v223, 33
v_readlane_b32 s10, v223, 34
v_readlane_b32 s11, v223, 35
v_readlane_b32 s12, v223, 36
v_readlane_b32 s13, v223, 37
v_readlane_b32 s14, v223, 38
v_readlane_b32 s15, v223, 39
v_readlane_b32 s92, v223, 40
v_readlane_b32 s93, v223, 41
v_readlane_b32 s94, v223, 42
v_readlane_b32 s95, v223, 43
v_readlane_b32 s96, v223, 44
v_readlane_b32 s97, v223, 45
v_readlane_b32 s98, v223, 46
v_readlane_b32 s99, v223, 47
v_readlane_b32 s0, v223, 48
v_readlane_b32 s1, v223, 49
v_readlane_b32 s2, v223, 50
v_readlane_b32 s3, v223, 51
v_readlane_b32 s4, v223, 52
v_readlane_b32 s5, v223, 53
v_readlane_b32 s6, v223, 54
v_readlane_b32 s7, v223, 55
v_readlane_b32 s16, v223, 56
v_readlane_b32 s17, v223, 57
v_readlane_b32 s18, v223, 58
v_readlane_b32 s19, v223, 59
v_readlane_b32 s72, v223, 60
v_readlane_b32 s73, v223, 61
s_lshl_b32 s64, s30, 4
s_lshl_b32 s65, s31, 4
v_lshlrev_b32 v252, 3, v16
v_and_b32 v253, 0x380, v252
v_and_b32 v254, 0x400, v252
v_lshl_add_u32 v253, v254, 1, v253
v_min_u32 v254, 0x680, v252
v_cmp_gt_u32 vcc, 0x800, v252
v_bfrev_b32 v255, 1
s_nop 0
v_cndmask_b32 v252, v255, v252, vcc
v_cndmask_b32 v253, v255, v253, vcc
s_getpc_b64 s[78:79]
.Lk1_base:
v_add_u32 v2, 0x10000, v0
v_add_u32 v3, 0x10000, v1
v_cmp_ne_u32_e64 s[76:77], 0, v19
s_sub_u32 s96, s96, s97
s_waitcnt vmcnt(28)
v_readfirstlane_b32 s82, v201
s_barrier
s_add_u32 s82, s82, 1
rt 64,4,0
rt 66,4,2048
rt 68,5,0
rt 70,5,2048
rt 72,6,0
rt 74,6,2048
rt 76,7,0
rt 78,7,2048
rt 80,8,0
rt 82,8,2048
rt 84,9,0
rt 86,9,2048
rqv 24,0,0
rqv 28,0,2048
rqv 32,0,4096
)ASM" :: "{v[0:3]}"(vin0), "{v[4:7]}"(vin0b), "{v[8:11]}"(vin0c), "{v[12:15]}"(vo), "{v[16:23]}"(vin1), "{v[220:235]}"(vin2), "{v[236:251]}"(vin4), "{s[20:35]}"(sa) : "v255", "a255", "s99", "vcc", "scc", "memory");
  asm volatile(R"ASM(
rqv 36,0,6144
rqv 40,0,8192
rqv 44,0,10240
rqv 48,0,12288
rqv 52,0,14336
s_bitcmp1_b32 s35, 1
s_cbranch_scc1 .Lk1_AN
.Lk1_AT:
s_waitcnt vmcnt(20) lgkmcnt(0)
s_barrier
.Lk1_AT_0:
zavv 64,64,24
rt 96,4,16384
zavv 68,68,24
s_add_u32 m0, s33, 0
zavv 72,72,24
s_mov_b32 s67, s29
zavv 76,76,24
rt 98,4,18432
zavv 80,80,24
dm 14,24,67
zavv 84,84,24
rt 100,5,16384
zavv 88,64,28
s_add_u32 m0, s32, 81920
rt 102,5,18432
zavv 92,68,28
rqv 24,1,0
zavv 96,72,28
s_add_u32 s61, s28, s64
zavv 100,76,28
dm 12,20,61
zavv 104,80,28
rt 104,6,16384
zavv 108,84,28
s_add_u32 m0, s33, 1024
zavv 112,64,32
rt 106,6,18432
s_add_u32 s67, s67, s31
rt 108,7,16384
zavv 116,68,32
rqv 28,1,2048
zavv 120,72,32
dm 15,24,67
zavv 124,76,32
rt 110,7,18432
zavv 128,80,32
s_add_u32 m0, s32, 82944
zavv 132,84,32
s_add_u32 s61, s61, s30
zavv 136,64,36
rt 112,8,16384
dm 13,20,61
zavv 140,68,36
rt 114,8,18432
zavv 144,72,36
rqv 32,1,4096
s_add_u32 m0, s33, 2048
zavv 148,76,36
rt 116,9,16384
zavv 152,80,36
s_add_u32 s67, s67, s31
zavv 156,84,36
rt 118,9,18432
zavv 160,64,40
dm 14,24,67
zavv 164,68,40
s_add_u32 m0, s32, 83968
rqv 36,1,6144
zavv 168,72,40
rqv 56,1,12288
zavv 172,76,40
s_add_u32 s61, s61, s30
zavv 176,80,40
dm 12,20,61
zavv 180,84,40
s_add_u32 m0, s33, 3072
zavv 184,64,44
rqv 40,1,8192
zavv 188,68,44
s_add_u32 s67, s67, s31
zavv 192,72,44
rqv 60,1,14336
zavv 196,76,44
dm 15,24,67
zavv 200,80,44
s_add_u32 m0, s32, 84992
zavv 204,84,44
s_add_u32 s61, s61, s30
zavv 208,64,48
rqv 44,1,10240
zavv 212,68,48
dm 13,20,61
zavv 216,72,48
s_add_u32 s29, s29, s65
zavv 220,76,48
s_add_u32 s28, s28, 128
zavv 224,80,48
zavv 228,84,48
s_add_u32 s74, s78, .Lk1_AT_5-.Lk1_base
s_addc_u32 s75, s79, 0
zavv 232,64,52
global_load_dword v19, v236, s[74:75]
zavv 236,68,52
zavv 240,72,52
buffer_load_dword v19, v253, s[36:39], s19 offen offset:0
zavv 244,76,52
zavv 248,80,52
zavv 252,84,52
s_waitcnt vmcnt(22) lgkmcnt(0)
s_barrier
.Lk1_AT_1:
mavv 64,96,24
rt 64,4,32768
mavv 68,100,24
s_add_u32 m0, s33, 16384
mavv 72,104,24
s_mov_b32 s67, s29
mavv 76,108,24
rt 66,4,34816
mavv 80,112,24
dm 14,24,67
mavv 84,116,24
rt 68,5,32768
mavv 88,96,28
s_add_u32 m0, s32, 0
mavv 92,100,28
rt 70,5,34816
mavv 96,104,28
rqv 24,0,32768
mavv 100,108,28
s_mov_b32 s61, s28
mavv 104,112,28
rt 72,6,32768
mavv 108,116,28
dm 12,20,61
mavv 112,96,32
rt 74,6,34816
s_add_u32 m0, s33, 17408
rt 76,7,32768
mavv 116,100,32
rqv 28,0,34816
mavv 120,104,32
s_add_u32 s67, s67, s31
mavv 124,108,32
rt 78,7,34816
mavv 128,112,32
dm 15,24,67
mavv 132,116,32
rt 80,8,32768
mavv 136,96,36
s_add_u32 m0, s32, 1024
rt 82,8,34816
mavv 140,100,36
rqv 32,0,36864
mavv 144,104,36
s_add_u32 s61, s61, s30
mavv 148,108,36
rt 84,9,32768
mavv 152,112,36
dm 13,20,61
mavv 156,116,36
rt 86,9,34816
mavv 160,96,40
s_add_u32 m0, s33, 18432
mavv 164,100,40
s_add_u32 s67, s67, s31
rqv 36,0,38912
mavv 168,104,40
rqv 48,0,45056
mavv 172,108,40
dm 14,24,67
mavv 176,112,40
s_add_u32 m0, s32, 2048
mavv 180,116,40
s_add_u32 s61, s61, s30
mavv 184,96,44
rqv 40,0,40960
mavv 188,100,44
rqv 52,0,47104
mavv 192,104,44
dm 12,20,61
mavv 196,108,44
s_add_u32 m0, s33, 19456
mavv 200,112,44
s_add_u32 s67, s67, s31
mavv 204,116,44
mavv 208,96,56
rqv 44,0,43008
mavv 212,100,56
dm 15,24,67
mavv 216,104,56
s_add_u32 m0, s32, 3072
mavv 220,108,56
mavv 224,112,56
s_add_u32 s61, s61, s30
mavv 228,116,56
mavv 232,96,60
dm 13,20,61
mavv 236,100,60
mavv 240,104,60
s_add_u32 s29, s29, s65
mavv 244,108,60
mavv 248,112,60
mavv 252,116,60
s_waitcnt vmcnt(18) lgkmcnt(0)
s_barrier
.Lk1_AT_2:
mavv 64,64,24
rt 96,4,49152
mavv 68,68,24
s_add_u32 m0, s33, 32768
mavv 72,72,24
s_mov_b32 s67, s29
mavv 76,76,24
rt 98,4,51200
mavv 80,80,24
dm 14,24,67
mavv 84,84,24
rt 100,5,49152
mavv 88,64,28
s_add_u32 m0, s32, 16384
mavv 92,68,28
rt 102,5,51200
mavv 96,72,28
rqv 24,1,32768
s_add_u32 s61, s28, s64
mavv 100,76,28
rt 104,6,49152
mavv 104,80,28
dm 12,20,61
mavv 108,84,28
rt 106,6,51200
mavv 112,64,32
s_add_u32 m0, s33, 33792
rt 108,7,49152
mavv 116,68,32
rqv 28,1,34816
mavv 120,72,32
s_add_u32 s67, s67, s31
mavv 124,76,32
rt 110,7,51200
mavv 128,80,32
dm 15,24,67
mavv 132,84,32
s_add_u32 m0, s32, 17408
mavv 136,64,36
rt 112,8,49152
s_add_u32 s61, s61, s30
mavv 140,68,36
rt 114,8,51200
mavv 144,72,36
rqv 32,1,36864
mavv 148,76,36
dm 13,20,61
mavv 152,80,36
rt 116,9,49152
mavv 156,84,36
s_add_u32 m0, s33, 34816
mavv 160,64,40
rt 118,9,51200
s_add_u32 s67, s67, s31
mavv 164,68,40
rqv 36,1,38912
mavv 168,72,40
dm 14,24,67
mavv 172,76,40
rqv 56,1,45056
mavv 176,80,40
s_add_u32 m0, s32, 18432
mavv 180,84,40
s_add_u32 s61, s61, s30
mavv 184,64,44
rqv 40,1,40960
mavv 188,68,44
dm 12,20,61
mavv 192,72,44
rqv 60,1,47104
mavv 196,76,44
s_add_u32 m0, s33, 35840
mavv 200,80,44
s_add_u32 s67, s67, s31
mavv 204,84,44
dm 15,24,67
mavv 208,64,48
rqv 44,1,43008
mavv 212,68,48
s_add_u32 m0, s32, 19456
mavv 216,72,48
s_add_u32 s61, s61, s30
mavv 220,76,48
mavv 224,80,48
dm 13,20,61
mavv 228,84,48
mavv 232,64,52
s_add_u32 s29, s29, s65
mavv 236,68,52
mavv 240,72,52
s_add_u32 s28, s28, 128
mavv 244,76,52
mavv 248,80,52
mavv 252,84,52
s_waitcnt vmcnt(16) lgkmcnt(0)
s_barrier
.Lk1_AT_3:
mavv 64,96,24
rt 64,4,0
mavv 68,100,24
s_add_u32 m0, s33, 49152
mavv 72,104,24
s_mov_b32 s67, s29
mavv 76,108,24
rt 66,4,2048
mavv 80,112,24
dm 14,24,67
mavv 84,116,24
rt 68,5,0
mavv 88,96,28
s_add_u32 m0, s32, 32768
rt 70,5,2048
mavv 92,100,28
rqv 24,2,0
mavv 96,104,28
s_mov_b32 s61, s28
mavv 100,108,28
rt 72,6,0
mavv 104,112,28
dm 12,20,61
mavv 108,116,28
rt 74,6,2048
mavv 112,96,32
s_add_u32 m0, s33, 50176
s_add_u32 s67, s67, s31
mavv 116,100,32
rt 76,7,0
mavv 120,104,32
rqv 28,2,2048
mavv 124,108,32
dm 15,24,67
mavv 128,112,32
rt 78,7,2048
mavv 132,116,32
s_add_u32 m0, s32, 33792
mavv 136,96,36
rt 80,8,0
s_add_u32 s61, s61, s30
rt 82,8,2048
mavv 140,100,36
rqv 32,2,4096
mavv 144,104,36
dm 13,20,61
mavv 148,108,36
rt 84,9,0
mavv 152,112,36
s_add_u32 m0, s33, 51200
mavv 156,116,36
rt 86,9,2048
mavv 160,96,40
s_add_u32 s67, s67, s31
mavv 164,100,40
dm 14,24,67
rqv 36,2,6144
mavv 168,104,40
rqv 48,2,12288
mavv 172,108,40
s_add_u32 m0, s32, 34816
mavv 176,112,40
s_add_u32 s61, s61, s30
mavv 180,116,40
dm 12,20,61
mavv 184,96,44
rqv 40,2,8192
mavv 188,100,44
s_add_u32 m0, s33, 52224
mavv 192,104,44
rqv 52,2,14336
mavv 196,108,44
s_add_u32 s67, s67, s31
mavv 200,112,44
dm 15,24,67
mavv 204,116,44
s_add_u32 m0, s32, 35840
mavv 208,96,56
rqv 44,2,10240
mavv 212,100,56
s_add_u32 s61, s61, s30
mavv 216,104,56
mavv 220,108,56
dm 13,20,61
mavv 224,112,56
s_add_u32 s29, s29, s65
mavv 228,116,56
mavv 232,96,60
buffer_load_dword v19, v252, s[40:43], s89 offen
mavv 236,100,60
mavv 240,104,60
s_add_u32 s89, s89, 0x800
mavv 244,108,60
mavv 248,112,60
mavv 252,116,60
s_waitcnt vmcnt(17) lgkmcnt(0)
s_barrier
.Lk1_AT_4:
mavv 64,64,24
rt 96,4,16384
mavv 68,68,24
s_add_u32 m0, s33, 0
mavv 72,72,24
s_mov_b32 s67, s29
mavv 76,76,24
rt 98,4,18432
mavv 80,80,24
dm 14,24,67
mavv 84,84,24
rt 100,5,16384
mavv 88,64,28
s_add_u32 m0, s32, 49152
rt 102,5,18432
mavv 92,68,28
rqv 24,3,0
mavv 96,72,28
s_add_u32 s61, s28, s64
mavv 100,76,28
rt 104,6,16384
mavv 104,80,28
dm 12,20,61
mavv 108,84,28
s_add_u32 m0, s33, 1024
mavv 112,64,32
rt 106,6,18432
s_add_u32 s67, s67, s31
mavv 116,68,32
rt 108,7,16384
mavv 120,72,32
rqv 28,3,2048
dm 15,24,67
mavv 124,76,32
rt 110,7,18432
mavv 128,80,32
s_add_u32 m0, s32, 50176
mavv 132,84,32
rt 112,8,16384
mavv 136,64,36
s_add_u32 s61, s61, s30
rt 114,8,18432
mavv 140,68,36
rqv 32,3,4096
mavv 144,72,36
dm 13,20,61
mavv 148,76,36
s_add_u32 m0, s33, 2048
mavv 152,80,36
rt 116,9,16384
mavv 156,84,36
s_add_u32 s67, s67, s31
mavv 160,64,40
rt 118,9,18432
dm 14,24,67
mavv 164,68,40
rqv 36,3,6144
mavv 168,72,40
s_add_u32 m0, s32, 51200
mavv 172,76,40
rqv 56,3,12288
mavv 176,80,40
s_add_u32 s61, s61, s30
mavv 180,84,40
dm 12,20,61
mavv 184,64,44
s_add_u32 m0, s33, 3072
mavv 188,68,44
rqv 40,3,8192
mavv 192,72,44
rqv 60,3,14336
mavv 196,76,44
s_add_u32 s67, s67, s31
mavv 200,80,44
dm 15,24,67
mavv 204,84,44
s_add_u32 m0, s32, 52224
mavv 208,64,48
rqv 44,3,10240
mavv 212,68,48
s_add_u32 s61, s61, s30
mavv 216,72,48
dm 13,20,61
mavv 220,76,48
s_add_u32 s29, s29, s65
mavv 224,80,48
mavv 228,84,48
s_add_u32 s28, s28, 128
mavv 232,64,52
s_add_u32 s74, s78, .Lk1_AT_9-.Lk1_base
s_addc_u32 s75, s79, 0
mavv 236,68,52
mavv 240,72,52
global_load_dword v19, v236, s[74:75]
mavv 244,76,52
mavv 248,80,52
mavv 252,84,52
s_waitcnt vmcnt(18) lgkmcnt(0)
s_barrier
.Lk1_AT_5:
mavv 64,96,24
rt 64,4,32768
mavv 68,100,24
s_add_u32 m0, s33, 16384
mavv 72,104,24
s_mov_b32 s67, s29
mavv 76,108,24
rt 66,4,34816
mavv 80,112,24
dm 14,24,67
mavv 84,116,24
rt 68,5,32768
mavv 88,96,28
s_add_u32 m0, s32, 65536
mavv 92,100,28
rt 70,5,34816
mavv 96,104,28
rqv 24,0,0
mavv 100,108,28
s_mov_b32 s61, s28
mavv 104,112,28
rt 72,6,32768
mavv 108,116,28
dm 12,20,61
mavv 112,96,32
rt 74,6,34816
s_add_u32 m0, s33, 17408
rt 76,7,32768
mavv 116,100,32
rqv 28,0,2048
mavv 120,104,32
s_add_u32 s67, s67, s31
mavv 124,108,32
rt 78,7,34816
mavv 128,112,32
dm 15,24,67
mavv 132,116,32
rt 80,8,32768
mavv 136,96,36
s_add_u32 m0, s32, 66560
rt 82,8,34816
mavv 140,100,36
rqv 32,0,4096
mavv 144,104,36
s_add_u32 s61, s61, s30
mavv 148,108,36
rt 84,9,32768
mavv 152,112,36
dm 13,20,61
mavv 156,116,36
rt 86,9,34816
mavv 160,96,40
s_add_u32 m0, s33, 18432
mavv 164,100,40
s_add_u32 s67, s67, s31
rqv 36,0,6144
mavv 168,104,40
rqv 48,0,12288
mavv 172,108,40
dm 14,24,67
mavv 176,112,40
s_add_u32 m0, s32, 67584
mavv 180,116,40
s_add_u32 s61, s61, s30
mavv 184,96,44
rqv 40,0,8192
mavv 188,100,44
rqv 52,0,14336
mavv 192,104,44
dm 12,20,61
mavv 196,108,44
s_add_u32 m0, s33, 19456
mavv 200,112,44
s_add_u32 s67, s67, s31
mavv 204,116,44
mavv 208,96,56
rqv 44,0,10240
mavv 212,100,56
dm 15,24,67
mavv 216,104,56
s_add_u32 m0, s32, 68608
mavv 220,108,56
mavv 224,112,56
s_add_u32 s61, s61, s30
mavv 228,116,56
mavv 232,96,60
dm 13,20,61
mavv 236,100,60
mavv 240,104,60
s_add_u32 s29, s29, s65
mavv 244,108,60
mavv 248,112,60
mavv 252,116,60
s_waitcnt vmcnt(17) lgkmcnt(0)
s_barrier
.Lk1_AT_6:
mavv 64,64,24
)ASM" ::: "memory");
  asm volatile(R"ASM(
rt 96,4,49152
mavv 68,68,24
s_add_u32 m0, s33, 32768
mavv 72,72,24
s_mov_b32 s67, s29
mavv 76,76,24
rt 98,4,51200
mavv 80,80,24
dm 14,24,67
mavv 84,84,24
rt 100,5,49152
mavv 88,64,28
s_add_u32 m0, s32, 81920
rt 102,5,51200
mavv 92,68,28
rqv 24,1,0
mavv 96,72,28
s_add_u32 s61, s28, s64
mavv 100,76,28
rt 104,6,49152
mavv 104,80,28
dm 12,20,61
mavv 108,84,28
s_add_u32 m0, s33, 33792
mavv 112,64,32
rt 106,6,51200
s_add_u32 s67, s67, s31
mavv 116,68,32
rt 108,7,49152
mavv 120,72,32
rqv 28,1,2048
dm 15,24,67
mavv 124,76,32
rt 110,7,51200
mavv 128,80,32
s_add_u32 m0, s32, 82944
mavv 132,84,32
rt 112,8,49152
mavv 136,64,36
s_add_u32 s61, s61, s30
rt 114,8,51200
mavv 140,68,36
rqv 32,1,4096
mavv 144,72,36
dm 13,20,61
mavv 148,76,36
s_add_u32 m0, s33, 34816
mavv 152,80,36
rt 116,9,49152
mavv 156,84,36
s_add_u32 s67, s67, s31
mavv 160,64,40
rt 118,9,51200
dm 14,24,67
mavv 164,68,40
rqv 36,1,6144
mavv 168,72,40
s_add_u32 m0, s32, 83968
mavv 172,76,40
rqv 56,1,12288
mavv 176,80,40
s_add_u32 s61, s61, s30
mavv 180,84,40
dm 12,20,61
mavv 184,64,44
s_add_u32 m0, s33, 35840
mavv 188,68,44
rqv 40,1,8192
mavv 192,72,44
rqv 60,1,14336
mavv 196,76,44
s_add_u32 s67, s67, s31
mavv 200,80,44
dm 15,24,67
mavv 204,84,44
s_add_u32 m0, s32, 84992
mavv 208,64,48
rqv 44,1,10240
mavv 212,68,48
s_add_u32 s61, s61, s30
mavv 216,72,48
dm 13,20,61
mavv 220,76,48
s_add_u32 s29, s29, s65
mavv 224,80,48
mavv 228,84,48
s_add_u32 s28, s28, 128
mavv 232,64,52
buffer_load_dword v19, v252, s[40:43], s89 offen
mavv 236,68,52
mavv 240,72,52
s_add_u32 s89, s89, 0x800
mavv 244,76,52
mavv 248,80,52
mavv 252,84,52
s_waitcnt vmcnt(17) lgkmcnt(0)
s_barrier
.Lk1_AT_7:
mavv 64,96,24
rt 64,4,0
mavv 68,100,24
s_add_u32 m0, s33, 49152
mavv 72,104,24
s_mov_b32 s67, s29
mavv 76,108,24
rt 66,4,2048
mavv 80,112,24
dm 14,24,67
mavv 84,116,24
rt 68,5,0
mavv 88,96,28
s_add_u32 m0, s32, 0
mavv 92,100,28
rt 70,5,2048
mavv 96,104,28
rqv 24,0,32768
mavv 100,108,28
s_mov_b32 s61, s28
mavv 104,112,28
rt 72,6,0
mavv 108,116,28
dm 12,20,61
mavv 112,96,32
rt 74,6,2048
s_add_u32 m0, s33, 50176
rt 76,7,0
mavv 116,100,32
rqv 28,0,34816
mavv 120,104,32
s_add_u32 s67, s67, s31
mavv 124,108,32
rt 78,7,2048
mavv 128,112,32
dm 15,24,67
mavv 132,116,32
rt 80,8,0
mavv 136,96,36
s_add_u32 m0, s32, 1024
rt 82,8,2048
mavv 140,100,36
rqv 32,0,36864
mavv 144,104,36
s_add_u32 s61, s61, s30
mavv 148,108,36
rt 84,9,0
mavv 152,112,36
dm 13,20,61
mavv 156,116,36
rt 86,9,2048
mavv 160,96,40
s_add_u32 m0, s33, 51200
mavv 164,100,40
s_add_u32 s67, s67, s31
rqv 36,0,38912
mavv 168,104,40
rqv 48,0,45056
mavv 172,108,40
dm 14,24,67
mavv 176,112,40
s_add_u32 m0, s32, 2048
mavv 180,116,40
s_add_u32 s61, s61, s30
mavv 184,96,44
rqv 40,0,40960
mavv 188,100,44
rqv 52,0,47104
mavv 192,104,44
dm 12,20,61
mavv 196,108,44
s_add_u32 m0, s33, 52224
mavv 200,112,44
s_add_u32 s67, s67, s31
mavv 204,116,44
mavv 208,96,56
rqv 44,0,43008
mavv 212,100,56
dm 15,24,67
mavv 216,104,56
s_add_u32 m0, s32, 3072
mavv 220,108,56
mavv 224,112,56
s_add_u32 s61, s61, s30
mavv 228,116,56
mavv 232,96,60
dm 13,20,61
mavv 236,100,60
mavv 240,104,60
s_add_u32 s29, s29, s65
mavv 244,108,60
mavv 248,112,60
mavv 252,116,60
s_waitcnt vmcnt(17) lgkmcnt(0)
s_barrier
.Lk1_AT_8:
mavv 64,64,24
rt 96,4,16384
mavv 68,68,24
s_add_u32 m0, s33, 0
mavv 72,72,24
s_mov_b32 s67, s29
mavv 76,76,24
rt 98,4,18432
mavv 80,80,24
dm 14,24,67
mavv 84,84,24
rt 100,5,16384
mavv 88,64,28
s_add_u32 m0, s32, 16384
rt 102,5,18432
mavv 92,68,28
rqv 24,1,32768
mavv 96,72,28
s_add_u32 s61, s28, s64
mavv 100,76,28
rt 104,6,16384
mavv 104,80,28
dm 12,20,61
mavv 108,84,28
s_add_u32 m0, s33, 1024
mavv 112,64,32
rt 106,6,18432
s_add_u32 s67, s67, s31
mavv 116,68,32
rt 108,7,16384
mavv 120,72,32
rqv 28,1,34816
dm 15,24,67
mavv 124,76,32
rt 110,7,18432
mavv 128,80,32
s_add_u32 m0, s32, 17408
mavv 132,84,32
rt 112,8,16384
mavv 136,64,36
s_add_u32 s61, s61, s30
rt 114,8,18432
mavv 140,68,36
rqv 32,1,36864
mavv 144,72,36
dm 13,20,61
mavv 148,76,36
s_add_u32 m0, s33, 2048
mavv 152,80,36
rt 116,9,16384
mavv 156,84,36
s_add_u32 s67, s67, s31
mavv 160,64,40
rt 118,9,18432
dm 14,24,67
mavv 164,68,40
rqv 36,1,38912
mavv 168,72,40
s_add_u32 m0, s32, 18432
mavv 172,76,40
rqv 56,1,45056
mavv 176,80,40
s_add_u32 s61, s61, s30
mavv 180,84,40
dm 12,20,61
mavv 184,64,44
s_add_u32 m0, s33, 3072
mavv 188,68,44
rqv 40,1,40960
mavv 192,72,44
rqv 60,1,47104
mavv 196,76,44
s_add_u32 s67, s67, s31
mavv 200,80,44
dm 15,24,67
mavv 204,84,44
s_add_u32 m0, s32, 19456
mavv 208,64,48
rqv 44,1,43008
mavv 212,68,48
s_add_u32 s61, s61, s30
mavv 216,72,48
dm 13,20,61
mavv 220,76,48
s_add_u32 s29, s29, s65
mavv 224,80,48
mavv 228,84,48
s_add_u32 s28, s28, 128
mavv 232,64,52
s_add_u32 s74, s78, .Lk1_AT_13-.Lk1_base
s_addc_u32 s75, s79, 0
mavv 236,68,52
mavv 240,72,52
global_load_dword v19, v236, s[74:75]
mavv 244,76,52
mavv 248,80,52
mavv 252,84,52
s_mov_b64 s[20:21], s[36:37]
s_mov_b64 s[22:23], s[38:39]
s_mov_b32 s28, s44
s_mov_b32 s30, s46
s_lshl_b32 s64, s46, 4
v_mov_b32 v12, v224
v_mov_b32 v13, v225
s_waitcnt vmcnt(17) lgkmcnt(0)
s_barrier
.Lk1_AT_9:
mavv 64,96,24
rt 64,4,32768
mavv 68,100,24
s_add_u32 m0, s33, 16384
mavv 72,104,24
s_mov_b32 s67, s29
mavv 76,108,24
rt 66,4,34816
mavv 80,112,24
dm 14,24,67
mavv 84,116,24
rt 68,5,32768
mavv 88,96,28
s_add_u32 m0, s32, 32768
rt 70,5,34816
mavv 92,100,28
rqv 24,2,0
mavv 96,104,28
s_mov_b32 s61, s28
mavv 100,108,28
dm 12,20,61
mavv 104,112,28
rt 72,6,32768
mavv 108,116,28
s_add_u32 m0, s33, 17408
mavv 112,96,32
rt 74,6,34816
s_add_u32 s67, s67, s31
rt 76,7,32768
mavv 116,100,32
rqv 28,2,2048
mavv 120,104,32
dm 15,24,67
mavv 124,108,32
rt 78,7,34816
mavv 128,112,32
s_add_u32 m0, s32, 33792
mavv 132,116,32
s_add_u32 s61, s61, s30
mavv 136,96,36
rt 80,8,32768
dm 13,20,61
mavv 140,100,36
rt 82,8,34816
mavv 144,104,36
rqv 32,2,4096
s_add_u32 m0, s33, 18432
mavv 148,108,36
rt 84,9,32768
mavv 152,112,36
s_add_u32 s67, s67, s31
mavv 156,116,36
rt 86,9,34816
mavv 160,96,40
dm 14,24,67
mavv 164,100,40
s_add_u32 m0, s32, 34816
rqv 36,2,6144
mavv 168,104,40
rqv 48,2,12288
mavv 172,108,40
s_add_u32 s61, s61, s30
mavv 176,112,40
dm 12,20,61
mavv 180,116,40
s_add_u32 m0, s33, 19456
mavv 184,96,44
rqv 40,2,8192
mavv 188,100,44
s_add_u32 s67, s67, s31
mavv 192,104,44
rqv 52,2,14336
mavv 196,108,44
dm 15,24,67
mavv 200,112,44
s_add_u32 m0, s32, 35840
mavv 204,116,44
s_add_u32 s61, s61, s30
mavv 208,96,56
rqv 44,2,10240
mavv 212,100,56
dm 13,20,61
mavv 216,104,56
s_add_u32 s29, s29, s65
mavv 220,108,56
s_add_u32 s74, s78, .Lk1_AT_pk-.Lk1_base
s_addc_u32 s75, s79, 0
mavv 224,112,56
mavv 228,116,56
global_load_dword v19, v236, s[74:75]
mavv 232,96,60
buffer_load_dword v19, v252, s[40:43], s89 offen
mavv 236,100,60
mavv 240,104,60
s_add_u32 s89, s89, 0x800
mavv 244,108,60
mavv 248,112,60
mavv 252,116,60
s_mov_b64 s[24:25], s[40:41]
s_mov_b64 s[26:27], s[42:43]
s_mov_b32 s29, s45
s_mov_b32 s31, s47
s_lshl_b32 s65, s47, 4
v_mov_b32 v14, v226
v_mov_b32 v15, v227
s_waitcnt vmcnt(19) lgkmcnt(0)
s_barrier
.Lk1_AT_10:
mavv 64,64,24
rt 96,4,49152
mavv 68,68,24
s_add_u32 m0, s33, 32768
mavv 72,72,24
s_mov_b32 s67, s29
mavv 76,76,24
rt 98,4,51200
mavv 80,80,24
dm 14,24,67
mavv 84,84,24
rt 100,5,49152
mavv 88,64,28
s_add_u32 m0, s32, 49152
rt 102,5,51200
mavv 92,68,28
rqv 24,3,0
mavv 96,72,28
s_add_u32 s61, s28, s64
mavv 100,76,28
rt 104,6,49152
mavv 104,80,28
dm 12,20,61
mavv 108,84,28
s_add_u32 m0, s33, 33792
mavv 112,64,32
rt 106,6,51200
s_add_u32 s67, s67, s31
mavv 116,68,32
rt 108,7,49152
mavv 120,72,32
rqv 28,3,2048
dm 15,24,67
mavv 124,76,32
rt 110,7,51200
mavv 128,80,32
s_add_u32 m0, s32, 50176
mavv 132,84,32
rt 112,8,49152
mavv 136,64,36
s_add_u32 s61, s61, s30
rt 114,8,51200
mavv 140,68,36
rqv 32,3,4096
mavv 144,72,36
dm 13,20,61
mavv 148,76,36
s_add_u32 m0, s33, 34816
mavv 152,80,36
rt 116,9,49152
mavv 156,84,36
s_add_u32 s67, s67, s31
mavv 160,64,40
rt 118,9,51200
dm 14,24,67
mavv 164,68,40
rqv 36,3,6144
mavv 168,72,40
s_add_u32 m0, s32, 51200
mavv 172,76,40
rqv 56,3,12288
mavv 176,80,40
s_add_u32 s61, s61, s30
mavv 180,84,40
dm 12,20,61
mavv 184,64,44
s_add_u32 m0, s33, 35840
mavv 188,68,44
rqv 40,3,8192
mavv 192,72,44
rqv 60,3,14336
mavv 196,76,44
s_add_u32 s67, s67, s31
mavv 200,80,44
dm 15,24,67
mavv 204,84,44
s_add_u32 m0, s32, 52224
mavv 208,64,48
rqv 44,3,10240
mavv 212,68,48
s_add_u32 s61, s61, s30
mavv 216,72,48
dm 13,20,61
mavv 220,76,48
s_add_u32 s29, s29, s65
mavv 224,80,48
mavv 228,84,48
s_add_u32 s28, s28, 128
mavv 232,64,52
s_add_u32 s74, s78, .Lk1_BTT-.Lk1_base
s_addc_u32 s75, s79, 0
mavv 236,68,52
mavv 240,72,52
global_load_dword v19, v236, s[74:75]
mavv 244,76,52
mavv 248,80,52
mavv 252,84,52
s_waitcnt vmcnt(19) lgkmcnt(0)
s_barrier
.Lk1_AT_11:
mavv 64,96,24
rt 64,4,0
mavv 68,100,24
s_add_u32 m0, s33, 49152
mavv 72,104,24
s_mov_b32 s67, s29
mavv 76,108,24
rt 66,4,2048
mavv 80,112,24
dm 14,24,67
mavv 84,116,24
rt 68,5,0
mavv 88,96,28
s_add_u32 m0, s32, 65536
rt 70,5,2048
mavv 92,100,28
rqv 24,0,0
mavv 96,104,28
s_mov_b32 s61, s28
mavv 100,108,28
rt 72,6,0
mavv 104,112,28
dm 12,20,61
mavv 108,116,28
rt 74,6,2048
mavv 112,96,32
s_add_u32 m0, s33, 50176
s_add_u32 s67, s67, s31
mavv 116,100,32
rt 76,7,0
mavv 120,104,32
rqv 28,0,2048
mavv 124,108,32
dm 15,24,67
mavv 128,112,32
rt 78,7,2048
mavv 132,116,32
s_add_u32 m0, s32, 66560
mavv 136,96,36
rt 80,8,0
s_add_u32 s61, s61, s30
rt 82,8,2048
mavv 140,100,36
rqv 32,0,4096
mavv 144,104,36
dm 13,20,61
mavv 148,108,36
rt 84,9,0
mavv 152,112,36
s_add_u32 m0, s33, 51200
mavv 156,116,36
rt 86,9,2048
mavv 160,96,40
s_add_u32 s67, s67, s31
mavv 164,100,40
dm 14,24,67
rqv 36,0,6144
mavv 168,104,40
rqv 48,0,12288
mavv 172,108,40
s_add_u32 m0, s32, 67584
mavv 176,112,40
s_add_u32 s61, s61, s30
mavv 180,116,40
dm 12,20,61
mavv 184,96,44
rqv 40,0,8192
mavv 188,100,44
s_add_u32 m0, s33, 52224
mavv 192,104,44
rqv 52,0,14336
mavv 196,108,44
s_add_u32 s67, s67, s31
mavv 200,112,44
dm 15,24,67
mavv 204,116,44
s_add_u32 m0, s32, 68608
mavv 208,96,56
rqv 44,0,10240
mavv 212,100,56
s_add_u32 s61, s61, s30
mavv 216,104,56
mavv 220,108,56
dm 13,20,61
mavv 224,112,56
s_add_u32 s29, s29, s65
mavv 228,116,56
mavv 232,96,60
s_add_u32 s74, s78, .Lk1_BNT-.Lk1_base
s_addc_u32 s75, s79, 0
mavv 236,100,60
)ASM" ::: "memory");
  asm volatile(R"ASM(
mavv 240,104,60
global_load_dword v19, v236, s[74:75]
mavv 244,108,60
mavv 248,112,60
mavv 252,116,60
s_waitcnt vmcnt(18) lgkmcnt(0)
s_barrier
.Lk1_AT_12:
mavv 64,64,24
rt 96,4,16384
mavv 68,68,24
s_add_u32 m0, s33, 0
mavv 72,72,24
s_mov_b32 s67, s29
mavv 76,76,24
rt 98,4,18432
mavv 80,80,24
dm 14,24,67
mavv 84,84,24
rt 100,5,16384
mavv 88,64,28
s_add_u32 m0, s32, 81920
rt 102,5,18432
mavv 92,68,28
rqv 24,1,0
mavv 96,72,28
s_add_u32 s61, s28, s64
mavv 100,76,28
dm 12,20,61
mavv 104,80,28
rt 104,6,16384
s_add_u32 m0, s33, 1024
mavv 108,84,28
rt 106,6,18432
mavv 112,64,32
s_add_u32 s67, s67, s31
rt 108,7,16384
mavv 116,68,32
rqv 28,1,2048
mavv 120,72,32
dm 15,24,67
mavv 124,76,32
s_add_u32 m0, s32, 82944
mavv 128,80,32
rt 110,7,18432
s_add_u32 s61, s61, s30
mavv 132,84,32
rt 112,8,16384
mavv 136,64,36
dm 13,20,61
rt 114,8,18432
mavv 140,68,36
rqv 32,1,4096
mavv 144,72,36
s_add_u32 m0, s33, 2048
mavv 148,76,36
s_add_u32 s67, s67, s31
mavv 152,80,36
rt 116,9,16384
mavv 156,84,36
dm 14,24,67
mavv 160,64,40
rt 118,9,18432
s_add_u32 m0, s32, 83968
mavv 164,68,40
rqv 36,1,6144
mavv 168,72,40
s_add_u32 s61, s61, s30
mavv 172,76,40
rqv 56,1,12288
mavv 176,80,40
dm 12,20,61
mavv 180,84,40
s_add_u32 m0, s33, 3072
mavv 184,64,44
s_add_u32 s67, s67, s31
mavv 188,68,44
rqv 40,1,8192
mavv 192,72,44
dm 15,24,67
mavv 196,76,44
rqv 60,1,14336
mavv 200,80,44
s_add_u32 m0, s32, 84992
mavv 204,84,44
s_add_u32 s61, s61, s30
mavv 208,64,48
dm 13,20,61
mavv 212,68,48
rqv 44,1,10240
mavv 216,72,48
s_add_u32 s29, s29, s65
mavv 220,76,48
s_add_u32 s28, s28, 128
mavv 224,80,48
s_add_u32 s74, s78, .Lk1_BTT+4096-.Lk1_base
s_addc_u32 s75, s79, 0
mavv 228,84,48
global_load_dword v19, v236, s[74:75]
mavv 232,64,52
buffer_load_dword v19, v252, s[40:43], s89 offen
mavv 236,68,52
mavv 240,72,52
s_add_u32 s89, s89, 0x800
mavv 244,76,52
mavv 248,80,52
mavv 252,84,52
v_mov_b32 v4, v228
v_mov_b32 v5, v229
v_mov_b32 v6, v230
v_mov_b32 v7, v231
v_mov_b32 v8, v232
v_mov_b32 v9, v233
v_mov_b32 v10, v234
v_mov_b32 v11, v235
s_waitcnt vmcnt(19) lgkmcnt(0)
s_barrier
.Lk1_AT_13:
mavv 64,96,24
rt 64,4,32768
mavv 68,100,24
s_add_u32 m0, s33, 16384
mavv 72,104,24
rt 66,4,34816
mavv 76,108,24
s_mov_b32 s67, s29
mavv 80,112,24
rt 68,5,32768
mavv 84,116,24
dm 14,24,67
mavv 88,96,28
rt 70,5,34816
s_add_u32 m0, s32, 0
rt 72,6,32768
mavv 92,100,28
rqv 24,0,32768
mavv 96,104,28
s_mov_b32 s61, s28
mavv 100,108,28
rt 74,6,34816
mavv 104,112,28
dm 12,20,61
rt 76,7,32768
mavv 108,116,28
rt 78,7,34816
mavv 112,96,32
s_add_u32 m0, s33, 17408
s_add_u32 s67, s67, s31
mavv 116,100,32
rt 80,8,32768
rqv 28,0,34816
mavv 120,104,32
rt 82,8,34816
mavv 124,108,32
dm 15,24,67
mavv 128,112,32
rt 84,9,32768
mavv 132,116,32
s_add_u32 m0, s32, 1024
mavv 136,96,36
rt 86,9,34816
s_add_u32 s61, s61, s30
rt 88,10,32768
mavv 140,100,36
rqv 32,0,36864
mavv 144,104,36
dm 13,20,61
mavv 148,108,36
rt 90,10,34816
mavv 152,112,36
s_add_u32 m0, s33, 18432
rt 92,11,32768
mavv 156,116,36
rt 94,11,34816
mavv 160,96,40
s_add_u32 s67, s67, s31
mavv 164,100,40
dm 14,24,67
rqv 36,0,38912
mavv 168,104,40
rqv 48,0,45056
mavv 172,108,40
s_add_u32 m0, s32, 2048
mavv 176,112,40
s_add_u32 s61, s61, s30
mavv 180,116,40
dm 12,20,61
mavv 184,96,44
rqv 40,0,40960
mavv 188,100,44
s_add_u32 m0, s33, 19456
mavv 192,104,44
rqv 52,0,47104
mavv 196,108,44
s_add_u32 s67, s67, s31
mavv 200,112,44
dm 15,24,67
mavv 204,116,44
s_add_u32 m0, s32, 3072
mavv 208,96,56
rqv 44,0,43008
mavv 212,100,56
s_add_u32 s61, s61, s30
mavv 216,104,56
mavv 220,108,56
dm 13,20,61
mavv 224,112,56
s_add_u32 s29, s29, s65
mavv 228,116,56
mavv 232,96,60
s_add_u32 s74, s78, .Lk1_BNT+4096-.Lk1_base
s_addc_u32 s75, s79, 0
mavv 236,100,60
mavv 240,104,60
global_load_dword v19, v236, s[74:75]
mavv 244,108,60
mavv 248,112,60
mavv 252,116,60
.Lk1_AT_pk:
s_cmp_lg_u32 s34, 0
s_cbranch_scc1 .Lk1_BNT
s_branch .Lk1_BTT
.Lk1_AN:
s_waitcnt vmcnt(20) lgkmcnt(0)
s_barrier
.Lk1_AN_0:
zavv 64,24,64
rt 96,4,16384
zavv 72,24,68
s_add_u32 m0, s33, 0
zavv 80,24,72
s_mov_b32 s67, s29
zavv 88,24,76
rt 98,4,18432
zavv 96,24,80
dm 14,24,67
zavv 104,24,84
rt 100,5,16384
zavv 68,28,64
s_add_u32 m0, s32, 81920
rt 102,5,18432
zavv 76,28,68
rqv 24,1,0
zavv 84,28,72
s_add_u32 s61, s28, s64
zavv 92,28,76
dm 12,20,61
zavv 100,28,80
rt 104,6,16384
zavv 108,28,84
s_add_u32 m0, s33, 1024
zavv 112,32,64
rt 106,6,18432
s_add_u32 s67, s67, s31
rt 108,7,16384
zavv 120,32,68
rqv 28,1,2048
zavv 128,32,72
dm 15,24,67
zavv 136,32,76
rt 110,7,18432
zavv 144,32,80
s_add_u32 m0, s32, 82944
zavv 152,32,84
s_add_u32 s61, s61, s30
zavv 116,36,64
rt 112,8,16384
dm 13,20,61
zavv 124,36,68
rt 114,8,18432
zavv 132,36,72
rqv 32,1,4096
s_add_u32 m0, s33, 2048
zavv 140,36,76
rt 116,9,16384
zavv 148,36,80
s_add_u32 s67, s67, s31
zavv 156,36,84
rt 118,9,18432
zavv 160,40,64
dm 14,24,67
zavv 168,40,68
s_add_u32 m0, s32, 83968
rqv 36,1,6144
zavv 176,40,72
rqv 56,1,12288
zavv 184,40,76
s_add_u32 s61, s61, s30
zavv 192,40,80
dm 12,20,61
zavv 200,40,84
s_add_u32 m0, s33, 3072
zavv 164,44,64
rqv 40,1,8192
zavv 172,44,68
s_add_u32 s67, s67, s31
zavv 180,44,72
rqv 60,1,14336
zavv 188,44,76
dm 15,24,67
zavv 196,44,80
s_add_u32 m0, s32, 84992
zavv 204,44,84
s_add_u32 s61, s61, s30
zavv 208,48,64
rqv 44,1,10240
zavv 216,48,68
dm 13,20,61
zavv 224,48,72
s_add_u32 s29, s29, s65
zavv 232,48,76
s_add_u32 s28, s28, 128
zavv 240,48,80
zavv 248,48,84
s_add_u32 s74, s78, .Lk1_AN_5-.Lk1_base
s_addc_u32 s75, s79, 0
zavv 212,52,64
global_load_dword v19, v236, s[74:75]
zavv 220,52,68
zavv 228,52,72
buffer_load_dword v19, v253, s[36:39], s19 offen offset:0
zavv 236,52,76
zavv 244,52,80
zavv 252,52,84
s_waitcnt vmcnt(22) lgkmcnt(0)
s_barrier
.Lk1_AN_1:
mavv 64,24,96
rt 64,4,32768
mavv 72,24,100
s_add_u32 m0, s33, 16384
mavv 80,24,104
s_mov_b32 s67, s29
mavv 88,24,108
rt 66,4,34816
mavv 96,24,112
dm 14,24,67
mavv 104,24,116
rt 68,5,32768
mavv 68,28,96
s_add_u32 m0, s32, 0
mavv 76,28,100
rt 70,5,34816
mavv 84,28,104
rqv 24,0,32768
mavv 92,28,108
s_mov_b32 s61, s28
mavv 100,28,112
rt 72,6,32768
mavv 108,28,116
dm 12,20,61
mavv 112,32,96
rt 74,6,34816
s_add_u32 m0, s33, 17408
rt 76,7,32768
mavv 120,32,100
rqv 28,0,34816
mavv 128,32,104
s_add_u32 s67, s67, s31
mavv 136,32,108
rt 78,7,34816
mavv 144,32,112
dm 15,24,67
mavv 152,32,116
rt 80,8,32768
mavv 116,36,96
s_add_u32 m0, s32, 1024
rt 82,8,34816
mavv 124,36,100
rqv 32,0,36864
mavv 132,36,104
s_add_u32 s61, s61, s30
mavv 140,36,108
rt 84,9,32768
mavv 148,36,112
dm 13,20,61
mavv 156,36,116
rt 86,9,34816
mavv 160,40,96
s_add_u32 m0, s33, 18432
mavv 168,40,100
s_add_u32 s67, s67, s31
rqv 36,0,38912
mavv 176,40,104
rqv 48,0,45056
mavv 184,40,108
dm 14,24,67
mavv 192,40,112
s_add_u32 m0, s32, 2048
mavv 200,40,116
s_add_u32 s61, s61, s30
mavv 164,44,96
rqv 40,0,40960
mavv 172,44,100
rqv 52,0,47104
mavv 180,44,104
dm 12,20,61
mavv 188,44,108
s_add_u32 m0, s33, 19456
mavv 196,44,112
s_add_u32 s67, s67, s31
mavv 204,44,116
mavv 208,56,96
rqv 44,0,43008
mavv 216,56,100
dm 15,24,67
mavv 224,56,104
s_add_u32 m0, s32, 3072
mavv 232,56,108
mavv 240,56,112
s_add_u32 s61, s61, s30
mavv 248,56,116
mavv 212,60,96
dm 13,20,61
mavv 220,60,100
mavv 228,60,104
s_add_u32 s29, s29, s65
mavv 236,60,108
mavv 244,60,112
mavv 252,60,116
s_waitcnt vmcnt(18) lgkmcnt(0)
s_barrier
.Lk1_AN_2:
mavv 64,24,64
rt 96,4,49152
mavv 72,24,68
s_add_u32 m0, s33, 32768
mavv 80,24,72
s_mov_b32 s67, s29
mavv 88,24,76
rt 98,4,51200
mavv 96,24,80
dm 14,24,67
mavv 104,24,84
rt 100,5,49152
mavv 68,28,64
s_add_u32 m0, s32, 16384
mavv 76,28,68
rt 102,5,51200
mavv 84,28,72
rqv 24,1,32768
s_add_u32 s61, s28, s64
mavv 92,28,76
rt 104,6,49152
mavv 100,28,80
dm 12,20,61
mavv 108,28,84
rt 106,6,51200
mavv 112,32,64
s_add_u32 m0, s33, 33792
rt 108,7,49152
mavv 120,32,68
rqv 28,1,34816
mavv 128,32,72
s_add_u32 s67, s67, s31
mavv 136,32,76
rt 110,7,51200
mavv 144,32,80
dm 15,24,67
mavv 152,32,84
s_add_u32 m0, s32, 17408
mavv 116,36,64
rt 112,8,49152
s_add_u32 s61, s61, s30
mavv 124,36,68
rt 114,8,51200
mavv 132,36,72
rqv 32,1,36864
mavv 140,36,76
dm 13,20,61
mavv 148,36,80
rt 116,9,49152
mavv 156,36,84
s_add_u32 m0, s33, 34816
mavv 160,40,64
rt 118,9,51200
s_add_u32 s67, s67, s31
mavv 168,40,68
rqv 36,1,38912
mavv 176,40,72
dm 14,24,67
mavv 184,40,76
rqv 56,1,45056
mavv 192,40,80
s_add_u32 m0, s32, 18432
mavv 200,40,84
s_add_u32 s61, s61, s30
mavv 164,44,64
rqv 40,1,40960
mavv 172,44,68
dm 12,20,61
mavv 180,44,72
rqv 60,1,47104
mavv 188,44,76
s_add_u32 m0, s33, 35840
mavv 196,44,80
s_add_u32 s67, s67, s31
mavv 204,44,84
dm 15,24,67
mavv 208,48,64
rqv 44,1,43008
mavv 216,48,68
s_add_u32 m0, s32, 19456
mavv 224,48,72
s_add_u32 s61, s61, s30
mavv 232,48,76
mavv 240,48,80
dm 13,20,61
mavv 248,48,84
mavv 212,52,64
s_add_u32 s29, s29, s65
mavv 220,52,68
mavv 228,52,72
s_add_u32 s28, s28, 128
mavv 236,52,76
mavv 244,52,80
mavv 252,52,84
s_waitcnt vmcnt(16) lgkmcnt(0)
s_barrier
.Lk1_AN_3:
mavv 64,24,96
rt 64,4,0
mavv 72,24,100
s_add_u32 m0, s33, 49152
mavv 80,24,104
s_mov_b32 s67, s29
mavv 88,24,108
rt 66,4,2048
mavv 96,24,112
dm 14,24,67
mavv 104,24,116
rt 68,5,0
mavv 68,28,96
s_add_u32 m0, s32, 32768
rt 70,5,2048
mavv 76,28,100
rqv 24,2,0
mavv 84,28,104
s_mov_b32 s61, s28
mavv 92,28,108
rt 72,6,0
mavv 100,28,112
dm 12,20,61
mavv 108,28,116
rt 74,6,2048
mavv 112,32,96
s_add_u32 m0, s33, 50176
s_add_u32 s67, s67, s31
mavv 120,32,100
rt 76,7,0
mavv 128,32,104
rqv 28,2,2048
mavv 136,32,108
dm 15,24,67
mavv 144,32,112
rt 78,7,2048
mavv 152,32,116
s_add_u32 m0, s32, 33792
mavv 116,36,96
rt 80,8,0
s_add_u32 s61, s61, s30
rt 82,8,2048
mavv 124,36,100
rqv 32,2,4096
mavv 132,36,104
dm 13,20,61
mavv 140,36,108
rt 84,9,0
mavv 148,36,112
s_add_u32 m0, s33, 51200
mavv 156,36,116
rt 86,9,2048
mavv 160,40,96
s_add_u32 s67, s67, s31
mavv 168,40,100
dm 14,24,67
rqv 36,2,6144
mavv 176,40,104
rqv 48,2,12288
mavv 184,40,108
s_add_u32 m0, s32, 34816
mavv 192,40,112
s_add_u32 s61, s61, s30
mavv 200,40,116
dm 12,20,61
mavv 164,44,96
rqv 40,2,8192
mavv 172,44,100
s_add_u32 m0, s33, 52224
mavv 180,44,104
rqv 52,2,14336
mavv 188,44,108
s_add_u32 s67, s67, s31
mavv 196,44,112
dm 15,24,67
mavv 204,44,116
s_add_u32 m0, s32, 35840
mavv 208,56,96
rqv 44,2,10240
mavv 216,56,100
)ASM" ::: "memory");
  asm volatile(R"ASM(
s_add_u32 s61, s61, s30
mavv 224,56,104
mavv 232,56,108
dm 13,20,61
mavv 240,56,112
s_add_u32 s29, s29, s65
mavv 248,56,116
mavv 212,60,96
buffer_load_dword v19, v252, s[40:43], s89 offen
mavv 220,60,100
mavv 228,60,104
s_add_u32 s89, s89, 0x800
mavv 236,60,108
mavv 244,60,112
mavv 252,60,116
s_waitcnt vmcnt(17) lgkmcnt(0)
s_barrier
.Lk1_AN_4:
mavv 64,24,64
rt 96,4,16384
mavv 72,24,68
s_add_u32 m0, s33, 0
mavv 80,24,72
s_mov_b32 s67, s29
mavv 88,24,76
rt 98,4,18432
mavv 96,24,80
dm 14,24,67
mavv 104,24,84
rt 100,5,16384
mavv 68,28,64
s_add_u32 m0, s32, 49152
rt 102,5,18432
mavv 76,28,68
rqv 24,3,0
mavv 84,28,72
s_add_u32 s61, s28, s64
mavv 92,28,76
rt 104,6,16384
mavv 100,28,80
dm 12,20,61
mavv 108,28,84
s_add_u32 m0, s33, 1024
mavv 112,32,64
rt 106,6,18432
s_add_u32 s67, s67, s31
mavv 120,32,68
rt 108,7,16384
mavv 128,32,72
rqv 28,3,2048
dm 15,24,67
mavv 136,32,76
rt 110,7,18432
mavv 144,32,80
s_add_u32 m0, s32, 50176
mavv 152,32,84
rt 112,8,16384
mavv 116,36,64
s_add_u32 s61, s61, s30
rt 114,8,18432
mavv 124,36,68
rqv 32,3,4096
mavv 132,36,72
dm 13,20,61
mavv 140,36,76
s_add_u32 m0, s33, 2048
mavv 148,36,80
rt 116,9,16384
mavv 156,36,84
s_add_u32 s67, s67, s31
mavv 160,40,64
rt 118,9,18432
dm 14,24,67
mavv 168,40,68
rqv 36,3,6144
mavv 176,40,72
s_add_u32 m0, s32, 51200
mavv 184,40,76
rqv 56,3,12288
mavv 192,40,80
s_add_u32 s61, s61, s30
mavv 200,40,84
dm 12,20,61
mavv 164,44,64
s_add_u32 m0, s33, 3072
mavv 172,44,68
rqv 40,3,8192
mavv 180,44,72
rqv 60,3,14336
mavv 188,44,76
s_add_u32 s67, s67, s31
mavv 196,44,80
dm 15,24,67
mavv 204,44,84
s_add_u32 m0, s32, 52224
mavv 208,48,64
rqv 44,3,10240
mavv 216,48,68
s_add_u32 s61, s61, s30
mavv 224,48,72
dm 13,20,61
mavv 232,48,76
s_add_u32 s29, s29, s65
mavv 240,48,80
mavv 248,48,84
s_add_u32 s28, s28, 128
mavv 212,52,64
s_add_u32 s74, s78, .Lk1_AN_9-.Lk1_base
s_addc_u32 s75, s79, 0
mavv 220,52,68
mavv 228,52,72
global_load_dword v19, v236, s[74:75]
mavv 236,52,76
mavv 244,52,80
mavv 252,52,84
s_waitcnt vmcnt(18) lgkmcnt(0)
s_barrier
.Lk1_AN_5:
mavv 64,24,96
rt 64,4,32768
mavv 72,24,100
s_add_u32 m0, s33, 16384
mavv 80,24,104
s_mov_b32 s67, s29
mavv 88,24,108
rt 66,4,34816
mavv 96,24,112
dm 14,24,67
mavv 104,24,116
rt 68,5,32768
mavv 68,28,96
s_add_u32 m0, s32, 65536
mavv 76,28,100
rt 70,5,34816
mavv 84,28,104
rqv 24,0,0
mavv 92,28,108
s_mov_b32 s61, s28
mavv 100,28,112
rt 72,6,32768
mavv 108,28,116
dm 12,20,61
mavv 112,32,96
rt 74,6,34816
s_add_u32 m0, s33, 17408
rt 76,7,32768
mavv 120,32,100
rqv 28,0,2048
mavv 128,32,104
s_add_u32 s67, s67, s31
mavv 136,32,108
rt 78,7,34816
mavv 144,32,112
dm 15,24,67
mavv 152,32,116
rt 80,8,32768
mavv 116,36,96
s_add_u32 m0, s32, 66560
rt 82,8,34816
mavv 124,36,100
rqv 32,0,4096
mavv 132,36,104
s_add_u32 s61, s61, s30
mavv 140,36,108
rt 84,9,32768
mavv 148,36,112
dm 13,20,61
mavv 156,36,116
rt 86,9,34816
mavv 160,40,96
s_add_u32 m0, s33, 18432
mavv 168,40,100
s_add_u32 s67, s67, s31
rqv 36,0,6144
mavv 176,40,104
rqv 48,0,12288
mavv 184,40,108
dm 14,24,67
mavv 192,40,112
s_add_u32 m0, s32, 67584
mavv 200,40,116
s_add_u32 s61, s61, s30
mavv 164,44,96
rqv 40,0,8192
mavv 172,44,100
rqv 52,0,14336
mavv 180,44,104
dm 12,20,61
mavv 188,44,108
s_add_u32 m0, s33, 19456
mavv 196,44,112
s_add_u32 s67, s67, s31
mavv 204,44,116
mavv 208,56,96
rqv 44,0,10240
mavv 216,56,100
dm 15,24,67
mavv 224,56,104
s_add_u32 m0, s32, 68608
mavv 232,56,108
mavv 240,56,112
s_add_u32 s61, s61, s30
mavv 248,56,116
mavv 212,60,96
dm 13,20,61
mavv 220,60,100
mavv 228,60,104
s_add_u32 s29, s29, s65
mavv 236,60,108
mavv 244,60,112
mavv 252,60,116
s_waitcnt vmcnt(17) lgkmcnt(0)
s_barrier
.Lk1_AN_6:
mavv 64,24,64
rt 96,4,49152
mavv 72,24,68
s_add_u32 m0, s33, 32768
mavv 80,24,72
s_mov_b32 s67, s29
mavv 88,24,76
rt 98,4,51200
mavv 96,24,80
dm 14,24,67
mavv 104,24,84
rt 100,5,49152
mavv 68,28,64
s_add_u32 m0, s32, 81920
rt 102,5,51200
mavv 76,28,68
rqv 24,1,0
mavv 84,28,72
s_add_u32 s61, s28, s64
mavv 92,28,76
rt 104,6,49152
mavv 100,28,80
dm 12,20,61
mavv 108,28,84
s_add_u32 m0, s33, 33792
mavv 112,32,64
rt 106,6,51200
s_add_u32 s67, s67, s31
mavv 120,32,68
rt 108,7,49152
mavv 128,32,72
rqv 28,1,2048
dm 15,24,67
mavv 136,32,76
rt 110,7,51200
mavv 144,32,80
s_add_u32 m0, s32, 82944
mavv 152,32,84
rt 112,8,49152
mavv 116,36,64
s_add_u32 s61, s61, s30
rt 114,8,51200
mavv 124,36,68
rqv 32,1,4096
mavv 132,36,72
dm 13,20,61
mavv 140,36,76
s_add_u32 m0, s33, 34816
mavv 148,36,80
rt 116,9,49152
mavv 156,36,84
s_add_u32 s67, s67, s31
mavv 160,40,64
rt 118,9,51200
dm 14,24,67
mavv 168,40,68
rqv 36,1,6144
mavv 176,40,72
s_add_u32 m0, s32, 83968
mavv 184,40,76
rqv 56,1,12288
mavv 192,40,80
s_add_u32 s61, s61, s30
mavv 200,40,84
dm 12,20,61
mavv 164,44,64
s_add_u32 m0, s33, 35840
mavv 172,44,68
rqv 40,1,8192
mavv 180,44,72
rqv 60,1,14336
mavv 188,44,76
s_add_u32 s67, s67, s31
mavv 196,44,80
dm 15,24,67
mavv 204,44,84
s_add_u32 m0, s32, 84992
mavv 208,48,64
rqv 44,1,10240
mavv 216,48,68
s_add_u32 s61, s61, s30
mavv 224,48,72
dm 13,20,61
mavv 232,48,76
s_add_u32 s29, s29, s65
mavv 240,48,80
mavv 248,48,84
s_add_u32 s28, s28, 128
mavv 212,52,64
buffer_load_dword v19, v252, s[40:43], s89 offen
mavv 220,52,68
mavv 228,52,72
s_add_u32 s89, s89, 0x800
mavv 236,52,76
mavv 244,52,80
mavv 252,52,84
s_waitcnt vmcnt(17) lgkmcnt(0)
s_barrier
.Lk1_AN_7:
mavv 64,24,96
rt 64,4,0
mavv 72,24,100
s_add_u32 m0, s33, 49152
mavv 80,24,104
s_mov_b32 s67, s29
mavv 88,24,108
rt 66,4,2048
mavv 96,24,112
dm 14,24,67
mavv 104,24,116
rt 68,5,0
mavv 68,28,96
s_add_u32 m0, s32, 0
mavv 76,28,100
rt 70,5,2048
mavv 84,28,104
rqv 24,0,32768
mavv 92,28,108
s_mov_b32 s61, s28
mavv 100,28,112
rt 72,6,0
mavv 108,28,116
dm 12,20,61
mavv 112,32,96
rt 74,6,2048
s_add_u32 m0, s33, 50176
rt 76,7,0
mavv 120,32,100
rqv 28,0,34816
mavv 128,32,104
s_add_u32 s67, s67, s31
mavv 136,32,108
rt 78,7,2048
mavv 144,32,112
dm 15,24,67
mavv 152,32,116
rt 80,8,0
mavv 116,36,96
s_add_u32 m0, s32, 1024
rt 82,8,2048
mavv 124,36,100
rqv 32,0,36864
mavv 132,36,104
s_add_u32 s61, s61, s30
mavv 140,36,108
rt 84,9,0
mavv 148,36,112
dm 13,20,61
mavv 156,36,116
rt 86,9,2048
mavv 160,40,96
s_add_u32 m0, s33, 51200
mavv 168,40,100
s_add_u32 s67, s67, s31
rqv 36,0,38912
mavv 176,40,104
rqv 48,0,45056
mavv 184,40,108
dm 14,24,67
mavv 192,40,112
s_add_u32 m0, s32, 2048
mavv 200,40,116
s_add_u32 s61, s61, s30
mavv 164,44,96
rqv 40,0,40960
mavv 172,44,100
rqv 52,0,47104
mavv 180,44,104
dm 12,20,61
mavv 188,44,108
s_add_u32 m0, s33, 52224
mavv 196,44,112
s_add_u32 s67, s67, s31
mavv 204,44,116
mavv 208,56,96
rqv 44,0,43008
mavv 216,56,100
dm 15,24,67
mavv 224,56,104
s_add_u32 m0, s32, 3072
mavv 232,56,108
mavv 240,56,112
s_add_u32 s61, s61, s30
mavv 248,56,116
mavv 212,60,96
dm 13,20,61
mavv 220,60,100
mavv 228,60,104
s_add_u32 s29, s29, s65
mavv 236,60,108
mavv 244,60,112
mavv 252,60,116
s_waitcnt vmcnt(17) lgkmcnt(0)
s_barrier
.Lk1_AN_8:
mavv 64,24,64
rt 96,4,16384
mavv 72,24,68
s_add_u32 m0, s33, 0
mavv 80,24,72
s_mov_b32 s67, s29
mavv 88,24,76
rt 98,4,18432
mavv 96,24,80
dm 14,24,67
mavv 104,24,84
rt 100,5,16384
mavv 68,28,64
s_add_u32 m0, s32, 16384
rt 102,5,18432
mavv 76,28,68
rqv 24,1,32768
mavv 84,28,72
s_add_u32 s61, s28, s64
mavv 92,28,76
rt 104,6,16384
mavv 100,28,80
dm 12,20,61
mavv 108,28,84
s_add_u32 m0, s33, 1024
mavv 112,32,64
rt 106,6,18432
s_add_u32 s67, s67, s31
mavv 120,32,68
rt 108,7,16384
mavv 128,32,72
rqv 28,1,34816
dm 15,24,67
mavv 136,32,76
rt 110,7,18432
mavv 144,32,80
s_add_u32 m0, s32, 17408
mavv 152,32,84
rt 112,8,16384
mavv 116,36,64
s_add_u32 s61, s61, s30
rt 114,8,18432
mavv 124,36,68
rqv 32,1,36864
mavv 132,36,72
dm 13,20,61
mavv 140,36,76
s_add_u32 m0, s33, 2048
mavv 148,36,80
rt 116,9,16384
mavv 156,36,84
s_add_u32 s67, s67, s31
mavv 160,40,64
rt 118,9,18432
dm 14,24,67
mavv 168,40,68
rqv 36,1,38912
mavv 176,40,72
s_add_u32 m0, s32, 18432
mavv 184,40,76
rqv 56,1,45056
mavv 192,40,80
s_add_u32 s61, s61, s30
mavv 200,40,84
dm 12,20,61
mavv 164,44,64
s_add_u32 m0, s33, 3072
mavv 172,44,68
rqv 40,1,40960
mavv 180,44,72
rqv 60,1,47104
mavv 188,44,76
s_add_u32 s67, s67, s31
mavv 196,44,80
dm 15,24,67
mavv 204,44,84
s_add_u32 m0, s32, 19456
mavv 208,48,64
rqv 44,1,43008
mavv 216,48,68
s_add_u32 s61, s61, s30
mavv 224,48,72
dm 13,20,61
mavv 232,48,76
s_add_u32 s29, s29, s65
mavv 240,48,80
mavv 248,48,84
s_add_u32 s28, s28, 128
mavv 212,52,64
s_add_u32 s74, s78, .Lk1_AN_13-.Lk1_base
s_addc_u32 s75, s79, 0
mavv 220,52,68
mavv 228,52,72
global_load_dword v19, v236, s[74:75]
mavv 236,52,76
mavv 244,52,80
mavv 252,52,84
s_mov_b64 s[20:21], s[36:37]
s_mov_b64 s[22:23], s[38:39]
s_mov_b32 s28, s44
s_mov_b32 s30, s46
s_lshl_b32 s64, s46, 4
v_mov_b32 v12, v224
v_mov_b32 v13, v225
s_waitcnt vmcnt(17) lgkmcnt(0)
s_barrier
.Lk1_AN_9:
mavv 64,24,96
rt 64,4,32768
mavv 72,24,100
s_add_u32 m0, s33, 16384
mavv 80,24,104
s_mov_b32 s67, s29
mavv 88,24,108
rt 66,4,34816
mavv 96,24,112
dm 14,24,67
mavv 104,24,116
rt 68,5,32768
mavv 68,28,96
s_add_u32 m0, s32, 32768
rt 70,5,34816
mavv 76,28,100
rqv 24,2,0
mavv 84,28,104
s_mov_b32 s61, s28
mavv 92,28,108
dm 12,20,61
mavv 100,28,112
rt 72,6,32768
mavv 108,28,116
s_add_u32 m0, s33, 17408
mavv 112,32,96
rt 74,6,34816
s_add_u32 s67, s67, s31
rt 76,7,32768
mavv 120,32,100
rqv 28,2,2048
mavv 128,32,104
dm 15,24,67
mavv 136,32,108
rt 78,7,34816
mavv 144,32,112
s_add_u32 m0, s32, 33792
mavv 152,32,116
s_add_u32 s61, s61, s30
mavv 116,36,96
rt 80,8,32768
dm 13,20,61
mavv 124,36,100
rt 82,8,34816
mavv 132,36,104
rqv 32,2,4096
s_add_u32 m0, s33, 18432
mavv 140,36,108
rt 84,9,32768
mavv 148,36,112
s_add_u32 s67, s67, s31
mavv 156,36,116
rt 86,9,34816
mavv 160,40,96
dm 14,24,67
mavv 168,40,100
s_add_u32 m0, s32, 34816
rqv 36,2,6144
mavv 176,40,104
rqv 48,2,12288
mavv 184,40,108
s_add_u32 s61, s61, s30
mavv 192,40,112
dm 12,20,61
mavv 200,40,116
s_add_u32 m0, s33, 19456
mavv 164,44,96
rqv 40,2,8192
mavv 172,44,100
s_add_u32 s67, s67, s31
mavv 180,44,104
rqv 52,2,14336
mavv 188,44,108
dm 15,24,67
mavv 196,44,112
s_add_u32 m0, s32, 35840
mavv 204,44,116
s_add_u32 s61, s61, s30
mavv 208,56,96
rqv 44,2,10240
mavv 216,56,100
dm 13,20,61
mavv 224,56,104
s_add_u32 s29, s29, s65
)ASM" ::: "memory");
  asm volatile(R"ASM(
mavv 232,56,108
s_add_u32 s74, s78, .Lk1_AN_pk-.Lk1_base
s_addc_u32 s75, s79, 0
mavv 240,56,112
mavv 248,56,116
global_load_dword v19, v236, s[74:75]
mavv 212,60,96
buffer_load_dword v19, v252, s[40:43], s89 offen
mavv 220,60,100
mavv 228,60,104
s_add_u32 s89, s89, 0x800
mavv 236,60,108
mavv 244,60,112
mavv 252,60,116
s_mov_b64 s[24:25], s[40:41]
s_mov_b64 s[26:27], s[42:43]
s_mov_b32 s29, s45
s_mov_b32 s31, s47
s_lshl_b32 s65, s47, 4
v_mov_b32 v14, v226
v_mov_b32 v15, v227
s_waitcnt vmcnt(19) lgkmcnt(0)
s_barrier
.Lk1_AN_10:
mavv 64,24,64
rt 96,4,49152
mavv 72,24,68
s_add_u32 m0, s33, 32768
mavv 80,24,72
s_mov_b32 s67, s29
mavv 88,24,76
rt 98,4,51200
mavv 96,24,80
dm 14,24,67
mavv 104,24,84
rt 100,5,49152
mavv 68,28,64
s_add_u32 m0, s32, 49152
rt 102,5,51200
mavv 76,28,68
rqv 24,3,0
mavv 84,28,72
s_add_u32 s61, s28, s64
mavv 92,28,76
rt 104,6,49152
mavv 100,28,80
dm 12,20,61
mavv 108,28,84
s_add_u32 m0, s33, 33792
mavv 112,32,64
rt 106,6,51200
s_add_u32 s67, s67, s31
mavv 120,32,68
rt 108,7,49152
mavv 128,32,72
rqv 28,3,2048
dm 15,24,67
mavv 136,32,76
rt 110,7,51200
mavv 144,32,80
s_add_u32 m0, s32, 50176
mavv 152,32,84
rt 112,8,49152
mavv 116,36,64
s_add_u32 s61, s61, s30
rt 114,8,51200
mavv 124,36,68
rqv 32,3,4096
mavv 132,36,72
dm 13,20,61
mavv 140,36,76
s_add_u32 m0, s33, 34816
mavv 148,36,80
rt 116,9,49152
mavv 156,36,84
s_add_u32 s67, s67, s31
mavv 160,40,64
rt 118,9,51200
dm 14,24,67
mavv 168,40,68
rqv 36,3,6144
mavv 176,40,72
s_add_u32 m0, s32, 51200
mavv 184,40,76
rqv 56,3,12288
mavv 192,40,80
s_add_u32 s61, s61, s30
mavv 200,40,84
dm 12,20,61
mavv 164,44,64
s_add_u32 m0, s33, 35840
mavv 172,44,68
rqv 40,3,8192
mavv 180,44,72
rqv 60,3,14336
mavv 188,44,76
s_add_u32 s67, s67, s31
mavv 196,44,80
dm 15,24,67
mavv 204,44,84
s_add_u32 m0, s32, 52224
mavv 208,48,64
rqv 44,3,10240
mavv 216,48,68
s_add_u32 s61, s61, s30
mavv 224,48,72
dm 13,20,61
mavv 232,48,76
s_add_u32 s29, s29, s65
mavv 240,48,80
mavv 248,48,84
s_add_u32 s28, s28, 128
mavv 212,52,64
s_add_u32 s74, s78, .Lk1_BTN-.Lk1_base
s_addc_u32 s75, s79, 0
mavv 220,52,68
mavv 228,52,72
global_load_dword v19, v236, s[74:75]
mavv 236,52,76
mavv 244,52,80
mavv 252,52,84
s_waitcnt vmcnt(19) lgkmcnt(0)
s_barrier
.Lk1_AN_11:
mavv 64,24,96
rt 64,4,0
mavv 72,24,100
s_add_u32 m0, s33, 49152
mavv 80,24,104
s_mov_b32 s67, s29
mavv 88,24,108
rt 66,4,2048
mavv 96,24,112
dm 14,24,67
mavv 104,24,116
rt 68,5,0
mavv 68,28,96
s_add_u32 m0, s32, 65536
rt 70,5,2048
mavv 76,28,100
rqv 24,0,0
mavv 84,28,104
s_mov_b32 s61, s28
mavv 92,28,108
rt 72,6,0
mavv 100,28,112
dm 12,20,61
mavv 108,28,116
rt 74,6,2048
mavv 112,32,96
s_add_u32 m0, s33, 50176
s_add_u32 s67, s67, s31
mavv 120,32,100
rt 76,7,0
mavv 128,32,104
rqv 28,0,2048
mavv 136,32,108
dm 15,24,67
mavv 144,32,112
rt 78,7,2048
mavv 152,32,116
s_add_u32 m0, s32, 66560
mavv 116,36,96
rt 80,8,0
s_add_u32 s61, s61, s30
rt 82,8,2048
mavv 124,36,100
rqv 32,0,4096
mavv 132,36,104
dm 13,20,61
mavv 140,36,108
rt 84,9,0
mavv 148,36,112
s_add_u32 m0, s33, 51200
mavv 156,36,116
rt 86,9,2048
mavv 160,40,96
s_add_u32 s67, s67, s31
mavv 168,40,100
dm 14,24,67
rqv 36,0,6144
mavv 176,40,104
rqv 48,0,12288
mavv 184,40,108
s_add_u32 m0, s32, 67584
mavv 192,40,112
s_add_u32 s61, s61, s30
mavv 200,40,116
dm 12,20,61
mavv 164,44,96
rqv 40,0,8192
mavv 172,44,100
s_add_u32 m0, s33, 52224
mavv 180,44,104
rqv 52,0,14336
mavv 188,44,108
s_add_u32 s67, s67, s31
mavv 196,44,112
dm 15,24,67
mavv 204,44,116
s_add_u32 m0, s32, 68608
mavv 208,56,96
rqv 44,0,10240
mavv 216,56,100
s_add_u32 s61, s61, s30
mavv 224,56,104
mavv 232,56,108
dm 13,20,61
mavv 240,56,112
s_add_u32 s29, s29, s65
mavv 248,56,116
mavv 212,60,96
s_add_u32 s74, s78, .Lk1_BNN-.Lk1_base
s_addc_u32 s75, s79, 0
mavv 220,60,100
mavv 228,60,104
global_load_dword v19, v236, s[74:75]
mavv 236,60,108
mavv 244,60,112
mavv 252,60,116
s_waitcnt vmcnt(18) lgkmcnt(0)
s_barrier
.Lk1_AN_12:
mavv 64,24,64
rt 96,4,16384
mavv 72,24,68
s_add_u32 m0, s33, 0
mavv 80,24,72
s_mov_b32 s67, s29
mavv 88,24,76
rt 98,4,18432
mavv 96,24,80
dm 14,24,67
mavv 104,24,84
rt 100,5,16384
mavv 68,28,64
s_add_u32 m0, s32, 81920
rt 102,5,18432
mavv 76,28,68
rqv 24,1,0
mavv 84,28,72
s_add_u32 s61, s28, s64
mavv 92,28,76
dm 12,20,61
mavv 100,28,80
rt 104,6,16384
s_add_u32 m0, s33, 1024
mavv 108,28,84
rt 106,6,18432
mavv 112,32,64
s_add_u32 s67, s67, s31
rt 108,7,16384
mavv 120,32,68
rqv 28,1,2048
mavv 128,32,72
dm 15,24,67
mavv 136,32,76
s_add_u32 m0, s32, 82944
mavv 144,32,80
rt 110,7,18432
s_add_u32 s61, s61, s30
mavv 152,32,84
rt 112,8,16384
mavv 116,36,64
dm 13,20,61
rt 114,8,18432
mavv 124,36,68
rqv 32,1,4096
mavv 132,36,72
s_add_u32 m0, s33, 2048
mavv 140,36,76
s_add_u32 s67, s67, s31
mavv 148,36,80
rt 116,9,16384
mavv 156,36,84
dm 14,24,67
mavv 160,40,64
rt 118,9,18432
s_add_u32 m0, s32, 83968
mavv 168,40,68
rqv 36,1,6144
mavv 176,40,72
s_add_u32 s61, s61, s30
mavv 184,40,76
rqv 56,1,12288
mavv 192,40,80
dm 12,20,61
mavv 200,40,84
s_add_u32 m0, s33, 3072
mavv 164,44,64
s_add_u32 s67, s67, s31
mavv 172,44,68
rqv 40,1,8192
mavv 180,44,72
dm 15,24,67
mavv 188,44,76
rqv 60,1,14336
mavv 196,44,80
s_add_u32 m0, s32, 84992
mavv 204,44,84
s_add_u32 s61, s61, s30
mavv 208,48,64
dm 13,20,61
mavv 216,48,68
rqv 44,1,10240
mavv 224,48,72
s_add_u32 s29, s29, s65
mavv 232,48,76
s_add_u32 s28, s28, 128
mavv 240,48,80
s_add_u32 s74, s78, .Lk1_BTN+4096-.Lk1_base
s_addc_u32 s75, s79, 0
mavv 248,48,84
global_load_dword v19, v236, s[74:75]
mavv 212,52,64
buffer_load_dword v19, v252, s[40:43], s89 offen
mavv 220,52,68
mavv 228,52,72
s_add_u32 s89, s89, 0x800
mavv 236,52,76
mavv 244,52,80
mavv 252,52,84
v_mov_b32 v4, v228
v_mov_b32 v5, v229
v_mov_b32 v6, v230
v_mov_b32 v7, v231
v_mov_b32 v8, v232
v_mov_b32 v9, v233
v_mov_b32 v10, v234
v_mov_b32 v11, v235
s_waitcnt vmcnt(19) lgkmcnt(0)
s_barrier
.Lk1_AN_13:
mavv 64,24,96
rt 64,4,32768
mavv 72,24,100
s_add_u32 m0, s33, 16384
mavv 80,24,104
rt 66,4,34816
mavv 88,24,108
s_mov_b32 s67, s29
mavv 96,24,112
rt 68,5,32768
mavv 104,24,116
dm 14,24,67
mavv 68,28,96
rt 70,5,34816
s_add_u32 m0, s32, 0
rt 72,6,32768
mavv 76,28,100
rqv 24,0,32768
mavv 84,28,104
s_mov_b32 s61, s28
mavv 92,28,108
rt 74,6,34816
mavv 100,28,112
dm 12,20,61
rt 76,7,32768
mavv 108,28,116
rt 78,7,34816
mavv 112,32,96
s_add_u32 m0, s33, 17408
s_add_u32 s67, s67, s31
mavv 120,32,100
rt 80,8,32768
rqv 28,0,34816
mavv 128,32,104
rt 82,8,34816
mavv 136,32,108
dm 15,24,67
mavv 144,32,112
rt 84,9,32768
mavv 152,32,116
s_add_u32 m0, s32, 1024
mavv 116,36,96
rt 86,9,34816
s_add_u32 s61, s61, s30
rt 88,10,32768
mavv 124,36,100
rqv 32,0,36864
mavv 132,36,104
dm 13,20,61
mavv 140,36,108
rt 90,10,34816
mavv 148,36,112
s_add_u32 m0, s33, 18432
rt 92,11,32768
mavv 156,36,116
rt 94,11,34816
mavv 160,40,96
s_add_u32 s67, s67, s31
mavv 168,40,100
dm 14,24,67
rqv 36,0,38912
mavv 176,40,104
rqv 48,0,45056
mavv 184,40,108
s_add_u32 m0, s32, 2048
mavv 192,40,112
s_add_u32 s61, s61, s30
mavv 200,40,116
dm 12,20,61
mavv 164,44,96
rqv 40,0,40960
mavv 172,44,100
s_add_u32 m0, s33, 19456
mavv 180,44,104
rqv 52,0,47104
mavv 188,44,108
s_add_u32 s67, s67, s31
mavv 196,44,112
dm 15,24,67
mavv 204,44,116
s_add_u32 m0, s32, 3072
mavv 208,56,96
rqv 44,0,43008
mavv 216,56,100
s_add_u32 s61, s61, s30
mavv 224,56,104
mavv 232,56,108
dm 13,20,61
mavv 240,56,112
s_add_u32 s29, s29, s65
mavv 248,56,116
mavv 212,60,96
s_add_u32 s74, s78, .Lk1_BNN+4096-.Lk1_base
s_addc_u32 s75, s79, 0
mavv 220,60,100
mavv 228,60,104
global_load_dword v19, v236, s[74:75]
mavv 236,60,108
mavv 244,60,112
mavv 252,60,116
.Lk1_AN_pk:
s_cmp_lg_u32 s34, 0
s_cbranch_scc1 .Lk1_BNN
s_branch .Lk1_BTN
.Lk1_BTT:
s_waitcnt vmcnt(19) lgkmcnt(0)
s_barrier
zavv 0,64,24
rt 96,4,49152
ar 224,64
ar 225,65
ar 226,66
ar 227,67
ar 228,68
ar 229,69
ar 230,70
zavv 4,68,24
s_add_u32 m0, s33, 32768
ar 231,71
v_pk_mul_f32 v[224:225], v[224:225], s[48:49] op_sel_hi:[1,0]
v_pk_mul_f32 v[226:227], v[226:227], s[48:49] op_sel_hi:[1,0]
v_pk_mul_f32 v[228:229], v[228:229], s[48:49] op_sel_hi:[1,0]
v_pk_mul_f32 v[230:231], v[230:231], s[48:49] op_sel_hi:[1,0]
cv 128,224,225
cv 129,226,227
zavv 8,72,24
rt 98,4,51200
cv 130,228,229
cv 131,230,231
v_cndmask_b32_e64 v130, v130, v17, s[76:77]
v_cndmask_b32_e64 v131, v131, v20, s[76:77]
ar 224,72
ar 225,73
ar 226,74
zavv 12,76,24
s_mov_b32 s67, s29
ar 227,75
ar 228,76
ar 229,77
ar 230,78
ar 231,79
v_pk_mul_f32 v[224:225], v[224:225], s[48:49] op_sel_hi:[1,0]
v_pk_mul_f32 v[226:227], v[226:227], s[48:49] op_sel_hi:[1,0]
zavv 16,80,24
rt 100,5,49152
v_pk_mul_f32 v[228:229], v[228:229], s[48:49] op_sel_hi:[1,0]
v_pk_mul_f32 v[230:231], v[230:231], s[48:49] op_sel_hi:[1,0]
cv 132,224,225
cv 133,226,227
cv 134,228,229
cv 135,230,231
v_cndmask_b32_e64 v134, v134, v17, s[76:77]
zavv 20,84,24
dm 14,24,67
v_cndmask_b32_e64 v135, v135, v20, s[76:77]
ar 224,80
ar 225,81
ar 226,82
ar 227,83
ar 228,84
ar 229,85
zavv 24,88,24
rt 102,5,51200
ar 230,86
ar 231,87
v_pk_mul_f32 v[224:225], v[224:225], s[48:49] op_sel_hi:[1,0]
v_pk_mul_f32 v[226:227], v[226:227], s[48:49] op_sel_hi:[1,0]
v_pk_mul_f32 v[228:229], v[228:229], s[48:49] op_sel_hi:[1,0]
v_pk_mul_f32 v[230:231], v[230:231], s[48:49] op_sel_hi:[1,0]
cv 136,224,225
zavv 28,92,24
s_add_u32 m0, s32, 16384
cv 137,226,227
cv 138,228,229
cv 139,230,231
v_cndmask_b32_e64 v138, v138, v17, s[76:77]
v_cndmask_b32_e64 v139, v139, v20, s[76:77]
ar 224,88
ar 225,89
zavv 32,64,28
rt 104,6,49152
ar 226,90
ar 227,91
ar 228,92
ar 229,93
ar 230,94
ar 231,95
v_pk_mul_f32 v[224:225], v[224:225], s[48:49] op_sel_hi:[1,0]
zavv 36,68,28
rqv 24,1,32768
v_pk_mul_f32 v[226:227], v[226:227], s[48:49] op_sel_hi:[1,0]
v_pk_mul_f32 v[228:229], v[228:229], s[48:49] op_sel_hi:[1,0]
v_pk_mul_f32 v[230:231], v[230:231], s[48:49] op_sel_hi:[1,0]
cv 140,224,225
cv 141,226,227
cv 142,228,229
cv 143,230,231
zavv 40,72,28
s_add_u32 s61, s28, s64
v_cndmask_b32_e64 v142, v142, v17, s[76:77]
v_cndmask_b32_e64 v143, v143, v20, s[76:77]
ar 224,96
ar 225,97
ar 226,98
ar 227,99
ar 228,100
zavv 44,76,28
rt 106,6,51200
ar 229,101
ar 230,102
ar 231,103
v_pk_mul_f32 v[224:225], v[224:225], s[48:49] op_sel_hi:[1,0]
v_pk_mul_f32 v[226:227], v[226:227], s[48:49] op_sel_hi:[1,0]
v_pk_mul_f32 v[228:229], v[228:229], s[48:49] op_sel_hi:[1,0]
v_pk_mul_f32 v[230:231], v[230:231], s[48:49] op_sel_hi:[1,0]
zavv 48,80,28
dm 12,20,61
cv 144,224,225
cv 145,226,227
cv 146,228,229
cv 147,230,231
v_cndmask_b32_e64 v146, v146, v17, s[76:77]
v_cndmask_b32_e64 v147, v147, v20, s[76:77]
ar 224,104
zavv 52,84,28
rt 108,7,49152
ar 225,105
ar 226,106
ar 227,107
ar 228,108
ar 229,109
ar 230,110
ar 231,111
zavv 56,88,28
s_add_u32 m0, s33, 33792
v_pk_mul_f32 v[224:225], v[224:225], s[48:49] op_sel_hi:[1,0]
v_pk_mul_f32 v[226:227], v[226:227], s[48:49] op_sel_hi:[1,0]
v_pk_mul_f32 v[228:229], v[228:229], s[48:49] op_sel_hi:[1,0]
v_pk_mul_f32 v[230:231], v[230:231], s[48:49] op_sel_hi:[1,0]
cv 148,224,225
cv 149,226,227
cv 150,228,229
zavv 60,92,28
rt 110,7,51200
cv 151,230,231
v_cndmask_b32_e64 v150, v150, v17, s[76:77]
v_cndmask_b32_e64 v151, v151, v20, s[76:77]
ar 224,112
ar 225,113
ar 226,114
ar 227,115
zavv 64,64,32
s_add_u32 s67, s67, s31
ar 228,116
ar 229,117
ar 230,118
ar 231,119
v_pk_mul_f32 v[224:225], v[224:225], s[48:49] op_sel_hi:[1,0]
v_pk_mul_f32 v[226:227], v[226:227], s[48:49] op_sel_hi:[1,0]
v_pk_mul_f32 v[228:229], v[228:229], s[48:49] op_sel_hi:[1,0]
zavv 68,68,32
rt 112,8,49152
v_pk_mul_f32 v[230:231], v[230:231], s[48:49] op_sel_hi:[1,0]
cv 152,224,225
cv 153,226,227
cv 154,228,229
cv 155,230,231
)ASM" ::: "memory");
  asm volatile(R"ASM(
v_cndmask_b32_e64 v154, v154, v17, s[76:77]
v_cndmask_b32_e64 v155, v155, v20, s[76:77]
zavv 72,72,32
rqv 28,1,34816
ar 224,120
ar 225,121
ar 226,122
ar 227,123
ar 228,124
ar 229,125
ar 230,126
zavv 76,76,32
dm 15,24,67
ar 231,127
v_pk_mul_f32 v[224:225], v[224:225], s[48:49] op_sel_hi:[1,0]
v_pk_mul_f32 v[226:227], v[226:227], s[48:49] op_sel_hi:[1,0]
v_pk_mul_f32 v[228:229], v[228:229], s[48:49] op_sel_hi:[1,0]
v_pk_mul_f32 v[230:231], v[230:231], s[48:49] op_sel_hi:[1,0]
cv 156,224,225
cv 157,226,227
zavv 80,80,32
rt 114,8,51200
cv 158,228,229
cv 159,230,231
v_cndmask_b32_e64 v158, v158, v17, s[76:77]
v_cndmask_b32_e64 v159, v159, v20, s[76:77]
ar 224,128
ar 225,129
ar 226,130
zavv 84,84,32
s_add_u32 m0, s32, 17408
ar 227,131
ar 228,132
ar 229,133
ar 230,134
ar 231,135
v_pk_mul_f32 v[224:225], v[224:225], s[48:49] op_sel_hi:[1,0]
v_pk_mul_f32 v[226:227], v[226:227], s[48:49] op_sel_hi:[1,0]
zavv 88,88,32
rt 116,9,49152
v_pk_mul_f32 v[228:229], v[228:229], s[48:49] op_sel_hi:[1,0]
v_pk_mul_f32 v[230:231], v[230:231], s[48:49] op_sel_hi:[1,0]
cv 160,224,225
cv 161,226,227
cv 162,228,229
cv 163,230,231
v_cndmask_b32_e64 v162, v162, v17, s[76:77]
zavv 92,92,32
rt 118,9,51200
v_cndmask_b32_e64 v163, v163, v20, s[76:77]
ar 224,136
ar 225,137
ar 226,138
ar 227,139
ar 228,140
ar 229,141
zavv 96,64,36
s_add_u32 s61, s61, s30
rt 120,10,49152
ar 230,142
ar 231,143
v_pk_mul_f32 v[224:225], v[224:225], s[48:49] op_sel_hi:[1,0]
v_pk_mul_f32 v[226:227], v[226:227], s[48:49] op_sel_hi:[1,0]
v_pk_mul_f32 v[228:229], v[228:229], s[48:49] op_sel_hi:[1,0]
v_pk_mul_f32 v[230:231], v[230:231], s[48:49] op_sel_hi:[1,0]
cv 164,224,225
zavv 100,68,36
rqv 32,1,36864
cv 165,226,227
cv 166,228,229
cv 167,230,231
v_cndmask_b32_e64 v166, v166, v17, s[76:77]
v_cndmask_b32_e64 v167, v167, v20, s[76:77]
ar 224,144
ar 225,145
zavv 104,72,36
dm 13,20,61
ar 226,146
ar 227,147
ar 228,148
ar 229,149
ar 230,150
ar 231,151
v_pk_mul_f32 v[224:225], v[224:225], s[48:49] op_sel_hi:[1,0]
zavv 108,76,36
rt 122,10,51200
v_pk_mul_f32 v[226:227], v[226:227], s[48:49] op_sel_hi:[1,0]
v_pk_mul_f32 v[228:229], v[228:229], s[48:49] op_sel_hi:[1,0]
v_pk_mul_f32 v[230:231], v[230:231], s[48:49] op_sel_hi:[1,0]
cv 168,224,225
cv 169,226,227
cv 170,228,229
cv 171,230,231
zavv 112,80,36
s_add_u32 m0, s33, 34816
v_cndmask_b32_e64 v170, v170, v17, s[76:77]
v_cndmask_b32_e64 v171, v171, v20, s[76:77]
ar 224,152
ar 225,153
ar 226,154
ar 227,155
ar 228,156
zavv 116,84,36
rt 124,11,49152
ar 229,157
ar 230,158
ar 231,159
v_pk_mul_f32 v[224:225], v[224:225], s[48:49] op_sel_hi:[1,0]
v_pk_mul_f32 v[226:227], v[226:227], s[48:49] op_sel_hi:[1,0]
v_pk_mul_f32 v[228:229], v[228:229], s[48:49] op_sel_hi:[1,0]
v_pk_mul_f32 v[230:231], v[230:231], s[48:49] op_sel_hi:[1,0]
zavv 120,88,36
s_add_u32 s67, s67, s31
cv 172,224,225
cv 173,226,227
cv 174,228,229
cv 175,230,231
v_cndmask_b32_e64 v174, v174, v17, s[76:77]
v_cndmask_b32_e64 v175, v175, v20, s[76:77]
ar 224,160
zavv 124,92,36
rt 126,11,51200
ar 225,161
ar 226,162
ar 227,163
ar 228,164
ar 229,165
ar 230,166
ar 231,167
zavv 128,64,40
dm 14,24,67
v_pk_mul_f32 v[224:225], v[224:225], s[48:49] op_sel_hi:[1,0]
v_pk_mul_f32 v[226:227], v[226:227], s[48:49] op_sel_hi:[1,0]
v_pk_mul_f32 v[228:229], v[228:229], s[48:49] op_sel_hi:[1,0]
v_pk_mul_f32 v[230:231], v[230:231], s[48:49] op_sel_hi:[1,0]
cv 176,224,225
cv 177,226,227
cv 178,228,229
zavv 132,68,40
rqv 36,1,38912
cv 179,230,231
v_cndmask_b32_e64 v178, v178, v17, s[76:77]
v_cndmask_b32_e64 v179, v179, v20, s[76:77]
ar 224,168
ar 225,169
ar 226,170
ar 227,171
zavv 136,72,40
rqv 56,1,45056
ar 228,172
ar 229,173
ar 230,174
ar 231,175
v_pk_mul_f32 v[224:225], v[224:225], s[48:49] op_sel_hi:[1,0]
v_pk_mul_f32 v[226:227], v[226:227], s[48:49] op_sel_hi:[1,0]
v_pk_mul_f32 v[228:229], v[228:229], s[48:49] op_sel_hi:[1,0]
zavv 140,76,40
s_add_u32 m0, s32, 18432
v_pk_mul_f32 v[230:231], v[230:231], s[48:49] op_sel_hi:[1,0]
cv 180,224,225
cv 181,226,227
cv 182,228,229
cv 183,230,231
v_cndmask_b32_e64 v182, v182, v17, s[76:77]
v_cndmask_b32_e64 v183, v183, v20, s[76:77]
zavv 144,80,40
s_add_u32 s61, s61, s30
ar 224,176
ar 225,177
ar 226,178
ar 227,179
ar 228,180
ar 229,181
ar 230,182
zavv 148,84,40
dm 12,20,61
ar 231,183
v_pk_mul_f32 v[224:225], v[224:225], s[48:49] op_sel_hi:[1,0]
v_pk_mul_f32 v[226:227], v[226:227], s[48:49] op_sel_hi:[1,0]
v_pk_mul_f32 v[228:229], v[228:229], s[48:49] op_sel_hi:[1,0]
v_pk_mul_f32 v[230:231], v[230:231], s[48:49] op_sel_hi:[1,0]
cv 184,224,225
cv 185,226,227
zavv 152,88,40
cv 186,228,229
cv 187,230,231
v_cndmask_b32_e64 v186, v186, v17, s[76:77]
v_cndmask_b32_e64 v187, v187, v20, s[76:77]
ar 224,184
ar 225,185
ar 226,186
zavv 156,92,40
s_add_u32 m0, s33, 35840
ar 227,187
ar 228,188
ar 229,189
ar 230,190
ar 231,191
v_pk_mul_f32 v[224:225], v[224:225], s[48:49] op_sel_hi:[1,0]
v_pk_mul_f32 v[226:227], v[226:227], s[48:49] op_sel_hi:[1,0]
zavv 160,64,44
rqv 40,1,40960
v_pk_mul_f32 v[228:229], v[228:229], s[48:49] op_sel_hi:[1,0]
v_pk_mul_f32 v[230:231], v[230:231], s[48:49] op_sel_hi:[1,0]
cv 188,224,225
cv 189,226,227
cv 190,228,229
cv 191,230,231
v_cndmask_b32_e64 v190, v190, v17, s[76:77]
zavv 164,68,44
rqv 60,1,47104
v_cndmask_b32_e64 v191, v191, v20, s[76:77]
ar 224,192
ar 225,193
ar 226,194
ar 227,195
ar 228,196
ar 229,197
zavv 168,72,44
s_add_u32 s67, s67, s31
ar 230,198
ar 231,199
v_pk_mul_f32 v[224:225], v[224:225], s[48:49] op_sel_hi:[1,0]
v_pk_mul_f32 v[226:227], v[226:227], s[48:49] op_sel_hi:[1,0]
v_pk_mul_f32 v[228:229], v[228:229], s[48:49] op_sel_hi:[1,0]
v_pk_mul_f32 v[230:231], v[230:231], s[48:49] op_sel_hi:[1,0]
cv 192,224,225
zavv 172,76,44
cv 193,226,227
cv 194,228,229
cv 195,230,231
v_cndmask_b32_e64 v194, v194, v17, s[76:77]
v_cndmask_b32_e64 v195, v195, v20, s[76:77]
ar 224,200
ar 225,201
zavv 176,80,44
dm 15,24,67
ar 226,202
ar 227,203
ar 228,204
ar 229,205
ar 230,206
ar 231,207
v_pk_mul_f32 v[224:225], v[224:225], s[48:49] op_sel_hi:[1,0]
zavv 180,84,44
v_pk_mul_f32 v[226:227], v[226:227], s[48:49] op_sel_hi:[1,0]
v_pk_mul_f32 v[228:229], v[228:229], s[48:49] op_sel_hi:[1,0]
v_pk_mul_f32 v[230:231], v[230:231], s[48:49] op_sel_hi:[1,0]
cv 196,224,225
cv 197,226,227
cv 198,228,229
cv 199,230,231
zavv 184,88,44
s_add_u32 m0, s32, 19456
v_cndmask_b32_e64 v198, v198, v17, s[76:77]
v_cndmask_b32_e64 v199, v199, v20, s[76:77]
ar 224,208
ar 225,209
ar 226,210
ar 227,211
ar 228,212
zavv 188,92,44
ar 229,213
ar 230,214
ar 231,215
v_pk_mul_f32 v[224:225], v[224:225], s[48:49] op_sel_hi:[1,0]
v_pk_mul_f32 v[226:227], v[226:227], s[48:49] op_sel_hi:[1,0]
v_pk_mul_f32 v[228:229], v[228:229], s[48:49] op_sel_hi:[1,0]
v_pk_mul_f32 v[230:231], v[230:231], s[48:49] op_sel_hi:[1,0]
zavv 192,64,48
rqv 44,1,43008
cv 200,224,225
cv 201,226,227
cv 202,228,229
cv 203,230,231
v_cndmask_b32_e64 v202, v202, v17, s[76:77]
v_cndmask_b32_e64 v203, v203, v20, s[76:77]
ar 224,216
zavv 196,68,48
s_add_u32 s61, s61, s30
ar 225,217
ar 226,218
ar 227,219
ar 228,220
ar 229,221
ar 230,222
ar 231,223
zavv 200,72,48
dm 13,20,61
v_pk_mul_f32 v[224:225], v[224:225], s[48:49] op_sel_hi:[1,0]
v_pk_mul_f32 v[226:227], v[226:227], s[48:49] op_sel_hi:[1,0]
v_pk_mul_f32 v[228:229], v[228:229], s[48:49] op_sel_hi:[1,0]
v_pk_mul_f32 v[230:231], v[230:231], s[48:49] op_sel_hi:[1,0]
cv 204,224,225
cv 205,226,227
cv 206,228,229
zavv 204,76,48
cv 207,230,231
v_cndmask_b32_e64 v206, v206, v17, s[76:77]
v_cndmask_b32_e64 v207, v207, v20, s[76:77]
ar 224,224
ar 225,225
ar 226,226
ar 227,227
zavv 208,80,48
ar 228,228
ar 229,229
ar 230,230
ar 231,231
v_pk_mul_f32 v[224:225], v[224:225], s[48:49] op_sel_hi:[1,0]
v_pk_mul_f32 v[226:227], v[226:227], s[48:49] op_sel_hi:[1,0]
v_pk_mul_f32 v[228:229], v[228:229], s[48:49] op_sel_hi:[1,0]
zavv 212,84,48
s_add_u32 s29, s29, s65
v_pk_mul_f32 v[230:231], v[230:231], s[48:49] op_sel_hi:[1,0]
cv 208,224,225
cv 209,226,227
cv 210,228,229
cv 211,230,231
v_cndmask_b32_e64 v210, v210, v17, s[76:77]
v_cndmask_b32_e64 v211, v211, v20, s[76:77]
zavv 216,88,48
ar 224,232
ar 225,233
ar 226,234
ar 227,235
ar 228,236
ar 229,237
ar 230,238
zavv 220,92,48
s_add_u32 s28, s28, 128
ar 231,239
v_pk_mul_f32 v[224:225], v[224:225], s[48:49] op_sel_hi:[1,0]
v_pk_mul_f32 v[226:227], v[226:227], s[48:49] op_sel_hi:[1,0]
v_pk_mul_f32 v[228:229], v[228:229], s[48:49] op_sel_hi:[1,0]
v_pk_mul_f32 v[230:231], v[230:231], s[48:49] op_sel_hi:[1,0]
cv 212,224,225
cv 213,226,227
zavv 224,64,52
cv 214,228,229
cv 215,230,231
v_cndmask_b32_e64 v214, v214, v17, s[76:77]
v_cndmask_b32_e64 v215, v215, v20, s[76:77]
ar 224,240
ar 225,241
ar 226,242
zavv 228,68,52
s_add_u32 s74, s78, .Lk1_BTT+8192-.Lk1_base
s_addc_u32 s75, s79, 0
ar 227,243
ar 228,244
ar 229,245
ar 230,246
ar 231,247
v_pk_mul_f32 v[224:225], v[224:225], s[48:49] op_sel_hi:[1,0]
v_pk_mul_f32 v[226:227], v[226:227], s[48:49] op_sel_hi:[1,0]
zavv 232,72,52
v_pk_mul_f32 v[228:229], v[228:229], s[48:49] op_sel_hi:[1,0]
v_pk_mul_f32 v[230:231], v[230:231], s[48:49] op_sel_hi:[1,0]
cv 216,224,225
cv 217,226,227
cv 218,228,229
cv 219,230,231
v_cndmask_b32_e64 v218, v218, v17, s[76:77]
zavv 236,76,52
global_load_dword v19, v236, s[74:75]
v_cndmask_b32_e64 v219, v219, v20, s[76:77]
ar 224,248
ar 225,249
ar 226,250
ar 227,251
ar 228,252
ar 229,253
zavv 240,80,52
ar 230,254
ar 231,255
v_pk_mul_f32 v[224:225], v[224:225], s[48:49] op_sel_hi:[1,0]
v_pk_mul_f32 v[226:227], v[226:227], s[48:49] op_sel_hi:[1,0]
v_pk_mul_f32 v[228:229], v[228:229], s[48:49] op_sel_hi:[1,0]
v_pk_mul_f32 v[230:231], v[230:231], s[48:49] op_sel_hi:[1,0]
cv 220,224,225
zavv 244,84,52
cv 221,226,227
cv 222,228,229
cv 223,230,231
v_cndmask_b32_e64 v222, v222, v17, s[76:77]
v_cndmask_b32_e64 v223, v223, v20, s[76:77]
zavv 248,88,52
zavv 252,92,52
s_waitcnt vmcnt(18) lgkmcnt(0)
s_barrier
mavv 0,96,24
rt 64,4,0
mavv 4,100,24
s_add_u32 m0, s33, 49152
mavv 8,104,24
rt 66,4,2048
mavv 12,108,24
s_mov_b32 s67, s29
mavv 16,112,24
rt 68,5,0
mavv 20,116,24
dm 14,24,67
mavv 24,120,24
rt 70,5,2048
mavv 28,124,24
s_add_u32 m0, s32, 32768
mavv 32,96,28
rt 72,6,0
mavv 36,100,28
rqv 24,2,0
mavv 40,104,28
s_mov_b32 s61, s28
mavv 44,108,28
rt 74,6,2048
mavv 48,112,28
dm 12,20,61
mavv 52,116,28
rt 76,7,0
mavv 56,120,28
s_add_u32 m0, s33, 50176
mavv 60,124,28
rt 78,7,2048
mavv 64,96,32
s_add_u32 s67, s67, s31
mavv 68,100,32
rt 80,8,0
mavv 72,104,32
rqv 28,2,2048
mavv 76,108,32
dm 15,24,67
mavv 80,112,32
rt 82,8,2048
mavv 84,116,32
s_add_u32 m0, s32, 33792
mavv 88,120,32
rt 84,9,0
mavv 92,124,32
rt 86,9,2048
mavv 96,96,36
s_add_u32 s61, s61, s30
rt 88,10,0
mavv 100,100,36
rqv 32,2,4096
mavv 104,104,36
dm 13,20,61
mavv 108,108,36
rt 90,10,2048
mavv 112,112,36
s_add_u32 m0, s33, 51200
mavv 116,116,36
rt 92,11,0
mavv 120,120,36
s_add_u32 s67, s67, s31
mavv 124,124,36
rt 94,11,2048
mavv 128,96,40
dm 14,24,67
mavv 132,100,40
rqv 36,2,6144
mavv 136,104,40
rqv 48,2,12288
mavv 140,108,40
s_add_u32 m0, s32, 34816
mavv 144,112,40
s_add_u32 s61, s61, s30
mavv 148,116,40
dm 12,20,61
mavv 152,120,40
mavv 156,124,40
s_add_u32 m0, s33, 52224
mavv 160,96,44
rqv 40,2,8192
mavv 164,100,44
rqv 52,2,14336
mavv 168,104,44
s_add_u32 s67, s67, s31
mavv 172,108,44
mavv 176,112,44
dm 15,24,67
mavv 180,116,44
mavv 184,120,44
s_add_u32 m0, s32, 35840
mavv 188,124,44
mavv 192,96,56
rqv 44,2,10240
mavv 196,100,56
s_add_u32 s61, s61, s30
mavv 200,104,56
dm 13,20,61
mavv 204,108,56
mavv 208,112,56
mavv 212,116,56
s_add_u32 s29, s29, s65
mavv 216,120,56
mavv 220,124,56
buffer_load_dwordx4 v[224:227], v16, s[12:15], 0 offen
mavv 224,96,60
mavv 228,100,60
buffer_load_dwordx4 v[228:231], v18, s[8:11], s16 offen
mavv 232,104,60
s_mov_b32 s68, s88
mavv 236,108,60
s_mov_b32 s69, s68
mavv 240,112,60
buffer_load_dwordx4 v[232:235], v18, s[8:11], s16 offen offset:896
mavv 244,116,60
buffer_store_dwordx4 v[128:131], v16, s[84:87], s69 offen sc1
mavv 248,120,60
s_add_u32 s69, s69, s51
mavv 252,124,60
buffer_store_dwordx4 v[132:135], v16, s[84:87], s69 offen sc1
s_waitcnt vmcnt(22) lgkmcnt(0)
s_barrier
mavv 0,64,24
rt 96,4,16384
mavv 4,68,24
s_add_u32 m0, s33, 0
mavv 8,72,24
rt 98,4,18432
mavv 12,76,24
s_mov_b32 s67, s29
mavv 16,80,24
rt 100,5,16384
mavv 20,84,24
dm 14,24,67
mavv 24,88,24
rt 102,5,18432
mavv 28,92,24
s_add_u32 m0, s32, 49152
mavv 32,64,28
rt 104,6,16384
mavv 36,68,28
rqv 24,3,0
mavv 40,72,28
s_add_u32 s61, s28, s64
mavv 44,76,28
rt 106,6,18432
mavv 48,80,28
dm 12,20,61
mavv 52,84,28
rt 108,7,16384
mavv 56,88,28
rt 110,7,18432
mavv 60,92,28
s_add_u32 m0, s33, 1024
mavv 64,64,32
rt 112,8,16384
mavv 68,68,32
rqv 28,3,2048
mavv 72,72,32
s_add_u32 s67, s67, s31
mavv 76,76,32
rt 114,8,18432
mavv 80,80,32
dm 15,24,67
mavv 84,84,32
rt 116,9,16384
mavv 88,88,32
s_add_u32 m0, s32, 50176
mavv 92,92,32
rt 118,9,18432
mavv 96,64,36
s_add_u32 s61, s61, s30
mavv 100,68,36
rt 120,10,16384
mavv 104,72,36
rqv 32,3,4096
mavv 108,76,36
rt 122,10,18432
mavv 112,80,36
dm 13,20,61
mavv 116,84,36
rt 124,11,16384
mavv 120,88,36
s_add_u32 m0, s33, 2048
mavv 124,92,36
rt 126,11,18432
mavv 128,64,40
s_add_u32 s67, s67, s31
mavv 132,68,40
rqv 36,3,6144
mavv 136,72,40
rqv 56,3,12288
mavv 140,76,40
dm 14,24,67
mavv 144,80,40
s_add_u32 m0, s32, 51200
mavv 148,84,40
mavv 152,88,40
s_add_u32 s61, s61, s30
mavv 156,92,40
mavv 160,64,44
rqv 40,3,8192
mavv 164,68,44
dm 12,20,61
mavv 168,72,44
rqv 60,3,14336
mavv 172,76,44
s_add_u32 m0, s33, 3072
)ASM" ::: "memory");
  asm volatile(R"ASM(
mavv 176,80,44
mavv 180,84,44
s_add_u32 s67, s67, s31
mavv 184,88,44
mavv 188,92,44
dm 15,24,67
mavv 192,64,48
rqv 44,3,10240
mavv 196,68,48
mavv 200,72,48
s_add_u32 m0, s32, 52224
mavv 204,76,48
mavv 208,80,48
s_add_u32 s61, s61, s30
mavv 212,84,48
mavv 216,88,48
dm 13,20,61
mavv 220,92,48
mavv 224,64,52
mavv 228,68,52
s_add_u32 s29, s29, s65
mavv 232,72,52
s_add_u32 s69, s69, s51
mavv 236,76,52
buffer_store_dwordx4 v[136:139], v16, s[84:87], s69 offen sc1
mavv 240,80,52
s_add_u32 s28, s28, 128
mavv 244,84,52
s_add_u32 s68, s68, 0x400
mavv 248,88,52
s_mov_b32 s69, s68
mavv 252,92,52
buffer_store_dwordx4 v[140:143], v16, s[84:87], s69 offen sc1
s_waitcnt vmcnt(23) lgkmcnt(0)
s_barrier
mavv 0,96,24
rt 64,4,32768
mavv 4,100,24
s_add_u32 m0, s33, 16384
mavv 8,104,24
rt 66,4,34816
mavv 12,108,24
s_mov_b32 s67, s29
mavv 16,112,24
rt 68,5,32768
mavv 20,116,24
dm 14,24,67
mavv 24,120,24
rt 70,5,34816
mavv 28,124,24
s_add_u32 m0, s32, 65536
mavv 32,96,28
rt 72,6,32768
mavv 36,100,28
rqv 24,0,0
mavv 40,104,28
s_mov_b32 s61, s28
mavv 44,108,28
rt 74,6,34816
mavv 48,112,28
dm 12,20,61
mavv 52,116,28
rt 76,7,32768
mavv 56,120,28
s_add_u32 m0, s33, 17408
mavv 60,124,28
rt 78,7,34816
mavv 64,96,32
rt 80,8,32768
mavv 68,100,32
rqv 28,0,2048
mavv 72,104,32
s_add_u32 s67, s67, s31
mavv 76,108,32
rt 82,8,34816
mavv 80,112,32
dm 15,24,67
mavv 84,116,32
rt 84,9,32768
mavv 88,120,32
s_add_u32 m0, s32, 66560
mavv 92,124,32
rt 86,9,34816
mavv 96,96,36
s_add_u32 s61, s61, s30
mavv 100,100,36
rt 88,10,32768
mavv 104,104,36
rqv 32,0,4096
mavv 108,108,36
dm 13,20,61
mavv 112,112,36
rt 90,10,34816
mavv 116,116,36
s_add_u32 m0, s33, 18432
mavv 120,120,36
rt 92,11,32768
mavv 124,124,36
s_add_u32 s67, s67, s31
mavv 128,96,40
rt 94,11,34816
mavv 132,100,40
rqv 36,0,6144
mavv 136,104,40
dm 14,24,67
mavv 140,108,40
rqv 48,0,12288
mavv 144,112,40
s_add_u32 m0, s32, 67584
mavv 148,116,40
s_add_u32 s61, s61, s30
mavv 152,120,40
mavv 156,124,40
dm 12,20,61
mavv 160,96,44
rqv 40,0,8192
mavv 164,100,44
rqv 52,0,14336
mavv 168,104,44
s_add_u32 m0, s33, 19456
mavv 172,108,44
s_add_u32 s67, s67, s31
mavv 176,112,44
mavv 180,116,44
dm 15,24,67
mavv 184,120,44
mavv 188,124,44
mavv 192,96,56
rqv 44,0,10240
mavv 196,100,56
s_add_u32 m0, s32, 68608
mavv 200,104,56
s_add_u32 s61, s61, s30
mavv 204,108,56
mavv 208,112,56
dm 13,20,61
mavv 212,116,56
mavv 216,120,56
mavv 220,124,56
s_add_u32 s29, s29, s65
mavv 224,96,60
mavv 228,100,60
s_add_u32 s74, s78, .Lk1_BTT+12288-.Lk1_base
s_addc_u32 s75, s79, 0
mavv 232,104,60
s_add_u32 s69, s69, s51
mavv 236,108,60
global_load_dword v19, v236, s[74:75]
mavv 240,112,60
buffer_store_dwordx4 v[144:147], v16, s[84:87], s69 offen sc1
mavv 244,116,60
s_add_u32 s69, s69, s51
mavv 248,120,60
buffer_store_dwordx4 v[148:151], v16, s[84:87], s69 offen sc1
mavv 252,124,60
s_waitcnt vmcnt(21) lgkmcnt(0)
s_barrier
mavv 0,64,24
rt 96,4,49152
mavv 4,68,24
s_add_u32 m0, s33, 32768
mavv 8,72,24
rt 98,4,51200
mavv 12,76,24
s_mov_b32 s67, s29
mavv 16,80,24
rt 100,5,49152
mavv 20,84,24
dm 14,24,67
mavv 24,88,24
rt 102,5,51200
mavv 28,92,24
s_add_u32 m0, s32, 81920
mavv 32,64,28
rt 104,6,49152
mavv 36,68,28
rqv 24,1,0
mavv 40,72,28
s_add_u32 s61, s28, s64
mavv 44,76,28
rt 106,6,51200
mavv 48,80,28
dm 12,20,61
mavv 52,84,28
rt 108,7,49152
mavv 56,88,28
rt 110,7,51200
mavv 60,92,28
s_add_u32 m0, s33, 33792
mavv 64,64,32
rt 112,8,49152
mavv 68,68,32
rqv 28,1,2048
mavv 72,72,32
s_add_u32 s67, s67, s31
mavv 76,76,32
rt 114,8,51200
mavv 80,80,32
dm 15,24,67
mavv 84,84,32
rt 116,9,49152
mavv 88,88,32
s_add_u32 m0, s32, 82944
mavv 92,92,32
rt 118,9,51200
mavv 96,64,36
s_add_u32 s61, s61, s30
mavv 100,68,36
rt 120,10,49152
mavv 104,72,36
rqv 32,1,4096
mavv 108,76,36
rt 122,10,51200
mavv 112,80,36
dm 13,20,61
mavv 116,84,36
rt 124,11,49152
mavv 120,88,36
s_add_u32 m0, s33, 34816
mavv 124,92,36
rt 126,11,51200
mavv 128,64,40
s_add_u32 s67, s67, s31
mavv 132,68,40
rqv 36,1,6144
mavv 136,72,40
rqv 56,1,12288
mavv 140,76,40
dm 14,24,67
mavv 144,80,40
s_add_u32 m0, s32, 83968
mavv 148,84,40
mavv 152,88,40
s_add_u32 s61, s61, s30
mavv 156,92,40
mavv 160,64,44
rqv 40,1,8192
mavv 164,68,44
dm 12,20,61
mavv 168,72,44
rqv 60,1,14336
mavv 172,76,44
s_add_u32 m0, s33, 35840
mavv 176,80,44
mavv 180,84,44
s_add_u32 s67, s67, s31
mavv 184,88,44
mavv 188,92,44
dm 15,24,67
mavv 192,64,48
rqv 44,1,10240
mavv 196,68,48
mavv 200,72,48
s_add_u32 m0, s32, 84992
mavv 204,76,48
mavv 208,80,48
s_add_u32 s61, s61, s30
mavv 212,84,48
mavv 216,88,48
dm 13,20,61
mavv 220,92,48
mavv 224,64,52
mavv 228,68,52
s_add_u32 s29, s29, s65
mavv 232,72,52
s_add_u32 s68, s68, 0x400
mavv 236,76,52
s_mov_b32 s69, s68
mavv 240,80,52
s_add_u32 s28, s28, 128
mavv 244,84,52
buffer_store_dwordx4 v[152:155], v16, s[84:87], s69 offen sc1
mavv 248,88,52
s_add_u32 s69, s69, s51
mavv 252,92,52
buffer_store_dwordx4 v[156:159], v16, s[84:87], s69 offen sc1
s_waitcnt vmcnt(21) lgkmcnt(0)
s_barrier
mavv 0,96,24
rt 64,4,0
mavv 4,100,24
s_add_u32 m0, s33, 49152
mavv 8,104,24
s_mov_b32 s67, s29
mavv 12,108,24
rt 66,4,2048
mavv 16,112,24
dm 14,24,67
mavv 20,116,24
s_add_u32 m0, s32, 0
mavv 24,120,24
rt 68,5,0
s_mov_b32 s61, s28
mavv 28,124,24
rt 70,5,2048
mavv 32,96,28
dm 12,20,61
s_add_u32 m0, s33, 50176
mavv 36,100,28
rt 72,6,0
mavv 40,104,28
rqv 24,0,32768
s_add_u32 s67, s67, s31
mavv 44,108,28
rt 74,6,2048
mavv 48,112,28
dm 15,24,67
mavv 52,116,28
s_add_u32 m0, s32, 1024
mavv 56,120,28
rt 76,7,0
s_add_u32 s61, s61, s30
mavv 60,124,28
rt 78,7,2048
mavv 64,96,32
dm 13,20,61
s_add_u32 m0, s33, 51200
mavv 68,100,32
rt 80,8,0
mavv 72,104,32
rqv 28,0,34816
s_add_u32 s67, s67, s31
mavv 76,108,32
rt 82,8,2048
mavv 80,112,32
dm 14,24,67
mavv 84,116,32
s_add_u32 m0, s32, 2048
mavv 88,120,32
rt 84,9,0
s_add_u32 s61, s61, s30
mavv 92,124,32
rt 86,9,2048
mavv 96,96,36
dm 12,20,61
s_add_u32 m0, s33, 52224
mavv 100,100,36
rt 88,10,0
mavv 104,104,36
rqv 32,0,36864
s_add_u32 s67, s67, s31
mavv 108,108,36
rt 90,10,2048
mavv 112,112,36
dm 15,24,67
mavv 116,116,36
s_add_u32 m0, s32, 3072
mavv 120,120,36
rt 92,11,0
s_add_u32 s61, s61, s30
mavv 124,124,36
rt 94,11,2048
mavv 128,96,40
dm 13,20,61
s_add_u32 s29, s29, s65
mavv 132,100,40
rqv 36,0,38912
mavv 136,104,40
v_dot2_f32_bf16 v20, v228, v224, 0
mavv 140,108,40
rqv 48,0,45056
mavv 144,112,40
v_dot2_f32_bf16 v21, v232, v224, 0
mavv 148,116,40
v_dot2_f32_bf16 v20, v229, v225, v20
mavv 152,120,40
v_dot2_f32_bf16 v21, v233, v225, v21
mavv 156,124,40
v_dot2_f32_bf16 v20, v230, v226, v20
mavv 160,96,44
v_dot2_f32_bf16 v21, v234, v226, v21
mavv 164,100,44
rqv 40,0,40960
mavv 168,104,44
v_dot2_f32_bf16 v20, v231, v227, v20
mavv 172,108,44
rqv 52,0,47104
mavv 176,112,44
v_dot2_f32_bf16 v21, v235, v227, v21
mavv 180,116,44
s_nop 1
mavv 184,120,44
v_add_f32_dpp v20, v20, v20 row_shr:8 row_mask:0xf bank_mask:0xf bound_ctrl:0
mavv 188,124,44
v_add_f32_dpp v21, v21, v21 row_shr:8 row_mask:0xf bank_mask:0xf bound_ctrl:0
mavv 192,96,56
v_add_f32_dpp v20, v20, v20 row_shr:4 row_mask:0xf bank_mask:0xf bound_ctrl:0
mavv 196,100,56
rqv 44,0,43008
mavv 200,104,56
v_add_f32_dpp v21, v21, v21 row_shr:4 row_mask:0xf bank_mask:0xf bound_ctrl:0
mavv 204,108,56
v_add_f32_dpp v20, v20, v20 row_shr:2 row_mask:0xf bank_mask:0xf bound_ctrl:0
mavv 208,112,56
v_add_f32_dpp v21, v21, v21 row_shr:2 row_mask:0xf bank_mask:0xf bound_ctrl:0
mavv 212,116,56
v_add_f32_dpp v20, v20, v20 row_shr:1 row_mask:0xf bank_mask:0xf bound_ctrl:0
mavv 216,120,56
v_add_f32_dpp v21, v21, v21 row_shr:1 row_mask:0xf bank_mask:0xf bound_ctrl:0
mavv 220,124,56
v_mov_b32 v224, v20
mavv 224,96,60
v_mov_b32 v225, v21
mavv 228,100,60
s_add_u32 s69, s69, s51
mavv 232,104,60
v_permlane16_swap_b32 v20, v224
mavv 236,108,60
buffer_store_dwordx4 v[160:163], v16, s[84:87], s69 offen sc1
mavv 240,112,60
v_permlane16_swap_b32 v21, v225
s_add_u32 s68, s68, 0x400
mavv 244,116,60
v_add_f32 v20, v20, v224
mavv 248,120,60
s_mov_b32 s69, s68
mavv 252,124,60
buffer_store_dwordx4 v[164:167], v16, s[84:87], s69 offen sc1
s_waitcnt vmcnt(20) lgkmcnt(0)
s_barrier
mavv 0,64,24
rt 96,4,16384
mavv 4,68,24
s_add_u32 m0, s33, 0
mavv 8,72,24
s_mov_b32 s67, s29
mavv 12,76,24
rt 98,4,18432
mavv 16,80,24
dm 14,24,67
s_add_u32 m0, s32, 16384
mavv 20,84,24
rt 100,5,16384
mavv 24,88,24
s_add_u32 s61, s28, s64
mavv 28,92,24
rt 102,5,18432
mavv 32,64,28
dm 12,20,61
s_add_u32 m0, s33, 1024
rt 104,6,16384
mavv 36,68,28
rqv 24,1,32768
mavv 40,72,28
s_add_u32 s67, s67, s31
dm 15,24,67
mavv 44,76,28
rt 106,6,18432
mavv 48,80,28
s_add_u32 m0, s32, 17408
mavv 52,84,28
rt 108,7,16384
mavv 56,88,28
s_add_u32 s61, s61, s30
dm 13,20,61
mavv 60,92,28
rt 110,7,18432
mavv 64,64,32
s_add_u32 m0, s33, 2048
s_add_u32 s67, s67, s31
mavv 68,68,32
rt 112,8,16384
mavv 72,72,32
rqv 28,1,34816
dm 14,24,67
mavv 76,76,32
rt 114,8,18432
mavv 80,80,32
s_add_u32 m0, s32, 18432
s_add_u32 s61, s61, s30
mavv 84,84,32
rt 116,9,16384
mavv 88,88,32
dm 12,20,61
mavv 92,92,32
s_add_u32 m0, s33, 3072
mavv 96,64,36
rt 118,9,18432
s_add_u32 s67, s67, s31
rt 120,10,16384
mavv 100,68,36
rqv 32,1,36864
mavv 104,72,36
dm 15,24,67
s_add_u32 m0, s32, 19456
mavv 108,76,36
rt 122,10,18432
mavv 112,80,36
s_add_u32 s61, s61, s30
mavv 116,84,36
dm 13,20,61
mavv 120,88,36
rt 124,11,16384
s_add_u32 s29, s29, s65
mavv 124,92,36
rt 126,11,18432
mavv 128,64,40
s_add_u32 s28, s28, 128
s_add_u32 s74, s78, .Lk1_BTT+16384-.Lk1_base
s_addc_u32 s75, s79, 0
mavv 132,68,40
rqv 36,1,38912
mavv 136,72,40
global_load_dword v19, v236, s[74:75]
mavv 140,76,40
rqv 56,1,45056
mavv 144,80,40
v_add_f32 v21, v21, v225
mavv 148,84,40
v_mov_b32 v224, v20
mavv 152,88,40
v_mov_b32 v225, v21
mavv 156,92,40
v_permlane32_swap_b32 v20, v224
mavv 160,64,44
v_permlane32_swap_b32 v21, v225
mavv 164,68,44
rqv 40,1,40960
v_add_f32 v20, v20, v224
mavv 168,72,44
rqv 60,1,47104
mavv 172,76,44
v_add_f32 v21, v21, v225
mavv 176,80,44
cv 20,20,21
mavv 180,84,44
v_and_b32 v21, 0xffff0000, v20
mavv 184,88,44
v_lshlrev_b32 v20, 16, v20
mavv 188,92,44
v_mul_f32 v20, 0xbfb8aa3b, v20
mavv 192,64,48
v_mul_f32 v21, 0xbfb8aa3b, v21
mavv 196,68,48
rqv 44,1,43008
mavv 200,72,48
ex 20
mavv 204,76,48
ex 21
mavv 208,80,48
v_add_f32 v20, 1.0, v20
mavv 212,84,48
v_add_f32 v21, 1.0, v21
mavv 216,88,48
v_rcp_f32 v20, v20
mavv 220,92,48
v_rcp_f32 v21, v21
mavv 224,64,52
v_mov_b32 v22, 0
mavv 228,68,52
cv 20,20,21
mavv 232,72,52
v_and_b32 v21, 0xffff0000, v20
mavv 236,76,52
s_add_u32 s69, s69, s51
mavv 240,80,52
v_lshlrev_b32 v20, 16, v20
buffer_store_dwordx4 v[168:171], v16, s[84:87], s69 offen sc1
mavv 244,84,52
s_mov_b32 exec_lo, 0
s_mov_b32 exec_hi, 0x80000000
global_store_dwordx2 v22, v[20:21], s[6:7] sc1
s_mov_b64 exec, -1
mavv 248,88,52
s_add_u32 s69, s69, s51
mavv 252,92,52
buffer_store_dwordx4 v[172:175], v16, s[84:87], s69 offen sc1
s_waitcnt vmcnt(22) lgkmcnt(0)
s_barrier
mavv 0,96,24
rt 64,4,32768
mavv 4,100,24
s_add_u32 m0, s33, 16384
mavv 8,104,24
rt 66,4,34816
mavv 12,108,24
s_mov_b32 s67, s29
mavv 16,112,24
rt 68,5,32768
mavv 20,116,24
dm 14,24,67
mavv 24,120,24
rt 70,5,34816
mavv 28,124,24
s_add_u32 m0, s32, 32768
mavv 32,96,28
rt 72,6,32768
mavv 36,100,28
rqv 24,2,0
mavv 40,104,28
s_mov_b32 s61, s28
mavv 44,108,28
rt 74,6,34816
mavv 48,112,28
rt 76,7,32768
mavv 52,116,28
dm 12,20,61
mavv 56,120,28
rt 78,7,34816
mavv 60,124,28
)ASM" ::: "memory");
  asm volatile(R"ASM(
s_add_u32 m0, s33, 17408
mavv 64,96,32
rt 80,8,32768
mavv 68,100,32
rqv 28,2,2048
mavv 72,104,32
s_add_u32 s67, s67, s31
mavv 76,108,32
rt 82,8,34816
mavv 80,112,32
dm 15,24,67
mavv 84,116,32
rt 84,9,32768
mavv 88,120,32
rt 86,9,34816
mavv 92,124,32
s_add_u32 m0, s32, 33792
mavv 96,96,36
rt 88,10,32768
mavv 100,100,36
rqv 32,2,4096
mavv 104,104,36
s_add_u32 s61, s61, s30
mavv 108,108,36
rt 90,10,34816
mavv 112,112,36
dm 13,20,61
mavv 116,116,36
rt 92,11,32768
mavv 120,120,36
s_add_u32 m0, s33, 18432
mavv 124,124,36
rt 94,11,34816
mavv 128,96,40
rqv 36,2,6144
mavv 132,100,40
s_add_u32 s67, s67, s31
mavv 136,104,40
rqv 48,2,12288
mavv 140,108,40
dm 14,24,67
mavv 144,112,40
mavv 148,116,40
s_add_u32 m0, s32, 34816
mavv 152,120,40
mavv 156,124,40
s_add_u32 s61, s61, s30
mavv 160,96,44
rqv 40,2,8192
mavv 164,100,44
rqv 52,2,14336
mavv 168,104,44
dm 12,20,61
mavv 172,108,44
mavv 176,112,44
s_add_u32 m0, s33, 19456
mavv 180,116,44
mavv 184,120,44
mavv 188,124,44
s_add_u32 s67, s67, s31
mavv 192,96,56
rqv 44,2,10240
mavv 196,100,56
dm 15,24,67
mavv 200,104,56
mavv 204,108,56
mavv 208,112,56
s_add_u32 m0, s32, 35840
mavv 212,116,56
mavv 216,120,56
s_add_u32 s61, s61, s30
mavv 220,124,56
mavv 224,96,60
mavv 228,100,60
dm 13,20,61
mavv 232,104,60
s_add_u32 s68, s68, 0x400
mavv 236,108,60
s_mov_b32 s69, s68
mavv 240,112,60
s_add_u32 s29, s29, s65
mavv 244,116,60
buffer_store_dwordx4 v[176:179], v16, s[84:87], s69 offen sc1
mavv 248,120,60
s_add_u32 s69, s69, s51
mavv 252,124,60
buffer_store_dwordx4 v[180:183], v16, s[84:87], s69 offen sc1
s_waitcnt vmcnt(22) lgkmcnt(0)
s_barrier
mavv 0,64,24
rt 96,4,49152
mavv 4,68,24
s_add_u32 m0, s33, 32768
mavv 8,72,24
rt 98,4,51200
mavv 12,76,24
s_mov_b32 s67, s29
mavv 16,80,24
rt 100,5,49152
mavv 20,84,24
dm 14,24,67
mavv 24,88,24
rt 102,5,51200
mavv 28,92,24
s_add_u32 m0, s32, 49152
mavv 32,64,28
rt 104,6,49152
mavv 36,68,28
rqv 24,3,0
mavv 40,72,28
s_add_u32 s61, s28, s64
mavv 44,76,28
rt 106,6,51200
mavv 48,80,28
dm 12,20,61
mavv 52,84,28
rt 108,7,49152
mavv 56,88,28
rt 110,7,51200
mavv 60,92,28
s_add_u32 m0, s33, 33792
mavv 64,64,32
rt 112,8,49152
mavv 68,68,32
rqv 28,3,2048
mavv 72,72,32
s_add_u32 s67, s67, s31
mavv 76,76,32
rt 114,8,51200
mavv 80,80,32
dm 15,24,67
mavv 84,84,32
rt 116,9,49152
mavv 88,88,32
s_add_u32 m0, s32, 50176
mavv 92,92,32
rt 118,9,51200
mavv 96,64,36
s_add_u32 s61, s61, s30
mavv 100,68,36
rt 120,10,49152
mavv 104,72,36
rqv 32,3,4096
mavv 108,76,36
rt 122,10,51200
mavv 112,80,36
dm 13,20,61
mavv 116,84,36
rt 124,11,49152
mavv 120,88,36
s_add_u32 m0, s33, 34816
mavv 124,92,36
rt 126,11,51200
mavv 128,64,40
s_add_u32 s67, s67, s31
mavv 132,68,40
rqv 36,3,6144
mavv 136,72,40
rqv 56,3,12288
mavv 140,76,40
dm 14,24,67
mavv 144,80,40
s_add_u32 m0, s32, 51200
mavv 148,84,40
mavv 152,88,40
s_add_u32 s61, s61, s30
mavv 156,92,40
mavv 160,64,44
rqv 40,3,8192
mavv 164,68,44
dm 12,20,61
mavv 168,72,44
rqv 60,3,14336
mavv 172,76,44
s_add_u32 m0, s33, 35840
mavv 176,80,44
mavv 180,84,44
s_add_u32 s67, s67, s31
mavv 184,88,44
mavv 188,92,44
dm 15,24,67
mavv 192,64,48
rqv 44,3,10240
mavv 196,68,48
mavv 200,72,48
s_add_u32 m0, s32, 52224
mavv 204,76,48
mavv 208,80,48
s_add_u32 s61, s61, s30
mavv 212,84,48
mavv 216,88,48
dm 13,20,61
mavv 220,92,48
mavv 224,64,52
mavv 228,68,52
s_add_u32 s29, s29, s65
mavv 232,72,52
s_add_u32 s69, s69, s51
mavv 236,76,52
buffer_store_dwordx4 v[184:187], v16, s[84:87], s69 offen sc1
mavv 240,80,52
s_add_u32 s28, s28, 128
mavv 244,84,52
s_add_u32 s68, s68, 0x400
mavv 248,88,52
s_mov_b32 s69, s68
mavv 252,92,52
buffer_store_dwordx4 v[188:191], v16, s[84:87], s69 offen sc1
s_waitcnt vmcnt(20) lgkmcnt(0)
s_barrier
mavv 0,96,24
rt 64,4,0
mavv 4,100,24
s_add_u32 m0, s33, 49152
mavv 8,104,24
rt 66,4,2048
mavv 12,108,24
s_mov_b32 s67, s29
mavv 16,112,24
rt 68,5,0
mavv 20,116,24
dm 14,24,67
mavv 24,120,24
rt 70,5,2048
mavv 28,124,24
s_add_u32 m0, s32, 65536
mavv 32,96,28
rt 72,6,0
mavv 36,100,28
rqv 24,0,0
mavv 40,104,28
s_mov_b32 s61, s28
mavv 44,108,28
rt 74,6,2048
mavv 48,112,28
dm 12,20,61
mavv 52,116,28
rt 76,7,0
mavv 56,120,28
s_add_u32 m0, s33, 50176
mavv 60,124,28
rt 78,7,2048
mavv 64,96,32
rt 80,8,0
mavv 68,100,32
rqv 28,0,2048
mavv 72,104,32
s_add_u32 s67, s67, s31
mavv 76,108,32
rt 82,8,2048
mavv 80,112,32
dm 15,24,67
mavv 84,116,32
rt 84,9,0
mavv 88,120,32
s_add_u32 m0, s32, 66560
mavv 92,124,32
rt 86,9,2048
mavv 96,96,36
s_add_u32 s61, s61, s30
mavv 100,100,36
rt 88,10,0
mavv 104,104,36
rqv 32,0,4096
mavv 108,108,36
dm 13,20,61
mavv 112,112,36
rt 90,10,2048
mavv 116,116,36
s_add_u32 m0, s33, 51200
mavv 120,120,36
rt 92,11,0
mavv 124,124,36
s_add_u32 s67, s67, s31
mavv 128,96,40
rt 94,11,2048
mavv 132,100,40
rqv 36,0,6144
mavv 136,104,40
dm 14,24,67
mavv 140,108,40
rqv 48,0,12288
mavv 144,112,40
s_add_u32 m0, s32, 67584
mavv 148,116,40
s_add_u32 s61, s61, s30
mavv 152,120,40
mavv 156,124,40
dm 12,20,61
mavv 160,96,44
rqv 40,0,8192
mavv 164,100,44
rqv 52,0,14336
mavv 168,104,44
s_add_u32 m0, s33, 52224
mavv 172,108,44
s_add_u32 s67, s67, s31
mavv 176,112,44
mavv 180,116,44
dm 15,24,67
mavv 184,120,44
mavv 188,124,44
mavv 192,96,56
rqv 44,0,10240
mavv 196,100,56
s_add_u32 m0, s32, 68608
mavv 200,104,56
s_add_u32 s61, s61, s30
mavv 204,108,56
mavv 208,112,56
dm 13,20,61
mavv 212,116,56
mavv 216,120,56
mavv 220,124,56
s_add_u32 s29, s29, s65
mavv 224,96,60
mavv 228,100,60
s_add_u32 s74, s78, .Lk1_BT+0-.Lk1_base
s_addc_u32 s75, s79, 0
mavv 232,104,60
s_add_u32 s69, s69, s51
mavv 236,108,60
global_load_dword v19, v236, s[74:75]
mavv 240,112,60
buffer_store_dwordx4 v[192:195], v16, s[84:87], s69 offen sc1
mavv 244,116,60
s_add_u32 s69, s69, s51
mavv 248,120,60
buffer_store_dwordx4 v[196:199], v16, s[84:87], s69 offen sc1
mavv 252,124,60
s_waitcnt vmcnt(21) lgkmcnt(0)
s_barrier
mavv 0,64,24
rt 96,4,16384
mavv 4,68,24
s_add_u32 m0, s33, 0
mavv 8,72,24
rt 98,4,18432
mavv 12,76,24
s_mov_b32 s67, s29
mavv 16,80,24
rt 100,5,16384
mavv 20,84,24
dm 14,24,67
mavv 24,88,24
rt 102,5,18432
mavv 28,92,24
s_add_u32 m0, s32, 81920
mavv 32,64,28
rt 104,6,16384
mavv 36,68,28
rqv 24,1,0
mavv 40,72,28
s_add_u32 s61, s28, s64
mavv 44,76,28
rt 106,6,18432
mavv 48,80,28
dm 12,20,61
mavv 52,84,28
rt 108,7,16384
mavv 56,88,28
rt 110,7,18432
mavv 60,92,28
s_add_u32 m0, s33, 1024
mavv 64,64,32
rt 112,8,16384
mavv 68,68,32
rqv 28,1,2048
mavv 72,72,32
s_add_u32 s67, s67, s31
mavv 76,76,32
rt 114,8,18432
mavv 80,80,32
dm 15,24,67
mavv 84,84,32
rt 116,9,16384
mavv 88,88,32
s_add_u32 m0, s32, 82944
mavv 92,92,32
rt 118,9,18432
mavv 96,64,36
s_add_u32 s61, s61, s30
mavv 100,68,36
rt 120,10,16384
mavv 104,72,36
rqv 32,1,4096
mavv 108,76,36
rt 122,10,18432
mavv 112,80,36
dm 13,20,61
mavv 116,84,36
rt 124,11,16384
mavv 120,88,36
s_add_u32 m0, s33, 2048
mavv 124,92,36
rt 126,11,18432
mavv 128,64,40
s_add_u32 s67, s67, s31
mavv 132,68,40
rqv 36,1,6144
mavv 136,72,40
rqv 56,1,12288
mavv 140,76,40
dm 14,24,67
mavv 144,80,40
s_add_u32 m0, s32, 83968
mavv 148,84,40
mavv 152,88,40
s_add_u32 s61, s61, s30
mavv 156,92,40
mavv 160,64,44
rqv 40,1,8192
mavv 164,68,44
dm 12,20,61
mavv 168,72,44
rqv 60,1,14336
mavv 172,76,44
s_add_u32 m0, s33, 3072
mavv 176,80,44
mavv 180,84,44
s_add_u32 s67, s67, s31
mavv 184,88,44
mavv 188,92,44
dm 15,24,67
mavv 192,64,48
rqv 44,1,10240
mavv 196,68,48
mavv 200,72,48
s_add_u32 m0, s32, 84992
mavv 204,76,48
mavv 208,80,48
s_add_u32 s61, s61, s30
mavv 212,84,48
mavv 216,88,48
dm 13,20,61
mavv 220,92,48
mavv 224,64,52
mavv 228,68,52
s_add_u32 s29, s29, s65
mavv 232,72,52
s_add_u32 s68, s68, 0x400
mavv 236,76,52
s_mov_b32 s69, s68
mavv 240,80,52
s_add_u32 s28, s28, 128
mavv 244,84,52
buffer_store_dwordx4 v[200:203], v16, s[84:87], s69 offen sc1
mavv 248,88,52
s_add_u32 s69, s69, s51
mavv 252,92,52
buffer_store_dwordx4 v[204:207], v16, s[84:87], s69 offen sc1
s_waitcnt vmcnt(21) lgkmcnt(0)
s_barrier
mavv 0,96,24
rt 64,4,32768
mavv 4,100,24
s_add_u32 m0, s33, 16384
mavv 8,104,24
rt 66,4,34816
mavv 12,108,24
s_mov_b32 s67, s29
mavv 16,112,24
rt 68,5,32768
mavv 20,116,24
dm 14,24,67
mavv 24,120,24
rt 70,5,34816
mavv 28,124,24
s_add_u32 m0, s32, 0
mavv 32,96,28
rt 72,6,32768
mavv 36,100,28
rqv 24,0,32768
mavv 40,104,28
s_mov_b32 s61, s28
mavv 44,108,28
rt 74,6,34816
mavv 48,112,28
rt 76,7,32768
mavv 52,116,28
dm 12,20,61
mavv 56,120,28
rt 78,7,34816
mavv 60,124,28
s_add_u32 m0, s33, 17408
mavv 64,96,32
rt 80,8,32768
mavv 68,100,32
rqv 28,0,34816
mavv 72,104,32
s_add_u32 s67, s67, s31
mavv 76,108,32
rt 82,8,34816
mavv 80,112,32
dm 15,24,67
mavv 84,116,32
rt 84,9,32768
mavv 88,120,32
rt 86,9,34816
mavv 92,124,32
s_add_u32 m0, s32, 1024
mavv 96,96,36
rt 88,10,32768
mavv 100,100,36
rqv 32,0,36864
mavv 104,104,36
s_add_u32 s61, s61, s30
mavv 108,108,36
rt 90,10,34816
mavv 112,112,36
dm 13,20,61
mavv 116,116,36
rt 92,11,32768
mavv 120,120,36
s_add_u32 m0, s33, 18432
mavv 124,124,36
rt 94,11,34816
mavv 128,96,40
rqv 36,0,38912
mavv 132,100,40
s_add_u32 s67, s67, s31
mavv 136,104,40
rqv 48,0,45056
mavv 140,108,40
dm 14,24,67
mavv 144,112,40
mavv 148,116,40
s_add_u32 m0, s32, 2048
mavv 152,120,40
mavv 156,124,40
s_add_u32 s61, s61, s30
mavv 160,96,44
rqv 40,0,40960
mavv 164,100,44
rqv 52,0,47104
mavv 168,104,44
dm 12,20,61
mavv 172,108,44
mavv 176,112,44
s_add_u32 m0, s33, 19456
mavv 180,116,44
mavv 184,120,44
mavv 188,124,44
s_add_u32 s67, s67, s31
mavv 192,96,56
rqv 44,0,43008
mavv 196,100,56
dm 15,24,67
mavv 200,104,56
mavv 204,108,56
mavv 208,112,56
s_add_u32 m0, s32, 3072
mavv 212,116,56
mavv 216,120,56
s_add_u32 s61, s61, s30
mavv 220,124,56
mavv 224,96,60
mavv 228,100,60
dm 13,20,61
mavv 232,104,60
s_add_u32 s69, s69, s51
mavv 236,108,60
buffer_store_dwordx4 v[208:211], v16, s[84:87], s69 offen sc1
mavv 240,112,60
s_add_u32 s29, s29, s65
mavv 244,116,60
s_add_u32 s68, s68, 0x400
mavv 248,120,60
s_mov_b32 s69, s68
mavv 252,124,60
buffer_store_dwordx4 v[212:215], v16, s[84:87], s69 offen sc1
s_waitcnt vmcnt(20) lgkmcnt(0)
s_barrier
mavv 0,64,24
rt 96,4,49152
mavv 4,68,24
s_add_u32 m0, s33, 32768
mavv 8,72,24
rt 98,4,51200
mavv 12,76,24
s_mov_b32 s67, s29
mavv 16,80,24
rt 100,5,49152
mavv 20,84,24
dm 14,24,67
mavv 24,88,24
rt 102,5,51200
mavv 28,92,24
s_add_u32 m0, s32, 16384
mavv 32,64,28
rt 104,6,49152
mavv 36,68,28
rqv 24,1,32768
mavv 40,72,28
s_add_u32 s61, s28, s64
mavv 44,76,28
rt 106,6,51200
mavv 48,80,28
dm 12,20,61
mavv 52,84,28
)ASM" ::: "memory");
  asm volatile(R"ASM(
rt 108,7,49152
mavv 56,88,28
s_add_u32 m0, s33, 33792
mavv 60,92,28
rt 110,7,51200
mavv 64,64,32
s_add_u32 s67, s67, s31
mavv 68,68,32
rt 112,8,49152
mavv 72,72,32
rqv 28,1,34816
mavv 76,76,32
dm 15,24,67
mavv 80,80,32
rt 114,8,51200
mavv 84,84,32
s_add_u32 m0, s32, 17408
mavv 88,88,32
rt 116,9,49152
mavv 92,92,32
rt 118,9,51200
mavv 96,64,36
s_add_u32 s61, s61, s30
rt 120,10,49152
mavv 100,68,36
rqv 32,1,36864
mavv 104,72,36
dm 13,20,61
mavv 108,76,36
rt 122,10,51200
mavv 112,80,36
s_add_u32 m0, s33, 34816
mavv 116,84,36
rt 124,11,49152
mavv 120,88,36
s_add_u32 s67, s67, s31
mavv 124,92,36
rt 126,11,51200
mavv 128,64,40
dm 14,24,67
mavv 132,68,40
rqv 36,1,38912
mavv 136,72,40
rqv 56,1,45056
mavv 140,76,40
s_add_u32 m0, s32, 18432
mavv 144,80,40
s_add_u32 s61, s61, s30
mavv 148,84,40
dm 12,20,61
mavv 152,88,40
mavv 156,92,40
s_add_u32 m0, s33, 35840
mavv 160,64,44
rqv 40,1,40960
mavv 164,68,44
rqv 60,1,47104
mavv 168,72,44
s_add_u32 s67, s67, s31
mavv 172,76,44
mavv 176,80,44
dm 15,24,67
mavv 180,84,44
mavv 184,88,44
s_add_u32 m0, s32, 19456
mavv 188,92,44
mavv 192,64,48
rqv 44,1,43008
mavv 196,68,48
s_add_u32 s61, s61, s30
mavv 200,72,48
dm 13,20,61
mavv 204,76,48
mavv 208,80,48
mavv 212,84,48
s_add_u32 s29, s29, s65
mavv 216,88,48
mavv 220,92,48
s_add_u32 s28, s28, 128
mavv 224,64,52
mavv 228,68,52
s_add_u32 s74, s78, .Lk1_BT+4096-.Lk1_base
s_addc_u32 s75, s79, 0
mavv 232,72,52
s_add_u32 s69, s69, s51
mavv 236,76,52
global_load_dword v19, v236, s[74:75]
mavv 240,80,52
buffer_store_dwordx4 v[216:219], v16, s[84:87], s69 offen sc1
mavv 244,84,52
s_add_u32 s69, s69, s51
mavv 248,88,52
buffer_store_dwordx4 v[220:223], v16, s[84:87], s69 offen sc1
mavv 252,92,52
s_branch .Lk1_BT
.Lk1_BTN:
s_waitcnt vmcnt(19) lgkmcnt(0)
s_barrier
zavv 0,64,24
rt 96,4,49152
ar 224,64
ar 225,65
ar 226,66
ar 227,67
ar 228,68
ar 229,69
ar 230,70
zavv 4,68,24
s_add_u32 m0, s33, 32768
ar 231,71
v_pk_mul_f32 v[224:225], v[224:225], s[48:49] op_sel_hi:[1,0]
v_pk_mul_f32 v[226:227], v[226:227], s[48:49] op_sel_hi:[1,0]
v_pk_mul_f32 v[228:229], v[228:229], s[48:49] op_sel_hi:[1,0]
v_pk_mul_f32 v[230:231], v[230:231], s[48:49] op_sel_hi:[1,0]
cv 128,224,225
cv 129,226,227
zavv 8,72,24
rt 98,4,51200
cv 130,228,229
cv 131,230,231
ar 224,72
ar 225,73
ar 226,74
ar 227,75
ar 228,76
zavv 12,76,24
s_mov_b32 s67, s29
ar 229,77
ar 230,78
ar 231,79
v_pk_mul_f32 v[224:225], v[224:225], s[48:49] op_sel_hi:[1,0]
v_pk_mul_f32 v[226:227], v[226:227], s[48:49] op_sel_hi:[1,0]
v_pk_mul_f32 v[228:229], v[228:229], s[48:49] op_sel_hi:[1,0]
v_pk_mul_f32 v[230:231], v[230:231], s[48:49] op_sel_hi:[1,0]
zavv 16,80,24
rt 100,5,49152
cv 132,224,225
cv 133,226,227
cv 134,228,229
cv 135,230,231
v_cndmask_b32_e64 v132, v132, v17, s[76:77]
v_cndmask_b32_e64 v133, v133, v17, s[76:77]
v_cndmask_b32_e64 v134, v134, v17, s[76:77]
zavv 20,84,24
dm 14,24,67
v_cndmask_b32_e64 v135, v135, v17, s[76:77]
ar 224,80
ar 225,81
ar 226,82
ar 227,83
ar 228,84
ar 229,85
zavv 24,88,24
rt 102,5,51200
ar 230,86
ar 231,87
v_pk_mul_f32 v[224:225], v[224:225], s[48:49] op_sel_hi:[1,0]
v_pk_mul_f32 v[226:227], v[226:227], s[48:49] op_sel_hi:[1,0]
v_pk_mul_f32 v[228:229], v[228:229], s[48:49] op_sel_hi:[1,0]
v_pk_mul_f32 v[230:231], v[230:231], s[48:49] op_sel_hi:[1,0]
cv 136,224,225
zavv 28,92,24
s_add_u32 m0, s32, 16384
cv 137,226,227
cv 138,228,229
cv 139,230,231
ar 224,88
ar 225,89
ar 226,90
ar 227,91
zavv 32,64,28
rt 104,6,49152
ar 228,92
ar 229,93
ar 230,94
ar 231,95
v_pk_mul_f32 v[224:225], v[224:225], s[48:49] op_sel_hi:[1,0]
v_pk_mul_f32 v[226:227], v[226:227], s[48:49] op_sel_hi:[1,0]
v_pk_mul_f32 v[228:229], v[228:229], s[48:49] op_sel_hi:[1,0]
zavv 36,68,28
rqv 24,1,32768
v_pk_mul_f32 v[230:231], v[230:231], s[48:49] op_sel_hi:[1,0]
cv 140,224,225
cv 141,226,227
cv 142,228,229
cv 143,230,231
v_cndmask_b32_e64 v140, v140, v17, s[76:77]
v_cndmask_b32_e64 v141, v141, v17, s[76:77]
zavv 40,72,28
s_add_u32 s61, s28, s64
v_cndmask_b32_e64 v142, v142, v17, s[76:77]
v_cndmask_b32_e64 v143, v143, v17, s[76:77]
ar 224,96
ar 225,97
ar 226,98
ar 227,99
ar 228,100
zavv 44,76,28
rt 106,6,51200
ar 229,101
ar 230,102
ar 231,103
v_pk_mul_f32 v[224:225], v[224:225], s[48:49] op_sel_hi:[1,0]
v_pk_mul_f32 v[226:227], v[226:227], s[48:49] op_sel_hi:[1,0]
v_pk_mul_f32 v[228:229], v[228:229], s[48:49] op_sel_hi:[1,0]
v_pk_mul_f32 v[230:231], v[230:231], s[48:49] op_sel_hi:[1,0]
zavv 48,80,28
dm 12,20,61
cv 144,224,225
cv 145,226,227
cv 146,228,229
cv 147,230,231
ar 224,104
ar 225,105
ar 226,106
zavv 52,84,28
rt 108,7,49152
ar 227,107
ar 228,108
ar 229,109
ar 230,110
ar 231,111
v_pk_mul_f32 v[224:225], v[224:225], s[48:49] op_sel_hi:[1,0]
v_pk_mul_f32 v[226:227], v[226:227], s[48:49] op_sel_hi:[1,0]
zavv 56,88,28
s_add_u32 m0, s33, 33792
v_pk_mul_f32 v[228:229], v[228:229], s[48:49] op_sel_hi:[1,0]
v_pk_mul_f32 v[230:231], v[230:231], s[48:49] op_sel_hi:[1,0]
cv 148,224,225
cv 149,226,227
cv 150,228,229
cv 151,230,231
v_cndmask_b32_e64 v148, v148, v17, s[76:77]
zavv 60,92,28
rt 110,7,51200
v_cndmask_b32_e64 v149, v149, v17, s[76:77]
v_cndmask_b32_e64 v150, v150, v17, s[76:77]
v_cndmask_b32_e64 v151, v151, v17, s[76:77]
ar 224,112
ar 225,113
ar 226,114
ar 227,115
zavv 64,64,32
s_add_u32 s67, s67, s31
ar 228,116
ar 229,117
ar 230,118
ar 231,119
v_pk_mul_f32 v[224:225], v[224:225], s[48:49] op_sel_hi:[1,0]
v_pk_mul_f32 v[226:227], v[226:227], s[48:49] op_sel_hi:[1,0]
v_pk_mul_f32 v[228:229], v[228:229], s[48:49] op_sel_hi:[1,0]
zavv 68,68,32
rt 112,8,49152
v_pk_mul_f32 v[230:231], v[230:231], s[48:49] op_sel_hi:[1,0]
cv 152,224,225
cv 153,226,227
cv 154,228,229
cv 155,230,231
ar 224,120
ar 225,121
zavv 72,72,32
rqv 28,1,34816
ar 226,122
ar 227,123
ar 228,124
ar 229,125
ar 230,126
ar 231,127
v_pk_mul_f32 v[224:225], v[224:225], s[48:49] op_sel_hi:[1,0]
zavv 76,76,32
dm 15,24,67
v_pk_mul_f32 v[226:227], v[226:227], s[48:49] op_sel_hi:[1,0]
v_pk_mul_f32 v[228:229], v[228:229], s[48:49] op_sel_hi:[1,0]
v_pk_mul_f32 v[230:231], v[230:231], s[48:49] op_sel_hi:[1,0]
cv 156,224,225
cv 157,226,227
cv 158,228,229
cv 159,230,231
zavv 80,80,32
rt 114,8,51200
v_cndmask_b32_e64 v156, v156, v17, s[76:77]
v_cndmask_b32_e64 v157, v157, v17, s[76:77]
v_cndmask_b32_e64 v158, v158, v17, s[76:77]
v_cndmask_b32_e64 v159, v159, v17, s[76:77]
ar 224,128
ar 225,129
ar 226,130
zavv 84,84,32
s_add_u32 m0, s32, 17408
ar 227,131
ar 228,132
ar 229,133
ar 230,134
ar 231,135
v_pk_mul_f32 v[224:225], v[224:225], s[48:49] op_sel_hi:[1,0]
v_pk_mul_f32 v[226:227], v[226:227], s[48:49] op_sel_hi:[1,0]
zavv 88,88,32
rt 116,9,49152
v_pk_mul_f32 v[228:229], v[228:229], s[48:49] op_sel_hi:[1,0]
v_pk_mul_f32 v[230:231], v[230:231], s[48:49] op_sel_hi:[1,0]
cv 160,224,225
cv 161,226,227
cv 162,228,229
cv 163,230,231
ar 224,136
zavv 92,92,32
rt 118,9,51200
ar 225,137
ar 226,138
ar 227,139
ar 228,140
ar 229,141
ar 230,142
ar 231,143
zavv 96,64,36
s_add_u32 s61, s61, s30
rt 120,10,49152
v_pk_mul_f32 v[224:225], v[224:225], s[48:49] op_sel_hi:[1,0]
v_pk_mul_f32 v[226:227], v[226:227], s[48:49] op_sel_hi:[1,0]
v_pk_mul_f32 v[228:229], v[228:229], s[48:49] op_sel_hi:[1,0]
v_pk_mul_f32 v[230:231], v[230:231], s[48:49] op_sel_hi:[1,0]
cv 164,224,225
cv 165,226,227
cv 166,228,229
zavv 100,68,36
rqv 32,1,36864
cv 167,230,231
v_cndmask_b32_e64 v164, v164, v17, s[76:77]
v_cndmask_b32_e64 v165, v165, v17, s[76:77]
v_cndmask_b32_e64 v166, v166, v17, s[76:77]
v_cndmask_b32_e64 v167, v167, v17, s[76:77]
ar 224,144
ar 225,145
zavv 104,72,36
dm 13,20,61
ar 226,146
ar 227,147
ar 228,148
ar 229,149
ar 230,150
ar 231,151
v_pk_mul_f32 v[224:225], v[224:225], s[48:49] op_sel_hi:[1,0]
zavv 108,76,36
rt 122,10,51200
v_pk_mul_f32 v[226:227], v[226:227], s[48:49] op_sel_hi:[1,0]
v_pk_mul_f32 v[228:229], v[228:229], s[48:49] op_sel_hi:[1,0]
v_pk_mul_f32 v[230:231], v[230:231], s[48:49] op_sel_hi:[1,0]
cv 168,224,225
cv 169,226,227
cv 170,228,229
cv 171,230,231
zavv 112,80,36
s_add_u32 m0, s33, 34816
ar 224,152
ar 225,153
ar 226,154
ar 227,155
ar 228,156
ar 229,157
ar 230,158
zavv 116,84,36
rt 124,11,49152
ar 231,159
v_pk_mul_f32 v[224:225], v[224:225], s[48:49] op_sel_hi:[1,0]
v_pk_mul_f32 v[226:227], v[226:227], s[48:49] op_sel_hi:[1,0]
v_pk_mul_f32 v[228:229], v[228:229], s[48:49] op_sel_hi:[1,0]
v_pk_mul_f32 v[230:231], v[230:231], s[48:49] op_sel_hi:[1,0]
cv 172,224,225
cv 173,226,227
zavv 120,88,36
s_add_u32 s67, s67, s31
cv 174,228,229
cv 175,230,231
v_cndmask_b32_e64 v172, v172, v17, s[76:77]
v_cndmask_b32_e64 v173, v173, v17, s[76:77]
v_cndmask_b32_e64 v174, v174, v17, s[76:77]
v_cndmask_b32_e64 v175, v175, v17, s[76:77]
ar 224,160
zavv 124,92,36
rt 126,11,51200
ar 225,161
ar 226,162
ar 227,163
ar 228,164
ar 229,165
ar 230,166
ar 231,167
zavv 128,64,40
dm 14,24,67
v_pk_mul_f32 v[224:225], v[224:225], s[48:49] op_sel_hi:[1,0]
v_pk_mul_f32 v[226:227], v[226:227], s[48:49] op_sel_hi:[1,0]
v_pk_mul_f32 v[228:229], v[228:229], s[48:49] op_sel_hi:[1,0]
v_pk_mul_f32 v[230:231], v[230:231], s[48:49] op_sel_hi:[1,0]
cv 176,224,225
cv 177,226,227
cv 178,228,229
zavv 132,68,40
rqv 36,1,38912
cv 179,230,231
ar 224,168
ar 225,169
ar 226,170
ar 227,171
ar 228,172
ar 229,173
zavv 136,72,40
rqv 56,1,45056
ar 230,174
ar 231,175
v_pk_mul_f32 v[224:225], v[224:225], s[48:49] op_sel_hi:[1,0]
v_pk_mul_f32 v[226:227], v[226:227], s[48:49] op_sel_hi:[1,0]
v_pk_mul_f32 v[228:229], v[228:229], s[48:49] op_sel_hi:[1,0]
v_pk_mul_f32 v[230:231], v[230:231], s[48:49] op_sel_hi:[1,0]
cv 180,224,225
zavv 140,76,40
s_add_u32 m0, s32, 18432
cv 181,226,227
cv 182,228,229
cv 183,230,231
v_cndmask_b32_e64 v180, v180, v17, s[76:77]
v_cndmask_b32_e64 v181, v181, v17, s[76:77]
v_cndmask_b32_e64 v182, v182, v17, s[76:77]
v_cndmask_b32_e64 v183, v183, v17, s[76:77]
zavv 144,80,40
s_add_u32 s61, s61, s30
ar 224,176
ar 225,177
ar 226,178
ar 227,179
ar 228,180
ar 229,181
ar 230,182
zavv 148,84,40
dm 12,20,61
ar 231,183
v_pk_mul_f32 v[224:225], v[224:225], s[48:49] op_sel_hi:[1,0]
v_pk_mul_f32 v[226:227], v[226:227], s[48:49] op_sel_hi:[1,0]
v_pk_mul_f32 v[228:229], v[228:229], s[48:49] op_sel_hi:[1,0]
v_pk_mul_f32 v[230:231], v[230:231], s[48:49] op_sel_hi:[1,0]
cv 184,224,225
cv 185,226,227
zavv 152,88,40
cv 186,228,229
cv 187,230,231
ar 224,184
ar 225,185
ar 226,186
ar 227,187
ar 228,188
zavv 156,92,40
s_add_u32 m0, s33, 35840
ar 229,189
ar 230,190
ar 231,191
v_pk_mul_f32 v[224:225], v[224:225], s[48:49] op_sel_hi:[1,0]
v_pk_mul_f32 v[226:227], v[226:227], s[48:49] op_sel_hi:[1,0]
v_pk_mul_f32 v[228:229], v[228:229], s[48:49] op_sel_hi:[1,0]
v_pk_mul_f32 v[230:231], v[230:231], s[48:49] op_sel_hi:[1,0]
zavv 160,64,44
rqv 40,1,40960
cv 188,224,225
cv 189,226,227
cv 190,228,229
cv 191,230,231
v_cndmask_b32_e64 v188, v188, v17, s[76:77]
v_cndmask_b32_e64 v189, v189, v17, s[76:77]
v_cndmask_b32_e64 v190, v190, v17, s[76:77]
zavv 164,68,44
rqv 60,1,47104
v_cndmask_b32_e64 v191, v191, v17, s[76:77]
ar 224,192
ar 225,193
ar 226,194
ar 227,195
ar 228,196
ar 229,197
zavv 168,72,44
s_add_u32 s67, s67, s31
ar 230,198
ar 231,199
v_pk_mul_f32 v[224:225], v[224:225], s[48:49] op_sel_hi:[1,0]
v_pk_mul_f32 v[226:227], v[226:227], s[48:49] op_sel_hi:[1,0]
v_pk_mul_f32 v[228:229], v[228:229], s[48:49] op_sel_hi:[1,0]
v_pk_mul_f32 v[230:231], v[230:231], s[48:49] op_sel_hi:[1,0]
cv 192,224,225
zavv 172,76,44
cv 193,226,227
cv 194,228,229
cv 195,230,231
ar 224,200
ar 225,201
ar 226,202
ar 227,203
zavv 176,80,44
dm 15,24,67
ar 228,204
ar 229,205
ar 230,206
ar 231,207
v_pk_mul_f32 v[224:225], v[224:225], s[48:49] op_sel_hi:[1,0]
v_pk_mul_f32 v[226:227], v[226:227], s[48:49] op_sel_hi:[1,0]
v_pk_mul_f32 v[228:229], v[228:229], s[48:49] op_sel_hi:[1,0]
zavv 180,84,44
v_pk_mul_f32 v[230:231], v[230:231], s[48:49] op_sel_hi:[1,0]
cv 196,224,225
cv 197,226,227
cv 198,228,229
cv 199,230,231
v_cndmask_b32_e64 v196, v196, v17, s[76:77]
v_cndmask_b32_e64 v197, v197, v17, s[76:77]
zavv 184,88,44
s_add_u32 m0, s32, 19456
v_cndmask_b32_e64 v198, v198, v17, s[76:77]
v_cndmask_b32_e64 v199, v199, v17, s[76:77]
ar 224,208
ar 225,209
ar 226,210
ar 227,211
ar 228,212
zavv 188,92,44
ar 229,213
ar 230,214
ar 231,215
v_pk_mul_f32 v[224:225], v[224:225], s[48:49] op_sel_hi:[1,0]
v_pk_mul_f32 v[226:227], v[226:227], s[48:49] op_sel_hi:[1,0]
v_pk_mul_f32 v[228:229], v[228:229], s[48:49] op_sel_hi:[1,0]
v_pk_mul_f32 v[230:231], v[230:231], s[48:49] op_sel_hi:[1,0]
zavv 192,64,48
rqv 44,1,43008
cv 200,224,225
cv 201,226,227
cv 202,228,229
cv 203,230,231
ar 224,216
ar 225,217
ar 226,218
zavv 196,68,48
s_add_u32 s61, s61, s30
ar 227,219
ar 228,220
ar 229,221
ar 230,222
ar 231,223
v_pk_mul_f32 v[224:225], v[224:225], s[48:49] op_sel_hi:[1,0]
v_pk_mul_f32 v[226:227], v[226:227], s[48:49] op_sel_hi:[1,0]
zavv 200,72,48
dm 13,20,61
v_pk_mul_f32 v[228:229], v[228:229], s[48:49] op_sel_hi:[1,0]
v_pk_mul_f32 v[230:231], v[230:231], s[48:49] op_sel_hi:[1,0]
cv 204,224,225
cv 205,226,227
cv 206,228,229
cv 207,230,231
v_cndmask_b32_e64 v204, v204, v17, s[76:77]
zavv 204,76,48
v_cndmask_b32_e64 v205, v205, v17, s[76:77]
v_cndmask_b32_e64 v206, v206, v17, s[76:77]
v_cndmask_b32_e64 v207, v207, v17, s[76:77]
ar 224,224
ar 225,225
ar 226,226
ar 227,227
zavv 208,80,48
ar 228,228
ar 229,229
ar 230,230
ar 231,231
v_pk_mul_f32 v[224:225], v[224:225], s[48:49] op_sel_hi:[1,0]
v_pk_mul_f32 v[226:227], v[226:227], s[48:49] op_sel_hi:[1,0]
v_pk_mul_f32 v[228:229], v[228:229], s[48:49] op_sel_hi:[1,0]
zavv 212,84,48
s_add_u32 s29, s29, s65
v_pk_mul_f32 v[230:231], v[230:231], s[48:49] op_sel_hi:[1,0]
cv 208,224,225
cv 209,226,227
cv 210,228,229
cv 211,230,231
ar 224,232
ar 225,233
zavv 216,88,48
ar 226,234
ar 227,235
ar 228,236
ar 229,237
ar 230,238
ar 231,239
v_pk_mul_f32 v[224:225], v[224:225], s[48:49] op_sel_hi:[1,0]
zavv 220,92,48
s_add_u32 s28, s28, 128
v_pk_mul_f32 v[226:227], v[226:227], s[48:49] op_sel_hi:[1,0]
v_pk_mul_f32 v[228:229], v[228:229], s[48:49] op_sel_hi:[1,0]
v_pk_mul_f32 v[230:231], v[230:231], s[48:49] op_sel_hi:[1,0]
cv 212,224,225
cv 213,226,227
cv 214,228,229
cv 215,230,231
zavv 224,64,52
v_cndmask_b32_e64 v212, v212, v17, s[76:77]
v_cndmask_b32_e64 v213, v213, v17, s[76:77]
v_cndmask_b32_e64 v214, v214, v17, s[76:77]
)ASM" ::: "memory");
  asm volatile(R"ASM(
v_cndmask_b32_e64 v215, v215, v17, s[76:77]
ar 224,240
ar 225,241
ar 226,242
zavv 228,68,52
s_add_u32 s74, s78, .Lk1_BTN+8192-.Lk1_base
s_addc_u32 s75, s79, 0
ar 227,243
ar 228,244
ar 229,245
ar 230,246
ar 231,247
v_pk_mul_f32 v[224:225], v[224:225], s[48:49] op_sel_hi:[1,0]
v_pk_mul_f32 v[226:227], v[226:227], s[48:49] op_sel_hi:[1,0]
zavv 232,72,52
v_pk_mul_f32 v[228:229], v[228:229], s[48:49] op_sel_hi:[1,0]
v_pk_mul_f32 v[230:231], v[230:231], s[48:49] op_sel_hi:[1,0]
cv 216,224,225
cv 217,226,227
cv 218,228,229
cv 219,230,231
ar 224,248
zavv 236,76,52
global_load_dword v19, v236, s[74:75]
ar 225,249
ar 226,250
ar 227,251
ar 228,252
ar 229,253
ar 230,254
ar 231,255
zavv 240,80,52
v_pk_mul_f32 v[224:225], v[224:225], s[48:49] op_sel_hi:[1,0]
v_pk_mul_f32 v[226:227], v[226:227], s[48:49] op_sel_hi:[1,0]
v_pk_mul_f32 v[228:229], v[228:229], s[48:49] op_sel_hi:[1,0]
v_pk_mul_f32 v[230:231], v[230:231], s[48:49] op_sel_hi:[1,0]
cv 220,224,225
cv 221,226,227
cv 222,228,229
zavv 244,84,52
cv 223,230,231
v_cndmask_b32_e64 v220, v220, v17, s[76:77]
v_cndmask_b32_e64 v221, v221, v17, s[76:77]
v_cndmask_b32_e64 v222, v222, v17, s[76:77]
v_cndmask_b32_e64 v223, v223, v17, s[76:77]
zavv 248,88,52
zavv 252,92,52
s_waitcnt vmcnt(18) lgkmcnt(0)
s_barrier
mavv 0,96,24
rt 64,4,0
mavv 4,100,24
s_add_u32 m0, s33, 49152
mavv 8,104,24
rt 66,4,2048
mavv 12,108,24
s_mov_b32 s67, s29
mavv 16,112,24
rt 68,5,0
mavv 20,116,24
dm 14,24,67
mavv 24,120,24
rt 70,5,2048
mavv 28,124,24
s_add_u32 m0, s32, 32768
mavv 32,96,28
rt 72,6,0
mavv 36,100,28
rqv 24,2,0
mavv 40,104,28
s_mov_b32 s61, s28
mavv 44,108,28
rt 74,6,2048
mavv 48,112,28
dm 12,20,61
mavv 52,116,28
rt 76,7,0
mavv 56,120,28
s_add_u32 m0, s33, 50176
mavv 60,124,28
rt 78,7,2048
mavv 64,96,32
s_add_u32 s67, s67, s31
mavv 68,100,32
rt 80,8,0
mavv 72,104,32
rqv 28,2,2048
mavv 76,108,32
dm 15,24,67
mavv 80,112,32
rt 82,8,2048
mavv 84,116,32
s_add_u32 m0, s32, 33792
mavv 88,120,32
rt 84,9,0
mavv 92,124,32
rt 86,9,2048
mavv 96,96,36
s_add_u32 s61, s61, s30
rt 88,10,0
mavv 100,100,36
rqv 32,2,4096
mavv 104,104,36
dm 13,20,61
mavv 108,108,36
rt 90,10,2048
mavv 112,112,36
s_add_u32 m0, s33, 51200
mavv 116,116,36
rt 92,11,0
mavv 120,120,36
s_add_u32 s67, s67, s31
mavv 124,124,36
rt 94,11,2048
mavv 128,96,40
dm 14,24,67
mavv 132,100,40
rqv 36,2,6144
mavv 136,104,40
rqv 48,2,12288
mavv 140,108,40
s_add_u32 m0, s32, 34816
mavv 144,112,40
s_add_u32 s61, s61, s30
mavv 148,116,40
dm 12,20,61
mavv 152,120,40
mavv 156,124,40
s_add_u32 m0, s33, 52224
mavv 160,96,44
rqv 40,2,8192
mavv 164,100,44
rqv 52,2,14336
mavv 168,104,44
s_add_u32 s67, s67, s31
mavv 172,108,44
mavv 176,112,44
dm 15,24,67
mavv 180,116,44
mavv 184,120,44
s_add_u32 m0, s32, 35840
mavv 188,124,44
mavv 192,96,56
rqv 44,2,10240
mavv 196,100,56
s_add_u32 s61, s61, s30
mavv 200,104,56
dm 13,20,61
mavv 204,108,56
mavv 208,112,56
mavv 212,116,56
s_add_u32 s29, s29, s65
mavv 216,120,56
mavv 220,124,56
buffer_load_dwordx4 v[224:227], v16, s[12:15], 0 offen
mavv 224,96,60
mavv 228,100,60
buffer_load_dwordx4 v[228:231], v18, s[8:11], s16 offen
mavv 232,104,60
s_mov_b32 s68, s88
mavv 236,108,60
s_mov_b32 s69, s68
mavv 240,112,60
buffer_load_dwordx4 v[232:235], v18, s[8:11], s16 offen offset:896
mavv 244,116,60
buffer_store_dwordx4 v[128:131], v16, s[84:87], s69 offen sc1
mavv 248,120,60
s_add_u32 s69, s69, s51
mavv 252,124,60
buffer_store_dwordx4 v[132:135], v16, s[84:87], s69 offen sc1
s_waitcnt vmcnt(22) lgkmcnt(0)
s_barrier
mavv 0,64,24
rt 96,4,16384
mavv 4,68,24
s_add_u32 m0, s33, 0
mavv 8,72,24
rt 98,4,18432
mavv 12,76,24
s_mov_b32 s67, s29
mavv 16,80,24
rt 100,5,16384
mavv 20,84,24
dm 14,24,67
mavv 24,88,24
rt 102,5,18432
mavv 28,92,24
s_add_u32 m0, s32, 49152
mavv 32,64,28
rt 104,6,16384
mavv 36,68,28
rqv 24,3,0
mavv 40,72,28
s_add_u32 s61, s28, s64
mavv 44,76,28
rt 106,6,18432
mavv 48,80,28
dm 12,20,61
mavv 52,84,28
rt 108,7,16384
mavv 56,88,28
rt 110,7,18432
mavv 60,92,28
s_add_u32 m0, s33, 1024
mavv 64,64,32
rt 112,8,16384
mavv 68,68,32
rqv 28,3,2048
mavv 72,72,32
s_add_u32 s67, s67, s31
mavv 76,76,32
rt 114,8,18432
mavv 80,80,32
dm 15,24,67
mavv 84,84,32
rt 116,9,16384
mavv 88,88,32
s_add_u32 m0, s32, 50176
mavv 92,92,32
rt 118,9,18432
mavv 96,64,36
s_add_u32 s61, s61, s30
mavv 100,68,36
rt 120,10,16384
mavv 104,72,36
rqv 32,3,4096
mavv 108,76,36
rt 122,10,18432
mavv 112,80,36
dm 13,20,61
mavv 116,84,36
rt 124,11,16384
mavv 120,88,36
s_add_u32 m0, s33, 2048
mavv 124,92,36
rt 126,11,18432
mavv 128,64,40
s_add_u32 s67, s67, s31
mavv 132,68,40
rqv 36,3,6144
mavv 136,72,40
rqv 56,3,12288
mavv 140,76,40
dm 14,24,67
mavv 144,80,40
s_add_u32 m0, s32, 51200
mavv 148,84,40
mavv 152,88,40
s_add_u32 s61, s61, s30
mavv 156,92,40
mavv 160,64,44
rqv 40,3,8192
mavv 164,68,44
dm 12,20,61
mavv 168,72,44
rqv 60,3,14336
mavv 172,76,44
s_add_u32 m0, s33, 3072
mavv 176,80,44
mavv 180,84,44
s_add_u32 s67, s67, s31
mavv 184,88,44
mavv 188,92,44
dm 15,24,67
mavv 192,64,48
rqv 44,3,10240
mavv 196,68,48
mavv 200,72,48
s_add_u32 m0, s32, 52224
mavv 204,76,48
mavv 208,80,48
s_add_u32 s61, s61, s30
mavv 212,84,48
mavv 216,88,48
dm 13,20,61
mavv 220,92,48
mavv 224,64,52
mavv 228,68,52
s_add_u32 s29, s29, s65
mavv 232,72,52
s_add_u32 s69, s69, s51
mavv 236,76,52
s_add_u32 s28, s28, 128
mavv 240,80,52
buffer_store_dwordx4 v[136:139], v16, s[84:87], s69 offen sc1
mavv 244,84,52
s_add_u32 s69, s69, s51
mavv 248,88,52
buffer_store_dwordx4 v[140:143], v16, s[84:87], s69 offen sc1
mavv 252,92,52
s_waitcnt vmcnt(23) lgkmcnt(0)
s_barrier
mavv 0,96,24
rt 64,4,32768
mavv 4,100,24
s_add_u32 m0, s33, 16384
mavv 8,104,24
rt 66,4,34816
mavv 12,108,24
s_mov_b32 s67, s29
mavv 16,112,24
rt 68,5,32768
mavv 20,116,24
dm 14,24,67
mavv 24,120,24
rt 70,5,34816
mavv 28,124,24
s_add_u32 m0, s32, 65536
mavv 32,96,28
rt 72,6,32768
mavv 36,100,28
rqv 24,0,0
mavv 40,104,28
s_mov_b32 s61, s28
mavv 44,108,28
rt 74,6,34816
mavv 48,112,28
dm 12,20,61
mavv 52,116,28
rt 76,7,32768
mavv 56,120,28
s_add_u32 m0, s33, 17408
mavv 60,124,28
rt 78,7,34816
mavv 64,96,32
rt 80,8,32768
mavv 68,100,32
rqv 28,0,2048
mavv 72,104,32
s_add_u32 s67, s67, s31
mavv 76,108,32
rt 82,8,34816
mavv 80,112,32
dm 15,24,67
mavv 84,116,32
rt 84,9,32768
mavv 88,120,32
s_add_u32 m0, s32, 66560
mavv 92,124,32
rt 86,9,34816
mavv 96,96,36
s_add_u32 s61, s61, s30
mavv 100,100,36
rt 88,10,32768
mavv 104,104,36
rqv 32,0,4096
mavv 108,108,36
dm 13,20,61
mavv 112,112,36
rt 90,10,34816
mavv 116,116,36
s_add_u32 m0, s33, 18432
mavv 120,120,36
rt 92,11,32768
mavv 124,124,36
s_add_u32 s67, s67, s31
mavv 128,96,40
rt 94,11,34816
mavv 132,100,40
rqv 36,0,6144
mavv 136,104,40
dm 14,24,67
mavv 140,108,40
rqv 48,0,12288
mavv 144,112,40
s_add_u32 m0, s32, 67584
mavv 148,116,40
s_add_u32 s61, s61, s30
mavv 152,120,40
mavv 156,124,40
dm 12,20,61
mavv 160,96,44
rqv 40,0,8192
mavv 164,100,44
rqv 52,0,14336
mavv 168,104,44
s_add_u32 m0, s33, 19456
mavv 172,108,44
s_add_u32 s67, s67, s31
mavv 176,112,44
mavv 180,116,44
dm 15,24,67
mavv 184,120,44
mavv 188,124,44
mavv 192,96,56
rqv 44,0,10240
mavv 196,100,56
s_add_u32 m0, s32, 68608
mavv 200,104,56
s_add_u32 s61, s61, s30
mavv 204,108,56
mavv 208,112,56
dm 13,20,61
mavv 212,116,56
mavv 216,120,56
mavv 220,124,56
s_add_u32 s29, s29, s65
mavv 224,96,60
mavv 228,100,60
s_add_u32 s74, s78, .Lk1_BTN+12288-.Lk1_base
s_addc_u32 s75, s79, 0
mavv 232,104,60
s_add_u32 s69, s69, s51
mavv 236,108,60
global_load_dword v19, v236, s[74:75]
mavv 240,112,60
buffer_store_dwordx4 v[144:147], v16, s[84:87], s69 offen sc1
mavv 244,116,60
s_add_u32 s69, s69, s51
mavv 248,120,60
buffer_store_dwordx4 v[148:151], v16, s[84:87], s69 offen sc1
mavv 252,124,60
s_waitcnt vmcnt(21) lgkmcnt(0)
s_barrier
mavv 0,64,24
rt 96,4,49152
mavv 4,68,24
s_add_u32 m0, s33, 32768
mavv 8,72,24
rt 98,4,51200
mavv 12,76,24
s_mov_b32 s67, s29
mavv 16,80,24
rt 100,5,49152
mavv 20,84,24
dm 14,24,67
mavv 24,88,24
rt 102,5,51200
mavv 28,92,24
s_add_u32 m0, s32, 81920
mavv 32,64,28
rt 104,6,49152
mavv 36,68,28
rqv 24,1,0
mavv 40,72,28
s_add_u32 s61, s28, s64
mavv 44,76,28
rt 106,6,51200
mavv 48,80,28
dm 12,20,61
mavv 52,84,28
rt 108,7,49152
mavv 56,88,28
rt 110,7,51200
mavv 60,92,28
s_add_u32 m0, s33, 33792
mavv 64,64,32
rt 112,8,49152
mavv 68,68,32
rqv 28,1,2048
mavv 72,72,32
s_add_u32 s67, s67, s31
mavv 76,76,32
rt 114,8,51200
mavv 80,80,32
dm 15,24,67
mavv 84,84,32
rt 116,9,49152
mavv 88,88,32
s_add_u32 m0, s32, 82944
mavv 92,92,32
rt 118,9,51200
mavv 96,64,36
s_add_u32 s61, s61, s30
mavv 100,68,36
rt 120,10,49152
mavv 104,72,36
rqv 32,1,4096
mavv 108,76,36
rt 122,10,51200
mavv 112,80,36
dm 13,20,61
mavv 116,84,36
rt 124,11,49152
mavv 120,88,36
s_add_u32 m0, s33, 34816
mavv 124,92,36
rt 126,11,51200
mavv 128,64,40
s_add_u32 s67, s67, s31
mavv 132,68,40
rqv 36,1,6144
mavv 136,72,40
rqv 56,1,12288
mavv 140,76,40
dm 14,24,67
mavv 144,80,40
s_add_u32 m0, s32, 83968
mavv 148,84,40
mavv 152,88,40
s_add_u32 s61, s61, s30
mavv 156,92,40
mavv 160,64,44
rqv 40,1,8192
mavv 164,68,44
dm 12,20,61
mavv 168,72,44
rqv 60,1,14336
mavv 172,76,44
s_add_u32 m0, s33, 35840
mavv 176,80,44
mavv 180,84,44
s_add_u32 s67, s67, s31
mavv 184,88,44
mavv 188,92,44
dm 15,24,67
mavv 192,64,48
rqv 44,1,10240
mavv 196,68,48
mavv 200,72,48
s_add_u32 m0, s32, 84992
mavv 204,76,48
mavv 208,80,48
s_add_u32 s61, s61, s30
mavv 212,84,48
mavv 216,88,48
dm 13,20,61
mavv 220,92,48
mavv 224,64,52
mavv 228,68,52
s_add_u32 s29, s29, s65
mavv 232,72,52
s_add_u32 s68, s68, s50
mavv 236,76,52
s_mov_b32 s69, s68
mavv 240,80,52
s_add_u32 s28, s28, 128
mavv 244,84,52
buffer_store_dwordx4 v[152:155], v16, s[84:87], s69 offen sc1
mavv 248,88,52
s_add_u32 s69, s69, s51
mavv 252,92,52
buffer_store_dwordx4 v[156:159], v16, s[84:87], s69 offen sc1
s_waitcnt vmcnt(21) lgkmcnt(0)
s_barrier
mavv 0,96,24
rt 64,4,0
mavv 4,100,24
s_add_u32 m0, s33, 49152
mavv 8,104,24
s_mov_b32 s67, s29
mavv 12,108,24
rt 66,4,2048
mavv 16,112,24
dm 14,24,67
mavv 20,116,24
s_add_u32 m0, s32, 0
mavv 24,120,24
rt 68,5,0
s_mov_b32 s61, s28
mavv 28,124,24
rt 70,5,2048
mavv 32,96,28
dm 12,20,61
s_add_u32 m0, s33, 50176
mavv 36,100,28
rt 72,6,0
mavv 40,104,28
rqv 24,0,32768
s_add_u32 s67, s67, s31
mavv 44,108,28
rt 74,6,2048
mavv 48,112,28
dm 15,24,67
mavv 52,116,28
s_add_u32 m0, s32, 1024
mavv 56,120,28
rt 76,7,0
s_add_u32 s61, s61, s30
mavv 60,124,28
rt 78,7,2048
mavv 64,96,32
dm 13,20,61
s_add_u32 m0, s33, 51200
mavv 68,100,32
rt 80,8,0
mavv 72,104,32
rqv 28,0,34816
s_add_u32 s67, s67, s31
mavv 76,108,32
rt 82,8,2048
mavv 80,112,32
dm 14,24,67
mavv 84,116,32
s_add_u32 m0, s32, 2048
mavv 88,120,32
rt 84,9,0
s_add_u32 s61, s61, s30
mavv 92,124,32
rt 86,9,2048
mavv 96,96,36
dm 12,20,61
s_add_u32 m0, s33, 52224
mavv 100,100,36
rt 88,10,0
mavv 104,104,36
rqv 32,0,36864
s_add_u32 s67, s67, s31
mavv 108,108,36
rt 90,10,2048
)ASM" ::: "memory");
  asm volatile(R"ASM(
mavv 112,112,36
dm 15,24,67
mavv 116,116,36
s_add_u32 m0, s32, 3072
mavv 120,120,36
rt 92,11,0
s_add_u32 s61, s61, s30
mavv 124,124,36
rt 94,11,2048
mavv 128,96,40
dm 13,20,61
s_add_u32 s29, s29, s65
mavv 132,100,40
rqv 36,0,38912
mavv 136,104,40
v_dot2_f32_bf16 v20, v228, v224, 0
mavv 140,108,40
rqv 48,0,45056
mavv 144,112,40
v_dot2_f32_bf16 v21, v232, v224, 0
mavv 148,116,40
v_dot2_f32_bf16 v20, v229, v225, v20
mavv 152,120,40
v_dot2_f32_bf16 v21, v233, v225, v21
mavv 156,124,40
v_dot2_f32_bf16 v20, v230, v226, v20
mavv 160,96,44
v_dot2_f32_bf16 v21, v234, v226, v21
mavv 164,100,44
rqv 40,0,40960
mavv 168,104,44
v_dot2_f32_bf16 v20, v231, v227, v20
mavv 172,108,44
rqv 52,0,47104
mavv 176,112,44
v_dot2_f32_bf16 v21, v235, v227, v21
mavv 180,116,44
s_nop 1
mavv 184,120,44
v_add_f32_dpp v20, v20, v20 row_shr:8 row_mask:0xf bank_mask:0xf bound_ctrl:0
mavv 188,124,44
v_add_f32_dpp v21, v21, v21 row_shr:8 row_mask:0xf bank_mask:0xf bound_ctrl:0
mavv 192,96,56
v_add_f32_dpp v20, v20, v20 row_shr:4 row_mask:0xf bank_mask:0xf bound_ctrl:0
mavv 196,100,56
rqv 44,0,43008
mavv 200,104,56
v_add_f32_dpp v21, v21, v21 row_shr:4 row_mask:0xf bank_mask:0xf bound_ctrl:0
mavv 204,108,56
v_add_f32_dpp v20, v20, v20 row_shr:2 row_mask:0xf bank_mask:0xf bound_ctrl:0
mavv 208,112,56
v_add_f32_dpp v21, v21, v21 row_shr:2 row_mask:0xf bank_mask:0xf bound_ctrl:0
mavv 212,116,56
v_add_f32_dpp v20, v20, v20 row_shr:1 row_mask:0xf bank_mask:0xf bound_ctrl:0
mavv 216,120,56
v_add_f32_dpp v21, v21, v21 row_shr:1 row_mask:0xf bank_mask:0xf bound_ctrl:0
mavv 220,124,56
v_mov_b32 v224, v20
mavv 224,96,60
v_mov_b32 v225, v21
mavv 228,100,60
mavv 232,104,60
v_permlane16_swap_b32 v20, v224
mavv 236,108,60
s_add_u32 s69, s69, s51
mavv 240,112,60
v_permlane16_swap_b32 v21, v225
buffer_store_dwordx4 v[160:163], v16, s[84:87], s69 offen sc1
mavv 244,116,60
v_add_f32 v20, v20, v224
mavv 248,120,60
s_add_u32 s69, s69, s51
mavv 252,124,60
buffer_store_dwordx4 v[164:167], v16, s[84:87], s69 offen sc1
s_waitcnt vmcnt(20) lgkmcnt(0)
s_barrier
mavv 0,64,24
rt 96,4,16384
mavv 4,68,24
s_add_u32 m0, s33, 0
mavv 8,72,24
s_mov_b32 s67, s29
mavv 12,76,24
rt 98,4,18432
mavv 16,80,24
dm 14,24,67
s_add_u32 m0, s32, 16384
mavv 20,84,24
rt 100,5,16384
mavv 24,88,24
s_add_u32 s61, s28, s64
mavv 28,92,24
rt 102,5,18432
mavv 32,64,28
dm 12,20,61
s_add_u32 m0, s33, 1024
rt 104,6,16384
mavv 36,68,28
rqv 24,1,32768
mavv 40,72,28
s_add_u32 s67, s67, s31
dm 15,24,67
mavv 44,76,28
rt 106,6,18432
mavv 48,80,28
s_add_u32 m0, s32, 17408
mavv 52,84,28
rt 108,7,16384
mavv 56,88,28
s_add_u32 s61, s61, s30
dm 13,20,61
mavv 60,92,28
rt 110,7,18432
mavv 64,64,32
s_add_u32 m0, s33, 2048
s_add_u32 s67, s67, s31
mavv 68,68,32
rt 112,8,16384
mavv 72,72,32
rqv 28,1,34816
dm 14,24,67
mavv 76,76,32
rt 114,8,18432
mavv 80,80,32
s_add_u32 m0, s32, 18432
s_add_u32 s61, s61, s30
mavv 84,84,32
rt 116,9,16384
mavv 88,88,32
dm 12,20,61
mavv 92,92,32
s_add_u32 m0, s33, 3072
mavv 96,64,36
rt 118,9,18432
s_add_u32 s67, s67, s31
rt 120,10,16384
mavv 100,68,36
rqv 32,1,36864
mavv 104,72,36
dm 15,24,67
s_add_u32 m0, s32, 19456
mavv 108,76,36
rt 122,10,18432
mavv 112,80,36
s_add_u32 s61, s61, s30
mavv 116,84,36
dm 13,20,61
mavv 120,88,36
rt 124,11,16384
s_add_u32 s29, s29, s65
mavv 124,92,36
rt 126,11,18432
mavv 128,64,40
s_add_u32 s28, s28, 128
s_add_u32 s74, s78, .Lk1_BTN+16384-.Lk1_base
s_addc_u32 s75, s79, 0
mavv 132,68,40
rqv 36,1,38912
mavv 136,72,40
global_load_dword v19, v236, s[74:75]
mavv 140,76,40
rqv 56,1,45056
mavv 144,80,40
v_add_f32 v21, v21, v225
mavv 148,84,40
v_mov_b32 v224, v20
mavv 152,88,40
v_mov_b32 v225, v21
mavv 156,92,40
v_permlane32_swap_b32 v20, v224
mavv 160,64,44
v_permlane32_swap_b32 v21, v225
mavv 164,68,44
rqv 40,1,40960
v_add_f32 v20, v20, v224
mavv 168,72,44
rqv 60,1,47104
mavv 172,76,44
v_add_f32 v21, v21, v225
mavv 176,80,44
cv 20,20,21
mavv 180,84,44
v_and_b32 v21, 0xffff0000, v20
mavv 184,88,44
v_lshlrev_b32 v20, 16, v20
mavv 188,92,44
v_mul_f32 v20, 0xbfb8aa3b, v20
mavv 192,64,48
v_mul_f32 v21, 0xbfb8aa3b, v21
mavv 196,68,48
rqv 44,1,43008
mavv 200,72,48
ex 20
mavv 204,76,48
ex 21
mavv 208,80,48
v_add_f32 v20, 1.0, v20
mavv 212,84,48
v_add_f32 v21, 1.0, v21
mavv 216,88,48
v_rcp_f32 v20, v20
mavv 220,92,48
v_rcp_f32 v21, v21
mavv 224,64,52
v_mov_b32 v22, 0
mavv 228,68,52
cv 20,20,21
mavv 232,72,52
v_and_b32 v21, 0xffff0000, v20
mavv 236,76,52
s_add_u32 s69, s69, s51
mavv 240,80,52
v_lshlrev_b32 v20, 16, v20
buffer_store_dwordx4 v[168:171], v16, s[84:87], s69 offen sc1
mavv 244,84,52
s_mov_b32 exec_lo, 0
s_mov_b32 exec_hi, 0x80000000
global_store_dwordx2 v22, v[20:21], s[6:7] sc1
s_mov_b64 exec, -1
mavv 248,88,52
s_add_u32 s69, s69, s51
mavv 252,92,52
buffer_store_dwordx4 v[172:175], v16, s[84:87], s69 offen sc1
s_waitcnt vmcnt(22) lgkmcnt(0)
s_barrier
mavv 0,96,24
rt 64,4,32768
mavv 4,100,24
s_add_u32 m0, s33, 16384
mavv 8,104,24
rt 66,4,34816
mavv 12,108,24
s_mov_b32 s67, s29
mavv 16,112,24
rt 68,5,32768
mavv 20,116,24
dm 14,24,67
mavv 24,120,24
rt 70,5,34816
mavv 28,124,24
s_add_u32 m0, s32, 32768
mavv 32,96,28
rt 72,6,32768
mavv 36,100,28
rqv 24,2,0
mavv 40,104,28
s_mov_b32 s61, s28
mavv 44,108,28
rt 74,6,34816
mavv 48,112,28
rt 76,7,32768
mavv 52,116,28
dm 12,20,61
mavv 56,120,28
rt 78,7,34816
mavv 60,124,28
s_add_u32 m0, s33, 17408
mavv 64,96,32
rt 80,8,32768
mavv 68,100,32
rqv 28,2,2048
mavv 72,104,32
s_add_u32 s67, s67, s31
mavv 76,108,32
rt 82,8,34816
mavv 80,112,32
dm 15,24,67
mavv 84,116,32
rt 84,9,32768
mavv 88,120,32
rt 86,9,34816
mavv 92,124,32
s_add_u32 m0, s32, 33792
mavv 96,96,36
rt 88,10,32768
mavv 100,100,36
rqv 32,2,4096
mavv 104,104,36
s_add_u32 s61, s61, s30
mavv 108,108,36
rt 90,10,34816
mavv 112,112,36
dm 13,20,61
mavv 116,116,36
rt 92,11,32768
mavv 120,120,36
s_add_u32 m0, s33, 18432
mavv 124,124,36
rt 94,11,34816
mavv 128,96,40
rqv 36,2,6144
mavv 132,100,40
s_add_u32 s67, s67, s31
mavv 136,104,40
rqv 48,2,12288
mavv 140,108,40
dm 14,24,67
mavv 144,112,40
mavv 148,116,40
s_add_u32 m0, s32, 34816
mavv 152,120,40
mavv 156,124,40
s_add_u32 s61, s61, s30
mavv 160,96,44
rqv 40,2,8192
mavv 164,100,44
rqv 52,2,14336
mavv 168,104,44
dm 12,20,61
mavv 172,108,44
mavv 176,112,44
s_add_u32 m0, s33, 19456
mavv 180,116,44
mavv 184,120,44
mavv 188,124,44
s_add_u32 s67, s67, s31
mavv 192,96,56
rqv 44,2,10240
mavv 196,100,56
dm 15,24,67
mavv 200,104,56
mavv 204,108,56
mavv 208,112,56
s_add_u32 m0, s32, 35840
mavv 212,116,56
mavv 216,120,56
s_add_u32 s61, s61, s30
mavv 220,124,56
mavv 224,96,60
mavv 228,100,60
dm 13,20,61
mavv 232,104,60
s_add_u32 s68, s68, s50
mavv 236,108,60
s_mov_b32 s69, s68
mavv 240,112,60
s_add_u32 s29, s29, s65
mavv 244,116,60
buffer_store_dwordx4 v[176:179], v16, s[84:87], s69 offen sc1
mavv 248,120,60
s_add_u32 s69, s69, s51
mavv 252,124,60
buffer_store_dwordx4 v[180:183], v16, s[84:87], s69 offen sc1
s_waitcnt vmcnt(22) lgkmcnt(0)
s_barrier
mavv 0,64,24
rt 96,4,49152
mavv 4,68,24
s_add_u32 m0, s33, 32768
mavv 8,72,24
rt 98,4,51200
mavv 12,76,24
s_mov_b32 s67, s29
mavv 16,80,24
rt 100,5,49152
mavv 20,84,24
dm 14,24,67
mavv 24,88,24
rt 102,5,51200
mavv 28,92,24
s_add_u32 m0, s32, 49152
mavv 32,64,28
rt 104,6,49152
mavv 36,68,28
rqv 24,3,0
mavv 40,72,28
s_add_u32 s61, s28, s64
mavv 44,76,28
rt 106,6,51200
mavv 48,80,28
dm 12,20,61
mavv 52,84,28
rt 108,7,49152
mavv 56,88,28
rt 110,7,51200
mavv 60,92,28
s_add_u32 m0, s33, 33792
mavv 64,64,32
rt 112,8,49152
mavv 68,68,32
rqv 28,3,2048
mavv 72,72,32
s_add_u32 s67, s67, s31
mavv 76,76,32
rt 114,8,51200
mavv 80,80,32
dm 15,24,67
mavv 84,84,32
rt 116,9,49152
mavv 88,88,32
s_add_u32 m0, s32, 50176
mavv 92,92,32
rt 118,9,51200
mavv 96,64,36
s_add_u32 s61, s61, s30
mavv 100,68,36
rt 120,10,49152
mavv 104,72,36
rqv 32,3,4096
mavv 108,76,36
rt 122,10,51200
mavv 112,80,36
dm 13,20,61
mavv 116,84,36
rt 124,11,49152
mavv 120,88,36
s_add_u32 m0, s33, 34816
mavv 124,92,36
rt 126,11,51200
mavv 128,64,40
s_add_u32 s67, s67, s31
mavv 132,68,40
rqv 36,3,6144
mavv 136,72,40
rqv 56,3,12288
mavv 140,76,40
dm 14,24,67
mavv 144,80,40
s_add_u32 m0, s32, 51200
mavv 148,84,40
mavv 152,88,40
s_add_u32 s61, s61, s30
mavv 156,92,40
mavv 160,64,44
rqv 40,3,8192
mavv 164,68,44
dm 12,20,61
mavv 168,72,44
rqv 60,3,14336
mavv 172,76,44
s_add_u32 m0, s33, 35840
mavv 176,80,44
mavv 180,84,44
s_add_u32 s67, s67, s31
mavv 184,88,44
mavv 188,92,44
dm 15,24,67
mavv 192,64,48
rqv 44,3,10240
mavv 196,68,48
mavv 200,72,48
s_add_u32 m0, s32, 52224
mavv 204,76,48
mavv 208,80,48
s_add_u32 s61, s61, s30
mavv 212,84,48
mavv 216,88,48
dm 13,20,61
mavv 220,92,48
mavv 224,64,52
mavv 228,68,52
s_add_u32 s29, s29, s65
mavv 232,72,52
s_add_u32 s69, s69, s51
mavv 236,76,52
s_add_u32 s28, s28, 128
mavv 240,80,52
buffer_store_dwordx4 v[184:187], v16, s[84:87], s69 offen sc1
mavv 244,84,52
s_add_u32 s69, s69, s51
mavv 248,88,52
buffer_store_dwordx4 v[188:191], v16, s[84:87], s69 offen sc1
mavv 252,92,52
s_waitcnt vmcnt(20) lgkmcnt(0)
s_barrier
mavv 0,96,24
rt 64,4,0
mavv 4,100,24
s_add_u32 m0, s33, 49152
mavv 8,104,24
rt 66,4,2048
mavv 12,108,24
s_mov_b32 s67, s29
mavv 16,112,24
rt 68,5,0
mavv 20,116,24
dm 14,24,67
mavv 24,120,24
rt 70,5,2048
mavv 28,124,24
s_add_u32 m0, s32, 65536
mavv 32,96,28
rt 72,6,0
mavv 36,100,28
rqv 24,0,0
mavv 40,104,28
s_mov_b32 s61, s28
mavv 44,108,28
rt 74,6,2048
mavv 48,112,28
dm 12,20,61
mavv 52,116,28
rt 76,7,0
mavv 56,120,28
s_add_u32 m0, s33, 50176
mavv 60,124,28
rt 78,7,2048
mavv 64,96,32
rt 80,8,0
mavv 68,100,32
rqv 28,0,2048
mavv 72,104,32
s_add_u32 s67, s67, s31
mavv 76,108,32
rt 82,8,2048
mavv 80,112,32
dm 15,24,67
mavv 84,116,32
rt 84,9,0
mavv 88,120,32
s_add_u32 m0, s32, 66560
mavv 92,124,32
rt 86,9,2048
mavv 96,96,36
s_add_u32 s61, s61, s30
mavv 100,100,36
rt 88,10,0
mavv 104,104,36
rqv 32,0,4096
mavv 108,108,36
dm 13,20,61
mavv 112,112,36
rt 90,10,2048
mavv 116,116,36
s_add_u32 m0, s33, 51200
mavv 120,120,36
rt 92,11,0
mavv 124,124,36
s_add_u32 s67, s67, s31
mavv 128,96,40
rt 94,11,2048
mavv 132,100,40
rqv 36,0,6144
mavv 136,104,40
dm 14,24,67
mavv 140,108,40
rqv 48,0,12288
mavv 144,112,40
s_add_u32 m0, s32, 67584
mavv 148,116,40
s_add_u32 s61, s61, s30
mavv 152,120,40
mavv 156,124,40
dm 12,20,61
mavv 160,96,44
rqv 40,0,8192
mavv 164,100,44
rqv 52,0,14336
mavv 168,104,44
s_add_u32 m0, s33, 52224
mavv 172,108,44
s_add_u32 s67, s67, s31
mavv 176,112,44
mavv 180,116,44
dm 15,24,67
mavv 184,120,44
mavv 188,124,44
mavv 192,96,56
rqv 44,0,10240
mavv 196,100,56
s_add_u32 m0, s32, 68608
mavv 200,104,56
s_add_u32 s61, s61, s30
mavv 204,108,56
mavv 208,112,56
dm 13,20,61
mavv 212,116,56
mavv 216,120,56
mavv 220,124,56
s_add_u32 s29, s29, s65
mavv 224,96,60
mavv 228,100,60
s_add_u32 s74, s78, .Lk1_BT+0-.Lk1_base
s_addc_u32 s75, s79, 0
mavv 232,104,60
s_add_u32 s69, s69, s51
mavv 236,108,60
global_load_dword v19, v236, s[74:75]
mavv 240,112,60
buffer_store_dwordx4 v[192:195], v16, s[84:87], s69 offen sc1
mavv 244,116,60
s_add_u32 s69, s69, s51
mavv 248,120,60
buffer_store_dwordx4 v[196:199], v16, s[84:87], s69 offen sc1
mavv 252,124,60
s_waitcnt vmcnt(21) lgkmcnt(0)
s_barrier
mavv 0,64,24
rt 96,4,16384
mavv 4,68,24
s_add_u32 m0, s33, 0
mavv 8,72,24
rt 98,4,18432
mavv 12,76,24
s_mov_b32 s67, s29
mavv 16,80,24
rt 100,5,16384
mavv 20,84,24
dm 14,24,67
mavv 24,88,24
)ASM" ::: "memory");
  asm volatile(R"ASM(
rt 102,5,18432
mavv 28,92,24
s_add_u32 m0, s32, 81920
mavv 32,64,28
rt 104,6,16384
mavv 36,68,28
rqv 24,1,0
mavv 40,72,28
s_add_u32 s61, s28, s64
mavv 44,76,28
rt 106,6,18432
mavv 48,80,28
dm 12,20,61
mavv 52,84,28
rt 108,7,16384
mavv 56,88,28
rt 110,7,18432
mavv 60,92,28
s_add_u32 m0, s33, 1024
mavv 64,64,32
rt 112,8,16384
mavv 68,68,32
rqv 28,1,2048
mavv 72,72,32
s_add_u32 s67, s67, s31
mavv 76,76,32
rt 114,8,18432
mavv 80,80,32
dm 15,24,67
mavv 84,84,32
rt 116,9,16384
mavv 88,88,32
s_add_u32 m0, s32, 82944
mavv 92,92,32
rt 118,9,18432
mavv 96,64,36
s_add_u32 s61, s61, s30
mavv 100,68,36
rt 120,10,16384
mavv 104,72,36
rqv 32,1,4096
mavv 108,76,36
rt 122,10,18432
mavv 112,80,36
dm 13,20,61
mavv 116,84,36
rt 124,11,16384
mavv 120,88,36
s_add_u32 m0, s33, 2048
mavv 124,92,36
rt 126,11,18432
mavv 128,64,40
s_add_u32 s67, s67, s31
mavv 132,68,40
rqv 36,1,6144
mavv 136,72,40
rqv 56,1,12288
mavv 140,76,40
dm 14,24,67
mavv 144,80,40
s_add_u32 m0, s32, 83968
mavv 148,84,40
mavv 152,88,40
s_add_u32 s61, s61, s30
mavv 156,92,40
mavv 160,64,44
rqv 40,1,8192
mavv 164,68,44
dm 12,20,61
mavv 168,72,44
rqv 60,1,14336
mavv 172,76,44
s_add_u32 m0, s33, 3072
mavv 176,80,44
mavv 180,84,44
s_add_u32 s67, s67, s31
mavv 184,88,44
mavv 188,92,44
dm 15,24,67
mavv 192,64,48
rqv 44,1,10240
mavv 196,68,48
mavv 200,72,48
s_add_u32 m0, s32, 84992
mavv 204,76,48
mavv 208,80,48
s_add_u32 s61, s61, s30
mavv 212,84,48
mavv 216,88,48
dm 13,20,61
mavv 220,92,48
mavv 224,64,52
mavv 228,68,52
s_add_u32 s29, s29, s65
mavv 232,72,52
s_add_u32 s68, s68, s50
mavv 236,76,52
s_mov_b32 s69, s68
mavv 240,80,52
s_add_u32 s28, s28, 128
mavv 244,84,52
buffer_store_dwordx4 v[200:203], v16, s[84:87], s69 offen sc1
mavv 248,88,52
s_add_u32 s69, s69, s51
mavv 252,92,52
buffer_store_dwordx4 v[204:207], v16, s[84:87], s69 offen sc1
s_waitcnt vmcnt(21) lgkmcnt(0)
s_barrier
mavv 0,96,24
rt 64,4,32768
mavv 4,100,24
s_add_u32 m0, s33, 16384
mavv 8,104,24
rt 66,4,34816
mavv 12,108,24
s_mov_b32 s67, s29
mavv 16,112,24
rt 68,5,32768
mavv 20,116,24
dm 14,24,67
mavv 24,120,24
rt 70,5,34816
mavv 28,124,24
s_add_u32 m0, s32, 0
mavv 32,96,28
rt 72,6,32768
mavv 36,100,28
rqv 24,0,32768
mavv 40,104,28
s_mov_b32 s61, s28
mavv 44,108,28
rt 74,6,34816
mavv 48,112,28
rt 76,7,32768
mavv 52,116,28
dm 12,20,61
mavv 56,120,28
rt 78,7,34816
mavv 60,124,28
s_add_u32 m0, s33, 17408
mavv 64,96,32
rt 80,8,32768
mavv 68,100,32
rqv 28,0,34816
mavv 72,104,32
s_add_u32 s67, s67, s31
mavv 76,108,32
rt 82,8,34816
mavv 80,112,32
dm 15,24,67
mavv 84,116,32
rt 84,9,32768
mavv 88,120,32
rt 86,9,34816
mavv 92,124,32
s_add_u32 m0, s32, 1024
mavv 96,96,36
rt 88,10,32768
mavv 100,100,36
rqv 32,0,36864
mavv 104,104,36
s_add_u32 s61, s61, s30
mavv 108,108,36
rt 90,10,34816
mavv 112,112,36
dm 13,20,61
mavv 116,116,36
rt 92,11,32768
mavv 120,120,36
s_add_u32 m0, s33, 18432
mavv 124,124,36
rt 94,11,34816
mavv 128,96,40
rqv 36,0,38912
mavv 132,100,40
s_add_u32 s67, s67, s31
mavv 136,104,40
rqv 48,0,45056
mavv 140,108,40
dm 14,24,67
mavv 144,112,40
mavv 148,116,40
s_add_u32 m0, s32, 2048
mavv 152,120,40
mavv 156,124,40
s_add_u32 s61, s61, s30
mavv 160,96,44
rqv 40,0,40960
mavv 164,100,44
rqv 52,0,47104
mavv 168,104,44
dm 12,20,61
mavv 172,108,44
mavv 176,112,44
s_add_u32 m0, s33, 19456
mavv 180,116,44
mavv 184,120,44
mavv 188,124,44
s_add_u32 s67, s67, s31
mavv 192,96,56
rqv 44,0,43008
mavv 196,100,56
dm 15,24,67
mavv 200,104,56
mavv 204,108,56
mavv 208,112,56
s_add_u32 m0, s32, 3072
mavv 212,116,56
mavv 216,120,56
s_add_u32 s61, s61, s30
mavv 220,124,56
mavv 224,96,60
mavv 228,100,60
dm 13,20,61
mavv 232,104,60
s_add_u32 s69, s69, s51
mavv 236,108,60
s_add_u32 s29, s29, s65
mavv 240,112,60
buffer_store_dwordx4 v[208:211], v16, s[84:87], s69 offen sc1
mavv 244,116,60
s_add_u32 s69, s69, s51
mavv 248,120,60
buffer_store_dwordx4 v[212:215], v16, s[84:87], s69 offen sc1
mavv 252,124,60
s_waitcnt vmcnt(20) lgkmcnt(0)
s_barrier
mavv 0,64,24
rt 96,4,49152
mavv 4,68,24
s_add_u32 m0, s33, 32768
mavv 8,72,24
rt 98,4,51200
mavv 12,76,24
s_mov_b32 s67, s29
mavv 16,80,24
rt 100,5,49152
mavv 20,84,24
dm 14,24,67
mavv 24,88,24
rt 102,5,51200
mavv 28,92,24
s_add_u32 m0, s32, 16384
mavv 32,64,28
rt 104,6,49152
mavv 36,68,28
rqv 24,1,32768
mavv 40,72,28
s_add_u32 s61, s28, s64
mavv 44,76,28
rt 106,6,51200
mavv 48,80,28
dm 12,20,61
mavv 52,84,28
rt 108,7,49152
mavv 56,88,28
s_add_u32 m0, s33, 33792
mavv 60,92,28
rt 110,7,51200
mavv 64,64,32
s_add_u32 s67, s67, s31
mavv 68,68,32
rt 112,8,49152
mavv 72,72,32
rqv 28,1,34816
mavv 76,76,32
dm 15,24,67
mavv 80,80,32
rt 114,8,51200
mavv 84,84,32
s_add_u32 m0, s32, 17408
mavv 88,88,32
rt 116,9,49152
mavv 92,92,32
rt 118,9,51200
mavv 96,64,36
s_add_u32 s61, s61, s30
rt 120,10,49152
mavv 100,68,36
rqv 32,1,36864
mavv 104,72,36
dm 13,20,61
mavv 108,76,36
rt 122,10,51200
mavv 112,80,36
s_add_u32 m0, s33, 34816
mavv 116,84,36
rt 124,11,49152
mavv 120,88,36
s_add_u32 s67, s67, s31
mavv 124,92,36
rt 126,11,51200
mavv 128,64,40
dm 14,24,67
mavv 132,68,40
rqv 36,1,38912
mavv 136,72,40
rqv 56,1,45056
mavv 140,76,40
s_add_u32 m0, s32, 18432
mavv 144,80,40
s_add_u32 s61, s61, s30
mavv 148,84,40
dm 12,20,61
mavv 152,88,40
mavv 156,92,40
s_add_u32 m0, s33, 35840
mavv 160,64,44
rqv 40,1,40960
mavv 164,68,44
rqv 60,1,47104
mavv 168,72,44
s_add_u32 s67, s67, s31
mavv 172,76,44
mavv 176,80,44
dm 15,24,67
mavv 180,84,44
mavv 184,88,44
s_add_u32 m0, s32, 19456
mavv 188,92,44
mavv 192,64,48
rqv 44,1,43008
mavv 196,68,48
s_add_u32 s61, s61, s30
mavv 200,72,48
dm 13,20,61
mavv 204,76,48
mavv 208,80,48
mavv 212,84,48
s_add_u32 s29, s29, s65
mavv 216,88,48
mavv 220,92,48
s_add_u32 s28, s28, 128
mavv 224,64,52
mavv 228,68,52
s_add_u32 s74, s78, .Lk1_BT+4096-.Lk1_base
s_addc_u32 s75, s79, 0
mavv 232,72,52
s_add_u32 s69, s69, s51
mavv 236,76,52
global_load_dword v19, v236, s[74:75]
mavv 240,80,52
buffer_store_dwordx4 v[216:219], v16, s[84:87], s69 offen sc1
mavv 244,84,52
s_add_u32 s69, s69, s51
mavv 248,88,52
buffer_store_dwordx4 v[220:223], v16, s[84:87], s69 offen sc1
mavv 252,92,52
s_branch .Lk1_BT
.Lk1_BNT:
s_waitcnt vmcnt(19) lgkmcnt(0)
s_barrier
zavv 0,24,64
rt 96,4,49152
ar 224,64
ar 225,65
ar 226,66
ar 227,67
ar 228,68
ar 229,69
ar 230,70
zavv 8,24,68
s_add_u32 m0, s33, 32768
ar 231,71
v_pk_mul_f32 v[224:225], v[224:225], s[48:49] op_sel_hi:[1,0]
v_pk_mul_f32 v[226:227], v[226:227], s[48:49] op_sel_hi:[1,0]
v_pk_mul_f32 v[228:229], v[228:229], s[48:49] op_sel_hi:[1,0]
v_pk_mul_f32 v[230:231], v[230:231], s[48:49] op_sel_hi:[1,0]
cv 128,224,225
cv 129,226,227
zavv 16,24,72
rt 98,4,51200
cv 130,228,229
cv 131,230,231
v_cndmask_b32_e64 v130, v130, v17, s[76:77]
v_cndmask_b32_e64 v131, v131, v20, s[76:77]
ar 224,72
ar 225,73
ar 226,74
zavv 24,24,76
s_mov_b32 s67, s29
ar 227,75
ar 228,76
ar 229,77
ar 230,78
ar 231,79
v_pk_mul_f32 v[224:225], v[224:225], s[48:49] op_sel_hi:[1,0]
v_pk_mul_f32 v[226:227], v[226:227], s[48:49] op_sel_hi:[1,0]
zavv 32,24,80
rt 100,5,49152
v_pk_mul_f32 v[228:229], v[228:229], s[48:49] op_sel_hi:[1,0]
v_pk_mul_f32 v[230:231], v[230:231], s[48:49] op_sel_hi:[1,0]
cv 132,224,225
cv 133,226,227
cv 134,228,229
cv 135,230,231
v_cndmask_b32_e64 v134, v134, v17, s[76:77]
zavv 40,24,84
dm 14,24,67
v_cndmask_b32_e64 v135, v135, v20, s[76:77]
ar 224,80
ar 225,81
ar 226,82
ar 227,83
ar 228,84
ar 229,85
zavv 48,24,88
rt 102,5,51200
ar 230,86
ar 231,87
v_pk_mul_f32 v[224:225], v[224:225], s[48:49] op_sel_hi:[1,0]
v_pk_mul_f32 v[226:227], v[226:227], s[48:49] op_sel_hi:[1,0]
v_pk_mul_f32 v[228:229], v[228:229], s[48:49] op_sel_hi:[1,0]
v_pk_mul_f32 v[230:231], v[230:231], s[48:49] op_sel_hi:[1,0]
cv 136,224,225
zavv 56,24,92
s_add_u32 m0, s32, 16384
cv 137,226,227
cv 138,228,229
cv 139,230,231
v_cndmask_b32_e64 v138, v138, v17, s[76:77]
v_cndmask_b32_e64 v139, v139, v20, s[76:77]
ar 224,88
ar 225,89
zavv 4,28,64
rt 104,6,49152
ar 226,90
ar 227,91
ar 228,92
ar 229,93
ar 230,94
ar 231,95
v_pk_mul_f32 v[224:225], v[224:225], s[48:49] op_sel_hi:[1,0]
zavv 12,28,68
rqv 24,1,32768
v_pk_mul_f32 v[226:227], v[226:227], s[48:49] op_sel_hi:[1,0]
v_pk_mul_f32 v[228:229], v[228:229], s[48:49] op_sel_hi:[1,0]
v_pk_mul_f32 v[230:231], v[230:231], s[48:49] op_sel_hi:[1,0]
cv 140,224,225
cv 141,226,227
cv 142,228,229
cv 143,230,231
zavv 20,28,72
s_add_u32 s61, s28, s64
v_cndmask_b32_e64 v142, v142, v17, s[76:77]
v_cndmask_b32_e64 v143, v143, v20, s[76:77]
ar 224,96
ar 225,97
ar 226,98
ar 227,99
ar 228,100
zavv 28,28,76
rt 106,6,51200
ar 229,101
ar 230,102
ar 231,103
v_pk_mul_f32 v[224:225], v[224:225], s[48:49] op_sel_hi:[1,0]
v_pk_mul_f32 v[226:227], v[226:227], s[48:49] op_sel_hi:[1,0]
v_pk_mul_f32 v[228:229], v[228:229], s[48:49] op_sel_hi:[1,0]
v_pk_mul_f32 v[230:231], v[230:231], s[48:49] op_sel_hi:[1,0]
zavv 36,28,80
dm 12,20,61
cv 144,224,225
cv 145,226,227
cv 146,228,229
cv 147,230,231
v_cndmask_b32_e64 v146, v146, v17, s[76:77]
v_cndmask_b32_e64 v147, v147, v20, s[76:77]
ar 224,104
zavv 44,28,84
rt 108,7,49152
ar 225,105
ar 226,106
ar 227,107
ar 228,108
ar 229,109
ar 230,110
ar 231,111
zavv 52,28,88
s_add_u32 m0, s33, 33792
v_pk_mul_f32 v[224:225], v[224:225], s[48:49] op_sel_hi:[1,0]
v_pk_mul_f32 v[226:227], v[226:227], s[48:49] op_sel_hi:[1,0]
v_pk_mul_f32 v[228:229], v[228:229], s[48:49] op_sel_hi:[1,0]
v_pk_mul_f32 v[230:231], v[230:231], s[48:49] op_sel_hi:[1,0]
cv 148,224,225
cv 149,226,227
cv 150,228,229
zavv 60,28,92
rt 110,7,51200
cv 151,230,231
v_cndmask_b32_e64 v150, v150, v17, s[76:77]
v_cndmask_b32_e64 v151, v151, v20, s[76:77]
ar 224,112
ar 225,113
ar 226,114
ar 227,115
zavv 64,32,64
s_add_u32 s67, s67, s31
ar 228,116
ar 229,117
ar 230,118
ar 231,119
v_pk_mul_f32 v[224:225], v[224:225], s[48:49] op_sel_hi:[1,0]
v_pk_mul_f32 v[226:227], v[226:227], s[48:49] op_sel_hi:[1,0]
v_pk_mul_f32 v[228:229], v[228:229], s[48:49] op_sel_hi:[1,0]
zavv 72,32,68
rt 112,8,49152
v_pk_mul_f32 v[230:231], v[230:231], s[48:49] op_sel_hi:[1,0]
cv 152,224,225
cv 153,226,227
cv 154,228,229
cv 155,230,231
v_cndmask_b32_e64 v154, v154, v17, s[76:77]
v_cndmask_b32_e64 v155, v155, v20, s[76:77]
zavv 80,32,72
rqv 28,1,34816
ar 224,120
ar 225,121
ar 226,122
ar 227,123
ar 228,124
ar 229,125
ar 230,126
zavv 88,32,76
dm 15,24,67
ar 231,127
v_pk_mul_f32 v[224:225], v[224:225], s[48:49] op_sel_hi:[1,0]
v_pk_mul_f32 v[226:227], v[226:227], s[48:49] op_sel_hi:[1,0]
v_pk_mul_f32 v[228:229], v[228:229], s[48:49] op_sel_hi:[1,0]
v_pk_mul_f32 v[230:231], v[230:231], s[48:49] op_sel_hi:[1,0]
cv 156,224,225
cv 157,226,227
zavv 96,32,80
rt 114,8,51200
cv 158,228,229
cv 159,230,231
v_cndmask_b32_e64 v158, v158, v17, s[76:77]
v_cndmask_b32_e64 v159, v159, v20, s[76:77]
ar 224,128
ar 225,129
ar 226,130
zavv 104,32,84
s_add_u32 m0, s32, 17408
ar 227,131
ar 228,132
ar 229,133
ar 230,134
ar 231,135
v_pk_mul_f32 v[224:225], v[224:225], s[48:49] op_sel_hi:[1,0]
v_pk_mul_f32 v[226:227], v[226:227], s[48:49] op_sel_hi:[1,0]
zavv 112,32,88
rt 116,9,49152
v_pk_mul_f32 v[228:229], v[228:229], s[48:49] op_sel_hi:[1,0]
v_pk_mul_f32 v[230:231], v[230:231], s[48:49] op_sel_hi:[1,0]
cv 160,224,225
cv 161,226,227
cv 162,228,229
cv 163,230,231
v_cndmask_b32_e64 v162, v162, v17, s[76:77]
zavv 120,32,92
rt 118,9,51200
v_cndmask_b32_e64 v163, v163, v20, s[76:77]
ar 224,136
ar 225,137
ar 226,138
ar 227,139
ar 228,140
ar 229,141
zavv 68,36,64
s_add_u32 s61, s61, s30
rt 120,10,49152
ar 230,142
ar 231,143
v_pk_mul_f32 v[224:225], v[224:225], s[48:49] op_sel_hi:[1,0]
v_pk_mul_f32 v[226:227], v[226:227], s[48:49] op_sel_hi:[1,0]
v_pk_mul_f32 v[228:229], v[228:229], s[48:49] op_sel_hi:[1,0]
v_pk_mul_f32 v[230:231], v[230:231], s[48:49] op_sel_hi:[1,0]
cv 164,224,225
zavv 76,36,68
rqv 32,1,36864
cv 165,226,227
cv 166,228,229
cv 167,230,231
v_cndmask_b32_e64 v166, v166, v17, s[76:77]
v_cndmask_b32_e64 v167, v167, v20, s[76:77]
ar 224,144
ar 225,145
zavv 84,36,72
dm 13,20,61
ar 226,146
ar 227,147
ar 228,148
ar 229,149
ar 230,150
ar 231,151
v_pk_mul_f32 v[224:225], v[224:225], s[48:49] op_sel_hi:[1,0]
zavv 92,36,76
rt 122,10,51200
v_pk_mul_f32 v[226:227], v[226:227], s[48:49] op_sel_hi:[1,0]
v_pk_mul_f32 v[228:229], v[228:229], s[48:49] op_sel_hi:[1,0]
)ASM" ::: "memory");
  asm volatile(R"ASM(
v_pk_mul_f32 v[230:231], v[230:231], s[48:49] op_sel_hi:[1,0]
cv 168,224,225
cv 169,226,227
cv 170,228,229
cv 171,230,231
zavv 100,36,80
s_add_u32 m0, s33, 34816
v_cndmask_b32_e64 v170, v170, v17, s[76:77]
v_cndmask_b32_e64 v171, v171, v20, s[76:77]
ar 224,152
ar 225,153
ar 226,154
ar 227,155
ar 228,156
zavv 108,36,84
rt 124,11,49152
ar 229,157
ar 230,158
ar 231,159
v_pk_mul_f32 v[224:225], v[224:225], s[48:49] op_sel_hi:[1,0]
v_pk_mul_f32 v[226:227], v[226:227], s[48:49] op_sel_hi:[1,0]
v_pk_mul_f32 v[228:229], v[228:229], s[48:49] op_sel_hi:[1,0]
v_pk_mul_f32 v[230:231], v[230:231], s[48:49] op_sel_hi:[1,0]
zavv 116,36,88
s_add_u32 s67, s67, s31
cv 172,224,225
cv 173,226,227
cv 174,228,229
cv 175,230,231
v_cndmask_b32_e64 v174, v174, v17, s[76:77]
v_cndmask_b32_e64 v175, v175, v20, s[76:77]
ar 224,160
zavv 124,36,92
rt 126,11,51200
ar 225,161
ar 226,162
ar 227,163
ar 228,164
ar 229,165
ar 230,166
ar 231,167
zavv 128,40,64
dm 14,24,67
v_pk_mul_f32 v[224:225], v[224:225], s[48:49] op_sel_hi:[1,0]
v_pk_mul_f32 v[226:227], v[226:227], s[48:49] op_sel_hi:[1,0]
v_pk_mul_f32 v[228:229], v[228:229], s[48:49] op_sel_hi:[1,0]
v_pk_mul_f32 v[230:231], v[230:231], s[48:49] op_sel_hi:[1,0]
cv 176,224,225
cv 177,226,227
cv 178,228,229
zavv 136,40,68
rqv 36,1,38912
cv 179,230,231
v_cndmask_b32_e64 v178, v178, v17, s[76:77]
v_cndmask_b32_e64 v179, v179, v20, s[76:77]
ar 224,168
ar 225,169
ar 226,170
ar 227,171
zavv 144,40,72
rqv 56,1,45056
ar 228,172
ar 229,173
ar 230,174
ar 231,175
v_pk_mul_f32 v[224:225], v[224:225], s[48:49] op_sel_hi:[1,0]
v_pk_mul_f32 v[226:227], v[226:227], s[48:49] op_sel_hi:[1,0]
v_pk_mul_f32 v[228:229], v[228:229], s[48:49] op_sel_hi:[1,0]
zavv 152,40,76
s_add_u32 m0, s32, 18432
v_pk_mul_f32 v[230:231], v[230:231], s[48:49] op_sel_hi:[1,0]
cv 180,224,225
cv 181,226,227
cv 182,228,229
cv 183,230,231
v_cndmask_b32_e64 v182, v182, v17, s[76:77]
v_cndmask_b32_e64 v183, v183, v20, s[76:77]
zavv 160,40,80
s_add_u32 s61, s61, s30
ar 224,176
ar 225,177
ar 226,178
ar 227,179
ar 228,180
ar 229,181
ar 230,182
zavv 168,40,84
dm 12,20,61
ar 231,183
v_pk_mul_f32 v[224:225], v[224:225], s[48:49] op_sel_hi:[1,0]
v_pk_mul_f32 v[226:227], v[226:227], s[48:49] op_sel_hi:[1,0]
v_pk_mul_f32 v[228:229], v[228:229], s[48:49] op_sel_hi:[1,0]
v_pk_mul_f32 v[230:231], v[230:231], s[48:49] op_sel_hi:[1,0]
cv 184,224,225
cv 185,226,227
zavv 176,40,88
cv 186,228,229
cv 187,230,231
v_cndmask_b32_e64 v186, v186, v17, s[76:77]
v_cndmask_b32_e64 v187, v187, v20, s[76:77]
ar 224,184
ar 225,185
ar 226,186
ar 227,187
zavv 184,40,92
s_add_u32 m0, s33, 35840
ar 228,188
ar 229,189
ar 230,190
ar 231,191
v_pk_mul_f32 v[224:225], v[224:225], s[48:49] op_sel_hi:[1,0]
v_pk_mul_f32 v[226:227], v[226:227], s[48:49] op_sel_hi:[1,0]
v_pk_mul_f32 v[228:229], v[228:229], s[48:49] op_sel_hi:[1,0]
zavv 132,44,64
rqv 40,1,40960
v_pk_mul_f32 v[230:231], v[230:231], s[48:49] op_sel_hi:[1,0]
cv 188,224,225
cv 189,226,227
cv 190,228,229
cv 191,230,231
v_cndmask_b32_e64 v190, v190, v17, s[76:77]
v_cndmask_b32_e64 v191, v191, v20, s[76:77]
zavv 140,44,68
rqv 60,1,47104
ar 224,192
ar 225,193
ar 226,194
ar 227,195
ar 228,196
ar 229,197
ar 230,198
zavv 148,44,72
s_add_u32 s67, s67, s31
ar 231,199
v_pk_mul_f32 v[224:225], v[224:225], s[48:49] op_sel_hi:[1,0]
v_pk_mul_f32 v[226:227], v[226:227], s[48:49] op_sel_hi:[1,0]
v_pk_mul_f32 v[228:229], v[228:229], s[48:49] op_sel_hi:[1,0]
v_pk_mul_f32 v[230:231], v[230:231], s[48:49] op_sel_hi:[1,0]
cv 192,224,225
cv 193,226,227
zavv 156,44,76
cv 194,228,229
cv 195,230,231
v_cndmask_b32_e64 v194, v194, v17, s[76:77]
v_cndmask_b32_e64 v195, v195, v20, s[76:77]
ar 224,200
ar 225,201
ar 226,202
zavv 164,44,80
dm 15,24,67
ar 227,203
ar 228,204
ar 229,205
ar 230,206
ar 231,207
v_pk_mul_f32 v[224:225], v[224:225], s[48:49] op_sel_hi:[1,0]
v_pk_mul_f32 v[226:227], v[226:227], s[48:49] op_sel_hi:[1,0]
zavv 172,44,84
v_pk_mul_f32 v[228:229], v[228:229], s[48:49] op_sel_hi:[1,0]
v_pk_mul_f32 v[230:231], v[230:231], s[48:49] op_sel_hi:[1,0]
cv 196,224,225
cv 197,226,227
cv 198,228,229
cv 199,230,231
v_cndmask_b32_e64 v198, v198, v17, s[76:77]
zavv 180,44,88
s_add_u32 m0, s32, 19456
v_cndmask_b32_e64 v199, v199, v20, s[76:77]
ar 224,208
ar 225,209
ar 226,210
ar 227,211
ar 228,212
ar 229,213
zavv 188,44,92
ar 230,214
ar 231,215
v_pk_mul_f32 v[224:225], v[224:225], s[48:49] op_sel_hi:[1,0]
v_pk_mul_f32 v[226:227], v[226:227], s[48:49] op_sel_hi:[1,0]
v_pk_mul_f32 v[228:229], v[228:229], s[48:49] op_sel_hi:[1,0]
v_pk_mul_f32 v[230:231], v[230:231], s[48:49] op_sel_hi:[1,0]
cv 200,224,225
zavv 192,48,64
rqv 44,1,43008
cv 201,226,227
cv 202,228,229
cv 203,230,231
v_cndmask_b32_e64 v202, v202, v17, s[76:77]
v_cndmask_b32_e64 v203, v203, v20, s[76:77]
ar 224,216
ar 225,217
zavv 200,48,68
s_add_u32 s61, s61, s30
ar 226,218
ar 227,219
ar 228,220
ar 229,221
ar 230,222
ar 231,223
v_pk_mul_f32 v[224:225], v[224:225], s[48:49] op_sel_hi:[1,0]
zavv 208,48,72
dm 13,20,61
v_pk_mul_f32 v[226:227], v[226:227], s[48:49] op_sel_hi:[1,0]
v_pk_mul_f32 v[228:229], v[228:229], s[48:49] op_sel_hi:[1,0]
v_pk_mul_f32 v[230:231], v[230:231], s[48:49] op_sel_hi:[1,0]
cv 204,224,225
cv 205,226,227
cv 206,228,229
cv 207,230,231
zavv 216,48,76
v_cndmask_b32_e64 v206, v206, v17, s[76:77]
v_cndmask_b32_e64 v207, v207, v20, s[76:77]
ar 224,224
ar 225,225
ar 226,226
ar 227,227
ar 228,228
zavv 224,48,80
ar 229,229
ar 230,230
ar 231,231
v_pk_mul_f32 v[224:225], v[224:225], s[48:49] op_sel_hi:[1,0]
v_pk_mul_f32 v[226:227], v[226:227], s[48:49] op_sel_hi:[1,0]
v_pk_mul_f32 v[228:229], v[228:229], s[48:49] op_sel_hi:[1,0]
v_pk_mul_f32 v[230:231], v[230:231], s[48:49] op_sel_hi:[1,0]
cv 208,224,225
cv 209,226,227
cv 210,228,229
cv 211,230,231
v_cndmask_b32_e64 v210, v210, v17, s[76:77]
v_cndmask_b32_e64 v211, v211, v20, s[76:77]
ar 224,232
ar 225,233
ar 226,234
ar 227,235
zavv 232,48,84
s_add_u32 s29, s29, s65
ar 228,236
ar 229,237
ar 230,238
ar 231,239
v_pk_mul_f32 v[224:225], v[224:225], s[48:49] op_sel_hi:[1,0]
v_pk_mul_f32 v[226:227], v[226:227], s[48:49] op_sel_hi:[1,0]
v_pk_mul_f32 v[228:229], v[228:229], s[48:49] op_sel_hi:[1,0]
v_pk_mul_f32 v[230:231], v[230:231], s[48:49] op_sel_hi:[1,0]
cv 212,224,225
cv 213,226,227
cv 214,228,229
cv 215,230,231
v_cndmask_b32_e64 v214, v214, v17, s[76:77]
v_cndmask_b32_e64 v215, v215, v20, s[76:77]
ar 224,240
ar 225,241
ar 226,242
ar 227,243
zavv 240,48,88
ar 228,244
ar 229,245
ar 230,246
ar 231,247
v_pk_mul_f32 v[224:225], v[224:225], s[48:49] op_sel_hi:[1,0]
v_pk_mul_f32 v[226:227], v[226:227], s[48:49] op_sel_hi:[1,0]
v_pk_mul_f32 v[228:229], v[228:229], s[48:49] op_sel_hi:[1,0]
v_pk_mul_f32 v[230:231], v[230:231], s[48:49] op_sel_hi:[1,0]
cv 216,224,225
cv 217,226,227
cv 218,228,229
cv 219,230,231
v_cndmask_b32_e64 v218, v218, v17, s[76:77]
v_cndmask_b32_e64 v219, v219, v20, s[76:77]
ar 224,248
ar 225,249
ar 226,250
ar 227,251
zavv 248,48,92
s_add_u32 s28, s28, 128
ar 228,252
ar 229,253
ar 230,254
ar 231,255
v_pk_mul_f32 v[224:225], v[224:225], s[48:49] op_sel_hi:[1,0]
v_pk_mul_f32 v[226:227], v[226:227], s[48:49] op_sel_hi:[1,0]
v_pk_mul_f32 v[228:229], v[228:229], s[48:49] op_sel_hi:[1,0]
zavv 196,52,64
v_pk_mul_f32 v[230:231], v[230:231], s[48:49] op_sel_hi:[1,0]
cv 220,224,225
cv 221,226,227
cv 222,228,229
cv 223,230,231
v_cndmask_b32_e64 v222, v222, v17, s[76:77]
v_cndmask_b32_e64 v223, v223, v20, s[76:77]
zavv 204,52,68
s_add_u32 s74, s78, .Lk1_BNT+8192-.Lk1_base
s_addc_u32 s75, s79, 0
zavv 212,52,72
zavv 220,52,76
global_load_dword v19, v236, s[74:75]
zavv 228,52,80
zavv 236,52,84
zavv 244,52,88
zavv 252,52,92
s_waitcnt vmcnt(18) lgkmcnt(0)
s_barrier
mavv 0,24,96
rt 64,4,0
mavv 8,24,100
s_add_u32 m0, s33, 49152
mavv 16,24,104
rt 66,4,2048
mavv 24,24,108
s_mov_b32 s67, s29
mavv 32,24,112
rt 68,5,0
mavv 40,24,116
dm 14,24,67
mavv 48,24,120
rt 70,5,2048
mavv 56,24,124
s_add_u32 m0, s32, 32768
mavv 4,28,96
rt 72,6,0
mavv 12,28,100
rqv 24,2,0
mavv 20,28,104
s_mov_b32 s61, s28
mavv 28,28,108
rt 74,6,2048
mavv 36,28,112
dm 12,20,61
mavv 44,28,116
rt 76,7,0
mavv 52,28,120
s_add_u32 m0, s33, 50176
mavv 60,28,124
rt 78,7,2048
mavv 64,32,96
s_add_u32 s67, s67, s31
mavv 72,32,100
rt 80,8,0
mavv 80,32,104
rqv 28,2,2048
mavv 88,32,108
dm 15,24,67
mavv 96,32,112
rt 82,8,2048
mavv 104,32,116
s_add_u32 m0, s32, 33792
mavv 112,32,120
rt 84,9,0
mavv 120,32,124
rt 86,9,2048
mavv 68,36,96
s_add_u32 s61, s61, s30
rt 88,10,0
mavv 76,36,100
rqv 32,2,4096
mavv 84,36,104
dm 13,20,61
mavv 92,36,108
rt 90,10,2048
mavv 100,36,112
s_add_u32 m0, s33, 51200
mavv 108,36,116
rt 92,11,0
mavv 116,36,120
s_add_u32 s67, s67, s31
mavv 124,36,124
rt 94,11,2048
mavv 128,40,96
dm 14,24,67
mavv 136,40,100
rqv 36,2,6144
mavv 144,40,104
rqv 48,2,12288
mavv 152,40,108
s_add_u32 m0, s32, 34816
mavv 160,40,112
s_add_u32 s61, s61, s30
mavv 168,40,116
dm 12,20,61
mavv 176,40,120
mavv 184,40,124
s_add_u32 m0, s33, 52224
mavv 132,44,96
rqv 40,2,8192
mavv 140,44,100
rqv 52,2,14336
mavv 148,44,104
s_add_u32 s67, s67, s31
mavv 156,44,108
mavv 164,44,112
dm 15,24,67
mavv 172,44,116
mavv 180,44,120
s_add_u32 m0, s32, 35840
mavv 188,44,124
mavv 192,56,96
rqv 44,2,10240
mavv 200,56,100
s_add_u32 s61, s61, s30
mavv 208,56,104
dm 13,20,61
mavv 216,56,108
mavv 224,56,112
mavv 232,56,116
s_add_u32 s29, s29, s65
mavv 240,56,120
mavv 248,56,124
buffer_load_dwordx4 v[224:227], v16, s[12:15], 0 offen
mavv 196,60,96
mavv 204,60,100
buffer_load_dwordx4 v[228:231], v18, s[8:11], s16 offen
mavv 212,60,104
s_mov_b32 s68, s88
mavv 220,60,108
s_mov_b32 s69, s68
mavv 228,60,112
buffer_load_dwordx4 v[232:235], v18, s[8:11], s16 offen offset:896
mavv 236,60,116
buffer_store_dwordx4 v[128:131], v16, s[84:87], s69 offen sc1
mavv 244,60,120
s_add_u32 s69, s69, s51
mavv 252,60,124
buffer_store_dwordx4 v[132:135], v16, s[84:87], s69 offen sc1
s_waitcnt vmcnt(22) lgkmcnt(0)
s_barrier
mavv 0,24,64
rt 96,4,16384
mavv 8,24,68
s_add_u32 m0, s33, 0
mavv 16,24,72
rt 98,4,18432
mavv 24,24,76
s_mov_b32 s67, s29
mavv 32,24,80
rt 100,5,16384
mavv 40,24,84
dm 14,24,67
mavv 48,24,88
rt 102,5,18432
mavv 56,24,92
s_add_u32 m0, s32, 49152
mavv 4,28,64
rt 104,6,16384
mavv 12,28,68
rqv 24,3,0
mavv 20,28,72
s_add_u32 s61, s28, s64
mavv 28,28,76
rt 106,6,18432
mavv 36,28,80
dm 12,20,61
mavv 44,28,84
rt 108,7,16384
mavv 52,28,88
rt 110,7,18432
mavv 60,28,92
s_add_u32 m0, s33, 1024
mavv 64,32,64
rt 112,8,16384
mavv 72,32,68
rqv 28,3,2048
mavv 80,32,72
s_add_u32 s67, s67, s31
mavv 88,32,76
rt 114,8,18432
mavv 96,32,80
dm 15,24,67
mavv 104,32,84
rt 116,9,16384
mavv 112,32,88
s_add_u32 m0, s32, 50176
mavv 120,32,92
rt 118,9,18432
mavv 68,36,64
s_add_u32 s61, s61, s30
mavv 76,36,68
rt 120,10,16384
mavv 84,36,72
rqv 32,3,4096
mavv 92,36,76
rt 122,10,18432
mavv 100,36,80
dm 13,20,61
mavv 108,36,84
rt 124,11,16384
mavv 116,36,88
s_add_u32 m0, s33, 2048
mavv 124,36,92
rt 126,11,18432
mavv 128,40,64
s_add_u32 s67, s67, s31
mavv 136,40,68
rqv 36,3,6144
mavv 144,40,72
rqv 56,3,12288
mavv 152,40,76
dm 14,24,67
mavv 160,40,80
s_add_u32 m0, s32, 51200
mavv 168,40,84
mavv 176,40,88
s_add_u32 s61, s61, s30
mavv 184,40,92
mavv 132,44,64
rqv 40,3,8192
mavv 140,44,68
dm 12,20,61
mavv 148,44,72
rqv 60,3,14336
mavv 156,44,76
s_add_u32 m0, s33, 3072
mavv 164,44,80
mavv 172,44,84
s_add_u32 s67, s67, s31
mavv 180,44,88
mavv 188,44,92
dm 15,24,67
mavv 192,48,64
rqv 44,3,10240
mavv 200,48,68
mavv 208,48,72
s_add_u32 m0, s32, 52224
mavv 216,48,76
mavv 224,48,80
s_add_u32 s61, s61, s30
mavv 232,48,84
mavv 240,48,88
dm 13,20,61
mavv 248,48,92
mavv 196,52,64
mavv 204,52,68
s_add_u32 s29, s29, s65
mavv 212,52,72
s_add_u32 s69, s69, s51
mavv 220,52,76
buffer_store_dwordx4 v[136:139], v16, s[84:87], s69 offen sc1
mavv 228,52,80
s_add_u32 s28, s28, 128
mavv 236,52,84
s_add_u32 s68, s68, 0x400
mavv 244,52,88
s_mov_b32 s69, s68
mavv 252,52,92
buffer_store_dwordx4 v[140:143], v16, s[84:87], s69 offen sc1
s_waitcnt vmcnt(23) lgkmcnt(0)
s_barrier
mavv 0,24,96
rt 64,4,32768
mavv 8,24,100
s_add_u32 m0, s33, 16384
mavv 16,24,104
rt 66,4,34816
mavv 24,24,108
s_mov_b32 s67, s29
mavv 32,24,112
rt 68,5,32768
mavv 40,24,116
dm 14,24,67
mavv 48,24,120
rt 70,5,34816
mavv 56,24,124
s_add_u32 m0, s32, 65536
mavv 4,28,96
rt 72,6,32768
mavv 12,28,100
rqv 24,0,0
mavv 20,28,104
s_mov_b32 s61, s28
mavv 28,28,108
rt 74,6,34816
mavv 36,28,112
dm 12,20,61
mavv 44,28,116
rt 76,7,32768
mavv 52,28,120
s_add_u32 m0, s33, 17408
mavv 60,28,124
rt 78,7,34816
mavv 64,32,96
rt 80,8,32768
mavv 72,32,100
rqv 28,0,2048
mavv 80,32,104
s_add_u32 s67, s67, s31
mavv 88,32,108
rt 82,8,34816
mavv 96,32,112
dm 15,24,67
mavv 104,32,116
rt 84,9,32768
mavv 112,32,120
s_add_u32 m0, s32, 66560
mavv 120,32,124
rt 86,9,34816
mavv 68,36,96
s_add_u32 s61, s61, s30
mavv 76,36,100
rt 88,10,32768
mavv 84,36,104
)ASM" ::: "memory");
  asm volatile(R"ASM(
rqv 32,0,4096
mavv 92,36,108
dm 13,20,61
mavv 100,36,112
rt 90,10,34816
mavv 108,36,116
s_add_u32 m0, s33, 18432
mavv 116,36,120
rt 92,11,32768
mavv 124,36,124
s_add_u32 s67, s67, s31
mavv 128,40,96
rt 94,11,34816
mavv 136,40,100
rqv 36,0,6144
mavv 144,40,104
dm 14,24,67
mavv 152,40,108
rqv 48,0,12288
mavv 160,40,112
s_add_u32 m0, s32, 67584
mavv 168,40,116
s_add_u32 s61, s61, s30
mavv 176,40,120
mavv 184,40,124
dm 12,20,61
mavv 132,44,96
rqv 40,0,8192
mavv 140,44,100
rqv 52,0,14336
mavv 148,44,104
s_add_u32 m0, s33, 19456
mavv 156,44,108
s_add_u32 s67, s67, s31
mavv 164,44,112
mavv 172,44,116
dm 15,24,67
mavv 180,44,120
mavv 188,44,124
mavv 192,56,96
rqv 44,0,10240
mavv 200,56,100
s_add_u32 m0, s32, 68608
mavv 208,56,104
s_add_u32 s61, s61, s30
mavv 216,56,108
mavv 224,56,112
dm 13,20,61
mavv 232,56,116
mavv 240,56,120
mavv 248,56,124
s_add_u32 s29, s29, s65
mavv 196,60,96
mavv 204,60,100
s_add_u32 s74, s78, .Lk1_BNT+12288-.Lk1_base
s_addc_u32 s75, s79, 0
mavv 212,60,104
s_add_u32 s69, s69, s51
mavv 220,60,108
global_load_dword v19, v236, s[74:75]
mavv 228,60,112
buffer_store_dwordx4 v[144:147], v16, s[84:87], s69 offen sc1
mavv 236,60,116
s_add_u32 s69, s69, s51
mavv 244,60,120
buffer_store_dwordx4 v[148:151], v16, s[84:87], s69 offen sc1
mavv 252,60,124
s_waitcnt vmcnt(21) lgkmcnt(0)
s_barrier
mavv 0,24,64
rt 96,4,49152
mavv 8,24,68
s_add_u32 m0, s33, 32768
mavv 16,24,72
rt 98,4,51200
mavv 24,24,76
s_mov_b32 s67, s29
mavv 32,24,80
rt 100,5,49152
mavv 40,24,84
dm 14,24,67
mavv 48,24,88
rt 102,5,51200
mavv 56,24,92
s_add_u32 m0, s32, 81920
mavv 4,28,64
rt 104,6,49152
mavv 12,28,68
rqv 24,1,0
mavv 20,28,72
s_add_u32 s61, s28, s64
mavv 28,28,76
rt 106,6,51200
mavv 36,28,80
dm 12,20,61
mavv 44,28,84
rt 108,7,49152
mavv 52,28,88
rt 110,7,51200
mavv 60,28,92
s_add_u32 m0, s33, 33792
mavv 64,32,64
rt 112,8,49152
mavv 72,32,68
rqv 28,1,2048
mavv 80,32,72
s_add_u32 s67, s67, s31
mavv 88,32,76
rt 114,8,51200
mavv 96,32,80
dm 15,24,67
mavv 104,32,84
rt 116,9,49152
mavv 112,32,88
s_add_u32 m0, s32, 82944
mavv 120,32,92
rt 118,9,51200
mavv 68,36,64
s_add_u32 s61, s61, s30
mavv 76,36,68
rt 120,10,49152
mavv 84,36,72
rqv 32,1,4096
mavv 92,36,76
rt 122,10,51200
mavv 100,36,80
dm 13,20,61
mavv 108,36,84
rt 124,11,49152
mavv 116,36,88
s_add_u32 m0, s33, 34816
mavv 124,36,92
rt 126,11,51200
mavv 128,40,64
s_add_u32 s67, s67, s31
mavv 136,40,68
rqv 36,1,6144
mavv 144,40,72
rqv 56,1,12288
mavv 152,40,76
dm 14,24,67
mavv 160,40,80
s_add_u32 m0, s32, 83968
mavv 168,40,84
mavv 176,40,88
s_add_u32 s61, s61, s30
mavv 184,40,92
mavv 132,44,64
rqv 40,1,8192
mavv 140,44,68
dm 12,20,61
mavv 148,44,72
rqv 60,1,14336
mavv 156,44,76
s_add_u32 m0, s33, 35840
mavv 164,44,80
mavv 172,44,84
s_add_u32 s67, s67, s31
mavv 180,44,88
mavv 188,44,92
dm 15,24,67
mavv 192,48,64
rqv 44,1,10240
mavv 200,48,68
mavv 208,48,72
s_add_u32 m0, s32, 84992
mavv 216,48,76
mavv 224,48,80
s_add_u32 s61, s61, s30
mavv 232,48,84
mavv 240,48,88
dm 13,20,61
mavv 248,48,92
mavv 196,52,64
mavv 204,52,68
s_add_u32 s29, s29, s65
mavv 212,52,72
s_add_u32 s68, s68, 0x400
mavv 220,52,76
s_mov_b32 s69, s68
mavv 228,52,80
s_add_u32 s28, s28, 128
mavv 236,52,84
buffer_store_dwordx4 v[152:155], v16, s[84:87], s69 offen sc1
mavv 244,52,88
s_add_u32 s69, s69, s51
mavv 252,52,92
buffer_store_dwordx4 v[156:159], v16, s[84:87], s69 offen sc1
s_waitcnt vmcnt(21) lgkmcnt(0)
s_barrier
mavv 0,24,96
rt 64,4,0
mavv 8,24,100
s_add_u32 m0, s33, 49152
mavv 16,24,104
s_mov_b32 s67, s29
mavv 24,24,108
rt 66,4,2048
mavv 32,24,112
dm 14,24,67
mavv 40,24,116
s_add_u32 m0, s32, 0
mavv 48,24,120
rt 68,5,0
s_mov_b32 s61, s28
mavv 56,24,124
rt 70,5,2048
mavv 4,28,96
dm 12,20,61
s_add_u32 m0, s33, 50176
mavv 12,28,100
rt 72,6,0
mavv 20,28,104
rqv 24,0,32768
s_add_u32 s67, s67, s31
mavv 28,28,108
rt 74,6,2048
mavv 36,28,112
dm 15,24,67
mavv 44,28,116
s_add_u32 m0, s32, 1024
mavv 52,28,120
rt 76,7,0
s_add_u32 s61, s61, s30
mavv 60,28,124
rt 78,7,2048
mavv 64,32,96
dm 13,20,61
s_add_u32 m0, s33, 51200
mavv 72,32,100
rt 80,8,0
mavv 80,32,104
rqv 28,0,34816
s_add_u32 s67, s67, s31
mavv 88,32,108
rt 82,8,2048
mavv 96,32,112
dm 14,24,67
mavv 104,32,116
s_add_u32 m0, s32, 2048
mavv 112,32,120
rt 84,9,0
s_add_u32 s61, s61, s30
mavv 120,32,124
rt 86,9,2048
mavv 68,36,96
dm 12,20,61
s_add_u32 m0, s33, 52224
mavv 76,36,100
rt 88,10,0
mavv 84,36,104
rqv 32,0,36864
s_add_u32 s67, s67, s31
mavv 92,36,108
rt 90,10,2048
mavv 100,36,112
dm 15,24,67
mavv 108,36,116
s_add_u32 m0, s32, 3072
mavv 116,36,120
rt 92,11,0
s_add_u32 s61, s61, s30
mavv 124,36,124
rt 94,11,2048
mavv 128,40,96
dm 13,20,61
s_add_u32 s29, s29, s65
mavv 136,40,100
rqv 36,0,38912
mavv 144,40,104
v_dot2_f32_bf16 v20, v228, v224, 0
mavv 152,40,108
rqv 48,0,45056
mavv 160,40,112
v_dot2_f32_bf16 v21, v232, v224, 0
mavv 168,40,116
v_dot2_f32_bf16 v20, v229, v225, v20
mavv 176,40,120
v_dot2_f32_bf16 v21, v233, v225, v21
mavv 184,40,124
v_dot2_f32_bf16 v20, v230, v226, v20
mavv 132,44,96
v_dot2_f32_bf16 v21, v234, v226, v21
mavv 140,44,100
rqv 40,0,40960
mavv 148,44,104
v_dot2_f32_bf16 v20, v231, v227, v20
mavv 156,44,108
rqv 52,0,47104
mavv 164,44,112
v_dot2_f32_bf16 v21, v235, v227, v21
mavv 172,44,116
s_nop 1
mavv 180,44,120
v_add_f32_dpp v20, v20, v20 row_shr:8 row_mask:0xf bank_mask:0xf bound_ctrl:0
mavv 188,44,124
v_add_f32_dpp v21, v21, v21 row_shr:8 row_mask:0xf bank_mask:0xf bound_ctrl:0
mavv 192,56,96
v_add_f32_dpp v20, v20, v20 row_shr:4 row_mask:0xf bank_mask:0xf bound_ctrl:0
mavv 200,56,100
rqv 44,0,43008
mavv 208,56,104
v_add_f32_dpp v21, v21, v21 row_shr:4 row_mask:0xf bank_mask:0xf bound_ctrl:0
mavv 216,56,108
v_add_f32_dpp v20, v20, v20 row_shr:2 row_mask:0xf bank_mask:0xf bound_ctrl:0
mavv 224,56,112
v_add_f32_dpp v21, v21, v21 row_shr:2 row_mask:0xf bank_mask:0xf bound_ctrl:0
mavv 232,56,116
v_add_f32_dpp v20, v20, v20 row_shr:1 row_mask:0xf bank_mask:0xf bound_ctrl:0
mavv 240,56,120
v_add_f32_dpp v21, v21, v21 row_shr:1 row_mask:0xf bank_mask:0xf bound_ctrl:0
mavv 248,56,124
v_mov_b32 v224, v20
mavv 196,60,96
v_mov_b32 v225, v21
mavv 204,60,100
s_add_u32 s69, s69, s51
mavv 212,60,104
v_permlane16_swap_b32 v20, v224
mavv 220,60,108
buffer_store_dwordx4 v[160:163], v16, s[84:87], s69 offen sc1
mavv 228,60,112
v_permlane16_swap_b32 v21, v225
s_add_u32 s68, s68, 0x400
mavv 236,60,116
v_add_f32 v20, v20, v224
mavv 244,60,120
s_mov_b32 s69, s68
mavv 252,60,124
buffer_store_dwordx4 v[164:167], v16, s[84:87], s69 offen sc1
s_waitcnt vmcnt(20) lgkmcnt(0)
s_barrier
mavv 0,24,64
rt 96,4,16384
mavv 8,24,68
s_add_u32 m0, s33, 0
mavv 16,24,72
s_mov_b32 s67, s29
mavv 24,24,76
rt 98,4,18432
mavv 32,24,80
dm 14,24,67
s_add_u32 m0, s32, 16384
mavv 40,24,84
rt 100,5,16384
mavv 48,24,88
s_add_u32 s61, s28, s64
mavv 56,24,92
rt 102,5,18432
mavv 4,28,64
dm 12,20,61
s_add_u32 m0, s33, 1024
rt 104,6,16384
mavv 12,28,68
rqv 24,1,32768
mavv 20,28,72
s_add_u32 s67, s67, s31
dm 15,24,67
mavv 28,28,76
rt 106,6,18432
mavv 36,28,80
s_add_u32 m0, s32, 17408
mavv 44,28,84
rt 108,7,16384
mavv 52,28,88
s_add_u32 s61, s61, s30
dm 13,20,61
mavv 60,28,92
rt 110,7,18432
mavv 64,32,64
s_add_u32 m0, s33, 2048
s_add_u32 s67, s67, s31
mavv 72,32,68
rt 112,8,16384
mavv 80,32,72
rqv 28,1,34816
dm 14,24,67
mavv 88,32,76
rt 114,8,18432
mavv 96,32,80
s_add_u32 m0, s32, 18432
s_add_u32 s61, s61, s30
mavv 104,32,84
rt 116,9,16384
mavv 112,32,88
dm 12,20,61
mavv 120,32,92
s_add_u32 m0, s33, 3072
mavv 68,36,64
rt 118,9,18432
s_add_u32 s67, s67, s31
rt 120,10,16384
mavv 76,36,68
rqv 32,1,36864
mavv 84,36,72
dm 15,24,67
s_add_u32 m0, s32, 19456
mavv 92,36,76
rt 122,10,18432
mavv 100,36,80
s_add_u32 s61, s61, s30
mavv 108,36,84
dm 13,20,61
mavv 116,36,88
rt 124,11,16384
s_add_u32 s29, s29, s65
mavv 124,36,92
rt 126,11,18432
mavv 128,40,64
s_add_u32 s28, s28, 128
s_add_u32 s74, s78, .Lk1_BNT+16384-.Lk1_base
s_addc_u32 s75, s79, 0
mavv 136,40,68
rqv 36,1,38912
mavv 144,40,72
global_load_dword v19, v236, s[74:75]
mavv 152,40,76
rqv 56,1,45056
mavv 160,40,80
v_add_f32 v21, v21, v225
mavv 168,40,84
v_mov_b32 v224, v20
mavv 176,40,88
v_mov_b32 v225, v21
mavv 184,40,92
v_permlane32_swap_b32 v20, v224
mavv 132,44,64
v_permlane32_swap_b32 v21, v225
mavv 140,44,68
rqv 40,1,40960
v_add_f32 v20, v20, v224
mavv 148,44,72
rqv 60,1,47104
mavv 156,44,76
v_add_f32 v21, v21, v225
mavv 164,44,80
cv 20,20,21
mavv 172,44,84
v_and_b32 v21, 0xffff0000, v20
mavv 180,44,88
v_lshlrev_b32 v20, 16, v20
mavv 188,44,92
v_mul_f32 v20, 0xbfb8aa3b, v20
mavv 192,48,64
v_mul_f32 v21, 0xbfb8aa3b, v21
mavv 200,48,68
rqv 44,1,43008
mavv 208,48,72
ex 20
mavv 216,48,76
ex 21
mavv 224,48,80
v_add_f32 v20, 1.0, v20
mavv 232,48,84
v_add_f32 v21, 1.0, v21
mavv 240,48,88
v_rcp_f32 v20, v20
mavv 248,48,92
v_rcp_f32 v21, v21
mavv 196,52,64
v_mov_b32 v22, 0
mavv 204,52,68
cv 20,20,21
mavv 212,52,72
v_and_b32 v21, 0xffff0000, v20
mavv 220,52,76
s_add_u32 s69, s69, s51
mavv 228,52,80
v_lshlrev_b32 v20, 16, v20
buffer_store_dwordx4 v[168:171], v16, s[84:87], s69 offen sc1
mavv 236,52,84
s_mov_b32 exec_lo, 0
s_mov_b32 exec_hi, 0x80000000
global_store_dwordx2 v22, v[20:21], s[6:7] sc1
s_mov_b64 exec, -1
mavv 244,52,88
s_add_u32 s69, s69, s51
mavv 252,52,92
buffer_store_dwordx4 v[172:175], v16, s[84:87], s69 offen sc1
s_waitcnt vmcnt(22) lgkmcnt(0)
s_barrier
mavv 0,24,96
rt 64,4,32768
mavv 8,24,100
s_add_u32 m0, s33, 16384
mavv 16,24,104
rt 66,4,34816
mavv 24,24,108
s_mov_b32 s67, s29
mavv 32,24,112
rt 68,5,32768
mavv 40,24,116
dm 14,24,67
mavv 48,24,120
rt 70,5,34816
mavv 56,24,124
s_add_u32 m0, s32, 32768
mavv 4,28,96
rt 72,6,32768
mavv 12,28,100
rqv 24,2,0
mavv 20,28,104
s_mov_b32 s61, s28
mavv 28,28,108
rt 74,6,34816
mavv 36,28,112
rt 76,7,32768
mavv 44,28,116
dm 12,20,61
mavv 52,28,120
rt 78,7,34816
mavv 60,28,124
s_add_u32 m0, s33, 17408
mavv 64,32,96
rt 80,8,32768
mavv 72,32,100
rqv 28,2,2048
mavv 80,32,104
s_add_u32 s67, s67, s31
mavv 88,32,108
rt 82,8,34816
mavv 96,32,112
dm 15,24,67
mavv 104,32,116
rt 84,9,32768
mavv 112,32,120
rt 86,9,34816
mavv 120,32,124
s_add_u32 m0, s32, 33792
mavv 68,36,96
rt 88,10,32768
mavv 76,36,100
rqv 32,2,4096
mavv 84,36,104
s_add_u32 s61, s61, s30
mavv 92,36,108
rt 90,10,34816
mavv 100,36,112
dm 13,20,61
mavv 108,36,116
rt 92,11,32768
mavv 116,36,120
s_add_u32 m0, s33, 18432
mavv 124,36,124
rt 94,11,34816
mavv 128,40,96
rqv 36,2,6144
mavv 136,40,100
s_add_u32 s67, s67, s31
mavv 144,40,104
rqv 48,2,12288
mavv 152,40,108
dm 14,24,67
mavv 160,40,112
mavv 168,40,116
s_add_u32 m0, s32, 34816
mavv 176,40,120
mavv 184,40,124
s_add_u32 s61, s61, s30
mavv 132,44,96
rqv 40,2,8192
mavv 140,44,100
rqv 52,2,14336
mavv 148,44,104
dm 12,20,61
mavv 156,44,108
mavv 164,44,112
s_add_u32 m0, s33, 19456
mavv 172,44,116
mavv 180,44,120
mavv 188,44,124
s_add_u32 s67, s67, s31
mavv 192,56,96
rqv 44,2,10240
mavv 200,56,100
dm 15,24,67
mavv 208,56,104
mavv 216,56,108
mavv 224,56,112
s_add_u32 m0, s32, 35840
mavv 232,56,116
mavv 240,56,120
s_add_u32 s61, s61, s30
mavv 248,56,124
mavv 196,60,96
mavv 204,60,100
dm 13,20,61
mavv 212,60,104
s_add_u32 s68, s68, 0x400
mavv 220,60,108
s_mov_b32 s69, s68
mavv 228,60,112
s_add_u32 s29, s29, s65
mavv 236,60,116
buffer_store_dwordx4 v[176:179], v16, s[84:87], s69 offen sc1
mavv 244,60,120
s_add_u32 s69, s69, s51
mavv 252,60,124
buffer_store_dwordx4 v[180:183], v16, s[84:87], s69 offen sc1
s_waitcnt vmcnt(22) lgkmcnt(0)
)ASM" ::: "memory");
  asm volatile(R"ASM(
s_barrier
mavv 0,24,64
rt 96,4,49152
mavv 8,24,68
s_add_u32 m0, s33, 32768
mavv 16,24,72
rt 98,4,51200
mavv 24,24,76
s_mov_b32 s67, s29
mavv 32,24,80
rt 100,5,49152
mavv 40,24,84
dm 14,24,67
mavv 48,24,88
rt 102,5,51200
mavv 56,24,92
s_add_u32 m0, s32, 49152
mavv 4,28,64
rt 104,6,49152
mavv 12,28,68
rqv 24,3,0
mavv 20,28,72
s_add_u32 s61, s28, s64
mavv 28,28,76
rt 106,6,51200
mavv 36,28,80
dm 12,20,61
mavv 44,28,84
rt 108,7,49152
mavv 52,28,88
rt 110,7,51200
mavv 60,28,92
s_add_u32 m0, s33, 33792
mavv 64,32,64
rt 112,8,49152
mavv 72,32,68
rqv 28,3,2048
mavv 80,32,72
s_add_u32 s67, s67, s31
mavv 88,32,76
rt 114,8,51200
mavv 96,32,80
dm 15,24,67
mavv 104,32,84
rt 116,9,49152
mavv 112,32,88
s_add_u32 m0, s32, 50176
mavv 120,32,92
rt 118,9,51200
mavv 68,36,64
s_add_u32 s61, s61, s30
mavv 76,36,68
rt 120,10,49152
mavv 84,36,72
rqv 32,3,4096
mavv 92,36,76
rt 122,10,51200
mavv 100,36,80
dm 13,20,61
mavv 108,36,84
rt 124,11,49152
mavv 116,36,88
s_add_u32 m0, s33, 34816
mavv 124,36,92
rt 126,11,51200
mavv 128,40,64
s_add_u32 s67, s67, s31
mavv 136,40,68
rqv 36,3,6144
mavv 144,40,72
rqv 56,3,12288
mavv 152,40,76
dm 14,24,67
mavv 160,40,80
s_add_u32 m0, s32, 51200
mavv 168,40,84
mavv 176,40,88
s_add_u32 s61, s61, s30
mavv 184,40,92
mavv 132,44,64
rqv 40,3,8192
mavv 140,44,68
dm 12,20,61
mavv 148,44,72
rqv 60,3,14336
mavv 156,44,76
s_add_u32 m0, s33, 35840
mavv 164,44,80
mavv 172,44,84
s_add_u32 s67, s67, s31
mavv 180,44,88
mavv 188,44,92
dm 15,24,67
mavv 192,48,64
rqv 44,3,10240
mavv 200,48,68
mavv 208,48,72
s_add_u32 m0, s32, 52224
mavv 216,48,76
mavv 224,48,80
s_add_u32 s61, s61, s30
mavv 232,48,84
mavv 240,48,88
dm 13,20,61
mavv 248,48,92
mavv 196,52,64
mavv 204,52,68
s_add_u32 s29, s29, s65
mavv 212,52,72
s_add_u32 s69, s69, s51
mavv 220,52,76
buffer_store_dwordx4 v[184:187], v16, s[84:87], s69 offen sc1
mavv 228,52,80
s_add_u32 s28, s28, 128
mavv 236,52,84
s_add_u32 s68, s68, 0x400
mavv 244,52,88
s_mov_b32 s69, s68
mavv 252,52,92
buffer_store_dwordx4 v[188:191], v16, s[84:87], s69 offen sc1
s_waitcnt vmcnt(20) lgkmcnt(0)
s_barrier
mavv 0,24,96
rt 64,4,0
mavv 8,24,100
s_add_u32 m0, s33, 49152
mavv 16,24,104
rt 66,4,2048
mavv 24,24,108
s_mov_b32 s67, s29
mavv 32,24,112
rt 68,5,0
mavv 40,24,116
dm 14,24,67
mavv 48,24,120
rt 70,5,2048
mavv 56,24,124
s_add_u32 m0, s32, 65536
mavv 4,28,96
rt 72,6,0
mavv 12,28,100
rqv 24,0,0
mavv 20,28,104
s_mov_b32 s61, s28
mavv 28,28,108
rt 74,6,2048
mavv 36,28,112
dm 12,20,61
mavv 44,28,116
rt 76,7,0
mavv 52,28,120
s_add_u32 m0, s33, 50176
mavv 60,28,124
rt 78,7,2048
mavv 64,32,96
rt 80,8,0
mavv 72,32,100
rqv 28,0,2048
mavv 80,32,104
s_add_u32 s67, s67, s31
mavv 88,32,108
rt 82,8,2048
mavv 96,32,112
dm 15,24,67
mavv 104,32,116
rt 84,9,0
mavv 112,32,120
s_add_u32 m0, s32, 66560
mavv 120,32,124
rt 86,9,2048
mavv 68,36,96
s_add_u32 s61, s61, s30
mavv 76,36,100
rt 88,10,0
mavv 84,36,104
rqv 32,0,4096
mavv 92,36,108
dm 13,20,61
mavv 100,36,112
rt 90,10,2048
mavv 108,36,116
s_add_u32 m0, s33, 51200
mavv 116,36,120
rt 92,11,0
mavv 124,36,124
s_add_u32 s67, s67, s31
mavv 128,40,96
rt 94,11,2048
mavv 136,40,100
rqv 36,0,6144
mavv 144,40,104
dm 14,24,67
mavv 152,40,108
rqv 48,0,12288
mavv 160,40,112
s_add_u32 m0, s32, 67584
mavv 168,40,116
s_add_u32 s61, s61, s30
mavv 176,40,120
mavv 184,40,124
dm 12,20,61
mavv 132,44,96
rqv 40,0,8192
mavv 140,44,100
rqv 52,0,14336
mavv 148,44,104
s_add_u32 m0, s33, 52224
mavv 156,44,108
s_add_u32 s67, s67, s31
mavv 164,44,112
mavv 172,44,116
dm 15,24,67
mavv 180,44,120
mavv 188,44,124
mavv 192,56,96
rqv 44,0,10240
mavv 200,56,100
s_add_u32 m0, s32, 68608
mavv 208,56,104
s_add_u32 s61, s61, s30
mavv 216,56,108
mavv 224,56,112
dm 13,20,61
mavv 232,56,116
mavv 240,56,120
mavv 248,56,124
s_add_u32 s29, s29, s65
mavv 196,60,96
mavv 204,60,100
s_add_u32 s74, s78, .Lk1_BN+0-.Lk1_base
s_addc_u32 s75, s79, 0
mavv 212,60,104
s_add_u32 s69, s69, s51
mavv 220,60,108
global_load_dword v19, v236, s[74:75]
mavv 228,60,112
buffer_store_dwordx4 v[192:195], v16, s[84:87], s69 offen sc1
mavv 236,60,116
s_add_u32 s69, s69, s51
mavv 244,60,120
buffer_store_dwordx4 v[196:199], v16, s[84:87], s69 offen sc1
mavv 252,60,124
s_waitcnt vmcnt(21) lgkmcnt(0)
s_barrier
mavv 0,24,64
rt 96,4,16384
mavv 8,24,68
s_add_u32 m0, s33, 0
mavv 16,24,72
rt 98,4,18432
mavv 24,24,76
s_mov_b32 s67, s29
mavv 32,24,80
rt 100,5,16384
mavv 40,24,84
dm 14,24,67
mavv 48,24,88
rt 102,5,18432
mavv 56,24,92
s_add_u32 m0, s32, 81920
mavv 4,28,64
rt 104,6,16384
mavv 12,28,68
rqv 24,1,0
mavv 20,28,72
s_add_u32 s61, s28, s64
mavv 28,28,76
rt 106,6,18432
mavv 36,28,80
dm 12,20,61
mavv 44,28,84
rt 108,7,16384
mavv 52,28,88
rt 110,7,18432
mavv 60,28,92
s_add_u32 m0, s33, 1024
mavv 64,32,64
rt 112,8,16384
mavv 72,32,68
rqv 28,1,2048
mavv 80,32,72
s_add_u32 s67, s67, s31
mavv 88,32,76
rt 114,8,18432
mavv 96,32,80
dm 15,24,67
mavv 104,32,84
rt 116,9,16384
mavv 112,32,88
s_add_u32 m0, s32, 82944
mavv 120,32,92
rt 118,9,18432
mavv 68,36,64
s_add_u32 s61, s61, s30
mavv 76,36,68
rt 120,10,16384
mavv 84,36,72
rqv 32,1,4096
mavv 92,36,76
rt 122,10,18432
mavv 100,36,80
dm 13,20,61
mavv 108,36,84
rt 124,11,16384
mavv 116,36,88
s_add_u32 m0, s33, 2048
mavv 124,36,92
rt 126,11,18432
mavv 128,40,64
s_add_u32 s67, s67, s31
mavv 136,40,68
rqv 36,1,6144
mavv 144,40,72
rqv 56,1,12288
mavv 152,40,76
dm 14,24,67
mavv 160,40,80
s_add_u32 m0, s32, 83968
mavv 168,40,84
mavv 176,40,88
s_add_u32 s61, s61, s30
mavv 184,40,92
mavv 132,44,64
rqv 40,1,8192
mavv 140,44,68
dm 12,20,61
mavv 148,44,72
rqv 60,1,14336
mavv 156,44,76
s_add_u32 m0, s33, 3072
mavv 164,44,80
mavv 172,44,84
s_add_u32 s67, s67, s31
mavv 180,44,88
mavv 188,44,92
dm 15,24,67
mavv 192,48,64
rqv 44,1,10240
mavv 200,48,68
mavv 208,48,72
s_add_u32 m0, s32, 84992
mavv 216,48,76
mavv 224,48,80
s_add_u32 s61, s61, s30
mavv 232,48,84
mavv 240,48,88
dm 13,20,61
mavv 248,48,92
mavv 196,52,64
mavv 204,52,68
s_add_u32 s29, s29, s65
mavv 212,52,72
s_add_u32 s68, s68, 0x400
mavv 220,52,76
s_mov_b32 s69, s68
mavv 228,52,80
s_add_u32 s28, s28, 128
mavv 236,52,84
buffer_store_dwordx4 v[200:203], v16, s[84:87], s69 offen sc1
mavv 244,52,88
s_add_u32 s69, s69, s51
mavv 252,52,92
buffer_store_dwordx4 v[204:207], v16, s[84:87], s69 offen sc1
s_waitcnt vmcnt(21) lgkmcnt(0)
s_barrier
mavv 0,24,96
rt 64,4,32768
mavv 8,24,100
s_add_u32 m0, s33, 16384
mavv 16,24,104
rt 66,4,34816
mavv 24,24,108
s_mov_b32 s67, s29
mavv 32,24,112
rt 68,5,32768
mavv 40,24,116
dm 14,24,67
mavv 48,24,120
rt 70,5,34816
mavv 56,24,124
s_add_u32 m0, s32, 0
mavv 4,28,96
rt 72,6,32768
mavv 12,28,100
rqv 24,0,32768
mavv 20,28,104
s_mov_b32 s61, s28
mavv 28,28,108
rt 74,6,34816
mavv 36,28,112
rt 76,7,32768
mavv 44,28,116
dm 12,20,61
mavv 52,28,120
rt 78,7,34816
mavv 60,28,124
s_add_u32 m0, s33, 17408
mavv 64,32,96
rt 80,8,32768
mavv 72,32,100
rqv 28,0,34816
mavv 80,32,104
s_add_u32 s67, s67, s31
mavv 88,32,108
rt 82,8,34816
mavv 96,32,112
dm 15,24,67
mavv 104,32,116
rt 84,9,32768
mavv 112,32,120
rt 86,9,34816
mavv 120,32,124
s_add_u32 m0, s32, 1024
mavv 68,36,96
rt 88,10,32768
mavv 76,36,100
rqv 32,0,36864
mavv 84,36,104
s_add_u32 s61, s61, s30
mavv 92,36,108
rt 90,10,34816
mavv 100,36,112
dm 13,20,61
mavv 108,36,116
rt 92,11,32768
mavv 116,36,120
s_add_u32 m0, s33, 18432
mavv 124,36,124
rt 94,11,34816
mavv 128,40,96
rqv 36,0,38912
mavv 136,40,100
s_add_u32 s67, s67, s31
mavv 144,40,104
rqv 48,0,45056
mavv 152,40,108
dm 14,24,67
mavv 160,40,112
mavv 168,40,116
s_add_u32 m0, s32, 2048
mavv 176,40,120
mavv 184,40,124
s_add_u32 s61, s61, s30
mavv 132,44,96
rqv 40,0,40960
mavv 140,44,100
rqv 52,0,47104
mavv 148,44,104
dm 12,20,61
mavv 156,44,108
mavv 164,44,112
s_add_u32 m0, s33, 19456
mavv 172,44,116
mavv 180,44,120
mavv 188,44,124
s_add_u32 s67, s67, s31
mavv 192,56,96
rqv 44,0,43008
mavv 200,56,100
dm 15,24,67
mavv 208,56,104
mavv 216,56,108
mavv 224,56,112
s_add_u32 m0, s32, 3072
mavv 232,56,116
mavv 240,56,120
s_add_u32 s61, s61, s30
mavv 248,56,124
mavv 196,60,96
mavv 204,60,100
dm 13,20,61
mavv 212,60,104
s_add_u32 s69, s69, s51
mavv 220,60,108
buffer_store_dwordx4 v[208:211], v16, s[84:87], s69 offen sc1
mavv 228,60,112
s_add_u32 s29, s29, s65
mavv 236,60,116
s_add_u32 s68, s68, 0x400
mavv 244,60,120
s_mov_b32 s69, s68
mavv 252,60,124
buffer_store_dwordx4 v[212:215], v16, s[84:87], s69 offen sc1
s_waitcnt vmcnt(20) lgkmcnt(0)
s_barrier
mavv 0,24,64
rt 96,4,49152
mavv 8,24,68
s_add_u32 m0, s33, 32768
mavv 16,24,72
rt 98,4,51200
mavv 24,24,76
s_mov_b32 s67, s29
mavv 32,24,80
rt 100,5,49152
mavv 40,24,84
dm 14,24,67
mavv 48,24,88
rt 102,5,51200
mavv 56,24,92
s_add_u32 m0, s32, 16384
mavv 4,28,64
rt 104,6,49152
mavv 12,28,68
rqv 24,1,32768
mavv 20,28,72
s_add_u32 s61, s28, s64
mavv 28,28,76
rt 106,6,51200
mavv 36,28,80
dm 12,20,61
mavv 44,28,84
rt 108,7,49152
mavv 52,28,88
s_add_u32 m0, s33, 33792
mavv 60,28,92
rt 110,7,51200
mavv 64,32,64
s_add_u32 s67, s67, s31
mavv 72,32,68
rt 112,8,49152
mavv 80,32,72
rqv 28,1,34816
mavv 88,32,76
dm 15,24,67
mavv 96,32,80
rt 114,8,51200
mavv 104,32,84
s_add_u32 m0, s32, 17408
mavv 112,32,88
rt 116,9,49152
mavv 120,32,92
rt 118,9,51200
mavv 68,36,64
s_add_u32 s61, s61, s30
rt 120,10,49152
mavv 76,36,68
rqv 32,1,36864
mavv 84,36,72
dm 13,20,61
mavv 92,36,76
rt 122,10,51200
mavv 100,36,80
s_add_u32 m0, s33, 34816
mavv 108,36,84
rt 124,11,49152
mavv 116,36,88
s_add_u32 s67, s67, s31
mavv 124,36,92
rt 126,11,51200
mavv 128,40,64
dm 14,24,67
mavv 136,40,68
rqv 36,1,38912
mavv 144,40,72
rqv 56,1,45056
mavv 152,40,76
s_add_u32 m0, s32, 18432
mavv 160,40,80
s_add_u32 s61, s61, s30
mavv 168,40,84
dm 12,20,61
mavv 176,40,88
mavv 184,40,92
s_add_u32 m0, s33, 35840
mavv 132,44,64
rqv 40,1,40960
mavv 140,44,68
rqv 60,1,47104
mavv 148,44,72
s_add_u32 s67, s67, s31
mavv 156,44,76
mavv 164,44,80
dm 15,24,67
mavv 172,44,84
mavv 180,44,88
s_add_u32 m0, s32, 19456
mavv 188,44,92
mavv 192,48,64
rqv 44,1,43008
mavv 200,48,68
s_add_u32 s61, s61, s30
mavv 208,48,72
dm 13,20,61
mavv 216,48,76
mavv 224,48,80
mavv 232,48,84
s_add_u32 s29, s29, s65
mavv 240,48,88
mavv 248,48,92
s_add_u32 s28, s28, 128
mavv 196,52,64
mavv 204,52,68
s_add_u32 s74, s78, .Lk1_BN+4096-.Lk1_base
s_addc_u32 s75, s79, 0
mavv 212,52,72
s_add_u32 s69, s69, s51
mavv 220,52,76
global_load_dword v19, v236, s[74:75]
mavv 228,52,80
)ASM" ::: "memory");
  asm volatile(R"ASM(
buffer_store_dwordx4 v[216:219], v16, s[84:87], s69 offen sc1
mavv 236,52,84
s_add_u32 s69, s69, s51
mavv 244,52,88
buffer_store_dwordx4 v[220:223], v16, s[84:87], s69 offen sc1
mavv 252,52,92
s_branch .Lk1_BN
.Lk1_BNN:
s_waitcnt vmcnt(19) lgkmcnt(0)
s_barrier
zavv 0,24,64
rt 96,4,49152
ar 224,64
ar 225,65
ar 226,66
ar 227,67
ar 228,68
ar 229,69
ar 230,70
zavv 8,24,68
s_add_u32 m0, s33, 32768
ar 231,71
v_pk_mul_f32 v[224:225], v[224:225], s[48:49] op_sel_hi:[1,0]
v_pk_mul_f32 v[226:227], v[226:227], s[48:49] op_sel_hi:[1,0]
v_pk_mul_f32 v[228:229], v[228:229], s[48:49] op_sel_hi:[1,0]
v_pk_mul_f32 v[230:231], v[230:231], s[48:49] op_sel_hi:[1,0]
cv 128,224,225
cv 129,226,227
zavv 16,24,72
rt 98,4,51200
cv 130,228,229
cv 131,230,231
ar 224,72
ar 225,73
ar 226,74
ar 227,75
ar 228,76
zavv 24,24,76
s_mov_b32 s67, s29
ar 229,77
ar 230,78
ar 231,79
v_pk_mul_f32 v[224:225], v[224:225], s[48:49] op_sel_hi:[1,0]
v_pk_mul_f32 v[226:227], v[226:227], s[48:49] op_sel_hi:[1,0]
v_pk_mul_f32 v[228:229], v[228:229], s[48:49] op_sel_hi:[1,0]
v_pk_mul_f32 v[230:231], v[230:231], s[48:49] op_sel_hi:[1,0]
zavv 32,24,80
rt 100,5,49152
cv 132,224,225
cv 133,226,227
cv 134,228,229
cv 135,230,231
v_cndmask_b32_e64 v132, v132, v17, s[76:77]
v_cndmask_b32_e64 v133, v133, v17, s[76:77]
v_cndmask_b32_e64 v134, v134, v17, s[76:77]
zavv 40,24,84
dm 14,24,67
v_cndmask_b32_e64 v135, v135, v17, s[76:77]
ar 224,80
ar 225,81
ar 226,82
ar 227,83
ar 228,84
ar 229,85
zavv 48,24,88
rt 102,5,51200
ar 230,86
ar 231,87
v_pk_mul_f32 v[224:225], v[224:225], s[48:49] op_sel_hi:[1,0]
v_pk_mul_f32 v[226:227], v[226:227], s[48:49] op_sel_hi:[1,0]
v_pk_mul_f32 v[228:229], v[228:229], s[48:49] op_sel_hi:[1,0]
v_pk_mul_f32 v[230:231], v[230:231], s[48:49] op_sel_hi:[1,0]
cv 136,224,225
zavv 56,24,92
s_add_u32 m0, s32, 16384
cv 137,226,227
cv 138,228,229
cv 139,230,231
ar 224,88
ar 225,89
ar 226,90
ar 227,91
zavv 4,28,64
rt 104,6,49152
ar 228,92
ar 229,93
ar 230,94
ar 231,95
v_pk_mul_f32 v[224:225], v[224:225], s[48:49] op_sel_hi:[1,0]
v_pk_mul_f32 v[226:227], v[226:227], s[48:49] op_sel_hi:[1,0]
v_pk_mul_f32 v[228:229], v[228:229], s[48:49] op_sel_hi:[1,0]
zavv 12,28,68
rqv 24,1,32768
v_pk_mul_f32 v[230:231], v[230:231], s[48:49] op_sel_hi:[1,0]
cv 140,224,225
cv 141,226,227
cv 142,228,229
cv 143,230,231
v_cndmask_b32_e64 v140, v140, v17, s[76:77]
v_cndmask_b32_e64 v141, v141, v17, s[76:77]
zavv 20,28,72
s_add_u32 s61, s28, s64
v_cndmask_b32_e64 v142, v142, v17, s[76:77]
v_cndmask_b32_e64 v143, v143, v17, s[76:77]
ar 224,96
ar 225,97
ar 226,98
ar 227,99
ar 228,100
zavv 28,28,76
rt 106,6,51200
ar 229,101
ar 230,102
ar 231,103
v_pk_mul_f32 v[224:225], v[224:225], s[48:49] op_sel_hi:[1,0]
v_pk_mul_f32 v[226:227], v[226:227], s[48:49] op_sel_hi:[1,0]
v_pk_mul_f32 v[228:229], v[228:229], s[48:49] op_sel_hi:[1,0]
v_pk_mul_f32 v[230:231], v[230:231], s[48:49] op_sel_hi:[1,0]
zavv 36,28,80
dm 12,20,61
cv 144,224,225
cv 145,226,227
cv 146,228,229
cv 147,230,231
ar 224,104
ar 225,105
ar 226,106
zavv 44,28,84
rt 108,7,49152
ar 227,107
ar 228,108
ar 229,109
ar 230,110
ar 231,111
v_pk_mul_f32 v[224:225], v[224:225], s[48:49] op_sel_hi:[1,0]
v_pk_mul_f32 v[226:227], v[226:227], s[48:49] op_sel_hi:[1,0]
zavv 52,28,88
s_add_u32 m0, s33, 33792
v_pk_mul_f32 v[228:229], v[228:229], s[48:49] op_sel_hi:[1,0]
v_pk_mul_f32 v[230:231], v[230:231], s[48:49] op_sel_hi:[1,0]
cv 148,224,225
cv 149,226,227
cv 150,228,229
cv 151,230,231
v_cndmask_b32_e64 v148, v148, v17, s[76:77]
zavv 60,28,92
rt 110,7,51200
v_cndmask_b32_e64 v149, v149, v17, s[76:77]
v_cndmask_b32_e64 v150, v150, v17, s[76:77]
v_cndmask_b32_e64 v151, v151, v17, s[76:77]
ar 224,112
ar 225,113
ar 226,114
ar 227,115
zavv 64,32,64
s_add_u32 s67, s67, s31
ar 228,116
ar 229,117
ar 230,118
ar 231,119
v_pk_mul_f32 v[224:225], v[224:225], s[48:49] op_sel_hi:[1,0]
v_pk_mul_f32 v[226:227], v[226:227], s[48:49] op_sel_hi:[1,0]
v_pk_mul_f32 v[228:229], v[228:229], s[48:49] op_sel_hi:[1,0]
zavv 72,32,68
rt 112,8,49152
v_pk_mul_f32 v[230:231], v[230:231], s[48:49] op_sel_hi:[1,0]
cv 152,224,225
cv 153,226,227
cv 154,228,229
cv 155,230,231
ar 224,120
ar 225,121
zavv 80,32,72
rqv 28,1,34816
ar 226,122
ar 227,123
ar 228,124
ar 229,125
ar 230,126
ar 231,127
v_pk_mul_f32 v[224:225], v[224:225], s[48:49] op_sel_hi:[1,0]
zavv 88,32,76
dm 15,24,67
v_pk_mul_f32 v[226:227], v[226:227], s[48:49] op_sel_hi:[1,0]
v_pk_mul_f32 v[228:229], v[228:229], s[48:49] op_sel_hi:[1,0]
v_pk_mul_f32 v[230:231], v[230:231], s[48:49] op_sel_hi:[1,0]
cv 156,224,225
cv 157,226,227
cv 158,228,229
cv 159,230,231
zavv 96,32,80
rt 114,8,51200
v_cndmask_b32_e64 v156, v156, v17, s[76:77]
v_cndmask_b32_e64 v157, v157, v17, s[76:77]
v_cndmask_b32_e64 v158, v158, v17, s[76:77]
v_cndmask_b32_e64 v159, v159, v17, s[76:77]
ar 224,128
ar 225,129
ar 226,130
zavv 104,32,84
s_add_u32 m0, s32, 17408
ar 227,131
ar 228,132
ar 229,133
ar 230,134
ar 231,135
v_pk_mul_f32 v[224:225], v[224:225], s[48:49] op_sel_hi:[1,0]
v_pk_mul_f32 v[226:227], v[226:227], s[48:49] op_sel_hi:[1,0]
zavv 112,32,88
rt 116,9,49152
v_pk_mul_f32 v[228:229], v[228:229], s[48:49] op_sel_hi:[1,0]
v_pk_mul_f32 v[230:231], v[230:231], s[48:49] op_sel_hi:[1,0]
cv 160,224,225
cv 161,226,227
cv 162,228,229
cv 163,230,231
ar 224,136
zavv 120,32,92
rt 118,9,51200
ar 225,137
ar 226,138
ar 227,139
ar 228,140
ar 229,141
ar 230,142
ar 231,143
zavv 68,36,64
s_add_u32 s61, s61, s30
rt 120,10,49152
v_pk_mul_f32 v[224:225], v[224:225], s[48:49] op_sel_hi:[1,0]
v_pk_mul_f32 v[226:227], v[226:227], s[48:49] op_sel_hi:[1,0]
v_pk_mul_f32 v[228:229], v[228:229], s[48:49] op_sel_hi:[1,0]
v_pk_mul_f32 v[230:231], v[230:231], s[48:49] op_sel_hi:[1,0]
cv 164,224,225
cv 165,226,227
cv 166,228,229
zavv 76,36,68
rqv 32,1,36864
cv 167,230,231
v_cndmask_b32_e64 v164, v164, v17, s[76:77]
v_cndmask_b32_e64 v165, v165, v17, s[76:77]
v_cndmask_b32_e64 v166, v166, v17, s[76:77]
v_cndmask_b32_e64 v167, v167, v17, s[76:77]
ar 224,144
ar 225,145
zavv 84,36,72
dm 13,20,61
ar 226,146
ar 227,147
ar 228,148
ar 229,149
ar 230,150
ar 231,151
v_pk_mul_f32 v[224:225], v[224:225], s[48:49] op_sel_hi:[1,0]
zavv 92,36,76
rt 122,10,51200
v_pk_mul_f32 v[226:227], v[226:227], s[48:49] op_sel_hi:[1,0]
v_pk_mul_f32 v[228:229], v[228:229], s[48:49] op_sel_hi:[1,0]
v_pk_mul_f32 v[230:231], v[230:231], s[48:49] op_sel_hi:[1,0]
cv 168,224,225
cv 169,226,227
cv 170,228,229
cv 171,230,231
zavv 100,36,80
s_add_u32 m0, s33, 34816
ar 224,152
ar 225,153
ar 226,154
ar 227,155
ar 228,156
ar 229,157
ar 230,158
zavv 108,36,84
rt 124,11,49152
ar 231,159
v_pk_mul_f32 v[224:225], v[224:225], s[48:49] op_sel_hi:[1,0]
v_pk_mul_f32 v[226:227], v[226:227], s[48:49] op_sel_hi:[1,0]
v_pk_mul_f32 v[228:229], v[228:229], s[48:49] op_sel_hi:[1,0]
v_pk_mul_f32 v[230:231], v[230:231], s[48:49] op_sel_hi:[1,0]
cv 172,224,225
cv 173,226,227
zavv 116,36,88
s_add_u32 s67, s67, s31
cv 174,228,229
cv 175,230,231
v_cndmask_b32_e64 v172, v172, v17, s[76:77]
v_cndmask_b32_e64 v173, v173, v17, s[76:77]
v_cndmask_b32_e64 v174, v174, v17, s[76:77]
v_cndmask_b32_e64 v175, v175, v17, s[76:77]
ar 224,160
zavv 124,36,92
rt 126,11,51200
ar 225,161
ar 226,162
ar 227,163
ar 228,164
ar 229,165
ar 230,166
ar 231,167
zavv 128,40,64
dm 14,24,67
v_pk_mul_f32 v[224:225], v[224:225], s[48:49] op_sel_hi:[1,0]
v_pk_mul_f32 v[226:227], v[226:227], s[48:49] op_sel_hi:[1,0]
v_pk_mul_f32 v[228:229], v[228:229], s[48:49] op_sel_hi:[1,0]
v_pk_mul_f32 v[230:231], v[230:231], s[48:49] op_sel_hi:[1,0]
cv 176,224,225
cv 177,226,227
cv 178,228,229
zavv 136,40,68
rqv 36,1,38912
cv 179,230,231
ar 224,168
ar 225,169
ar 226,170
ar 227,171
ar 228,172
ar 229,173
zavv 144,40,72
rqv 56,1,45056
ar 230,174
ar 231,175
v_pk_mul_f32 v[224:225], v[224:225], s[48:49] op_sel_hi:[1,0]
v_pk_mul_f32 v[226:227], v[226:227], s[48:49] op_sel_hi:[1,0]
v_pk_mul_f32 v[228:229], v[228:229], s[48:49] op_sel_hi:[1,0]
v_pk_mul_f32 v[230:231], v[230:231], s[48:49] op_sel_hi:[1,0]
cv 180,224,225
zavv 152,40,76
s_add_u32 m0, s32, 18432
cv 181,226,227
cv 182,228,229
cv 183,230,231
v_cndmask_b32_e64 v180, v180, v17, s[76:77]
v_cndmask_b32_e64 v181, v181, v17, s[76:77]
v_cndmask_b32_e64 v182, v182, v17, s[76:77]
v_cndmask_b32_e64 v183, v183, v17, s[76:77]
zavv 160,40,80
s_add_u32 s61, s61, s30
ar 224,176
ar 225,177
ar 226,178
ar 227,179
ar 228,180
ar 229,181
ar 230,182
zavv 168,40,84
dm 12,20,61
ar 231,183
v_pk_mul_f32 v[224:225], v[224:225], s[48:49] op_sel_hi:[1,0]
v_pk_mul_f32 v[226:227], v[226:227], s[48:49] op_sel_hi:[1,0]
v_pk_mul_f32 v[228:229], v[228:229], s[48:49] op_sel_hi:[1,0]
v_pk_mul_f32 v[230:231], v[230:231], s[48:49] op_sel_hi:[1,0]
cv 184,224,225
cv 185,226,227
zavv 176,40,88
cv 186,228,229
cv 187,230,231
ar 224,184
ar 225,185
ar 226,186
ar 227,187
ar 228,188
zavv 184,40,92
s_add_u32 m0, s33, 35840
ar 229,189
ar 230,190
ar 231,191
v_pk_mul_f32 v[224:225], v[224:225], s[48:49] op_sel_hi:[1,0]
v_pk_mul_f32 v[226:227], v[226:227], s[48:49] op_sel_hi:[1,0]
v_pk_mul_f32 v[228:229], v[228:229], s[48:49] op_sel_hi:[1,0]
v_pk_mul_f32 v[230:231], v[230:231], s[48:49] op_sel_hi:[1,0]
zavv 132,44,64
rqv 40,1,40960
cv 188,224,225
cv 189,226,227
cv 190,228,229
cv 191,230,231
v_cndmask_b32_e64 v188, v188, v17, s[76:77]
v_cndmask_b32_e64 v189, v189, v17, s[76:77]
v_cndmask_b32_e64 v190, v190, v17, s[76:77]
zavv 140,44,68
rqv 60,1,47104
v_cndmask_b32_e64 v191, v191, v17, s[76:77]
ar 224,192
ar 225,193
ar 226,194
ar 227,195
ar 228,196
ar 229,197
zavv 148,44,72
s_add_u32 s67, s67, s31
ar 230,198
ar 231,199
v_pk_mul_f32 v[224:225], v[224:225], s[48:49] op_sel_hi:[1,0]
v_pk_mul_f32 v[226:227], v[226:227], s[48:49] op_sel_hi:[1,0]
v_pk_mul_f32 v[228:229], v[228:229], s[48:49] op_sel_hi:[1,0]
v_pk_mul_f32 v[230:231], v[230:231], s[48:49] op_sel_hi:[1,0]
cv 192,224,225
zavv 156,44,76
cv 193,226,227
cv 194,228,229
cv 195,230,231
ar 224,200
ar 225,201
ar 226,202
ar 227,203
zavv 164,44,80
dm 15,24,67
ar 228,204
ar 229,205
ar 230,206
ar 231,207
v_pk_mul_f32 v[224:225], v[224:225], s[48:49] op_sel_hi:[1,0]
v_pk_mul_f32 v[226:227], v[226:227], s[48:49] op_sel_hi:[1,0]
v_pk_mul_f32 v[228:229], v[228:229], s[48:49] op_sel_hi:[1,0]
zavv 172,44,84
v_pk_mul_f32 v[230:231], v[230:231], s[48:49] op_sel_hi:[1,0]
cv 196,224,225
cv 197,226,227
cv 198,228,229
cv 199,230,231
v_cndmask_b32_e64 v196, v196, v17, s[76:77]
v_cndmask_b32_e64 v197, v197, v17, s[76:77]
zavv 180,44,88
s_add_u32 m0, s32, 19456
v_cndmask_b32_e64 v198, v198, v17, s[76:77]
v_cndmask_b32_e64 v199, v199, v17, s[76:77]
ar 224,208
ar 225,209
ar 226,210
ar 227,211
ar 228,212
zavv 188,44,92
ar 229,213
ar 230,214
ar 231,215
v_pk_mul_f32 v[224:225], v[224:225], s[48:49] op_sel_hi:[1,0]
v_pk_mul_f32 v[226:227], v[226:227], s[48:49] op_sel_hi:[1,0]
v_pk_mul_f32 v[228:229], v[228:229], s[48:49] op_sel_hi:[1,0]
v_pk_mul_f32 v[230:231], v[230:231], s[48:49] op_sel_hi:[1,0]
zavv 192,48,64
rqv 44,1,43008
cv 200,224,225
cv 201,226,227
cv 202,228,229
cv 203,230,231
ar 224,216
ar 225,217
ar 226,218
zavv 200,48,68
s_add_u32 s61, s61, s30
ar 227,219
ar 228,220
ar 229,221
ar 230,222
ar 231,223
v_pk_mul_f32 v[224:225], v[224:225], s[48:49] op_sel_hi:[1,0]
v_pk_mul_f32 v[226:227], v[226:227], s[48:49] op_sel_hi:[1,0]
zavv 208,48,72
dm 13,20,61
v_pk_mul_f32 v[228:229], v[228:229], s[48:49] op_sel_hi:[1,0]
v_pk_mul_f32 v[230:231], v[230:231], s[48:49] op_sel_hi:[1,0]
cv 204,224,225
cv 205,226,227
cv 206,228,229
cv 207,230,231
v_cndmask_b32_e64 v204, v204, v17, s[76:77]
zavv 216,48,76
v_cndmask_b32_e64 v205, v205, v17, s[76:77]
v_cndmask_b32_e64 v206, v206, v17, s[76:77]
v_cndmask_b32_e64 v207, v207, v17, s[76:77]
ar 224,224
ar 225,225
ar 226,226
ar 227,227
zavv 224,48,80
ar 228,228
ar 229,229
ar 230,230
ar 231,231
v_pk_mul_f32 v[224:225], v[224:225], s[48:49] op_sel_hi:[1,0]
v_pk_mul_f32 v[226:227], v[226:227], s[48:49] op_sel_hi:[1,0]
v_pk_mul_f32 v[228:229], v[228:229], s[48:49] op_sel_hi:[1,0]
v_pk_mul_f32 v[230:231], v[230:231], s[48:49] op_sel_hi:[1,0]
cv 208,224,225
cv 209,226,227
cv 210,228,229
cv 211,230,231
ar 224,232
ar 225,233
ar 226,234
ar 227,235
zavv 232,48,84
s_add_u32 s29, s29, s65
ar 228,236
ar 229,237
ar 230,238
ar 231,239
v_pk_mul_f32 v[224:225], v[224:225], s[48:49] op_sel_hi:[1,0]
v_pk_mul_f32 v[226:227], v[226:227], s[48:49] op_sel_hi:[1,0]
v_pk_mul_f32 v[228:229], v[228:229], s[48:49] op_sel_hi:[1,0]
v_pk_mul_f32 v[230:231], v[230:231], s[48:49] op_sel_hi:[1,0]
cv 212,224,225
cv 213,226,227
cv 214,228,229
cv 215,230,231
v_cndmask_b32_e64 v212, v212, v17, s[76:77]
v_cndmask_b32_e64 v213, v213, v17, s[76:77]
v_cndmask_b32_e64 v214, v214, v17, s[76:77]
v_cndmask_b32_e64 v215, v215, v17, s[76:77]
ar 224,240
ar 225,241
ar 226,242
ar 227,243
zavv 240,48,88
ar 228,244
ar 229,245
ar 230,246
ar 231,247
v_pk_mul_f32 v[224:225], v[224:225], s[48:49] op_sel_hi:[1,0]
v_pk_mul_f32 v[226:227], v[226:227], s[48:49] op_sel_hi:[1,0]
v_pk_mul_f32 v[228:229], v[228:229], s[48:49] op_sel_hi:[1,0]
v_pk_mul_f32 v[230:231], v[230:231], s[48:49] op_sel_hi:[1,0]
cv 216,224,225
cv 217,226,227
cv 218,228,229
cv 219,230,231
ar 224,248
ar 225,249
ar 226,250
ar 227,251
zavv 248,48,92
s_add_u32 s28, s28, 128
ar 228,252
ar 229,253
ar 230,254
ar 231,255
v_pk_mul_f32 v[224:225], v[224:225], s[48:49] op_sel_hi:[1,0]
v_pk_mul_f32 v[226:227], v[226:227], s[48:49] op_sel_hi:[1,0]
v_pk_mul_f32 v[228:229], v[228:229], s[48:49] op_sel_hi:[1,0]
zavv 196,52,64
v_pk_mul_f32 v[230:231], v[230:231], s[48:49] op_sel_hi:[1,0]
cv 220,224,225
cv 221,226,227
cv 222,228,229
cv 223,230,231
v_cndmask_b32_e64 v220, v220, v17, s[76:77]
v_cndmask_b32_e64 v221, v221, v17, s[76:77]
zavv 204,52,68
s_add_u32 s74, s78, .Lk1_BNN+8192-.Lk1_base
s_addc_u32 s75, s79, 0
v_cndmask_b32_e64 v222, v222, v17, s[76:77]
v_cndmask_b32_e64 v223, v223, v17, s[76:77]
zavv 212,52,72
zavv 220,52,76
global_load_dword v19, v236, s[74:75]
zavv 228,52,80
zavv 236,52,84
zavv 244,52,88
zavv 252,52,92
s_waitcnt vmcnt(18) lgkmcnt(0)
s_barrier
mavv 0,24,96
rt 64,4,0
mavv 8,24,100
s_add_u32 m0, s33, 49152
mavv 16,24,104
rt 66,4,2048
mavv 24,24,108
s_mov_b32 s67, s29
mavv 32,24,112
rt 68,5,0
mavv 40,24,116
dm 14,24,67
mavv 48,24,120
rt 70,5,2048
mavv 56,24,124
s_add_u32 m0, s32, 32768
mavv 4,28,96
rt 72,6,0
mavv 12,28,100
rqv 24,2,0
mavv 20,28,104
s_mov_b32 s61, s28
mavv 28,28,108
rt 74,6,2048
mavv 36,28,112
dm 12,20,61
mavv 44,28,116
rt 76,7,0
mavv 52,28,120
s_add_u32 m0, s33, 50176
mavv 60,28,124
rt 78,7,2048
mavv 64,32,96
s_add_u32 s67, s67, s31
mavv 72,32,100
rt 80,8,0
mavv 80,32,104
rqv 28,2,2048
mavv 88,32,108
)ASM" ::: "memory");
  asm volatile(R"ASM(
dm 15,24,67
mavv 96,32,112
rt 82,8,2048
mavv 104,32,116
s_add_u32 m0, s32, 33792
mavv 112,32,120
rt 84,9,0
mavv 120,32,124
rt 86,9,2048
mavv 68,36,96
s_add_u32 s61, s61, s30
rt 88,10,0
mavv 76,36,100
rqv 32,2,4096
mavv 84,36,104
dm 13,20,61
mavv 92,36,108
rt 90,10,2048
mavv 100,36,112
s_add_u32 m0, s33, 51200
mavv 108,36,116
rt 92,11,0
mavv 116,36,120
s_add_u32 s67, s67, s31
mavv 124,36,124
rt 94,11,2048
mavv 128,40,96
dm 14,24,67
mavv 136,40,100
rqv 36,2,6144
mavv 144,40,104
rqv 48,2,12288
mavv 152,40,108
s_add_u32 m0, s32, 34816
mavv 160,40,112
s_add_u32 s61, s61, s30
mavv 168,40,116
dm 12,20,61
mavv 176,40,120
mavv 184,40,124
s_add_u32 m0, s33, 52224
mavv 132,44,96
rqv 40,2,8192
mavv 140,44,100
rqv 52,2,14336
mavv 148,44,104
s_add_u32 s67, s67, s31
mavv 156,44,108
mavv 164,44,112
dm 15,24,67
mavv 172,44,116
mavv 180,44,120
s_add_u32 m0, s32, 35840
mavv 188,44,124
mavv 192,56,96
rqv 44,2,10240
mavv 200,56,100
s_add_u32 s61, s61, s30
mavv 208,56,104
dm 13,20,61
mavv 216,56,108
mavv 224,56,112
mavv 232,56,116
s_add_u32 s29, s29, s65
mavv 240,56,120
mavv 248,56,124
buffer_load_dwordx4 v[224:227], v16, s[12:15], 0 offen
mavv 196,60,96
mavv 204,60,100
buffer_load_dwordx4 v[228:231], v18, s[8:11], s16 offen
mavv 212,60,104
s_mov_b32 s68, s88
mavv 220,60,108
s_mov_b32 s69, s68
mavv 228,60,112
buffer_load_dwordx4 v[232:235], v18, s[8:11], s16 offen offset:896
mavv 236,60,116
buffer_store_dwordx4 v[128:131], v16, s[84:87], s69 offen sc1
mavv 244,60,120
s_add_u32 s69, s69, s51
mavv 252,60,124
buffer_store_dwordx4 v[132:135], v16, s[84:87], s69 offen sc1
s_waitcnt vmcnt(22) lgkmcnt(0)
s_barrier
mavv 0,24,64
rt 96,4,16384
mavv 8,24,68
s_add_u32 m0, s33, 0
mavv 16,24,72
rt 98,4,18432
mavv 24,24,76
s_mov_b32 s67, s29
mavv 32,24,80
rt 100,5,16384
mavv 40,24,84
dm 14,24,67
mavv 48,24,88
rt 102,5,18432
mavv 56,24,92
s_add_u32 m0, s32, 49152
mavv 4,28,64
rt 104,6,16384
mavv 12,28,68
rqv 24,3,0
mavv 20,28,72
s_add_u32 s61, s28, s64
mavv 28,28,76
rt 106,6,18432
mavv 36,28,80
dm 12,20,61
mavv 44,28,84
rt 108,7,16384
mavv 52,28,88
rt 110,7,18432
mavv 60,28,92
s_add_u32 m0, s33, 1024
mavv 64,32,64
rt 112,8,16384
mavv 72,32,68
rqv 28,3,2048
mavv 80,32,72
s_add_u32 s67, s67, s31
mavv 88,32,76
rt 114,8,18432
mavv 96,32,80
dm 15,24,67
mavv 104,32,84
rt 116,9,16384
mavv 112,32,88
s_add_u32 m0, s32, 50176
mavv 120,32,92
rt 118,9,18432
mavv 68,36,64
s_add_u32 s61, s61, s30
mavv 76,36,68
rt 120,10,16384
mavv 84,36,72
rqv 32,3,4096
mavv 92,36,76
rt 122,10,18432
mavv 100,36,80
dm 13,20,61
mavv 108,36,84
rt 124,11,16384
mavv 116,36,88
s_add_u32 m0, s33, 2048
mavv 124,36,92
rt 126,11,18432
mavv 128,40,64
s_add_u32 s67, s67, s31
mavv 136,40,68
rqv 36,3,6144
mavv 144,40,72
rqv 56,3,12288
mavv 152,40,76
dm 14,24,67
mavv 160,40,80
s_add_u32 m0, s32, 51200
mavv 168,40,84
mavv 176,40,88
s_add_u32 s61, s61, s30
mavv 184,40,92
mavv 132,44,64
rqv 40,3,8192
mavv 140,44,68
dm 12,20,61
mavv 148,44,72
rqv 60,3,14336
mavv 156,44,76
s_add_u32 m0, s33, 3072
mavv 164,44,80
mavv 172,44,84
s_add_u32 s67, s67, s31
mavv 180,44,88
mavv 188,44,92
dm 15,24,67
mavv 192,48,64
rqv 44,3,10240
mavv 200,48,68
mavv 208,48,72
s_add_u32 m0, s32, 52224
mavv 216,48,76
mavv 224,48,80
s_add_u32 s61, s61, s30
mavv 232,48,84
mavv 240,48,88
dm 13,20,61
mavv 248,48,92
mavv 196,52,64
mavv 204,52,68
s_add_u32 s29, s29, s65
mavv 212,52,72
s_add_u32 s69, s69, s51
mavv 220,52,76
s_add_u32 s28, s28, 128
mavv 228,52,80
buffer_store_dwordx4 v[136:139], v16, s[84:87], s69 offen sc1
mavv 236,52,84
s_add_u32 s69, s69, s51
mavv 244,52,88
buffer_store_dwordx4 v[140:143], v16, s[84:87], s69 offen sc1
mavv 252,52,92
s_waitcnt vmcnt(23) lgkmcnt(0)
s_barrier
mavv 0,24,96
rt 64,4,32768
mavv 8,24,100
s_add_u32 m0, s33, 16384
mavv 16,24,104
rt 66,4,34816
mavv 24,24,108
s_mov_b32 s67, s29
mavv 32,24,112
rt 68,5,32768
mavv 40,24,116
dm 14,24,67
mavv 48,24,120
rt 70,5,34816
mavv 56,24,124
s_add_u32 m0, s32, 65536
mavv 4,28,96
rt 72,6,32768
mavv 12,28,100
rqv 24,0,0
mavv 20,28,104
s_mov_b32 s61, s28
mavv 28,28,108
rt 74,6,34816
mavv 36,28,112
dm 12,20,61
mavv 44,28,116
rt 76,7,32768
mavv 52,28,120
s_add_u32 m0, s33, 17408
mavv 60,28,124
rt 78,7,34816
mavv 64,32,96
rt 80,8,32768
mavv 72,32,100
rqv 28,0,2048
mavv 80,32,104
s_add_u32 s67, s67, s31
mavv 88,32,108
rt 82,8,34816
mavv 96,32,112
dm 15,24,67
mavv 104,32,116
rt 84,9,32768
mavv 112,32,120
s_add_u32 m0, s32, 66560
mavv 120,32,124
rt 86,9,34816
mavv 68,36,96
s_add_u32 s61, s61, s30
mavv 76,36,100
rt 88,10,32768
mavv 84,36,104
rqv 32,0,4096
mavv 92,36,108
dm 13,20,61
mavv 100,36,112
rt 90,10,34816
mavv 108,36,116
s_add_u32 m0, s33, 18432
mavv 116,36,120
rt 92,11,32768
mavv 124,36,124
s_add_u32 s67, s67, s31
mavv 128,40,96
rt 94,11,34816
mavv 136,40,100
rqv 36,0,6144
mavv 144,40,104
dm 14,24,67
mavv 152,40,108
rqv 48,0,12288
mavv 160,40,112
s_add_u32 m0, s32, 67584
mavv 168,40,116
s_add_u32 s61, s61, s30
mavv 176,40,120
mavv 184,40,124
dm 12,20,61
mavv 132,44,96
rqv 40,0,8192
mavv 140,44,100
rqv 52,0,14336
mavv 148,44,104
s_add_u32 m0, s33, 19456
mavv 156,44,108
s_add_u32 s67, s67, s31
mavv 164,44,112
mavv 172,44,116
dm 15,24,67
mavv 180,44,120
mavv 188,44,124
mavv 192,56,96
rqv 44,0,10240
mavv 200,56,100
s_add_u32 m0, s32, 68608
mavv 208,56,104
s_add_u32 s61, s61, s30
mavv 216,56,108
mavv 224,56,112
dm 13,20,61
mavv 232,56,116
mavv 240,56,120
mavv 248,56,124
s_add_u32 s29, s29, s65
mavv 196,60,96
mavv 204,60,100
s_add_u32 s74, s78, .Lk1_BNN+12288-.Lk1_base
s_addc_u32 s75, s79, 0
mavv 212,60,104
s_add_u32 s69, s69, s51
mavv 220,60,108
global_load_dword v19, v236, s[74:75]
mavv 228,60,112
buffer_store_dwordx4 v[144:147], v16, s[84:87], s69 offen sc1
mavv 236,60,116
s_add_u32 s69, s69, s51
mavv 244,60,120
buffer_store_dwordx4 v[148:151], v16, s[84:87], s69 offen sc1
mavv 252,60,124
s_waitcnt vmcnt(21) lgkmcnt(0)
s_barrier
mavv 0,24,64
rt 96,4,49152
mavv 8,24,68
s_add_u32 m0, s33, 32768
mavv 16,24,72
rt 98,4,51200
mavv 24,24,76
s_mov_b32 s67, s29
mavv 32,24,80
rt 100,5,49152
mavv 40,24,84
dm 14,24,67
mavv 48,24,88
rt 102,5,51200
mavv 56,24,92
s_add_u32 m0, s32, 81920
mavv 4,28,64
rt 104,6,49152
mavv 12,28,68
rqv 24,1,0
mavv 20,28,72
s_add_u32 s61, s28, s64
mavv 28,28,76
rt 106,6,51200
mavv 36,28,80
dm 12,20,61
mavv 44,28,84
rt 108,7,49152
mavv 52,28,88
rt 110,7,51200
mavv 60,28,92
s_add_u32 m0, s33, 33792
mavv 64,32,64
rt 112,8,49152
mavv 72,32,68
rqv 28,1,2048
mavv 80,32,72
s_add_u32 s67, s67, s31
mavv 88,32,76
rt 114,8,51200
mavv 96,32,80
dm 15,24,67
mavv 104,32,84
rt 116,9,49152
mavv 112,32,88
s_add_u32 m0, s32, 82944
mavv 120,32,92
rt 118,9,51200
mavv 68,36,64
s_add_u32 s61, s61, s30
mavv 76,36,68
rt 120,10,49152
mavv 84,36,72
rqv 32,1,4096
mavv 92,36,76
rt 122,10,51200
mavv 100,36,80
dm 13,20,61
mavv 108,36,84
rt 124,11,49152
mavv 116,36,88
s_add_u32 m0, s33, 34816
mavv 124,36,92
rt 126,11,51200
mavv 128,40,64
s_add_u32 s67, s67, s31
mavv 136,40,68
rqv 36,1,6144
mavv 144,40,72
rqv 56,1,12288
mavv 152,40,76
dm 14,24,67
mavv 160,40,80
s_add_u32 m0, s32, 83968
mavv 168,40,84
mavv 176,40,88
s_add_u32 s61, s61, s30
mavv 184,40,92
mavv 132,44,64
rqv 40,1,8192
mavv 140,44,68
dm 12,20,61
mavv 148,44,72
rqv 60,1,14336
mavv 156,44,76
s_add_u32 m0, s33, 35840
mavv 164,44,80
mavv 172,44,84
s_add_u32 s67, s67, s31
mavv 180,44,88
mavv 188,44,92
dm 15,24,67
mavv 192,48,64
rqv 44,1,10240
mavv 200,48,68
mavv 208,48,72
s_add_u32 m0, s32, 84992
mavv 216,48,76
mavv 224,48,80
s_add_u32 s61, s61, s30
mavv 232,48,84
mavv 240,48,88
dm 13,20,61
mavv 248,48,92
mavv 196,52,64
mavv 204,52,68
s_add_u32 s29, s29, s65
mavv 212,52,72
s_add_u32 s68, s68, s50
mavv 220,52,76
s_mov_b32 s69, s68
mavv 228,52,80
s_add_u32 s28, s28, 128
mavv 236,52,84
buffer_store_dwordx4 v[152:155], v16, s[84:87], s69 offen sc1
mavv 244,52,88
s_add_u32 s69, s69, s51
mavv 252,52,92
buffer_store_dwordx4 v[156:159], v16, s[84:87], s69 offen sc1
s_waitcnt vmcnt(21) lgkmcnt(0)
s_barrier
mavv 0,24,96
rt 64,4,0
mavv 8,24,100
s_add_u32 m0, s33, 49152
mavv 16,24,104
s_mov_b32 s67, s29
mavv 24,24,108
rt 66,4,2048
mavv 32,24,112
dm 14,24,67
mavv 40,24,116
s_add_u32 m0, s32, 0
mavv 48,24,120
rt 68,5,0
s_mov_b32 s61, s28
mavv 56,24,124
rt 70,5,2048
mavv 4,28,96
dm 12,20,61
s_add_u32 m0, s33, 50176
mavv 12,28,100
rt 72,6,0
mavv 20,28,104
rqv 24,0,32768
s_add_u32 s67, s67, s31
mavv 28,28,108
rt 74,6,2048
mavv 36,28,112
dm 15,24,67
mavv 44,28,116
s_add_u32 m0, s32, 1024
mavv 52,28,120
rt 76,7,0
s_add_u32 s61, s61, s30
mavv 60,28,124
rt 78,7,2048
mavv 64,32,96
dm 13,20,61
s_add_u32 m0, s33, 51200
mavv 72,32,100
rt 80,8,0
mavv 80,32,104
rqv 28,0,34816
s_add_u32 s67, s67, s31
mavv 88,32,108
rt 82,8,2048
mavv 96,32,112
dm 14,24,67
mavv 104,32,116
s_add_u32 m0, s32, 2048
mavv 112,32,120
rt 84,9,0
s_add_u32 s61, s61, s30
mavv 120,32,124
rt 86,9,2048
mavv 68,36,96
dm 12,20,61
s_add_u32 m0, s33, 52224
mavv 76,36,100
rt 88,10,0
mavv 84,36,104
rqv 32,0,36864
s_add_u32 s67, s67, s31
mavv 92,36,108
rt 90,10,2048
mavv 100,36,112
dm 15,24,67
mavv 108,36,116
s_add_u32 m0, s32, 3072
mavv 116,36,120
rt 92,11,0
s_add_u32 s61, s61, s30
mavv 124,36,124
rt 94,11,2048
mavv 128,40,96
dm 13,20,61
s_add_u32 s29, s29, s65
mavv 136,40,100
rqv 36,0,38912
mavv 144,40,104
v_dot2_f32_bf16 v20, v228, v224, 0
mavv 152,40,108
rqv 48,0,45056
mavv 160,40,112
v_dot2_f32_bf16 v21, v232, v224, 0
mavv 168,40,116
v_dot2_f32_bf16 v20, v229, v225, v20
mavv 176,40,120
v_dot2_f32_bf16 v21, v233, v225, v21
mavv 184,40,124
v_dot2_f32_bf16 v20, v230, v226, v20
mavv 132,44,96
v_dot2_f32_bf16 v21, v234, v226, v21
mavv 140,44,100
rqv 40,0,40960
mavv 148,44,104
v_dot2_f32_bf16 v20, v231, v227, v20
mavv 156,44,108
rqv 52,0,47104
mavv 164,44,112
v_dot2_f32_bf16 v21, v235, v227, v21
mavv 172,44,116
s_nop 1
mavv 180,44,120
v_add_f32_dpp v20, v20, v20 row_shr:8 row_mask:0xf bank_mask:0xf bound_ctrl:0
mavv 188,44,124
v_add_f32_dpp v21, v21, v21 row_shr:8 row_mask:0xf bank_mask:0xf bound_ctrl:0
mavv 192,56,96
v_add_f32_dpp v20, v20, v20 row_shr:4 row_mask:0xf bank_mask:0xf bound_ctrl:0
mavv 200,56,100
rqv 44,0,43008
mavv 208,56,104
v_add_f32_dpp v21, v21, v21 row_shr:4 row_mask:0xf bank_mask:0xf bound_ctrl:0
mavv 216,56,108
v_add_f32_dpp v20, v20, v20 row_shr:2 row_mask:0xf bank_mask:0xf bound_ctrl:0
mavv 224,56,112
v_add_f32_dpp v21, v21, v21 row_shr:2 row_mask:0xf bank_mask:0xf bound_ctrl:0
mavv 232,56,116
v_add_f32_dpp v20, v20, v20 row_shr:1 row_mask:0xf bank_mask:0xf bound_ctrl:0
mavv 240,56,120
v_add_f32_dpp v21, v21, v21 row_shr:1 row_mask:0xf bank_mask:0xf bound_ctrl:0
mavv 248,56,124
v_mov_b32 v224, v20
mavv 196,60,96
v_mov_b32 v225, v21
mavv 204,60,100
mavv 212,60,104
v_permlane16_swap_b32 v20, v224
mavv 220,60,108
s_add_u32 s69, s69, s51
mavv 228,60,112
v_permlane16_swap_b32 v21, v225
buffer_store_dwordx4 v[160:163], v16, s[84:87], s69 offen sc1
mavv 236,60,116
v_add_f32 v20, v20, v224
mavv 244,60,120
s_add_u32 s69, s69, s51
mavv 252,60,124
buffer_store_dwordx4 v[164:167], v16, s[84:87], s69 offen sc1
s_waitcnt vmcnt(20) lgkmcnt(0)
s_barrier
mavv 0,24,64
rt 96,4,16384
mavv 8,24,68
s_add_u32 m0, s33, 0
mavv 16,24,72
s_mov_b32 s67, s29
mavv 24,24,76
rt 98,4,18432
mavv 32,24,80
dm 14,24,67
s_add_u32 m0, s32, 16384
mavv 40,24,84
)ASM" ::: "memory");
  asm volatile(R"ASM(
rt 100,5,16384
mavv 48,24,88
s_add_u32 s61, s28, s64
mavv 56,24,92
rt 102,5,18432
mavv 4,28,64
dm 12,20,61
s_add_u32 m0, s33, 1024
rt 104,6,16384
mavv 12,28,68
rqv 24,1,32768
mavv 20,28,72
s_add_u32 s67, s67, s31
dm 15,24,67
mavv 28,28,76
rt 106,6,18432
mavv 36,28,80
s_add_u32 m0, s32, 17408
mavv 44,28,84
rt 108,7,16384
mavv 52,28,88
s_add_u32 s61, s61, s30
dm 13,20,61
mavv 60,28,92
rt 110,7,18432
mavv 64,32,64
s_add_u32 m0, s33, 2048
s_add_u32 s67, s67, s31
mavv 72,32,68
rt 112,8,16384
mavv 80,32,72
rqv 28,1,34816
dm 14,24,67
mavv 88,32,76
rt 114,8,18432
mavv 96,32,80
s_add_u32 m0, s32, 18432
s_add_u32 s61, s61, s30
mavv 104,32,84
rt 116,9,16384
mavv 112,32,88
dm 12,20,61
mavv 120,32,92
s_add_u32 m0, s33, 3072
mavv 68,36,64
rt 118,9,18432
s_add_u32 s67, s67, s31
rt 120,10,16384
mavv 76,36,68
rqv 32,1,36864
mavv 84,36,72
dm 15,24,67
s_add_u32 m0, s32, 19456
mavv 92,36,76
rt 122,10,18432
mavv 100,36,80
s_add_u32 s61, s61, s30
mavv 108,36,84
dm 13,20,61
mavv 116,36,88
rt 124,11,16384
s_add_u32 s29, s29, s65
mavv 124,36,92
rt 126,11,18432
mavv 128,40,64
s_add_u32 s28, s28, 128
s_add_u32 s74, s78, .Lk1_BNN+16384-.Lk1_base
s_addc_u32 s75, s79, 0
mavv 136,40,68
rqv 36,1,38912
mavv 144,40,72
global_load_dword v19, v236, s[74:75]
mavv 152,40,76
rqv 56,1,45056
mavv 160,40,80
v_add_f32 v21, v21, v225
mavv 168,40,84
v_mov_b32 v224, v20
mavv 176,40,88
v_mov_b32 v225, v21
mavv 184,40,92
v_permlane32_swap_b32 v20, v224
mavv 132,44,64
v_permlane32_swap_b32 v21, v225
mavv 140,44,68
rqv 40,1,40960
v_add_f32 v20, v20, v224
mavv 148,44,72
rqv 60,1,47104
mavv 156,44,76
v_add_f32 v21, v21, v225
mavv 164,44,80
cv 20,20,21
mavv 172,44,84
v_and_b32 v21, 0xffff0000, v20
mavv 180,44,88
v_lshlrev_b32 v20, 16, v20
mavv 188,44,92
v_mul_f32 v20, 0xbfb8aa3b, v20
mavv 192,48,64
v_mul_f32 v21, 0xbfb8aa3b, v21
mavv 200,48,68
rqv 44,1,43008
mavv 208,48,72
ex 20
mavv 216,48,76
ex 21
mavv 224,48,80
v_add_f32 v20, 1.0, v20
mavv 232,48,84
v_add_f32 v21, 1.0, v21
mavv 240,48,88
v_rcp_f32 v20, v20
mavv 248,48,92
v_rcp_f32 v21, v21
mavv 196,52,64
v_mov_b32 v22, 0
mavv 204,52,68
cv 20,20,21
mavv 212,52,72
v_and_b32 v21, 0xffff0000, v20
mavv 220,52,76
s_add_u32 s69, s69, s51
mavv 228,52,80
v_lshlrev_b32 v20, 16, v20
buffer_store_dwordx4 v[168:171], v16, s[84:87], s69 offen sc1
mavv 236,52,84
s_mov_b32 exec_lo, 0
s_mov_b32 exec_hi, 0x80000000
global_store_dwordx2 v22, v[20:21], s[6:7] sc1
s_mov_b64 exec, -1
mavv 244,52,88
s_add_u32 s69, s69, s51
mavv 252,52,92
buffer_store_dwordx4 v[172:175], v16, s[84:87], s69 offen sc1
s_waitcnt vmcnt(22) lgkmcnt(0)
s_barrier
mavv 0,24,96
rt 64,4,32768
mavv 8,24,100
s_add_u32 m0, s33, 16384
mavv 16,24,104
rt 66,4,34816
mavv 24,24,108
s_mov_b32 s67, s29
mavv 32,24,112
rt 68,5,32768
mavv 40,24,116
dm 14,24,67
mavv 48,24,120
rt 70,5,34816
mavv 56,24,124
s_add_u32 m0, s32, 32768
mavv 4,28,96
rt 72,6,32768
mavv 12,28,100
rqv 24,2,0
mavv 20,28,104
s_mov_b32 s61, s28
mavv 28,28,108
rt 74,6,34816
mavv 36,28,112
rt 76,7,32768
mavv 44,28,116
dm 12,20,61
mavv 52,28,120
rt 78,7,34816
mavv 60,28,124
s_add_u32 m0, s33, 17408
mavv 64,32,96
rt 80,8,32768
mavv 72,32,100
rqv 28,2,2048
mavv 80,32,104
s_add_u32 s67, s67, s31
mavv 88,32,108
rt 82,8,34816
mavv 96,32,112
dm 15,24,67
mavv 104,32,116
rt 84,9,32768
mavv 112,32,120
rt 86,9,34816
mavv 120,32,124
s_add_u32 m0, s32, 33792
mavv 68,36,96
rt 88,10,32768
mavv 76,36,100
rqv 32,2,4096
mavv 84,36,104
s_add_u32 s61, s61, s30
mavv 92,36,108
rt 90,10,34816
mavv 100,36,112
dm 13,20,61
mavv 108,36,116
rt 92,11,32768
mavv 116,36,120
s_add_u32 m0, s33, 18432
mavv 124,36,124
rt 94,11,34816
mavv 128,40,96
rqv 36,2,6144
mavv 136,40,100
s_add_u32 s67, s67, s31
mavv 144,40,104
rqv 48,2,12288
mavv 152,40,108
dm 14,24,67
mavv 160,40,112
mavv 168,40,116
s_add_u32 m0, s32, 34816
mavv 176,40,120
mavv 184,40,124
s_add_u32 s61, s61, s30
mavv 132,44,96
rqv 40,2,8192
mavv 140,44,100
rqv 52,2,14336
mavv 148,44,104
dm 12,20,61
mavv 156,44,108
mavv 164,44,112
s_add_u32 m0, s33, 19456
mavv 172,44,116
mavv 180,44,120
mavv 188,44,124
s_add_u32 s67, s67, s31
mavv 192,56,96
rqv 44,2,10240
mavv 200,56,100
dm 15,24,67
mavv 208,56,104
mavv 216,56,108
mavv 224,56,112
s_add_u32 m0, s32, 35840
mavv 232,56,116
mavv 240,56,120
s_add_u32 s61, s61, s30
mavv 248,56,124
mavv 196,60,96
mavv 204,60,100
dm 13,20,61
mavv 212,60,104
s_add_u32 s68, s68, s50
mavv 220,60,108
s_mov_b32 s69, s68
mavv 228,60,112
s_add_u32 s29, s29, s65
mavv 236,60,116
buffer_store_dwordx4 v[176:179], v16, s[84:87], s69 offen sc1
mavv 244,60,120
s_add_u32 s69, s69, s51
mavv 252,60,124
buffer_store_dwordx4 v[180:183], v16, s[84:87], s69 offen sc1
s_waitcnt vmcnt(22) lgkmcnt(0)
s_barrier
mavv 0,24,64
rt 96,4,49152
mavv 8,24,68
s_add_u32 m0, s33, 32768
mavv 16,24,72
rt 98,4,51200
mavv 24,24,76
s_mov_b32 s67, s29
mavv 32,24,80
rt 100,5,49152
mavv 40,24,84
dm 14,24,67
mavv 48,24,88
rt 102,5,51200
mavv 56,24,92
s_add_u32 m0, s32, 49152
mavv 4,28,64
rt 104,6,49152
mavv 12,28,68
rqv 24,3,0
mavv 20,28,72
s_add_u32 s61, s28, s64
mavv 28,28,76
rt 106,6,51200
mavv 36,28,80
dm 12,20,61
mavv 44,28,84
rt 108,7,49152
mavv 52,28,88
rt 110,7,51200
mavv 60,28,92
s_add_u32 m0, s33, 33792
mavv 64,32,64
rt 112,8,49152
mavv 72,32,68
rqv 28,3,2048
mavv 80,32,72
s_add_u32 s67, s67, s31
mavv 88,32,76
rt 114,8,51200
mavv 96,32,80
dm 15,24,67
mavv 104,32,84
rt 116,9,49152
mavv 112,32,88
s_add_u32 m0, s32, 50176
mavv 120,32,92
rt 118,9,51200
mavv 68,36,64
s_add_u32 s61, s61, s30
mavv 76,36,68
rt 120,10,49152
mavv 84,36,72
rqv 32,3,4096
mavv 92,36,76
rt 122,10,51200
mavv 100,36,80
dm 13,20,61
mavv 108,36,84
rt 124,11,49152
mavv 116,36,88
s_add_u32 m0, s33, 34816
mavv 124,36,92
rt 126,11,51200
mavv 128,40,64
s_add_u32 s67, s67, s31
mavv 136,40,68
rqv 36,3,6144
mavv 144,40,72
rqv 56,3,12288
mavv 152,40,76
dm 14,24,67
mavv 160,40,80
s_add_u32 m0, s32, 51200
mavv 168,40,84
mavv 176,40,88
s_add_u32 s61, s61, s30
mavv 184,40,92
mavv 132,44,64
rqv 40,3,8192
mavv 140,44,68
dm 12,20,61
mavv 148,44,72
rqv 60,3,14336
mavv 156,44,76
s_add_u32 m0, s33, 35840
mavv 164,44,80
mavv 172,44,84
s_add_u32 s67, s67, s31
mavv 180,44,88
mavv 188,44,92
dm 15,24,67
mavv 192,48,64
rqv 44,3,10240
mavv 200,48,68
mavv 208,48,72
s_add_u32 m0, s32, 52224
mavv 216,48,76
mavv 224,48,80
s_add_u32 s61, s61, s30
mavv 232,48,84
mavv 240,48,88
dm 13,20,61
mavv 248,48,92
mavv 196,52,64
mavv 204,52,68
s_add_u32 s29, s29, s65
mavv 212,52,72
s_add_u32 s69, s69, s51
mavv 220,52,76
s_add_u32 s28, s28, 128
mavv 228,52,80
buffer_store_dwordx4 v[184:187], v16, s[84:87], s69 offen sc1
mavv 236,52,84
s_add_u32 s69, s69, s51
mavv 244,52,88
buffer_store_dwordx4 v[188:191], v16, s[84:87], s69 offen sc1
mavv 252,52,92
s_waitcnt vmcnt(20) lgkmcnt(0)
s_barrier
mavv 0,24,96
rt 64,4,0
mavv 8,24,100
s_add_u32 m0, s33, 49152
mavv 16,24,104
rt 66,4,2048
mavv 24,24,108
s_mov_b32 s67, s29
mavv 32,24,112
rt 68,5,0
mavv 40,24,116
dm 14,24,67
mavv 48,24,120
rt 70,5,2048
mavv 56,24,124
s_add_u32 m0, s32, 65536
mavv 4,28,96
rt 72,6,0
mavv 12,28,100
rqv 24,0,0
mavv 20,28,104
s_mov_b32 s61, s28
mavv 28,28,108
rt 74,6,2048
mavv 36,28,112
dm 12,20,61
mavv 44,28,116
rt 76,7,0
mavv 52,28,120
s_add_u32 m0, s33, 50176
mavv 60,28,124
rt 78,7,2048
mavv 64,32,96
rt 80,8,0
mavv 72,32,100
rqv 28,0,2048
mavv 80,32,104
s_add_u32 s67, s67, s31
mavv 88,32,108
rt 82,8,2048
mavv 96,32,112
dm 15,24,67
mavv 104,32,116
rt 84,9,0
mavv 112,32,120
s_add_u32 m0, s32, 66560
mavv 120,32,124
rt 86,9,2048
mavv 68,36,96
s_add_u32 s61, s61, s30
mavv 76,36,100
rt 88,10,0
mavv 84,36,104
rqv 32,0,4096
mavv 92,36,108
dm 13,20,61
mavv 100,36,112
rt 90,10,2048
mavv 108,36,116
s_add_u32 m0, s33, 51200
mavv 116,36,120
rt 92,11,0
mavv 124,36,124
s_add_u32 s67, s67, s31
mavv 128,40,96
rt 94,11,2048
mavv 136,40,100
rqv 36,0,6144
mavv 144,40,104
dm 14,24,67
mavv 152,40,108
rqv 48,0,12288
mavv 160,40,112
s_add_u32 m0, s32, 67584
mavv 168,40,116
s_add_u32 s61, s61, s30
mavv 176,40,120
mavv 184,40,124
dm 12,20,61
mavv 132,44,96
rqv 40,0,8192
mavv 140,44,100
rqv 52,0,14336
mavv 148,44,104
s_add_u32 m0, s33, 52224
mavv 156,44,108
s_add_u32 s67, s67, s31
mavv 164,44,112
mavv 172,44,116
dm 15,24,67
mavv 180,44,120
mavv 188,44,124
mavv 192,56,96
rqv 44,0,10240
mavv 200,56,100
s_add_u32 m0, s32, 68608
mavv 208,56,104
s_add_u32 s61, s61, s30
mavv 216,56,108
mavv 224,56,112
dm 13,20,61
mavv 232,56,116
mavv 240,56,120
mavv 248,56,124
s_add_u32 s29, s29, s65
mavv 196,60,96
mavv 204,60,100
s_add_u32 s74, s78, .Lk1_BN+0-.Lk1_base
s_addc_u32 s75, s79, 0
mavv 212,60,104
s_add_u32 s69, s69, s51
mavv 220,60,108
global_load_dword v19, v236, s[74:75]
mavv 228,60,112
buffer_store_dwordx4 v[192:195], v16, s[84:87], s69 offen sc1
mavv 236,60,116
s_add_u32 s69, s69, s51
mavv 244,60,120
buffer_store_dwordx4 v[196:199], v16, s[84:87], s69 offen sc1
mavv 252,60,124
s_waitcnt vmcnt(21) lgkmcnt(0)
s_barrier
mavv 0,24,64
rt 96,4,16384
mavv 8,24,68
s_add_u32 m0, s33, 0
mavv 16,24,72
rt 98,4,18432
mavv 24,24,76
s_mov_b32 s67, s29
mavv 32,24,80
rt 100,5,16384
mavv 40,24,84
dm 14,24,67
mavv 48,24,88
rt 102,5,18432
mavv 56,24,92
s_add_u32 m0, s32, 81920
mavv 4,28,64
rt 104,6,16384
mavv 12,28,68
rqv 24,1,0
mavv 20,28,72
s_add_u32 s61, s28, s64
mavv 28,28,76
rt 106,6,18432
mavv 36,28,80
dm 12,20,61
mavv 44,28,84
rt 108,7,16384
mavv 52,28,88
rt 110,7,18432
mavv 60,28,92
s_add_u32 m0, s33, 1024
mavv 64,32,64
rt 112,8,16384
mavv 72,32,68
rqv 28,1,2048
mavv 80,32,72
s_add_u32 s67, s67, s31
mavv 88,32,76
rt 114,8,18432
mavv 96,32,80
dm 15,24,67
mavv 104,32,84
rt 116,9,16384
mavv 112,32,88
s_add_u32 m0, s32, 82944
mavv 120,32,92
rt 118,9,18432
mavv 68,36,64
s_add_u32 s61, s61, s30
mavv 76,36,68
rt 120,10,16384
mavv 84,36,72
rqv 32,1,4096
mavv 92,36,76
rt 122,10,18432
mavv 100,36,80
dm 13,20,61
mavv 108,36,84
rt 124,11,16384
mavv 116,36,88
s_add_u32 m0, s33, 2048
mavv 124,36,92
rt 126,11,18432
mavv 128,40,64
s_add_u32 s67, s67, s31
mavv 136,40,68
rqv 36,1,6144
mavv 144,40,72
rqv 56,1,12288
mavv 152,40,76
dm 14,24,67
mavv 160,40,80
s_add_u32 m0, s32, 83968
mavv 168,40,84
mavv 176,40,88
s_add_u32 s61, s61, s30
mavv 184,40,92
mavv 132,44,64
rqv 40,1,8192
mavv 140,44,68
dm 12,20,61
mavv 148,44,72
rqv 60,1,14336
mavv 156,44,76
s_add_u32 m0, s33, 3072
mavv 164,44,80
mavv 172,44,84
s_add_u32 s67, s67, s31
mavv 180,44,88
mavv 188,44,92
dm 15,24,67
mavv 192,48,64
rqv 44,1,10240
mavv 200,48,68
mavv 208,48,72
s_add_u32 m0, s32, 84992
mavv 216,48,76
mavv 224,48,80
s_add_u32 s61, s61, s30
mavv 232,48,84
)ASM" ::: "memory");
  asm volatile(R"ASM(
mavv 240,48,88
dm 13,20,61
mavv 248,48,92
mavv 196,52,64
mavv 204,52,68
s_add_u32 s29, s29, s65
mavv 212,52,72
s_add_u32 s68, s68, s50
mavv 220,52,76
s_mov_b32 s69, s68
mavv 228,52,80
s_add_u32 s28, s28, 128
mavv 236,52,84
buffer_store_dwordx4 v[200:203], v16, s[84:87], s69 offen sc1
mavv 244,52,88
s_add_u32 s69, s69, s51
mavv 252,52,92
buffer_store_dwordx4 v[204:207], v16, s[84:87], s69 offen sc1
s_waitcnt vmcnt(21) lgkmcnt(0)
s_barrier
mavv 0,24,96
rt 64,4,32768
mavv 8,24,100
s_add_u32 m0, s33, 16384
mavv 16,24,104
rt 66,4,34816
mavv 24,24,108
s_mov_b32 s67, s29
mavv 32,24,112
rt 68,5,32768
mavv 40,24,116
dm 14,24,67
mavv 48,24,120
rt 70,5,34816
mavv 56,24,124
s_add_u32 m0, s32, 0
mavv 4,28,96
rt 72,6,32768
mavv 12,28,100
rqv 24,0,32768
mavv 20,28,104
s_mov_b32 s61, s28
mavv 28,28,108
rt 74,6,34816
mavv 36,28,112
rt 76,7,32768
mavv 44,28,116
dm 12,20,61
mavv 52,28,120
rt 78,7,34816
mavv 60,28,124
s_add_u32 m0, s33, 17408
mavv 64,32,96
rt 80,8,32768
mavv 72,32,100
rqv 28,0,34816
mavv 80,32,104
s_add_u32 s67, s67, s31
mavv 88,32,108
rt 82,8,34816
mavv 96,32,112
dm 15,24,67
mavv 104,32,116
rt 84,9,32768
mavv 112,32,120
rt 86,9,34816
mavv 120,32,124
s_add_u32 m0, s32, 1024
mavv 68,36,96
rt 88,10,32768
mavv 76,36,100
rqv 32,0,36864
mavv 84,36,104
s_add_u32 s61, s61, s30
mavv 92,36,108
rt 90,10,34816
mavv 100,36,112
dm 13,20,61
mavv 108,36,116
rt 92,11,32768
mavv 116,36,120
s_add_u32 m0, s33, 18432
mavv 124,36,124
rt 94,11,34816
mavv 128,40,96
rqv 36,0,38912
mavv 136,40,100
s_add_u32 s67, s67, s31
mavv 144,40,104
rqv 48,0,45056
mavv 152,40,108
dm 14,24,67
mavv 160,40,112
mavv 168,40,116
s_add_u32 m0, s32, 2048
mavv 176,40,120
mavv 184,40,124
s_add_u32 s61, s61, s30
mavv 132,44,96
rqv 40,0,40960
mavv 140,44,100
rqv 52,0,47104
mavv 148,44,104
dm 12,20,61
mavv 156,44,108
mavv 164,44,112
s_add_u32 m0, s33, 19456
mavv 172,44,116
mavv 180,44,120
mavv 188,44,124
s_add_u32 s67, s67, s31
mavv 192,56,96
rqv 44,0,43008
mavv 200,56,100
dm 15,24,67
mavv 208,56,104
mavv 216,56,108
mavv 224,56,112
s_add_u32 m0, s32, 3072
mavv 232,56,116
mavv 240,56,120
s_add_u32 s61, s61, s30
mavv 248,56,124
mavv 196,60,96
mavv 204,60,100
dm 13,20,61
mavv 212,60,104
s_add_u32 s69, s69, s51
mavv 220,60,108
s_add_u32 s29, s29, s65
mavv 228,60,112
buffer_store_dwordx4 v[208:211], v16, s[84:87], s69 offen sc1
mavv 236,60,116
s_add_u32 s69, s69, s51
mavv 244,60,120
buffer_store_dwordx4 v[212:215], v16, s[84:87], s69 offen sc1
mavv 252,60,124
s_waitcnt vmcnt(20) lgkmcnt(0)
s_barrier
mavv 0,24,64
rt 96,4,49152
mavv 8,24,68
s_add_u32 m0, s33, 32768
mavv 16,24,72
rt 98,4,51200
mavv 24,24,76
s_mov_b32 s67, s29
mavv 32,24,80
rt 100,5,49152
mavv 40,24,84
dm 14,24,67
mavv 48,24,88
rt 102,5,51200
mavv 56,24,92
s_add_u32 m0, s32, 16384
mavv 4,28,64
rt 104,6,49152
mavv 12,28,68
rqv 24,1,32768
mavv 20,28,72
s_add_u32 s61, s28, s64
mavv 28,28,76
rt 106,6,51200
mavv 36,28,80
dm 12,20,61
mavv 44,28,84
rt 108,7,49152
mavv 52,28,88
s_add_u32 m0, s33, 33792
mavv 60,28,92
rt 110,7,51200
mavv 64,32,64
s_add_u32 s67, s67, s31
mavv 72,32,68
rt 112,8,49152
mavv 80,32,72
rqv 28,1,34816
mavv 88,32,76
dm 15,24,67
mavv 96,32,80
rt 114,8,51200
mavv 104,32,84
s_add_u32 m0, s32, 17408
mavv 112,32,88
rt 116,9,49152
mavv 120,32,92
rt 118,9,51200
mavv 68,36,64
s_add_u32 s61, s61, s30
rt 120,10,49152
mavv 76,36,68
rqv 32,1,36864
mavv 84,36,72
dm 13,20,61
mavv 92,36,76
rt 122,10,51200
mavv 100,36,80
s_add_u32 m0, s33, 34816
mavv 108,36,84
rt 124,11,49152
mavv 116,36,88
s_add_u32 s67, s67, s31
mavv 124,36,92
rt 126,11,51200
mavv 128,40,64
dm 14,24,67
mavv 136,40,68
rqv 36,1,38912
mavv 144,40,72
rqv 56,1,45056
mavv 152,40,76
s_add_u32 m0, s32, 18432
mavv 160,40,80
s_add_u32 s61, s61, s30
mavv 168,40,84
dm 12,20,61
mavv 176,40,88
mavv 184,40,92
s_add_u32 m0, s33, 35840
mavv 132,44,64
rqv 40,1,40960
mavv 140,44,68
rqv 60,1,47104
mavv 148,44,72
s_add_u32 s67, s67, s31
mavv 156,44,76
mavv 164,44,80
dm 15,24,67
mavv 172,44,84
mavv 180,44,88
s_add_u32 m0, s32, 19456
mavv 188,44,92
mavv 192,48,64
rqv 44,1,43008
mavv 200,48,68
s_add_u32 s61, s61, s30
mavv 208,48,72
dm 13,20,61
mavv 216,48,76
mavv 224,48,80
mavv 232,48,84
s_add_u32 s29, s29, s65
mavv 240,48,88
mavv 248,48,92
s_add_u32 s28, s28, 128
mavv 196,52,64
mavv 204,52,68
s_add_u32 s74, s78, .Lk1_BN+4096-.Lk1_base
s_addc_u32 s75, s79, 0
mavv 212,52,72
s_add_u32 s69, s69, s51
mavv 220,52,76
global_load_dword v19, v236, s[74:75]
mavv 228,52,80
buffer_store_dwordx4 v[216:219], v16, s[84:87], s69 offen sc1
mavv 236,52,84
s_add_u32 s69, s69, s51
mavv 244,52,88
buffer_store_dwordx4 v[220:223], v16, s[84:87], s69 offen sc1
mavv 252,52,92
s_branch .Lk1_BN
.Lk1_atr0:
s_getpc_b64 s[72:73]
s_add_u32 s72, s72, .Lk1_aret0-.Lk1_atr0-4
s_addc_u32 s73, s73, 0
s_mov_b32 s70, 65536
s_branch .Lk1_arare0
.Lk1_atr1:
s_getpc_b64 s[72:73]
s_add_u32 s72, s72, .Lk1_aret1-.Lk1_atr1-4
s_addc_u32 s73, s73, 0
s_mov_b32 s70, 69632
s_branch .Lk1_arare1
.Lk1_atr2:
s_getpc_b64 s[72:73]
s_add_u32 s72, s72, .Lk1_aret2-.Lk1_atr2-4
s_addc_u32 s73, s73, 0
s_mov_b32 s70, 73728
s_branch .Lk1_arare0
.Lk1_atr3:
s_getpc_b64 s[72:73]
s_add_u32 s72, s72, .Lk1_aret3-.Lk1_atr3-4
s_addc_u32 s73, s73, 0
s_mov_b32 s70, 77824
s_branch .Lk1_arare1
.Lk1_atr4:
s_getpc_b64 s[72:73]
s_add_u32 s72, s72, .Lk1_aret4-.Lk1_atr4-4
s_addc_u32 s73, s73, 0
s_mov_b32 s70, 81920
s_branch .Lk1_arare0
.Lk1_atr5:
s_getpc_b64 s[72:73]
s_add_u32 s72, s72, .Lk1_aret5-.Lk1_atr5-4
s_addc_u32 s73, s73, 0
s_mov_b32 s70, 86016
s_branch .Lk1_arare1
.Lk1_atr6:
s_getpc_b64 s[72:73]
s_add_u32 s72, s72, .Lk1_aret6-.Lk1_atr6-4
s_addc_u32 s73, s73, 0
s_mov_b32 s70, 90112
s_branch .Lk1_arare0
.Lk1_atr7:
s_getpc_b64 s[72:73]
s_add_u32 s72, s72, .Lk1_aret7-.Lk1_atr7-4
s_addc_u32 s73, s73, 0
s_mov_b32 s70, 94208
s_branch .Lk1_arare1
.Lk1_atr8:
s_getpc_b64 s[72:73]
s_add_u32 s72, s72, .Lk1_aret8-.Lk1_atr8-4
s_addc_u32 s73, s73, 0
s_mov_b32 s70, 131072
s_branch .Lk1_arare0
.Lk1_atr9:
s_getpc_b64 s[72:73]
s_add_u32 s72, s72, .Lk1_aret9-.Lk1_atr9-4
s_addc_u32 s73, s73, 0
s_mov_b32 s70, 135168
s_branch .Lk1_arare1
.Lk1_atr10:
s_getpc_b64 s[72:73]
s_add_u32 s72, s72, .Lk1_aret10-.Lk1_atr10-4
s_addc_u32 s73, s73, 0
s_mov_b32 s70, 139264
s_branch .Lk1_arare0
.Lk1_atr11:
s_getpc_b64 s[72:73]
s_add_u32 s72, s72, .Lk1_aret11-.Lk1_atr11-4
s_addc_u32 s73, s73, 0
s_mov_b32 s70, 143360
s_branch .Lk1_arare1
.Lk1_atr12:
s_getpc_b64 s[72:73]
s_add_u32 s72, s72, .Lk1_aret12-.Lk1_atr12-4
s_addc_u32 s73, s73, 0
s_mov_b32 s70, 0
s_branch .Lk1_arare0
.Lk1_atr13:
s_getpc_b64 s[72:73]
s_add_u32 s72, s72, .Lk1_aret13-.Lk1_atr13-4
s_addc_u32 s73, s73, 0
s_mov_b32 s70, 4096
s_branch .Lk1_arare1
.Lk1_atr14:
s_getpc_b64 s[72:73]
s_add_u32 s72, s72, .Lk1_aret14-.Lk1_atr14-4
s_addc_u32 s73, s73, 0
s_mov_b32 s70, 8192
s_branch .Lk1_arare0
.Lk1_atr15:
s_getpc_b64 s[72:73]
s_add_u32 s72, s72, .Lk1_aret15-.Lk1_atr15-4
s_addc_u32 s73, s73, 0
s_mov_b32 s70, 12288
s_branch .Lk1_arare1
.Lk1_atr16:
s_getpc_b64 s[72:73]
s_add_u32 s72, s72, .Lk1_aret16-.Lk1_atr16-4
s_addc_u32 s73, s73, 0
s_mov_b32 s70, 16384
s_branch .Lk1_arare0
.Lk1_atr17:
s_getpc_b64 s[72:73]
s_add_u32 s72, s72, .Lk1_aret17-.Lk1_atr17-4
s_addc_u32 s73, s73, 0
s_mov_b32 s70, 20480
s_branch .Lk1_arare1
.Lk1_atr18:
s_getpc_b64 s[72:73]
s_add_u32 s72, s72, .Lk1_aret18-.Lk1_atr18-4
s_addc_u32 s73, s73, 0
s_mov_b32 s70, 24576
s_branch .Lk1_arare0
.Lk1_atr19:
s_getpc_b64 s[72:73]
s_add_u32 s72, s72, .Lk1_aret19-.Lk1_atr19-4
s_addc_u32 s73, s73, 0
s_mov_b32 s70, 28672
s_branch .Lk1_arare1
.Lk1_atr20:
s_getpc_b64 s[72:73]
s_add_u32 s72, s72, .Lk1_aret20-.Lk1_atr20-4
s_addc_u32 s73, s73, 0
s_mov_b32 s70, 147456
s_branch .Lk1_arare0
.Lk1_atr21:
s_getpc_b64 s[72:73]
s_add_u32 s72, s72, .Lk1_aret21-.Lk1_atr21-4
s_addc_u32 s73, s73, 0
s_mov_b32 s70, 151552
s_branch .Lk1_arare1
.Lk1_atr22:
s_getpc_b64 s[72:73]
s_add_u32 s72, s72, .Lk1_aret22-.Lk1_atr22-4
s_addc_u32 s73, s73, 0
s_mov_b32 s70, 155648
s_branch .Lk1_arare0
.Lk1_atr23:
s_getpc_b64 s[72:73]
s_add_u32 s72, s72, .Lk1_aret23-.Lk1_atr23-4
s_addc_u32 s73, s73, 0
s_mov_b32 s70, 159744
s_branch .Lk1_arare1
.Lk1_atr24:
s_getpc_b64 s[72:73]
s_add_u32 s72, s72, .Lk1_aret24-.Lk1_atr24-4
s_addc_u32 s73, s73, 0
s_mov_b32 s70, 98304
s_branch .Lk1_arare0
.Lk1_atr25:
s_getpc_b64 s[72:73]
s_add_u32 s72, s72, .Lk1_aret25-.Lk1_atr25-4
s_addc_u32 s73, s73, 0
s_mov_b32 s70, 102400
s_branch .Lk1_arare1
.Lk1_atr26:
s_getpc_b64 s[72:73]
s_add_u32 s72, s72, .Lk1_aret26-.Lk1_atr26-4
s_addc_u32 s73, s73, 0
s_mov_b32 s70, 106496
s_branch .Lk1_arare0
.Lk1_atr27:
s_getpc_b64 s[72:73]
s_add_u32 s72, s72, .Lk1_aret27-.Lk1_atr27-4
s_addc_u32 s73, s73, 0
s_mov_b32 s70, 110592
s_branch .Lk1_arare1
.Lk1_atr28:
s_getpc_b64 s[72:73]
s_add_u32 s72, s72, .Lk1_aret28-.Lk1_atr28-4
s_addc_u32 s73, s73, 0
s_mov_b32 s70, 114688
s_branch .Lk1_arare0
.Lk1_atr29:
s_getpc_b64 s[72:73]
s_add_u32 s72, s72, .Lk1_aret29-.Lk1_atr29-4
s_addc_u32 s73, s73, 0
s_mov_b32 s70, 118784
s_branch .Lk1_arare1
.Lk1_atr30:
s_getpc_b64 s[72:73]
s_add_u32 s72, s72, .Lk1_aret30-.Lk1_atr30-4
s_addc_u32 s73, s73, 0
s_mov_b32 s70, 122880
s_branch .Lk1_arare0
.Lk1_atr31:
s_getpc_b64 s[72:73]
s_add_u32 s72, s72, .Lk1_aret31-.Lk1_atr31-4
s_addc_u32 s73, s73, 0
s_mov_b32 s70, 126976
s_branch .Lk1_arare1
.Lk1_arare0:
v_add_u32 v8, s70, v1
ds_read_b128 v[16:19], v8
rqv 20,8,512
s_waitcnt lgkmcnt(0)
v_mfma_f32_32x32x16_bf16 v[48:63], v[16:19], v[64:67], 0
v_mfma_f32_32x32x16_bf16 v[80:95], v[16:19], v[72:75], 0
v_mfma_f32_32x32x16_bf16 v[48:63], v[20:23], v[68:71], v[48:63]
v_mfma_f32_32x32x16_bf16 v[80:95], v[20:23], v[76:79], v[80:95]
s_nop 15
m3 8,48,49,50
m3 9,51,52,53
m3 9,9,54,55
m3 8,8,56,57
m3 9,9,58,59
m3 8,8,60,61
m3 9,9,62,63
v_max_f32 v8, v8, v9
v_mov_b32 v9, v8
s_nop 1
v_permlane32_swap_b32 v8, v9
s_nop 0
v_max_f32 v8, v8, v9
v_max_f32 v8, 0, v8
v_add_f32 v8, v8, v4
cv 24,8,8
v_and_b32 v24, 0xffff0000, v24
v_sub_f32 v8, v8, v24
cv 25,8,8
v_and_b32 v25, 0xffff0000, v25
v_sub_f32 v8, v25, v8
v_cvt_pk_bf16_f32 v26, -v24, -v25
v_cvt_pk_bf16_f32 v27, v8, 0
v_add_f32 v24, v24, v25
v_lshlrev_b32 v8, 16, v27
v_sub_f32 v8, v24, v8
v_sub_f32 v9, v8, v4
v_mov_b32 v4, v8
v_sub_f32 v10, 0, v9
ex 10
v_cndmask_b32_e64 v70, v70, v26, s[76:77]
v_cndmask_b32_e64 v71, v71, v27, s[76:77]
v_sub_f32 v48, v48, v9
v_sub_f32 v49, v49, v9
v_sub_f32 v50, v50, v9
v_sub_f32 v51, v51, v9
v_sub_f32 v52, v52, v9
v_sub_f32 v53, v53, v9
v_sub_f32 v54, v54, v9
v_sub_f32 v55, v55, v9
v_sub_f32 v56, v56, v9
v_sub_f32 v57, v57, v9
v_sub_f32 v58, v58, v9
v_sub_f32 v59, v59, v9
v_sub_f32 v60, v60, v9
v_sub_f32 v61, v61, v9
v_sub_f32 v62, v62, v9
v_sub_f32 v63, v63, v9
v_sub_f32 v96, v96, v9
v_sub_f32 v97, v97, v9
v_sub_f32 v98, v98, v9
v_sub_f32 v99, v99, v9
v_sub_f32 v100, v100, v9
v_sub_f32 v101, v101, v9
v_sub_f32 v102, v102, v9
v_sub_f32 v103, v103, v9
v_sub_f32 v104, v104, v9
v_sub_f32 v105, v105, v9
v_sub_f32 v106, v106, v9
v_sub_f32 v107, v107, v9
v_sub_f32 v108, v108, v9
v_sub_f32 v109, v109, v9
v_sub_f32 v110, v110, v9
v_sub_f32 v111, v111, v9
ar 8,64
v_mul_f32 v8, v10, v8
v_accvgpr_write_b32 a64, v8
ar 9,65
v_mul_f32 v9, v10, v9
v_accvgpr_write_b32 a65, v9
ar 8,66
v_mul_f32 v8, v10, v8
v_accvgpr_write_b32 a66, v8
ar 9,67
v_mul_f32 v9, v10, v9
v_accvgpr_write_b32 a67, v9
ar 8,68
v_mul_f32 v8, v10, v8
v_accvgpr_write_b32 a68, v8
ar 9,69
v_mul_f32 v9, v10, v9
v_accvgpr_write_b32 a69, v9
ar 8,70
v_mul_f32 v8, v10, v8
v_accvgpr_write_b32 a70, v8
ar 9,71
v_mul_f32 v9, v10, v9
v_accvgpr_write_b32 a71, v9
ar 8,72
v_mul_f32 v8, v10, v8
v_accvgpr_write_b32 a72, v8
ar 9,73
v_mul_f32 v9, v10, v9
v_accvgpr_write_b32 a73, v9
ar 8,74
v_mul_f32 v8, v10, v8
v_accvgpr_write_b32 a74, v8
ar 9,75
v_mul_f32 v9, v10, v9
v_accvgpr_write_b32 a75, v9
ar 8,76
v_mul_f32 v8, v10, v8
v_accvgpr_write_b32 a76, v8
ar 9,77
v_mul_f32 v9, v10, v9
v_accvgpr_write_b32 a77, v9
ar 8,78
v_mul_f32 v8, v10, v8
v_accvgpr_write_b32 a78, v8
ar 9,79
v_mul_f32 v9, v10, v9
v_accvgpr_write_b32 a79, v9
ex 48
ex 49
ex 50
ex 51
ex 52
ex 53
ex 54
ex 55
ex 56
ex 57
ex 58
ex 59
ex 60
ex 61
ex 62
ex 63
s_nop 0
cv 16,48,49
cv 17,50,51
cv 18,52,53
cv 19,54,55
cv 20,56,57
cv 21,58,59
cv 22,60,61
cv 23,62,63
)ASM" ::: "memory");
  asm volatile(R"ASM(
m3 8,80,81,82
m3 9,83,84,85
m3 9,9,86,87
m3 8,8,88,89
m3 9,9,90,91
m3 8,8,92,93
m3 9,9,94,95
v_max_f32 v8, v8, v9
v_mov_b32 v9, v8
s_nop 1
v_permlane32_swap_b32 v8, v9
s_nop 0
v_max_f32 v8, v8, v9
v_max_f32 v8, 0, v8
v_add_f32 v8, v8, v5
cv 24,8,8
v_and_b32 v24, 0xffff0000, v24
v_sub_f32 v8, v8, v24
cv 25,8,8
v_and_b32 v25, 0xffff0000, v25
v_sub_f32 v8, v25, v8
v_cvt_pk_bf16_f32 v26, -v24, -v25
v_cvt_pk_bf16_f32 v27, v8, 0
v_add_f32 v24, v24, v25
v_lshlrev_b32 v8, 16, v27
v_sub_f32 v8, v24, v8
v_sub_f32 v9, v8, v5
v_mov_b32 v5, v8
v_sub_f32 v10, 0, v9
ex 10
v_cndmask_b32_e64 v78, v78, v26, s[76:77]
v_cndmask_b32_e64 v79, v79, v27, s[76:77]
v_sub_f32 v80, v80, v9
v_sub_f32 v81, v81, v9
v_sub_f32 v82, v82, v9
v_sub_f32 v83, v83, v9
v_sub_f32 v84, v84, v9
v_sub_f32 v85, v85, v9
v_sub_f32 v86, v86, v9
v_sub_f32 v87, v87, v9
v_sub_f32 v88, v88, v9
v_sub_f32 v89, v89, v9
v_sub_f32 v90, v90, v9
v_sub_f32 v91, v91, v9
v_sub_f32 v92, v92, v9
v_sub_f32 v93, v93, v9
v_sub_f32 v94, v94, v9
v_sub_f32 v95, v95, v9
v_sub_f32 v112, v112, v9
v_sub_f32 v113, v113, v9
v_sub_f32 v114, v114, v9
v_sub_f32 v115, v115, v9
v_sub_f32 v116, v116, v9
v_sub_f32 v117, v117, v9
v_sub_f32 v118, v118, v9
v_sub_f32 v119, v119, v9
v_sub_f32 v120, v120, v9
v_sub_f32 v121, v121, v9
v_sub_f32 v122, v122, v9
v_sub_f32 v123, v123, v9
v_sub_f32 v124, v124, v9
v_sub_f32 v125, v125, v9
v_sub_f32 v126, v126, v9
v_sub_f32 v127, v127, v9
ar 8,80
v_mul_f32 v8, v10, v8
v_accvgpr_write_b32 a80, v8
ar 9,81
v_mul_f32 v9, v10, v9
v_accvgpr_write_b32 a81, v9
ar 8,82
v_mul_f32 v8, v10, v8
v_accvgpr_write_b32 a82, v8
ar 9,83
v_mul_f32 v9, v10, v9
v_accvgpr_write_b32 a83, v9
ar 8,84
v_mul_f32 v8, v10, v8
v_accvgpr_write_b32 a84, v8
ar 9,85
v_mul_f32 v9, v10, v9
v_accvgpr_write_b32 a85, v9
ar 8,86
v_mul_f32 v8, v10, v8
v_accvgpr_write_b32 a86, v8
ar 9,87
v_mul_f32 v9, v10, v9
v_accvgpr_write_b32 a87, v9
ar 8,88
v_mul_f32 v8, v10, v8
v_accvgpr_write_b32 a88, v8
ar 9,89
v_mul_f32 v9, v10, v9
v_accvgpr_write_b32 a89, v9
ar 8,90
v_mul_f32 v8, v10, v8
v_accvgpr_write_b32 a90, v8
ar 9,91
v_mul_f32 v9, v10, v9
v_accvgpr_write_b32 a91, v9
ar 8,92
v_mul_f32 v8, v10, v8
v_accvgpr_write_b32 a92, v8
ar 9,93
v_mul_f32 v9, v10, v9
v_accvgpr_write_b32 a93, v9
ar 8,94
v_mul_f32 v8, v10, v8
v_accvgpr_write_b32 a94, v8
ar 9,95
v_mul_f32 v9, v10, v9
v_accvgpr_write_b32 a95, v9
ex 80
ex 81
ex 82
ex 83
ex 84
ex 85
ex 86
ex 87
ex 88
ex 89
ex 90
ex 91
ex 92
ex 93
ex 94
ex 95
s_nop 0
cv 24,80,81
cv 25,82,83
cv 26,84,85
cv 27,86,87
cv 28,88,89
cv 29,90,91
cv 30,92,93
cv 31,94,95
s_nop 1
s_setpc_b64 s[72:73]
.Lk1_arare1:
v_add_u32 v8, s70, v1
ds_read_b128 v[32:35], v8
rqv 36,8,512
s_waitcnt lgkmcnt(0)
v_mfma_f32_32x32x16_bf16 v[96:111], v[32:35], v[64:67], 0
v_mfma_f32_32x32x16_bf16 v[112:127], v[32:35], v[72:75], 0
v_mfma_f32_32x32x16_bf16 v[96:111], v[36:39], v[68:71], v[96:111]
v_mfma_f32_32x32x16_bf16 v[112:127], v[36:39], v[76:79], v[112:127]
s_nop 15
m3 8,96,97,98
m3 9,99,100,101
m3 9,9,102,103
m3 8,8,104,105
m3 9,9,106,107
m3 8,8,108,109
m3 9,9,110,111
v_max_f32 v8, v8, v9
v_mov_b32 v9, v8
s_nop 1
v_permlane32_swap_b32 v8, v9
s_nop 0
v_max_f32 v8, v8, v9
v_max_f32 v8, 0, v8
v_add_f32 v8, v8, v4
cv 40,8,8
v_and_b32 v40, 0xffff0000, v40
v_sub_f32 v8, v8, v40
cv 41,8,8
v_and_b32 v41, 0xffff0000, v41
v_sub_f32 v8, v41, v8
v_cvt_pk_bf16_f32 v42, -v40, -v41
v_cvt_pk_bf16_f32 v43, v8, 0
v_add_f32 v40, v40, v41
v_lshlrev_b32 v8, 16, v43
v_sub_f32 v8, v40, v8
v_sub_f32 v9, v8, v4
v_mov_b32 v4, v8
v_sub_f32 v10, 0, v9
ex 10
v_cndmask_b32_e64 v70, v70, v42, s[76:77]
v_cndmask_b32_e64 v71, v71, v43, s[76:77]
v_sub_f32 v96, v96, v9
v_sub_f32 v97, v97, v9
v_sub_f32 v98, v98, v9
v_sub_f32 v99, v99, v9
v_sub_f32 v100, v100, v9
v_sub_f32 v101, v101, v9
v_sub_f32 v102, v102, v9
v_sub_f32 v103, v103, v9
v_sub_f32 v104, v104, v9
v_sub_f32 v105, v105, v9
v_sub_f32 v106, v106, v9
v_sub_f32 v107, v107, v9
v_sub_f32 v108, v108, v9
v_sub_f32 v109, v109, v9
v_sub_f32 v110, v110, v9
v_sub_f32 v111, v111, v9
v_sub_f32 v48, v48, v9
v_sub_f32 v49, v49, v9
v_sub_f32 v50, v50, v9
v_sub_f32 v51, v51, v9
v_sub_f32 v52, v52, v9
v_sub_f32 v53, v53, v9
v_sub_f32 v54, v54, v9
v_sub_f32 v55, v55, v9
v_sub_f32 v56, v56, v9
v_sub_f32 v57, v57, v9
v_sub_f32 v58, v58, v9
v_sub_f32 v59, v59, v9
v_sub_f32 v60, v60, v9
v_sub_f32 v61, v61, v9
v_sub_f32 v62, v62, v9
v_sub_f32 v63, v63, v9
ar 8,64
v_mul_f32 v8, v10, v8
v_accvgpr_write_b32 a64, v8
ar 9,65
v_mul_f32 v9, v10, v9
v_accvgpr_write_b32 a65, v9
ar 8,66
v_mul_f32 v8, v10, v8
v_accvgpr_write_b32 a66, v8
ar 9,67
v_mul_f32 v9, v10, v9
v_accvgpr_write_b32 a67, v9
ar 8,68
v_mul_f32 v8, v10, v8
v_accvgpr_write_b32 a68, v8
ar 9,69
v_mul_f32 v9, v10, v9
v_accvgpr_write_b32 a69, v9
ar 8,70
v_mul_f32 v8, v10, v8
v_accvgpr_write_b32 a70, v8
ar 9,71
v_mul_f32 v9, v10, v9
v_accvgpr_write_b32 a71, v9
ar 8,72
v_mul_f32 v8, v10, v8
v_accvgpr_write_b32 a72, v8
ar 9,73
v_mul_f32 v9, v10, v9
v_accvgpr_write_b32 a73, v9
ar 8,74
v_mul_f32 v8, v10, v8
v_accvgpr_write_b32 a74, v8
ar 9,75
v_mul_f32 v9, v10, v9
v_accvgpr_write_b32 a75, v9
ar 8,76
v_mul_f32 v8, v10, v8
v_accvgpr_write_b32 a76, v8
ar 9,77
v_mul_f32 v9, v10, v9
v_accvgpr_write_b32 a77, v9
ar 8,78
v_mul_f32 v8, v10, v8
v_accvgpr_write_b32 a78, v8
ar 9,79
v_mul_f32 v9, v10, v9
v_accvgpr_write_b32 a79, v9
ex 96
ex 97
ex 98
ex 99
ex 100
ex 101
ex 102
ex 103
ex 104
ex 105
ex 106
ex 107
ex 108
ex 109
ex 110
ex 111
s_nop 0
cv 32,96,97
cv 33,98,99
cv 34,100,101
cv 35,102,103
cv 36,104,105
cv 37,106,107
cv 38,108,109
cv 39,110,111
m3 8,112,113,114
m3 9,115,116,117
m3 9,9,118,119
m3 8,8,120,121
m3 9,9,122,123
m3 8,8,124,125
m3 9,9,126,127
v_max_f32 v8, v8, v9
v_mov_b32 v9, v8
s_nop 1
v_permlane32_swap_b32 v8, v9
s_nop 0
v_max_f32 v8, v8, v9
v_max_f32 v8, 0, v8
v_add_f32 v8, v8, v5
cv 40,8,8
v_and_b32 v40, 0xffff0000, v40
v_sub_f32 v8, v8, v40
cv 41,8,8
v_and_b32 v41, 0xffff0000, v41
v_sub_f32 v8, v41, v8
v_cvt_pk_bf16_f32 v42, -v40, -v41
v_cvt_pk_bf16_f32 v43, v8, 0
v_add_f32 v40, v40, v41
v_lshlrev_b32 v8, 16, v43
v_sub_f32 v8, v40, v8
v_sub_f32 v9, v8, v5
v_mov_b32 v5, v8
v_sub_f32 v10, 0, v9
ex 10
v_cndmask_b32_e64 v78, v78, v42, s[76:77]
v_cndmask_b32_e64 v79, v79, v43, s[76:77]
v_sub_f32 v112, v112, v9
v_sub_f32 v113, v113, v9
v_sub_f32 v114, v114, v9
v_sub_f32 v115, v115, v9
v_sub_f32 v116, v116, v9
v_sub_f32 v117, v117, v9
v_sub_f32 v118, v118, v9
v_sub_f32 v119, v119, v9
v_sub_f32 v120, v120, v9
v_sub_f32 v121, v121, v9
v_sub_f32 v122, v122, v9
v_sub_f32 v123, v123, v9
v_sub_f32 v124, v124, v9
v_sub_f32 v125, v125, v9
v_sub_f32 v126, v126, v9
v_sub_f32 v127, v127, v9
v_sub_f32 v80, v80, v9
v_sub_f32 v81, v81, v9
v_sub_f32 v82, v82, v9
v_sub_f32 v83, v83, v9
v_sub_f32 v84, v84, v9
v_sub_f32 v85, v85, v9
v_sub_f32 v86, v86, v9
v_sub_f32 v87, v87, v9
v_sub_f32 v88, v88, v9
v_sub_f32 v89, v89, v9
v_sub_f32 v90, v90, v9
v_sub_f32 v91, v91, v9
v_sub_f32 v92, v92, v9
v_sub_f32 v93, v93, v9
v_sub_f32 v94, v94, v9
v_sub_f32 v95, v95, v9
ar 8,80
v_mul_f32 v8, v10, v8
v_accvgpr_write_b32 a80, v8
ar 9,81
v_mul_f32 v9, v10, v9
v_accvgpr_write_b32 a81, v9
ar 8,82
v_mul_f32 v8, v10, v8
v_accvgpr_write_b32 a82, v8
ar 9,83
v_mul_f32 v9, v10, v9
v_accvgpr_write_b32 a83, v9
ar 8,84
v_mul_f32 v8, v10, v8
v_accvgpr_write_b32 a84, v8
ar 9,85
v_mul_f32 v9, v10, v9
v_accvgpr_write_b32 a85, v9
ar 8,86
v_mul_f32 v8, v10, v8
v_accvgpr_write_b32 a86, v8
ar 9,87
v_mul_f32 v9, v10, v9
v_accvgpr_write_b32 a87, v9
ar 8,88
v_mul_f32 v8, v10, v8
v_accvgpr_write_b32 a88, v8
ar 9,89
v_mul_f32 v9, v10, v9
v_accvgpr_write_b32 a89, v9
ar 8,90
v_mul_f32 v8, v10, v8
v_accvgpr_write_b32 a90, v8
ar 9,91
v_mul_f32 v9, v10, v9
v_accvgpr_write_b32 a91, v9
ar 8,92
v_mul_f32 v8, v10, v8
v_accvgpr_write_b32 a92, v8
ar 9,93
v_mul_f32 v9, v10, v9
v_accvgpr_write_b32 a93, v9
ar 8,94
v_mul_f32 v8, v10, v8
v_accvgpr_write_b32 a94, v8
ar 9,95
v_mul_f32 v9, v10, v9
v_accvgpr_write_b32 a95, v9
ex 112
ex 113
ex 114
ex 115
ex 116
ex 117
ex 118
ex 119
ex 120
ex 121
ex 122
ex 123
ex 124
ex 125
ex 126
ex 127
s_nop 0
cv 40,112,113
cv 41,114,115
cv 42,116,117
cv 43,118,119
cv 44,120,121
cv 45,122,123
cv 46,124,125
cv 47,126,127
s_nop 1
s_setpc_b64 s[72:73]
.Lk1_BT:
s_waitcnt vmcnt(21) lgkmcnt(0)
s_barrier
mavv 0,96,24
rt 64,4,0
mavv 4,100,24
s_add_u32 m0, s33, 49152
mavv 8,104,24
rt 66,4,2048
mavv 12,108,24
s_mov_b32 s67, s29
mavv 16,112,24
rt 68,5,0
mavv 20,116,24
dm 14,24,67
mavv 24,120,24
rt 70,5,2048
mavv 28,124,24
s_add_u32 m0, s32, 32768
mavv 32,96,28
rt 72,6,0
mavv 36,100,28
rqv 24,2,0
mavv 40,104,28
s_mov_b32 s61, s28
mavv 44,108,28
rt 74,6,2048
mavv 48,112,28
dm 12,20,61
mavv 52,116,28
rt 76,7,0
mavv 56,120,28
s_add_u32 m0, s33, 50176
mavv 60,124,28
rt 78,7,2048
mavv 64,96,32
rt 80,8,0
mavv 68,100,32
rqv 28,2,2048
mavv 72,104,32
s_add_u32 s67, s67, s31
mavv 76,108,32
rt 82,8,2048
mavv 80,112,32
dm 15,24,67
mavv 84,116,32
rt 84,9,0
mavv 88,120,32
s_add_u32 m0, s32, 33792
mavv 92,124,32
rt 86,9,2048
mavv 96,96,36
s_add_u32 s61, s61, s30
mavv 100,100,36
rt 88,10,0
mavv 104,104,36
rqv 32,2,4096
mavv 108,108,36
dm 13,20,61
mavv 112,112,36
rt 90,10,2048
mavv 116,116,36
s_add_u32 m0, s33, 51200
mavv 120,120,36
rt 92,11,0
mavv 124,124,36
s_add_u32 s67, s67, s31
mavv 128,96,40
rt 94,11,2048
mavv 132,100,40
rqv 36,2,6144
mavv 136,104,40
dm 14,24,67
mavv 140,108,40
rqv 48,2,12288
mavv 144,112,40
s_add_u32 m0, s32, 34816
mavv 148,116,40
s_add_u32 s61, s61, s30
mavv 152,120,40
mavv 156,124,40
dm 12,20,61
mavv 160,96,44
rqv 40,2,8192
mavv 164,100,44
rqv 52,2,14336
mavv 168,104,44
s_add_u32 m0, s33, 52224
mavv 172,108,44
s_add_u32 s67, s67, s31
mavv 176,112,44
mavv 180,116,44
dm 15,24,67
mavv 184,120,44
mavv 188,124,44
mavv 192,96,56
rqv 44,2,10240
mavv 196,100,56
s_add_u32 m0, s32, 35840
mavv 200,104,56
s_add_u32 s61, s61, s30
mavv 204,108,56
mavv 208,112,56
dm 13,20,61
mavv 212,116,56
mavv 216,120,56
mavv 220,124,56
s_add_u32 s29, s29, s65
mavv 224,96,60
mavv 228,100,60
s_add_u32 s74, s78, .Lk1_BT+8192-.Lk1_base
s_addc_u32 s75, s79, 0
mavv 232,104,60
mavv 236,108,60
global_load_dword v19, v236, s[74:75]
mavv 240,112,60
mavv 244,116,60
mavv 248,120,60
mavv 252,124,60
s_waitcnt vmcnt(20) lgkmcnt(0)
s_barrier
mavv 0,64,24
rt 96,4,16384
mavv 4,68,24
s_add_u32 m0, s33, 0
mavv 8,72,24
rt 98,4,18432
mavv 12,76,24
s_mov_b32 s67, s29
mavv 16,80,24
rt 100,5,16384
mavv 20,84,24
dm 14,24,67
mavv 24,88,24
rt 102,5,18432
mavv 28,92,24
s_add_u32 m0, s32, 49152
mavv 32,64,28
rt 104,6,16384
mavv 36,68,28
rqv 24,3,0
mavv 40,72,28
s_add_u32 s61, s28, s64
mavv 44,76,28
rt 106,6,18432
mavv 48,80,28
dm 12,20,61
mavv 52,84,28
rt 108,7,16384
mavv 56,88,28
rt 110,7,18432
mavv 60,92,28
s_add_u32 m0, s33, 1024
mavv 64,64,32
rt 112,8,16384
mavv 68,68,32
rqv 28,3,2048
mavv 72,72,32
s_add_u32 s67, s67, s31
mavv 76,76,32
rt 114,8,18432
mavv 80,80,32
dm 15,24,67
mavv 84,84,32
rt 116,9,16384
mavv 88,88,32
s_add_u32 m0, s32, 50176
mavv 92,92,32
rt 118,9,18432
mavv 96,64,36
s_add_u32 s61, s61, s30
mavv 100,68,36
rt 120,10,16384
mavv 104,72,36
rqv 32,3,4096
)ASM" ::: "memory");
  asm volatile(R"ASM(
mavv 108,76,36
rt 122,10,18432
mavv 112,80,36
dm 13,20,61
mavv 116,84,36
rt 124,11,16384
mavv 120,88,36
s_add_u32 m0, s33, 2048
mavv 124,92,36
rt 126,11,18432
mavv 128,64,40
s_add_u32 s67, s67, s31
mavv 132,68,40
rqv 36,3,6144
mavv 136,72,40
rqv 56,3,12288
mavv 140,76,40
dm 14,24,67
mavv 144,80,40
s_add_u32 m0, s32, 51200
mavv 148,84,40
mavv 152,88,40
s_add_u32 s61, s61, s30
mavv 156,92,40
mavv 160,64,44
rqv 40,3,8192
mavv 164,68,44
dm 12,20,61
mavv 168,72,44
rqv 60,3,14336
mavv 172,76,44
s_add_u32 m0, s33, 3072
mavv 176,80,44
mavv 180,84,44
s_add_u32 s67, s67, s31
mavv 184,88,44
mavv 188,92,44
dm 15,24,67
mavv 192,64,48
rqv 44,3,10240
mavv 196,68,48
mavv 200,72,48
s_add_u32 m0, s32, 52224
mavv 204,76,48
mavv 208,80,48
s_add_u32 s61, s61, s30
mavv 212,84,48
mavv 216,88,48
dm 13,20,61
mavv 220,92,48
mavv 224,64,52
mavv 228,68,52
s_add_u32 s29, s29, s65
mavv 232,72,52
mavv 236,76,52
s_add_u32 s28, s28, 128
mavv 240,80,52
mavv 244,84,52
mavv 248,88,52
mavv 252,92,52
s_waitcnt vmcnt(17) lgkmcnt(0)
s_barrier
s_cmp_lg_u32 s49, 0
s_cbranch_scc1 .Lk1_BT_nf
v_mov_b32 v20, s59
v_mov_b32 v21, s82
s_mov_b64 exec, 1
global_store_dword v20, v21, s[4:5] sc1
s_mov_b64 exec, -1
.Lk1_BT_nf:
mavv 0,96,24
rt 64,4,32768
mavv 4,100,24
s_add_u32 m0, s33, 16384
mavv 8,104,24
rt 66,4,34816
mavv 12,108,24
s_mov_b32 s67, s29
mavv 16,112,24
rt 68,5,32768
mavv 20,116,24
dm 14,24,67
mavv 24,120,24
rt 70,5,34816
mavv 28,124,24
s_add_u32 m0, s32, 65536
mavv 32,96,28
rt 72,6,32768
mavv 36,100,28
rqv 24,0,0
mavv 40,104,28
s_mov_b32 s61, s28
mavv 44,108,28
rt 74,6,34816
mavv 48,112,28
rt 76,7,32768
mavv 52,116,28
dm 12,20,61
mavv 56,120,28
rt 78,7,34816
mavv 60,124,28
s_add_u32 m0, s33, 17408
mavv 64,96,32
rt 80,8,32768
mavv 68,100,32
rqv 28,0,2048
mavv 72,104,32
s_add_u32 s67, s67, s31
mavv 76,108,32
rt 82,8,34816
mavv 80,112,32
dm 15,24,67
mavv 84,116,32
rt 84,9,32768
mavv 88,120,32
rt 86,9,34816
mavv 92,124,32
s_add_u32 m0, s32, 66560
mavv 96,96,36
rt 88,10,32768
mavv 100,100,36
rqv 32,0,4096
mavv 104,104,36
s_add_u32 s61, s61, s30
mavv 108,108,36
rt 90,10,34816
mavv 112,112,36
dm 13,20,61
mavv 116,116,36
rt 92,11,32768
mavv 120,120,36
s_add_u32 m0, s33, 18432
mavv 124,124,36
rt 94,11,34816
mavv 128,96,40
rqv 36,0,6144
mavv 132,100,40
s_add_u32 s67, s67, s31
mavv 136,104,40
rqv 48,0,12288
mavv 140,108,40
dm 14,24,67
mavv 144,112,40
mavv 148,116,40
s_add_u32 m0, s32, 67584
mavv 152,120,40
mavv 156,124,40
s_add_u32 s61, s61, s30
mavv 160,96,44
rqv 40,0,8192
mavv 164,100,44
rqv 52,0,14336
mavv 168,104,44
dm 12,20,61
mavv 172,108,44
mavv 176,112,44
s_add_u32 m0, s33, 19456
mavv 180,116,44
mavv 184,120,44
mavv 188,124,44
s_add_u32 s67, s67, s31
mavv 192,96,56
rqv 44,0,10240
mavv 196,100,56
dm 15,24,67
mavv 200,104,56
mavv 204,108,56
mavv 208,112,56
s_add_u32 m0, s32, 68608
mavv 212,116,56
mavv 216,120,56
s_add_u32 s61, s61, s30
mavv 220,124,56
mavv 224,96,60
mavv 228,100,60
dm 13,20,61
mavv 232,104,60
mavv 236,108,60
s_add_u32 s29, s29, s65
mavv 240,112,60
mavv 244,116,60
mavv 248,120,60
mavv 252,124,60
s_waitcnt vmcnt(16) lgkmcnt(0)
s_barrier
mavv 0,64,24
rt 96,4,49152
mavv 4,68,24
s_add_u32 m0, s33, 32768
mavv 8,72,24
rt 98,4,51200
mavv 12,76,24
s_mov_b32 s67, s29
mavv 16,80,24
rt 100,5,49152
mavv 20,84,24
dm 14,24,67
mavv 24,88,24
rt 102,5,51200
mavv 28,92,24
s_add_u32 m0, s32, 81920
mavv 32,64,28
rt 104,6,49152
mavv 36,68,28
rqv 24,1,0
mavv 40,72,28
s_add_u32 s61, s28, s64
mavv 44,76,28
rt 106,6,51200
mavv 48,80,28
dm 12,20,61
mavv 52,84,28
rt 108,7,49152
mavv 56,88,28
s_add_u32 m0, s33, 33792
mavv 60,92,28
rt 110,7,51200
mavv 64,64,32
s_add_u32 s67, s67, s31
mavv 68,68,32
rt 112,8,49152
mavv 72,72,32
rqv 28,1,2048
mavv 76,76,32
dm 15,24,67
mavv 80,80,32
rt 114,8,51200
mavv 84,84,32
s_add_u32 m0, s32, 82944
mavv 88,88,32
rt 116,9,49152
mavv 92,92,32
s_add_u32 s61, s61, s30
mavv 96,64,36
rt 118,9,51200
dm 13,20,61
mavv 100,68,36
rt 120,10,49152
mavv 104,72,36
rqv 32,1,4096
mavv 108,76,36
s_add_u32 m0, s33, 34816
mavv 112,80,36
rt 122,10,51200
mavv 116,84,36
s_add_u32 s67, s67, s31
mavv 120,88,36
rt 124,11,49152
mavv 124,92,36
dm 14,24,67
mavv 128,64,40
rt 126,11,51200
mavv 132,68,40
rqv 36,1,6144
mavv 136,72,40
s_add_u32 m0, s32, 83968
mavv 140,76,40
rqv 56,1,12288
mavv 144,80,40
s_add_u32 s61, s61, s30
mavv 148,84,40
dm 12,20,61
mavv 152,88,40
s_add_u32 m0, s33, 35840
mavv 156,92,40
mavv 160,64,44
rqv 40,1,8192
mavv 164,68,44
s_add_u32 s67, s67, s31
mavv 168,72,44
rqv 60,1,14336
mavv 172,76,44
dm 15,24,67
mavv 176,80,44
s_add_u32 m0, s32, 84992
mavv 180,84,44
mavv 184,88,44
mavv 188,92,44
s_add_u32 s61, s61, s30
mavv 192,64,48
rqv 44,1,10240
mavv 196,68,48
dm 13,20,61
mavv 200,72,48
mavv 204,76,48
s_add_u32 s29, s29, s65
mavv 208,80,48
mavv 212,84,48
s_add_u32 s28, s28, 128
mavv 216,88,48
mavv 220,92,48
s_add_u32 s74, s78, .Lk1_BT+12288-.Lk1_base
s_addc_u32 s75, s79, 0
mavv 224,64,52
mavv 228,68,52
global_load_dword v19, v236, s[74:75]
mavv 232,72,52
mavv 236,76,52
global_load_dword v19, v254, s[72:73] offset:0
mavv 240,80,52
mavv 244,84,52
mavv 248,88,52
mavv 252,92,52
s_waitcnt vmcnt(18) lgkmcnt(0)
s_barrier
mavv 0,96,24
rt 64,4,0
mavv 4,100,24
s_add_u32 m0, s33, 49152
mavv 8,104,24
rt 66,4,2048
mavv 12,108,24
s_mov_b32 s67, s29
mavv 16,112,24
rt 68,5,0
mavv 20,116,24
dm 14,24,67
mavv 24,120,24
rt 70,5,2048
mavv 28,124,24
s_add_u32 m0, s32, 0
mavv 32,96,28
rt 72,6,0
mavv 36,100,28
rqv 24,0,32768
mavv 40,104,28
s_mov_b32 s61, s28
mavv 44,108,28
rt 74,6,2048
mavv 48,112,28
rt 76,7,0
mavv 52,116,28
dm 12,20,61
mavv 56,120,28
rt 78,7,2048
mavv 60,124,28
s_add_u32 m0, s33, 50176
mavv 64,96,32
rt 80,8,0
mavv 68,100,32
rqv 28,0,34816
mavv 72,104,32
s_add_u32 s67, s67, s31
mavv 76,108,32
rt 82,8,2048
mavv 80,112,32
dm 15,24,67
mavv 84,116,32
rt 84,9,0
mavv 88,120,32
rt 86,9,2048
mavv 92,124,32
s_add_u32 m0, s32, 1024
mavv 96,96,36
rt 88,10,0
mavv 100,100,36
rqv 32,0,36864
mavv 104,104,36
s_add_u32 s61, s61, s30
mavv 108,108,36
rt 90,10,2048
mavv 112,112,36
dm 13,20,61
mavv 116,116,36
rt 92,11,0
mavv 120,120,36
s_add_u32 m0, s33, 51200
mavv 124,124,36
rt 94,11,2048
mavv 128,96,40
rqv 36,0,38912
mavv 132,100,40
s_add_u32 s67, s67, s31
mavv 136,104,40
rqv 48,0,45056
mavv 140,108,40
dm 14,24,67
mavv 144,112,40
mavv 148,116,40
s_add_u32 m0, s32, 2048
mavv 152,120,40
mavv 156,124,40
s_add_u32 s61, s61, s30
mavv 160,96,44
rqv 40,0,40960
mavv 164,100,44
rqv 52,0,47104
mavv 168,104,44
dm 12,20,61
mavv 172,108,44
mavv 176,112,44
s_add_u32 m0, s33, 52224
mavv 180,116,44
mavv 184,120,44
mavv 188,124,44
s_add_u32 s67, s67, s31
mavv 192,96,56
rqv 44,0,43008
mavv 196,100,56
dm 15,24,67
mavv 200,104,56
mavv 204,108,56
mavv 208,112,56
s_add_u32 m0, s32, 3072
mavv 212,116,56
mavv 216,120,56
s_add_u32 s61, s61, s30
mavv 220,124,56
mavv 224,96,60
mavv 228,100,60
dm 13,20,61
mavv 232,104,60
mavv 236,108,60
s_add_u32 s29, s29, s65
mavv 240,112,60
mavv 244,116,60
mavv 248,120,60
mavv 252,124,60
s_waitcnt vmcnt(18) lgkmcnt(0)
s_barrier
mavv 0,64,24
rt 96,4,16384
mavv 4,68,24
s_add_u32 m0, s33, 0
mavv 8,72,24
rt 98,4,18432
mavv 12,76,24
s_mov_b32 s67, s29
mavv 16,80,24
rt 100,5,16384
mavv 20,84,24
dm 14,24,67
mavv 24,88,24
rt 102,5,18432
mavv 28,92,24
s_add_u32 m0, s32, 16384
mavv 32,64,28
rt 104,6,16384
mavv 36,68,28
rqv 24,1,32768
mavv 40,72,28
s_add_u32 s61, s28, s64
mavv 44,76,28
rt 106,6,18432
mavv 48,80,28
dm 12,20,61
mavv 52,84,28
rt 108,7,16384
mavv 56,88,28
rt 110,7,18432
mavv 60,92,28
s_add_u32 m0, s33, 1024
mavv 64,64,32
rt 112,8,16384
mavv 68,68,32
rqv 28,1,34816
mavv 72,72,32
s_add_u32 s67, s67, s31
mavv 76,76,32
rt 114,8,18432
mavv 80,80,32
dm 15,24,67
mavv 84,84,32
rt 116,9,16384
mavv 88,88,32
s_add_u32 m0, s32, 17408
mavv 92,92,32
rt 118,9,18432
mavv 96,64,36
s_add_u32 s61, s61, s30
mavv 100,68,36
rt 120,10,16384
mavv 104,72,36
rqv 32,1,36864
mavv 108,76,36
rt 122,10,18432
mavv 112,80,36
dm 13,20,61
mavv 116,84,36
rt 124,11,16384
mavv 120,88,36
s_add_u32 m0, s33, 2048
mavv 124,92,36
rt 126,11,18432
mavv 128,64,40
s_add_u32 s67, s67, s31
mavv 132,68,40
rqv 36,1,38912
mavv 136,72,40
rqv 56,1,45056
mavv 140,76,40
dm 14,24,67
mavv 144,80,40
s_add_u32 m0, s32, 18432
mavv 148,84,40
mavv 152,88,40
s_add_u32 s61, s61, s30
mavv 156,92,40
mavv 160,64,44
rqv 40,1,40960
mavv 164,68,44
dm 12,20,61
mavv 168,72,44
rqv 60,1,47104
mavv 172,76,44
s_add_u32 m0, s33, 3072
mavv 176,80,44
mavv 180,84,44
s_add_u32 s67, s67, s31
mavv 184,88,44
mavv 188,92,44
dm 15,24,67
mavv 192,64,48
rqv 44,1,43008
mavv 196,68,48
mavv 200,72,48
s_add_u32 m0, s32, 19456
mavv 204,76,48
mavv 208,80,48
s_add_u32 s61, s61, s30
mavv 212,84,48
mavv 216,88,48
dm 13,20,61
mavv 220,92,48
mavv 224,64,52
mavv 228,68,52
s_add_u32 s29, s29, s65
mavv 232,72,52
mavv 236,76,52
s_add_u32 s28, s28, 128
mavv 240,80,52
mavv 244,84,52
mavv 248,88,52
mavv 252,92,52
s_waitcnt vmcnt(16) lgkmcnt(0)
s_barrier
global_load_dwordx4 v[224:227], v16, s[4:5] sc1
mavv 0,96,24
rt 64,4,32768
mavv 4,100,24
s_add_u32 m0, s33, 16384
mavv 8,104,24
rt 66,4,34816
mavv 12,108,24
s_mov_b32 s67, s29
mavv 16,112,24
rt 68,5,32768
mavv 20,116,24
dm 14,24,67
mavv 24,120,24
rt 70,5,34816
mavv 28,124,24
s_add_u32 m0, s32, 32768
mavv 32,96,28
rt 72,6,32768
mavv 36,100,28
rqv 24,2,0
mavv 40,104,28
s_mov_b32 s61, s28
mavv 44,108,28
rt 74,6,34816
mavv 48,112,28
dm 12,20,61
mavv 52,116,28
rt 76,7,32768
mavv 56,120,28
s_add_u32 m0, s33, 17408
mavv 60,124,28
rt 78,7,34816
mavv 64,96,32
rt 80,8,32768
mavv 68,100,32
rqv 28,2,2048
mavv 72,104,32
s_add_u32 s67, s67, s31
mavv 76,108,32
rt 82,8,34816
mavv 80,112,32
dm 15,24,67
mavv 84,116,32
rt 84,9,32768
mavv 88,120,32
s_add_u32 m0, s32, 33792
mavv 92,124,32
rt 86,9,34816
mavv 96,96,36
s_add_u32 s61, s61, s30
mavv 100,100,36
rt 88,10,32768
mavv 104,104,36
rqv 32,2,4096
mavv 108,108,36
dm 13,20,61
mavv 112,112,36
rt 90,10,34816
mavv 116,116,36
s_add_u32 m0, s33, 18432
mavv 120,120,36
rt 92,11,32768
mavv 124,124,36
)ASM" ::: "memory");
  asm volatile(R"ASM(
s_add_u32 s67, s67, s31
mavv 128,96,40
rt 94,11,34816
mavv 132,100,40
rqv 36,2,6144
mavv 136,104,40
dm 14,24,67
mavv 140,108,40
rqv 48,2,12288
mavv 144,112,40
s_add_u32 m0, s32, 34816
mavv 148,116,40
s_add_u32 s61, s61, s30
mavv 152,120,40
mavv 156,124,40
dm 12,20,61
mavv 160,96,44
rqv 40,2,8192
mavv 164,100,44
rqv 52,2,14336
mavv 168,104,44
s_add_u32 m0, s33, 19456
mavv 172,108,44
s_add_u32 s67, s67, s31
mavv 176,112,44
mavv 180,116,44
dm 15,24,67
mavv 184,120,44
mavv 188,124,44
mavv 192,96,56
rqv 44,2,10240
mavv 196,100,56
s_add_u32 m0, s32, 35840
mavv 200,104,56
s_add_u32 s61, s61, s30
mavv 204,108,56
mavv 208,112,56
dm 13,20,61
mavv 212,116,56
mavv 216,120,56
mavv 220,124,56
s_add_u32 s29, s29, s65
mavv 224,96,60
mavv 228,100,60
s_add_u32 s74, s78, .Lk1_BT+16384-.Lk1_base
s_addc_u32 s75, s79, 0
mavv 232,104,60
mavv 236,108,60
global_load_dword v19, v236, s[74:75]
mavv 240,112,60
mavv 244,116,60
mavv 248,120,60
mavv 252,124,60
s_waitcnt vmcnt(18) lgkmcnt(0)
s_barrier
mavv 0,64,24
rt 96,4,49152
mavv 4,68,24
s_add_u32 m0, s33, 32768
mavv 8,72,24
rt 98,4,51200
mavv 12,76,24
s_mov_b32 s67, s29
mavv 16,80,24
rt 100,5,49152
mavv 20,84,24
dm 14,24,67
mavv 24,88,24
rt 102,5,51200
mavv 28,92,24
s_add_u32 m0, s32, 49152
mavv 32,64,28
rt 104,6,49152
mavv 36,68,28
rqv 24,3,0
mavv 40,72,28
s_add_u32 s61, s28, s64
mavv 44,76,28
rt 106,6,51200
mavv 48,80,28
dm 12,20,61
mavv 52,84,28
rt 108,7,49152
mavv 56,88,28
s_add_u32 m0, s33, 33792
mavv 60,92,28
rt 110,7,51200
mavv 64,64,32
rt 112,8,49152
mavv 68,68,32
rqv 28,3,2048
mavv 72,72,32
s_add_u32 s67, s67, s31
mavv 76,76,32
rt 114,8,51200
mavv 80,80,32
dm 15,24,67
mavv 84,84,32
rt 116,9,49152
mavv 88,88,32
s_add_u32 m0, s32, 50176
mavv 92,92,32
rt 118,9,51200
mavv 96,64,36
s_add_u32 s61, s61, s30
mavv 100,68,36
rt 120,10,49152
mavv 104,72,36
rqv 32,3,4096
mavv 108,76,36
dm 13,20,61
mavv 112,80,36
rt 122,10,51200
mavv 116,84,36
s_add_u32 m0, s33, 34816
mavv 120,88,36
rt 124,11,49152
mavv 124,92,36
s_add_u32 s67, s67, s31
mavv 128,64,40
rt 126,11,51200
mavv 132,68,40
rqv 36,3,6144
mavv 136,72,40
dm 14,24,67
mavv 140,76,40
rqv 56,3,12288
mavv 144,80,40
s_add_u32 m0, s32, 51200
mavv 148,84,40
s_add_u32 s61, s61, s30
mavv 152,88,40
mavv 156,92,40
dm 12,20,61
mavv 160,64,44
rqv 40,3,8192
mavv 164,68,44
rqv 60,3,14336
mavv 168,72,44
s_add_u32 m0, s33, 35840
mavv 172,76,44
s_add_u32 s67, s67, s31
mavv 176,80,44
mavv 180,84,44
dm 15,24,67
mavv 184,88,44
mavv 188,92,44
mavv 192,64,48
rqv 44,3,10240
mavv 196,68,48
s_add_u32 m0, s32, 52224
mavv 200,72,48
s_add_u32 s61, s61, s30
mavv 204,76,48
mavv 208,80,48
dm 13,20,61
mavv 212,84,48
mavv 216,88,48
mavv 220,92,48
s_add_u32 s29, s29, s65
mavv 224,64,52
mavv 228,68,52
s_add_u32 s28, s28, 128
mavv 232,72,52
mavv 236,76,52
global_load_dword v19, v254, s[72:73] offset:1792
mavv 240,80,52
mavv 244,84,52
mavv 248,88,52
mavv 252,92,52
s_waitcnt vmcnt(19) lgkmcnt(0)
s_barrier
mavv 0,96,24
rt 64,4,0
mavv 4,100,24
s_add_u32 m0, s33, 49152
mavv 8,104,24
rt 66,4,2048
mavv 12,108,24
s_mov_b32 s67, s29
mavv 16,112,24
rt 68,5,0
mavv 20,116,24
dm 14,24,67
mavv 24,120,24
rt 70,5,2048
mavv 28,124,24
s_add_u32 m0, s32, 65536
mavv 32,96,28
rt 72,6,0
mavv 36,100,28
rqv 24,0,0
mavv 40,104,28
s_mov_b32 s61, s28
mavv 44,108,28
rt 74,6,2048
mavv 48,112,28
rt 76,7,0
mavv 52,116,28
dm 12,20,61
mavv 56,120,28
rt 78,7,2048
mavv 60,124,28
s_add_u32 m0, s33, 50176
mavv 64,96,32
rt 80,8,0
mavv 68,100,32
rqv 28,0,2048
mavv 72,104,32
s_add_u32 s67, s67, s31
mavv 76,108,32
rt 82,8,2048
mavv 80,112,32
dm 15,24,67
mavv 84,116,32
rt 84,9,0
mavv 88,120,32
rt 86,9,2048
mavv 92,124,32
s_add_u32 m0, s32, 66560
mavv 96,96,36
rt 88,10,0
mavv 100,100,36
rqv 32,0,4096
mavv 104,104,36
s_add_u32 s61, s61, s30
mavv 108,108,36
rt 90,10,2048
mavv 112,112,36
dm 13,20,61
mavv 116,116,36
rt 92,11,0
mavv 120,120,36
s_add_u32 m0, s33, 51200
mavv 124,124,36
rt 94,11,2048
mavv 128,96,40
rqv 36,0,6144
mavv 132,100,40
s_add_u32 s67, s67, s31
mavv 136,104,40
rqv 48,0,12288
mavv 140,108,40
dm 14,24,67
mavv 144,112,40
mavv 148,116,40
s_add_u32 m0, s32, 67584
mavv 152,120,40
mavv 156,124,40
s_add_u32 s61, s61, s30
mavv 160,96,44
rqv 40,0,8192
mavv 164,100,44
rqv 52,0,14336
mavv 168,104,44
dm 12,20,61
mavv 172,108,44
mavv 176,112,44
s_add_u32 m0, s33, 52224
mavv 180,116,44
mavv 184,120,44
mavv 188,124,44
s_add_u32 s67, s67, s31
mavv 192,96,56
rqv 44,0,10240
mavv 196,100,56
dm 15,24,67
mavv 200,104,56
mavv 204,108,56
mavv 208,112,56
s_add_u32 m0, s32, 68608
mavv 212,116,56
mavv 216,120,56
s_add_u32 s61, s61, s30
mavv 220,124,56
mavv 224,96,60
mavv 228,100,60
dm 13,20,61
mavv 232,104,60
mavv 236,108,60
s_add_u32 s29, s29, s65
mavv 240,112,60
mavv 244,116,60
mavv 248,120,60
mavv 252,124,60
s_cmp_lg_u32 s49, 0
s_cbranch_scc1 .Lk1_BT_acq
s_waitcnt vmcnt(16)
s_mov_b32 s63, 0x400000
.Lk1_BT_chk:
v_xor_b32 v224, s82, v224
v_xor_b32 v225, s82, v225
v_xor_b32 v226, s82, v226
v_xor_b32 v227, s82, v227
v_or3_b32 v224, v224, v225, v226
v_or_b32 v224, v224, v227
v_cmp_ne_u32 vcc, 0, v224
s_nop 4
s_cbranch_vccz .Lk1_BT_ok
s_sleep 8
global_load_dwordx4 v[224:227], v16, s[4:5] sc1
s_waitcnt vmcnt(0)
s_sub_u32 s63, s63, 1
s_cmp_lg_u32 s63, 0
s_cbranch_scc1 .Lk1_BT_chk
.Lk1_BT_ok:
s_cmp_lg_u32 s59, 0
s_cbranch_scc1 .Lk1_BT_acq
v_mov_b32 v20, 0
v_mov_b32 v21, s82
s_mov_b64 exec, 1
global_store_dword v20, v21, s[4:5] offset:1024 sc1
s_mov_b64 exec, -1
.Lk1_BT_acq:
s_waitcnt vmcnt(17) lgkmcnt(0)
s_barrier
mavv 0,64,24
rt 96,4,16384
mavv 4,68,24
s_add_u32 m0, s33, 0
mavv 8,72,24
rt 98,4,18432
mavv 12,76,24
s_mov_b32 s67, s29
mavv 16,80,24
rt 100,5,16384
mavv 20,84,24
dm 14,24,67
mavv 24,88,24
rt 102,5,18432
mavv 28,92,24
s_add_u32 m0, s32, 81920
mavv 32,64,28
rt 104,6,16384
mavv 36,68,28
rqv 24,1,0
mavv 40,72,28
s_add_u32 s61, s28, s64
mavv 44,76,28
rt 106,6,18432
mavv 48,80,28
dm 12,20,61
mavv 52,84,28
rt 108,7,16384
mavv 56,88,28
s_add_u32 m0, s33, 1024
mavv 60,92,28
rt 110,7,18432
mavv 64,64,32
s_add_u32 s67, s67, s31
mavv 68,68,32
rt 112,8,16384
mavv 72,72,32
rqv 28,1,2048
mavv 76,76,32
dm 15,24,67
mavv 80,80,32
rt 114,8,18432
mavv 84,84,32
s_add_u32 m0, s32, 82944
mavv 88,88,32
rt 116,9,16384
mavv 92,92,32
rt 118,9,18432
mavv 96,64,36
s_add_u32 s61, s61, s30
rt 120,10,16384
mavv 100,68,36
rqv 32,1,4096
mavv 104,72,36
dm 13,20,61
mavv 108,76,36
rt 122,10,18432
mavv 112,80,36
s_add_u32 m0, s33, 2048
mavv 116,84,36
rt 124,11,16384
mavv 120,88,36
s_add_u32 s67, s67, s31
mavv 124,92,36
rt 126,11,18432
mavv 128,64,40
dm 14,24,67
mavv 132,68,40
rqv 36,1,6144
mavv 136,72,40
rqv 56,1,12288
mavv 140,76,40
s_add_u32 m0, s32, 83968
mavv 144,80,40
s_add_u32 s61, s61, s30
mavv 148,84,40
dm 12,20,61
mavv 152,88,40
mavv 156,92,40
s_add_u32 m0, s33, 3072
mavv 160,64,44
rqv 40,1,8192
mavv 164,68,44
rqv 60,1,14336
mavv 168,72,44
s_add_u32 s67, s67, s31
mavv 172,76,44
mavv 176,80,44
dm 15,24,67
mavv 180,84,44
mavv 184,88,44
s_add_u32 m0, s32, 84992
mavv 188,92,44
mavv 192,64,48
rqv 44,1,10240
mavv 196,68,48
s_add_u32 s61, s61, s30
mavv 200,72,48
dm 13,20,61
mavv 204,76,48
mavv 208,80,48
mavv 212,84,48
s_add_u32 s29, s29, s65
mavv 216,88,48
mavv 220,92,48
s_add_u32 s28, s28, 128
mavv 224,64,52
mavv 228,68,52
s_add_u32 s74, s78, .Lk1_BT+20480-.Lk1_base
s_addc_u32 s75, s79, 0
mavv 232,72,52
mavv 236,76,52
global_load_dword v19, v236, s[74:75]
mavv 240,80,52
mavv 244,84,52
mavv 248,88,52
mavv 252,92,52
s_waitcnt vmcnt(17) lgkmcnt(0)
s_barrier
mavv 0,96,24
rt 64,4,32768
mavv 4,100,24
s_add_u32 m0, s33, 16384
mavv 8,104,24
rt 66,4,34816
mavv 12,108,24
s_mov_b32 s67, s29
mavv 16,112,24
rt 68,5,32768
mavv 20,116,24
dm 14,24,67
mavv 24,120,24
rt 70,5,34816
mavv 28,124,24
s_add_u32 m0, s32, 0
mavv 32,96,28
rt 72,6,32768
mavv 36,100,28
rqv 24,0,32768
mavv 40,104,28
s_mov_b32 s61, s28
mavv 44,108,28
rt 74,6,34816
mavv 48,112,28
rt 76,7,32768
mavv 52,116,28
dm 12,20,61
mavv 56,120,28
rt 78,7,34816
mavv 60,124,28
s_add_u32 m0, s33, 17408
mavv 64,96,32
rt 80,8,32768
mavv 68,100,32
rqv 28,0,34816
mavv 72,104,32
s_add_u32 s67, s67, s31
mavv 76,108,32
rt 82,8,34816
mavv 80,112,32
dm 15,24,67
mavv 84,116,32
rt 84,9,32768
mavv 88,120,32
rt 86,9,34816
mavv 92,124,32
s_add_u32 m0, s32, 1024
mavv 96,96,36
rt 88,10,32768
mavv 100,100,36
rqv 32,0,36864
mavv 104,104,36
s_add_u32 s61, s61, s30
mavv 108,108,36
rt 90,10,34816
mavv 112,112,36
dm 13,20,61
mavv 116,116,36
rt 92,11,32768
mavv 120,120,36
s_add_u32 m0, s33, 18432
mavv 124,124,36
rt 94,11,34816
mavv 128,96,40
rqv 36,0,38912
mavv 132,100,40
s_add_u32 s67, s67, s31
mavv 136,104,40
rqv 48,0,45056
mavv 140,108,40
dm 14,24,67
mavv 144,112,40
mavv 148,116,40
s_add_u32 m0, s32, 2048
mavv 152,120,40
mavv 156,124,40
s_add_u32 s61, s61, s30
mavv 160,96,44
rqv 40,0,40960
mavv 164,100,44
rqv 52,0,47104
mavv 168,104,44
dm 12,20,61
mavv 172,108,44
mavv 176,112,44
s_add_u32 m0, s33, 19456
mavv 180,116,44
mavv 184,120,44
mavv 188,124,44
s_add_u32 s67, s67, s31
mavv 192,96,56
rqv 44,0,43008
mavv 196,100,56
dm 15,24,67
mavv 200,104,56
mavv 204,108,56
mavv 208,112,56
s_add_u32 m0, s32, 3072
mavv 212,116,56
mavv 216,120,56
s_add_u32 s61, s61, s30
mavv 220,124,56
mavv 224,96,60
mavv 228,100,60
dm 13,20,61
mavv 232,104,60
mavv 236,108,60
s_add_u32 s29, s29, s65
mavv 240,112,60
mavv 244,116,60
mavv 248,120,60
mavv 252,124,60
s_waitcnt vmcnt(17) lgkmcnt(0)
s_barrier
mavv 0,64,24
rt 96,4,49152
mavv 4,68,24
s_add_u32 m0, s33, 32768
mavv 8,72,24
rt 98,4,51200
mavv 12,76,24
s_mov_b32 s67, s29
mavv 16,80,24
rt 100,5,49152
mavv 20,84,24
dm 14,24,67
mavv 24,88,24
rt 102,5,51200
mavv 28,92,24
s_add_u32 m0, s32, 16384
mavv 32,64,28
rt 104,6,49152
mavv 36,68,28
rqv 24,1,32768
mavv 40,72,28
s_add_u32 s61, s28, s64
mavv 44,76,28
rt 106,6,51200
mavv 48,80,28
dm 12,20,61
mavv 52,84,28
rt 108,7,49152
mavv 56,88,28
s_add_u32 m0, s33, 33792
mavv 60,92,28
rt 110,7,51200
mavv 64,64,32
s_add_u32 s67, s67, s31
mavv 68,68,32
rt 112,8,49152
mavv 72,72,32
rqv 28,1,34816
mavv 76,76,32
dm 15,24,67
mavv 80,80,32
rt 114,8,51200
mavv 84,84,32
s_add_u32 m0, s32, 17408
mavv 88,88,32
rt 116,9,49152
mavv 92,92,32
rt 118,9,51200
mavv 96,64,36
s_add_u32 s61, s61, s30
)ASM" ::: "memory");
  asm volatile(R"ASM(
rt 120,10,49152
mavv 100,68,36
rqv 32,1,36864
mavv 104,72,36
dm 13,20,61
mavv 108,76,36
rt 122,10,51200
mavv 112,80,36
s_add_u32 m0, s33, 34816
mavv 116,84,36
rt 124,11,49152
mavv 120,88,36
s_add_u32 s67, s67, s31
mavv 124,92,36
rt 126,11,51200
mavv 128,64,40
dm 14,24,67
mavv 132,68,40
rqv 36,1,38912
mavv 136,72,40
rqv 56,1,45056
mavv 140,76,40
s_add_u32 m0, s32, 18432
mavv 144,80,40
s_add_u32 s61, s61, s30
mavv 148,84,40
dm 12,20,61
mavv 152,88,40
mavv 156,92,40
s_add_u32 m0, s33, 35840
mavv 160,64,44
rqv 40,1,40960
mavv 164,68,44
rqv 60,1,47104
mavv 168,72,44
s_add_u32 s67, s67, s31
mavv 172,76,44
mavv 176,80,44
dm 15,24,67
mavv 180,84,44
mavv 184,88,44
s_add_u32 m0, s32, 19456
mavv 188,92,44
mavv 192,64,48
rqv 44,1,43008
mavv 196,68,48
s_add_u32 s61, s61, s30
mavv 200,72,48
dm 13,20,61
mavv 204,76,48
mavv 208,80,48
mavv 212,84,48
s_add_u32 s29, s29, s65
mavv 216,88,48
mavv 220,92,48
s_add_u32 s28, s28, 128
mavv 224,64,52
mavv 228,68,52
s_add_u32 s74, s78, .Lk1_packB+0-.Lk1_base
s_addc_u32 s75, s79, 0
mavv 232,72,52
mavv 236,76,52
global_load_dword v19, v236, s[74:75]
mavv 240,80,52
mavv 244,84,52
mavv 248,88,52
mavv 252,92,52
s_waitcnt vmcnt(17) lgkmcnt(0)
s_barrier
mavv 0,96,24
rt 64,4,0
mavv 4,100,24
s_add_u32 m0, s33, 49152
mavv 8,104,24
rt 66,4,2048
mavv 12,108,24
s_mov_b32 s67, s29
mavv 16,112,24
rt 68,5,0
mavv 20,116,24
dm 14,24,67
mavv 24,120,24
rt 70,5,2048
mavv 28,124,24
s_add_u32 m0, s32, 32768
mavv 32,96,28
rt 72,6,0
mavv 36,100,28
rqv 24,2,0
mavv 40,104,28
s_mov_b32 s61, s28
mavv 44,108,28
rt 74,6,2048
mavv 48,112,28
dm 12,20,61
mavv 52,116,28
rt 76,7,0
mavv 56,120,28
s_add_u32 m0, s33, 50176
mavv 60,124,28
rt 78,7,2048
mavv 64,96,32
rt 80,8,0
mavv 68,100,32
rqv 28,2,2048
mavv 72,104,32
s_add_u32 s67, s67, s31
mavv 76,108,32
rt 82,8,2048
mavv 80,112,32
dm 15,24,67
mavv 84,116,32
rt 84,9,0
mavv 88,120,32
s_add_u32 m0, s32, 33792
mavv 92,124,32
rt 86,9,2048
mavv 96,96,36
s_add_u32 s61, s61, s30
mavv 100,100,36
rt 88,10,0
mavv 104,104,36
rqv 32,2,4096
mavv 108,108,36
dm 13,20,61
mavv 112,112,36
rt 90,10,2048
mavv 116,116,36
s_add_u32 m0, s33, 51200
mavv 120,120,36
rt 92,11,0
mavv 124,124,36
s_add_u32 s67, s67, s31
mavv 128,96,40
rt 94,11,2048
mavv 132,100,40
rqv 36,2,6144
mavv 136,104,40
dm 14,24,67
mavv 140,108,40
rqv 48,2,12288
mavv 144,112,40
s_add_u32 m0, s32, 34816
mavv 148,116,40
s_add_u32 s61, s61, s30
mavv 152,120,40
mavv 156,124,40
dm 12,20,61
mavv 160,96,44
rqv 40,2,8192
mavv 164,100,44
rqv 52,2,14336
mavv 168,104,44
s_add_u32 m0, s33, 52224
mavv 172,108,44
s_add_u32 s67, s67, s31
mavv 176,112,44
mavv 180,116,44
dm 15,24,67
mavv 184,120,44
mavv 188,124,44
mavv 192,96,56
rqv 44,2,10240
mavv 196,100,56
s_add_u32 m0, s32, 35840
mavv 200,104,56
s_add_u32 s61, s61, s30
mavv 204,108,56
mavv 208,112,56
dm 13,20,61
mavv 212,116,56
mavv 216,120,56
mavv 220,124,56
s_add_u32 s29, s29, s65
mavv 224,96,60
mavv 228,100,60
s_add_u32 s74, s78, .Lk1_packB+4096-.Lk1_base
s_addc_u32 s75, s79, 0
mavv 232,104,60
mavv 236,108,60
global_load_dword v19, v236, s[74:75]
mavv 240,112,60
mavv 244,116,60
mavv 248,120,60
mavv 252,124,60
s_waitcnt vmcnt(18) lgkmcnt(0)
s_barrier
v_and_b32 v22, 0xf0, v16
v_and_b32 v21, 0x100, v16
v_lshl_add_u32 v22, v21, 2, v22
v_lshrrev_b32 v21, 1, v16
v_and_b32 v21, 0x100, v21
v_add_u32 v22, v22, v21
mavv 0,64,24
rt 96,4,16384
mavv 4,68,24
s_add_u32 m0, s33, 0
mavv 8,72,24
rt 98,4,18432
mavv 12,76,24
s_mov_b32 s67, s29
mavv 16,80,24
rt 100,5,16384
mavv 20,84,24
dm 14,24,67
mavv 24,88,24
rt 102,5,18432
mavv 28,92,24
s_add_u32 m0, s32, 49152
mavv 32,64,28
rt 104,6,16384
mavv 36,68,28
rqv 24,3,0
mavv 40,72,28
s_add_u32 s61, s28, s64
mavv 44,76,28
rt 106,6,18432
mavv 48,80,28
dm 12,20,61
mavv 52,84,28
rt 108,7,16384
mavv 56,88,28
s_add_u32 m0, s33, 1024
mavv 60,92,28
rt 110,7,18432
mavv 64,64,32
s_add_u32 s67, s67, s31
mavv 68,68,32
rt 112,8,16384
mavv 72,72,32
rqv 28,3,2048
mavv 76,76,32
dm 15,24,67
mavv 80,80,32
rt 114,8,18432
mavv 84,84,32
s_add_u32 m0, s32, 50176
mavv 88,88,32
rt 116,9,16384
mavv 92,92,32
rt 118,9,18432
mavv 96,64,36
s_add_u32 s61, s61, s30
rt 120,10,16384
mavv 100,68,36
rqv 32,3,4096
mavv 104,72,36
dm 13,20,61
mavv 108,76,36
rt 122,10,18432
mavv 112,80,36
s_add_u32 m0, s33, 2048
mavv 116,84,36
rt 124,11,16384
mavv 120,88,36
s_add_u32 s67, s67, s31
mavv 124,92,36
rt 126,11,18432
mavv 128,64,40
dm 14,24,67
mavv 132,68,40
rqv 36,3,6144
mavv 136,72,40
rqv 56,3,12288
mavv 140,76,40
s_add_u32 m0, s32, 51200
mavv 144,80,40
s_add_u32 s61, s61, s30
mavv 148,84,40
dm 12,20,61
mavv 152,88,40
mavv 156,92,40
s_add_u32 m0, s33, 3072
mavv 160,64,44
rqv 40,3,8192
mavv 164,68,44
rqv 60,3,14336
mavv 168,72,44
s_add_u32 s67, s67, s31
mavv 172,76,44
mavv 176,80,44
dm 15,24,67
mavv 180,84,44
mavv 184,88,44
s_add_u32 m0, s32, 52224
mavv 188,92,44
mavv 192,64,48
rqv 44,3,10240
mavv 196,68,48
s_add_u32 s61, s61, s30
mavv 200,72,48
dm 13,20,61
mavv 204,76,48
mavv 208,80,48
mavv 212,84,48
s_add_u32 s29, s29, s65
mavv 216,88,48
mavv 220,92,48
s_add_u32 s28, s28, 128
mavv 224,64,52
mavv 228,68,52
s_add_u32 s74, s78, .Lk1_packB+8192-.Lk1_base
s_addc_u32 s75, s79, 0
mavv 232,72,52
mavv 236,76,52
global_load_dword v19, v236, s[74:75]
mavv 240,80,52
mavv 244,84,52
mavv 248,88,52
mavv 252,92,52
s_waitcnt vmcnt(18) lgkmcnt(0)
s_barrier
mavv 0,96,24
rt 64,4,32768
mavv 4,100,24
s_add_u32 m0, s33, 16384
mavv 8,104,24
s_mov_b32 s67, s29
mavv 12,108,24
rt 66,4,34816
mavv 16,112,24
dm 14,24,67
mavv 20,116,24
rt 68,5,32768
mavv 24,120,24
s_add_u32 m0, s33, 17408
mavv 28,124,24
rt 70,5,34816
mavv 32,96,28
s_add_u32 s67, s67, s31
dm 15,24,67
mavv 36,100,28
rt 72,6,32768
mavv 40,104,28
rqv 24,0,0
mavv 44,108,28
s_add_u32 m0, s33, 18432
mavv 48,112,28
rt 74,6,34816
s_add_u32 s67, s67, s31
mavv 52,116,28
rt 76,7,32768
mavv 56,120,28
dm 14,24,67
mavv 60,124,28
rt 78,7,34816
mavv 64,96,32
s_add_u32 m0, s33, 19456
s_add_u32 s67, s67, s31
mavv 68,100,32
rt 80,8,32768
mavv 72,104,32
rqv 28,0,2048
mavv 76,108,32
dm 15,24,67
mavv 80,112,32
rt 82,8,34816
s_add_u32 s29, s29, s65
mavv 84,116,32
rt 84,9,32768
mavv 88,120,32
s_add_u32 m0, s17, 65536
mavv 92,124,32
rt 86,9,34816
mavv 96,96,36
s_add_u32 s96, s96, s97
buffer_load_dwordx4 v16, s[92:95], s96 offen sc1 lds
mavv 100,100,36
rt 88,10,32768
mavv 104,104,36
rqv 32,0,4096
mavv 108,108,36
s_add_u32 m0, s17, 69632
mavv 112,112,36
rt 90,10,34816
s_add_u32 s96, s96, s97
mavv 116,116,36
rt 92,11,32768
mavv 120,120,36
buffer_load_dwordx4 v16, s[92:95], s96 offen sc1 lds
mavv 124,124,36
rt 94,11,34816
mavv 128,96,40
s_add_u32 m0, s17, 73728
mavv 132,100,40
s_add_u32 s96, s96, s97
rqv 36,0,6144
mavv 136,104,40
rqv 48,0,12288
mavv 140,108,40
buffer_load_dwordx4 v16, s[92:95], s96 offen sc1 lds
mavv 144,112,40
s_add_u32 m0, s17, 77824
mavv 148,116,40
s_add_u32 s96, s96, s97
mavv 152,120,40
buffer_load_dwordx4 v16, s[92:95], s96 offen sc1 lds
mavv 156,124,40
s_add_u32 m0, s17, 81920
mavv 160,96,44
rqv 40,0,8192
mavv 164,100,44
rqv 52,0,14336
mavv 168,104,44
s_add_u32 s96, s96, s97
mavv 172,108,44
buffer_load_dwordx4 v16, s[92:95], s96 offen sc1 lds
mavv 176,112,44
s_add_u32 m0, s17, 86016
mavv 180,116,44
mavv 184,120,44
s_add_u32 s96, s96, s97
mavv 188,124,44
buffer_load_dwordx4 v16, s[92:95], s96 offen sc1 lds
mavv 192,96,56
rqv 44,0,10240
mavv 196,100,56
s_add_u32 m0, s17, 90112
mavv 200,104,56
s_add_u32 s96, s96, s97
mavv 204,108,56
mavv 208,112,56
buffer_load_dwordx4 v16, s[92:95], s96 offen sc1 lds
mavv 212,116,56
mavv 216,120,56
s_add_u32 m0, s17, 94208
mavv 220,124,56
s_add_u32 s96, s96, s97
mavv 224,96,60
mavv 228,100,60
buffer_load_dwordx4 v16, s[92:95], s96 offen sc1 lds
mavv 232,104,60
s_add_u32 s74, s78, .Lk1_packB+12288-.Lk1_base
s_addc_u32 s75, s79, 0
mavv 236,108,60
mavv 240,112,60
global_load_dword v19, v236, s[74:75]
mavv 244,116,60
mavv 248,120,60
mavv 252,124,60
s_waitcnt vmcnt(22) lgkmcnt(0)
s_barrier
mavv 0,64,24
rt 96,4,49152
mavv 4,68,24
s_add_u32 m0, s17, 131072
mavv 8,72,24
rt 98,4,51200
mavv 12,76,24
mavv 16,80,24
rt 100,5,49152
mavv 20,84,24
s_add_u32 s96, s96, s97
mavv 24,88,24
rt 102,5,51200
mavv 28,92,24
mavv 32,64,28
rt 104,6,49152
mavv 36,68,28
rqv 24,1,0
mavv 40,72,28
buffer_load_dwordx4 v16, s[92:95], s96 offen sc1 lds
mavv 44,76,28
rt 106,6,51200
mavv 48,80,28
rt 108,7,49152
mavv 52,84,28
s_add_u32 m0, s17, 135168
mavv 56,88,28
rt 110,7,51200
mavv 60,92,28
mavv 64,64,32
rt 112,8,49152
mavv 68,68,32
rqv 28,1,2048
mavv 72,72,32
s_add_u32 s96, s96, s97
mavv 76,76,32
rt 114,8,51200
mavv 80,80,32
rt 116,9,49152
mavv 84,84,32
mavv 88,88,32
rt 118,9,51200
mavv 92,92,32
buffer_load_dwordx4 v16, s[92:95], s96 offen sc1 lds
mavv 96,64,36
rt 120,10,49152
mavv 100,68,36
rqv 32,1,4096
mavv 104,72,36
rt 122,10,51200
mavv 108,76,36
s_add_u32 m0, s17, 139264
mavv 112,80,36
rt 124,11,49152
mavv 116,84,36
mavv 120,88,36
rt 126,11,51200
mavv 124,92,36
s_add_u32 s96, s96, s97
mavv 128,64,40
rqv 36,1,6144
mavv 132,68,40
rqv 56,1,12288
mavv 136,72,40
mavv 140,76,40
buffer_load_dwordx4 v16, s[92:95], s96 offen sc1 lds
mavv 144,80,40
mavv 148,84,40
mavv 152,88,40
mavv 156,92,40
s_add_u32 m0, s17, 143360
mavv 160,64,44
rqv 40,1,8192
mavv 164,68,44
rqv 60,1,14336
mavv 168,72,44
mavv 172,76,44
mavv 176,80,44
s_add_u32 s96, s96, s97
mavv 180,84,44
mavv 184,88,44
mavv 188,92,44
mavv 192,64,48
rqv 44,1,10240
mavv 196,68,48
buffer_load_dwordx4 v16, s[92:95], s96 offen sc1 lds
mavv 200,72,48
mavv 204,76,48
mavv 208,80,48
mavv 212,84,48
s_add_u32 s74, s78, .Lk1_packB+16384-.Lk1_base
s_addc_u32 s75, s79, 0
mavv 216,88,48
mavv 220,92,48
mavv 224,64,52
mavv 228,68,52
global_load_dword v19, v236, s[74:75]
mavv 232,72,52
mavv 236,76,52
mavv 240,80,52
mavv 244,84,52
mavv 248,88,52
mavv 252,92,52
s_waitcnt vmcnt(18) lgkmcnt(0)
s_barrier
mavv 0,96,24
rt 64,4,0
mavv 4,100,24
s_add_u32 m0, s17, 0
mavv 8,104,24
rt 66,4,2048
mavv 12,108,24
s_add_u32 s96, s96, s97
mavv 16,112,24
rt 68,5,0
mavv 20,116,24
buffer_load_dwordx4 v16, s[92:95], s96 offen sc1 lds
mavv 24,120,24
rt 70,5,2048
mavv 28,124,24
s_add_u32 m0, s17, 4096
mavv 32,96,28
rt 72,6,0
mavv 36,100,28
rqv 24,0,32768
mavv 40,104,28
s_add_u32 s96, s96, s97
mavv 44,108,28
rt 74,6,2048
mavv 48,112,28
buffer_load_dwordx4 v16, s[92:95], s96 offen sc1 lds
mavv 52,116,28
rt 76,7,0
mavv 56,120,28
rt 78,7,2048
mavv 60,124,28
s_add_u32 m0, s17, 8192
mavv 64,96,32
rt 80,8,0
mavv 68,100,32
rqv 28,0,34816
mavv 72,104,32
s_add_u32 s96, s96, s97
mavv 76,108,32
rt 82,8,2048
mavv 80,112,32
buffer_load_dwordx4 v16, s[92:95], s96 offen sc1 lds
mavv 84,116,32
rt 84,9,0
mavv 88,120,32
s_add_u32 m0, s17, 12288
mavv 92,124,32
rt 86,9,2048
mavv 96,96,36
s_add_u32 s96, s96, s97
mavv 100,100,36
rt 88,10,0
mavv 104,104,36
)ASM" ::: "memory");
  asm volatile(R"ASM(
rqv 32,0,36864
mavv 108,108,36
rt 90,10,2048
mavv 112,112,36
buffer_load_dwordx4 v16, s[92:95], s96 offen sc1 lds
mavv 116,116,36
rt 92,11,0
mavv 120,120,36
s_add_u32 m0, s17, 16384
mavv 124,124,36
rt 94,11,2048
mavv 128,96,40
s_add_u32 s96, s96, s97
mavv 132,100,40
rqv 36,0,38912
mavv 136,104,40
rqv 48,0,45056
mavv 140,108,40
buffer_load_dwordx4 v16, s[92:95], s96 offen sc1 lds
mavv 144,112,40
s_add_u32 m0, s17, 20480
mavv 148,116,40
mavv 152,120,40
s_add_u32 s96, s96, s97
mavv 156,124,40
mavv 160,96,44
rqv 40,0,40960
mavv 164,100,44
buffer_load_dwordx4 v16, s[92:95], s96 offen sc1 lds
mavv 168,104,44
rqv 52,0,47104
mavv 172,108,44
s_add_u32 m0, s17, 24576
mavv 176,112,44
mavv 180,116,44
s_add_u32 s96, s96, s97
mavv 184,120,44
mavv 188,124,44
buffer_load_dwordx4 v16, s[92:95], s96 offen sc1 lds
mavv 192,96,56
rqv 44,0,43008
mavv 196,100,56
mavv 200,104,56
s_add_u32 m0, s17, 28672
mavv 204,108,56
mavv 208,112,56
s_add_u32 s96, s96, s97
mavv 212,116,56
mavv 216,120,56
buffer_load_dwordx4 v16, s[92:95], s96 offen sc1 lds
mavv 220,124,56
mavv 224,96,60
mavv 228,100,60
s_add_u32 s74, s78, .Lk1_packB+20480-.Lk1_base
s_addc_u32 s75, s79, 0
mavv 232,104,60
mavv 236,108,60
global_load_dword v19, v236, s[74:75]
mavv 240,112,60
mavv 244,116,60
mavv 248,120,60
mavv 252,124,60
s_waitcnt vmcnt(14) lgkmcnt(0)
s_barrier
mavv 0,64,24
rt 96,4,16384
mavv 4,68,24
s_add_u32 m0, s17, 147456
mavv 8,72,24
rt 98,4,18432
mavv 12,76,24
s_add_u32 s96, s96, s97
mavv 16,80,24
rt 100,5,16384
mavv 20,84,24
buffer_load_dwordx4 v16, s[92:95], s96 offen sc1 lds
mavv 24,88,24
rt 102,5,18432
mavv 28,92,24
s_add_u32 m0, s17, 151552
mavv 32,64,28
rt 104,6,16384
mavv 36,68,28
rqv 24,1,32768
mavv 40,72,28
s_add_u32 s96, s96, s97
mavv 44,76,28
rt 106,6,18432
mavv 48,80,28
buffer_load_dwordx4 v16, s[92:95], s96 offen sc1 lds
mavv 52,84,28
rt 108,7,16384
mavv 56,88,28
rt 110,7,18432
mavv 60,92,28
s_add_u32 m0, s17, 155648
mavv 64,64,32
rt 112,8,16384
mavv 68,68,32
rqv 28,1,34816
mavv 72,72,32
s_add_u32 s96, s96, s97
mavv 76,76,32
rt 114,8,18432
mavv 80,80,32
buffer_load_dwordx4 v16, s[92:95], s96 offen sc1 lds
mavv 84,84,32
rt 116,9,16384
mavv 88,88,32
s_add_u32 m0, s17, 159744
mavv 92,92,32
rt 118,9,18432
mavv 96,64,36
s_add_u32 s96, s96, s97
mavv 100,68,36
rt 120,10,16384
mavv 104,72,36
rqv 32,1,36864
mavv 108,76,36
rt 122,10,18432
mavv 112,80,36
buffer_load_dwordx4 v16, s[92:95], s96 offen sc1 lds
mavv 116,84,36
rt 124,11,16384
mavv 120,88,36
s_add_u32 m0, s17, 98304
mavv 124,92,36
rt 126,11,18432
mavv 128,64,40
s_add_u32 s96, s96, s97
mavv 132,68,40
rqv 36,1,38912
mavv 136,72,40
rqv 56,1,45056
mavv 140,76,40
buffer_load_dwordx4 v16, s[92:95], s96 offen sc1 lds
mavv 144,80,40
s_add_u32 m0, s17, 102400
mavv 148,84,40
mavv 152,88,40
s_add_u32 s96, s96, s97
mavv 156,92,40
mavv 160,64,44
rqv 40,1,40960
mavv 164,68,44
buffer_load_dwordx4 v16, s[92:95], s96 offen sc1 lds
mavv 168,72,44
rqv 60,1,47104
mavv 172,76,44
s_add_u32 m0, s17, 106496
mavv 176,80,44
mavv 180,84,44
s_add_u32 s96, s96, s97
mavv 184,88,44
mavv 188,92,44
buffer_load_dwordx4 v16, s[92:95], s96 offen sc1 lds
mavv 192,64,48
rqv 44,1,43008
mavv 196,68,48
mavv 200,72,48
s_add_u32 m0, s17, 110592
mavv 204,76,48
mavv 208,80,48
s_add_u32 s96, s96, s97
mavv 212,84,48
mavv 216,88,48
buffer_load_dwordx4 v16, s[92:95], s96 offen sc1 lds
mavv 220,92,48
mavv 224,64,52
mavv 228,68,52
s_add_u32 s74, s78, .Lk1_packB+24576-.Lk1_base
s_addc_u32 s75, s79, 0
mavv 232,72,52
mavv 236,76,52
global_load_dword v19, v236, s[74:75]
mavv 240,80,52
mavv 244,84,52
mavv 248,88,52
mavv 252,92,52
s_waitcnt vmcnt(18) lgkmcnt(0)
s_barrier
mavv 0,96,24
s_add_u32 s74, s78, .Lk1_packB+28672-.Lk1_base
s_addc_u32 s75, s79, 0
mavv 4,100,24
mavv 8,104,24
mavv 12,108,24
global_load_dword v19, v236, s[74:75]
mavv 16,112,24
mavv 20,116,24
ar 80,0
ar 81,1
ar 82,2
ar 83,3
ar 84,4
ar 85,5
mavv 24,120,24
s_add_u32 m0, s17, 114688
ar 86,6
ar 87,7
cv 128,80,81
cv 129,82,83
cv 130,84,85
cv 131,86,87
mavv 28,124,24
ar 88,8
ar 89,9
ar 90,10
ar 91,11
ar 92,12
ar 93,13
mavv 32,96,28
ar 94,14
ar 95,15
cv 132,88,89
cv 133,90,91
cv 134,92,93
cv 135,94,95
mavv 36,100,28
ar 80,16
ar 81,17
ar 82,18
ar 83,19
ar 84,20
ar 85,21
mavv 40,104,28
s_add_u32 s96, s96, s97
ar 86,22
ar 87,23
cv 136,80,81
cv 137,82,83
cv 138,84,85
cv 139,86,87
mavv 44,108,28
ar 88,24
ar 89,25
ar 90,26
ar 91,27
ar 92,28
ar 93,29
mavv 48,112,28
ar 94,30
ar 95,31
cv 140,88,89
cv 141,90,91
cv 142,92,93
cv 143,94,95
mavv 52,116,28
buffer_load_dwordx4 v16, s[92:95], s96 offen sc1 lds
ar 80,32
ar 81,33
ar 82,34
ar 83,35
ar 84,36
ar 85,37
mavv 56,120,28
ar 86,38
ar 87,39
cv 144,80,81
cv 145,82,83
cv 146,84,85
cv 147,86,87
mavv 60,124,28
ar 88,40
ar 89,41
ar 90,42
ar 91,43
ar 92,44
ar 93,45
mavv 64,96,32
ar 94,46
ar 95,47
cv 148,88,89
cv 149,90,91
cv 150,92,93
cv 151,94,95
mavv 68,100,32
s_add_u32 m0, s17, 118784
ar 80,48
ar 81,49
ar 82,50
ar 83,51
ar 84,52
ar 85,53
mavv 72,104,32
ar 86,54
ar 87,55
cv 152,80,81
cv 153,82,83
cv 154,84,85
cv 155,86,87
mavv 76,108,32
ar 88,56
ar 89,57
ar 90,58
ar 91,59
ar 92,60
ar 93,61
mavv 80,112,32
s_add_u32 s96, s96, s97
ar 94,62
ar 95,63
cv 156,88,89
cv 157,90,91
cv 158,92,93
cv 159,94,95
mavv 84,116,32
ar 80,64
ar 81,65
ar 82,66
ar 83,67
ar 84,68
ar 85,69
mavv 88,120,32
ar 86,70
ar 87,71
cv 160,80,81
cv 161,82,83
cv 162,84,85
cv 163,86,87
mavv 92,124,32
ar 88,72
ar 89,73
ar 90,74
ar 91,75
ar 92,76
ar 93,77
mavv 96,96,36
buffer_load_dwordx4 v16, s[92:95], s96 offen sc1 lds
ar 94,78
ar 95,79
cv 164,88,89
cv 165,90,91
cv 166,92,93
cv 167,94,95
mavv 100,100,36
ar 80,80
ar 81,81
ar 82,82
ar 83,83
ar 84,84
ar 85,85
mavv 104,104,36
ar 86,86
ar 87,87
cv 168,80,81
cv 169,82,83
cv 170,84,85
cv 171,86,87
mavv 108,108,36
s_add_u32 m0, s17, 122880
ar 88,88
ar 89,89
ar 90,90
ar 91,91
ar 92,92
ar 93,93
mavv 112,112,36
ar 94,94
ar 95,95
cv 172,88,89
cv 173,90,91
cv 174,92,93
cv 175,94,95
mavv 116,116,36
ar 80,96
ar 81,97
ar 82,98
ar 83,99
ar 84,100
ar 85,101
mavv 120,120,36
ar 86,102
ar 87,103
cv 176,80,81
cv 177,82,83
cv 178,84,85
cv 179,86,87
mavv 124,124,36
s_add_u32 s96, s96, s97
ar 88,104
ar 89,105
ar 90,106
ar 91,107
ar 92,108
ar 93,109
mavv 128,96,40
ar 94,110
ar 95,111
cv 180,88,89
cv 181,90,91
cv 182,92,93
cv 183,94,95
mavv 132,100,40
ar 80,112
ar 81,113
ar 82,114
ar 83,115
ar 84,116
ar 85,117
mavv 136,104,40
buffer_load_dwordx4 v16, s[92:95], s96 offen sc1 lds
ar 86,118
ar 87,119
cv 184,80,81
cv 185,82,83
cv 186,84,85
cv 187,86,87
mavv 140,108,40
ar 88,120
ar 89,121
ar 90,122
ar 91,123
ar 92,124
ar 93,125
mavv 144,112,40
ar 94,126
ar 95,127
cv 188,88,89
cv 189,90,91
cv 190,92,93
cv 191,94,95
mavv 148,116,40
s_add_u32 m0, s17, 126976
ar 80,128
ar 81,129
ar 82,130
ar 83,131
ar 84,132
ar 85,133
mavv 152,120,40
ar 86,134
ar 87,135
cv 192,80,81
cv 193,82,83
cv 194,84,85
cv 195,86,87
mavv 156,124,40
ar 88,136
ar 89,137
ar 90,138
ar 91,139
ar 92,140
ar 93,141
mavv 160,96,44
ar 94,142
ar 95,143
cv 196,88,89
cv 197,90,91
cv 198,92,93
cv 199,94,95
mavv 164,100,44
s_add_u32 s96, s96, s97
ar 80,144
ar 81,145
ar 82,146
ar 83,147
ar 84,148
ar 85,149
mavv 168,104,44
ar 86,150
ar 87,151
cv 200,80,81
cv 201,82,83
cv 202,84,85
cv 203,86,87
mavv 172,108,44
ar 88,152
ar 89,153
ar 90,154
ar 91,155
ar 92,156
ar 93,157
mavv 176,112,44
buffer_load_dwordx4 v16, s[92:95], s96 offen sc1 lds
ar 94,158
ar 95,159
cv 204,88,89
cv 205,90,91
cv 206,92,93
cv 207,94,95
mavv 180,116,44
ar 80,160
ar 81,161
ar 82,162
ar 83,163
ar 84,164
ar 85,165
mavv 184,120,44
ar 86,166
ar 87,167
cv 208,80,81
cv 209,82,83
cv 210,84,85
cv 211,86,87
mavv 188,124,44
ar 88,168
ar 89,169
ar 90,170
ar 91,171
ar 92,172
ar 93,173
mavv 192,96,56
global_load_dwordx4 v[64:67], v22, s[98:99] offset:0 sc1
ar 94,174
ar 95,175
cv 212,88,89
cv 213,90,91
cv 214,92,93
cv 215,94,95
mavv 196,100,56
ar 80,176
ar 81,177
ar 82,178
ar 83,179
ar 84,180
ar 85,181
mavv 200,104,56
ar 86,182
ar 87,183
cv 216,80,81
cv 217,82,83
cv 218,84,85
cv 219,86,87
mavv 204,108,56
global_load_dwordx4 v[68:71], v22, s[98:99] offset:512 sc1
ar 88,184
ar 89,185
ar 90,186
ar 91,187
ar 92,188
ar 93,189
mavv 208,112,56
ar 94,190
ar 95,191
cv 220,88,89
cv 221,90,91
cv 222,92,93
cv 223,94,95
mavv 212,116,56
ar 80,192
ar 81,193
ar 82,194
ar 83,195
ar 84,196
ar 85,197
mavv 216,120,56
ar 86,198
ar 87,199
cv 224,80,81
cv 225,82,83
cv 226,84,85
cv 227,86,87
mavv 220,124,56
global_load_dwordx4 v[72:75], v22, s[98:99] offset:2048 sc1
ar 88,200
ar 89,201
ar 90,202
ar 91,203
ar 92,204
ar 93,205
mavv 224,96,60
ar 94,206
ar 95,207
cv 228,88,89
cv 229,90,91
cv 230,92,93
cv 231,94,95
mavv 228,100,60
v_accvgpr_write_b32 a192, v237
v_accvgpr_write_b32 a193, v238
v_accvgpr_write_b32 a194, v239
v_accvgpr_write_b32 a195, v240
v_accvgpr_write_b32 a196, v241
v_accvgpr_write_b32 a197, v242
mavv 232,104,60
global_load_dwordx4 v[76:79], v22, s[98:99] offset:2560 sc1
v_accvgpr_write_b32 a198, v243
v_accvgpr_write_b32 a199, v244
v_accvgpr_write_b32 a200, v245
v_accvgpr_write_b32 a201, v246
v_accvgpr_write_b32 a202, v247
v_accvgpr_write_b32 a203, v248
mavv 236,108,60
v_accvgpr_write_b32 a204, v249
v_accvgpr_write_b32 a205, v250
v_accvgpr_write_b32 a206, v251
ar 80,208
ar 81,209
ar 82,210
mavv 240,112,60
ar 83,211
ar 84,212
ar 85,213
ar 86,214
ar 87,215
cv 232,80,81
mavv 244,116,60
cv 233,82,83
cv 234,84,85
)ASM" ::: "memory");
  asm volatile(R"ASM(
cv 235,86,87
ar 88,216
ar 89,217
ar 90,218
mavv 248,120,60
ar 91,219
ar 92,220
ar 93,221
ar 94,222
ar 95,223
cv 236,88,89
mavv 252,124,60
cv 237,90,91
cv 238,92,93
cv 239,94,95
ar 80,224
ar 81,225
ar 82,226
s_nop 5
ar 83,227
ar 84,228
ar 85,229
ar 86,230
ar 87,231
cv 240,80,81
cv 241,82,83
cv 242,84,85
cv 243,86,87
ar 88,232
ar 89,233
ar 90,234
ar 91,235
ar 92,236
ar 93,237
ar 94,238
ar 95,239
cv 244,88,89
cv 245,90,91
cv 246,92,93
cv 247,94,95
ar 80,240
ar 81,241
ar 82,242
ar 83,243
ar 84,244
ar 85,245
ar 86,246
ar 87,247
cv 248,80,81
cv 249,82,83
cv 250,84,85
cv 251,86,87
ar 88,248
ar 89,249
ar 90,250
ar 91,251
ar 92,252
ar 93,253
ar 94,254
ar 95,255
cv 252,88,89
cv 253,90,91
cv 254,92,93
cv 255,94,95
s_branch .Lk1_packB
.Lk1_BN:
s_waitcnt vmcnt(21) lgkmcnt(0)
s_barrier
mavv 0,24,96
rt 64,4,0
mavv 8,24,100
s_add_u32 m0, s33, 49152
mavv 16,24,104
rt 66,4,2048
mavv 24,24,108
s_mov_b32 s67, s29
mavv 32,24,112
rt 68,5,0
mavv 40,24,116
dm 14,24,67
mavv 48,24,120
rt 70,5,2048
mavv 56,24,124
s_add_u32 m0, s32, 32768
mavv 4,28,96
rt 72,6,0
mavv 12,28,100
rqv 24,2,0
mavv 20,28,104
s_mov_b32 s61, s28
mavv 28,28,108
rt 74,6,2048
mavv 36,28,112
dm 12,20,61
mavv 44,28,116
rt 76,7,0
mavv 52,28,120
s_add_u32 m0, s33, 50176
mavv 60,28,124
rt 78,7,2048
mavv 64,32,96
rt 80,8,0
mavv 72,32,100
rqv 28,2,2048
mavv 80,32,104
s_add_u32 s67, s67, s31
mavv 88,32,108
rt 82,8,2048
mavv 96,32,112
dm 15,24,67
mavv 104,32,116
rt 84,9,0
mavv 112,32,120
s_add_u32 m0, s32, 33792
mavv 120,32,124
rt 86,9,2048
mavv 68,36,96
s_add_u32 s61, s61, s30
mavv 76,36,100
rt 88,10,0
mavv 84,36,104
rqv 32,2,4096
mavv 92,36,108
dm 13,20,61
mavv 100,36,112
rt 90,10,2048
mavv 108,36,116
s_add_u32 m0, s33, 51200
mavv 116,36,120
rt 92,11,0
mavv 124,36,124
s_add_u32 s67, s67, s31
mavv 128,40,96
rt 94,11,2048
mavv 136,40,100
rqv 36,2,6144
mavv 144,40,104
dm 14,24,67
mavv 152,40,108
rqv 48,2,12288
mavv 160,40,112
s_add_u32 m0, s32, 34816
mavv 168,40,116
s_add_u32 s61, s61, s30
mavv 176,40,120
mavv 184,40,124
dm 12,20,61
mavv 132,44,96
rqv 40,2,8192
mavv 140,44,100
rqv 52,2,14336
mavv 148,44,104
s_add_u32 m0, s33, 52224
mavv 156,44,108
s_add_u32 s67, s67, s31
mavv 164,44,112
mavv 172,44,116
dm 15,24,67
mavv 180,44,120
mavv 188,44,124
mavv 192,56,96
rqv 44,2,10240
mavv 200,56,100
s_add_u32 m0, s32, 35840
mavv 208,56,104
s_add_u32 s61, s61, s30
mavv 216,56,108
mavv 224,56,112
dm 13,20,61
mavv 232,56,116
mavv 240,56,120
mavv 248,56,124
s_add_u32 s29, s29, s65
mavv 196,60,96
mavv 204,60,100
s_add_u32 s74, s78, .Lk1_BN+8192-.Lk1_base
s_addc_u32 s75, s79, 0
mavv 212,60,104
mavv 220,60,108
global_load_dword v19, v236, s[74:75]
mavv 228,60,112
mavv 236,60,116
mavv 244,60,120
mavv 252,60,124
s_waitcnt vmcnt(20) lgkmcnt(0)
s_barrier
mavv 0,24,64
rt 96,4,16384
mavv 8,24,68
s_add_u32 m0, s33, 0
mavv 16,24,72
rt 98,4,18432
mavv 24,24,76
s_mov_b32 s67, s29
mavv 32,24,80
rt 100,5,16384
mavv 40,24,84
dm 14,24,67
mavv 48,24,88
rt 102,5,18432
mavv 56,24,92
s_add_u32 m0, s32, 49152
mavv 4,28,64
rt 104,6,16384
mavv 12,28,68
rqv 24,3,0
mavv 20,28,72
s_add_u32 s61, s28, s64
mavv 28,28,76
rt 106,6,18432
mavv 36,28,80
dm 12,20,61
mavv 44,28,84
rt 108,7,16384
mavv 52,28,88
rt 110,7,18432
mavv 60,28,92
s_add_u32 m0, s33, 1024
mavv 64,32,64
rt 112,8,16384
mavv 72,32,68
rqv 28,3,2048
mavv 80,32,72
s_add_u32 s67, s67, s31
mavv 88,32,76
rt 114,8,18432
mavv 96,32,80
dm 15,24,67
mavv 104,32,84
rt 116,9,16384
mavv 112,32,88
s_add_u32 m0, s32, 50176
mavv 120,32,92
rt 118,9,18432
mavv 68,36,64
s_add_u32 s61, s61, s30
mavv 76,36,68
rt 120,10,16384
mavv 84,36,72
rqv 32,3,4096
mavv 92,36,76
rt 122,10,18432
mavv 100,36,80
dm 13,20,61
mavv 108,36,84
rt 124,11,16384
mavv 116,36,88
s_add_u32 m0, s33, 2048
mavv 124,36,92
rt 126,11,18432
mavv 128,40,64
s_add_u32 s67, s67, s31
mavv 136,40,68
rqv 36,3,6144
mavv 144,40,72
rqv 56,3,12288
mavv 152,40,76
dm 14,24,67
mavv 160,40,80
s_add_u32 m0, s32, 51200
mavv 168,40,84
mavv 176,40,88
s_add_u32 s61, s61, s30
mavv 184,40,92
mavv 132,44,64
rqv 40,3,8192
mavv 140,44,68
dm 12,20,61
mavv 148,44,72
rqv 60,3,14336
mavv 156,44,76
s_add_u32 m0, s33, 3072
mavv 164,44,80
mavv 172,44,84
s_add_u32 s67, s67, s31
mavv 180,44,88
mavv 188,44,92
dm 15,24,67
mavv 192,48,64
rqv 44,3,10240
mavv 200,48,68
mavv 208,48,72
s_add_u32 m0, s32, 52224
mavv 216,48,76
mavv 224,48,80
s_add_u32 s61, s61, s30
mavv 232,48,84
mavv 240,48,88
dm 13,20,61
mavv 248,48,92
mavv 196,52,64
mavv 204,52,68
s_add_u32 s29, s29, s65
mavv 212,52,72
mavv 220,52,76
s_add_u32 s28, s28, 128
mavv 228,52,80
mavv 236,52,84
mavv 244,52,88
mavv 252,52,92
s_waitcnt vmcnt(17) lgkmcnt(0)
s_barrier
s_cmp_lg_u32 s49, 0
s_cbranch_scc1 .Lk1_BN_nf
v_mov_b32 v20, s59
v_mov_b32 v21, s82
s_mov_b64 exec, 1
global_store_dword v20, v21, s[4:5] sc1
s_mov_b64 exec, -1
.Lk1_BN_nf:
mavv 0,24,96
rt 64,4,32768
mavv 8,24,100
s_add_u32 m0, s33, 16384
mavv 16,24,104
rt 66,4,34816
mavv 24,24,108
s_mov_b32 s67, s29
mavv 32,24,112
rt 68,5,32768
mavv 40,24,116
dm 14,24,67
mavv 48,24,120
rt 70,5,34816
mavv 56,24,124
s_add_u32 m0, s32, 65536
mavv 4,28,96
rt 72,6,32768
mavv 12,28,100
rqv 24,0,0
mavv 20,28,104
s_mov_b32 s61, s28
mavv 28,28,108
rt 74,6,34816
mavv 36,28,112
rt 76,7,32768
mavv 44,28,116
dm 12,20,61
mavv 52,28,120
rt 78,7,34816
mavv 60,28,124
s_add_u32 m0, s33, 17408
mavv 64,32,96
rt 80,8,32768
mavv 72,32,100
rqv 28,0,2048
mavv 80,32,104
s_add_u32 s67, s67, s31
mavv 88,32,108
rt 82,8,34816
mavv 96,32,112
dm 15,24,67
mavv 104,32,116
rt 84,9,32768
mavv 112,32,120
rt 86,9,34816
mavv 120,32,124
s_add_u32 m0, s32, 66560
mavv 68,36,96
rt 88,10,32768
mavv 76,36,100
rqv 32,0,4096
mavv 84,36,104
s_add_u32 s61, s61, s30
mavv 92,36,108
rt 90,10,34816
mavv 100,36,112
dm 13,20,61
mavv 108,36,116
rt 92,11,32768
mavv 116,36,120
s_add_u32 m0, s33, 18432
mavv 124,36,124
rt 94,11,34816
mavv 128,40,96
rqv 36,0,6144
mavv 136,40,100
s_add_u32 s67, s67, s31
mavv 144,40,104
rqv 48,0,12288
mavv 152,40,108
dm 14,24,67
mavv 160,40,112
mavv 168,40,116
s_add_u32 m0, s32, 67584
mavv 176,40,120
mavv 184,40,124
s_add_u32 s61, s61, s30
mavv 132,44,96
rqv 40,0,8192
mavv 140,44,100
rqv 52,0,14336
mavv 148,44,104
dm 12,20,61
mavv 156,44,108
mavv 164,44,112
s_add_u32 m0, s33, 19456
mavv 172,44,116
mavv 180,44,120
mavv 188,44,124
s_add_u32 s67, s67, s31
mavv 192,56,96
rqv 44,0,10240
mavv 200,56,100
dm 15,24,67
mavv 208,56,104
mavv 216,56,108
mavv 224,56,112
s_add_u32 m0, s32, 68608
mavv 232,56,116
mavv 240,56,120
s_add_u32 s61, s61, s30
mavv 248,56,124
mavv 196,60,96
mavv 204,60,100
dm 13,20,61
mavv 212,60,104
mavv 220,60,108
s_add_u32 s29, s29, s65
mavv 228,60,112
mavv 236,60,116
mavv 244,60,120
mavv 252,60,124
s_waitcnt vmcnt(16) lgkmcnt(0)
s_barrier
mavv 0,24,64
rt 96,4,49152
mavv 8,24,68
s_add_u32 m0, s33, 32768
mavv 16,24,72
rt 98,4,51200
mavv 24,24,76
s_mov_b32 s67, s29
mavv 32,24,80
rt 100,5,49152
mavv 40,24,84
dm 14,24,67
mavv 48,24,88
rt 102,5,51200
mavv 56,24,92
s_add_u32 m0, s32, 81920
mavv 4,28,64
rt 104,6,49152
mavv 12,28,68
rqv 24,1,0
mavv 20,28,72
s_add_u32 s61, s28, s64
mavv 28,28,76
rt 106,6,51200
mavv 36,28,80
dm 12,20,61
mavv 44,28,84
rt 108,7,49152
mavv 52,28,88
s_add_u32 m0, s33, 33792
mavv 60,28,92
rt 110,7,51200
mavv 64,32,64
s_add_u32 s67, s67, s31
mavv 72,32,68
rt 112,8,49152
mavv 80,32,72
rqv 28,1,2048
mavv 88,32,76
dm 15,24,67
mavv 96,32,80
rt 114,8,51200
mavv 104,32,84
s_add_u32 m0, s32, 82944
mavv 112,32,88
rt 116,9,49152
mavv 120,32,92
s_add_u32 s61, s61, s30
mavv 68,36,64
rt 118,9,51200
dm 13,20,61
mavv 76,36,68
rt 120,10,49152
mavv 84,36,72
rqv 32,1,4096
mavv 92,36,76
s_add_u32 m0, s33, 34816
mavv 100,36,80
rt 122,10,51200
mavv 108,36,84
s_add_u32 s67, s67, s31
mavv 116,36,88
rt 124,11,49152
mavv 124,36,92
dm 14,24,67
mavv 128,40,64
rt 126,11,51200
mavv 136,40,68
rqv 36,1,6144
mavv 144,40,72
s_add_u32 m0, s32, 83968
mavv 152,40,76
rqv 56,1,12288
mavv 160,40,80
s_add_u32 s61, s61, s30
mavv 168,40,84
dm 12,20,61
mavv 176,40,88
s_add_u32 m0, s33, 35840
mavv 184,40,92
mavv 132,44,64
rqv 40,1,8192
mavv 140,44,68
s_add_u32 s67, s67, s31
mavv 148,44,72
rqv 60,1,14336
mavv 156,44,76
dm 15,24,67
mavv 164,44,80
s_add_u32 m0, s32, 84992
mavv 172,44,84
mavv 180,44,88
mavv 188,44,92
s_add_u32 s61, s61, s30
mavv 192,48,64
rqv 44,1,10240
mavv 200,48,68
dm 13,20,61
mavv 208,48,72
mavv 216,48,76
s_add_u32 s29, s29, s65
mavv 224,48,80
mavv 232,48,84
s_add_u32 s28, s28, 128
mavv 240,48,88
mavv 248,48,92
s_add_u32 s74, s78, .Lk1_BN+12288-.Lk1_base
s_addc_u32 s75, s79, 0
mavv 196,52,64
mavv 204,52,68
global_load_dword v19, v236, s[74:75]
mavv 212,52,72
mavv 220,52,76
global_load_dword v19, v254, s[72:73] offset:0
mavv 228,52,80
mavv 236,52,84
mavv 244,52,88
mavv 252,52,92
s_waitcnt vmcnt(18) lgkmcnt(0)
s_barrier
mavv 0,24,96
rt 64,4,0
mavv 8,24,100
s_add_u32 m0, s33, 49152
mavv 16,24,104
rt 66,4,2048
mavv 24,24,108
s_mov_b32 s67, s29
mavv 32,24,112
rt 68,5,0
mavv 40,24,116
dm 14,24,67
mavv 48,24,120
rt 70,5,2048
mavv 56,24,124
s_add_u32 m0, s32, 0
mavv 4,28,96
rt 72,6,0
mavv 12,28,100
rqv 24,0,32768
mavv 20,28,104
s_mov_b32 s61, s28
mavv 28,28,108
rt 74,6,2048
mavv 36,28,112
rt 76,7,0
mavv 44,28,116
dm 12,20,61
mavv 52,28,120
rt 78,7,2048
mavv 60,28,124
s_add_u32 m0, s33, 50176
mavv 64,32,96
rt 80,8,0
mavv 72,32,100
rqv 28,0,34816
mavv 80,32,104
s_add_u32 s67, s67, s31
mavv 88,32,108
rt 82,8,2048
mavv 96,32,112
dm 15,24,67
mavv 104,32,116
rt 84,9,0
mavv 112,32,120
rt 86,9,2048
mavv 120,32,124
s_add_u32 m0, s32, 1024
mavv 68,36,96
rt 88,10,0
mavv 76,36,100
rqv 32,0,36864
mavv 84,36,104
s_add_u32 s61, s61, s30
mavv 92,36,108
)ASM" ::: "memory");
  asm volatile(R"ASM(
rt 90,10,2048
mavv 100,36,112
dm 13,20,61
mavv 108,36,116
rt 92,11,0
mavv 116,36,120
s_add_u32 m0, s33, 51200
mavv 124,36,124
rt 94,11,2048
mavv 128,40,96
rqv 36,0,38912
mavv 136,40,100
s_add_u32 s67, s67, s31
mavv 144,40,104
rqv 48,0,45056
mavv 152,40,108
dm 14,24,67
mavv 160,40,112
mavv 168,40,116
s_add_u32 m0, s32, 2048
mavv 176,40,120
mavv 184,40,124
s_add_u32 s61, s61, s30
mavv 132,44,96
rqv 40,0,40960
mavv 140,44,100
rqv 52,0,47104
mavv 148,44,104
dm 12,20,61
mavv 156,44,108
mavv 164,44,112
s_add_u32 m0, s33, 52224
mavv 172,44,116
mavv 180,44,120
mavv 188,44,124
s_add_u32 s67, s67, s31
mavv 192,56,96
rqv 44,0,43008
mavv 200,56,100
dm 15,24,67
mavv 208,56,104
mavv 216,56,108
mavv 224,56,112
s_add_u32 m0, s32, 3072
mavv 232,56,116
mavv 240,56,120
s_add_u32 s61, s61, s30
mavv 248,56,124
mavv 196,60,96
mavv 204,60,100
dm 13,20,61
mavv 212,60,104
mavv 220,60,108
s_add_u32 s29, s29, s65
mavv 228,60,112
mavv 236,60,116
mavv 244,60,120
mavv 252,60,124
s_waitcnt vmcnt(18) lgkmcnt(0)
s_barrier
mavv 0,24,64
rt 96,4,16384
mavv 8,24,68
s_add_u32 m0, s33, 0
mavv 16,24,72
rt 98,4,18432
mavv 24,24,76
s_mov_b32 s67, s29
mavv 32,24,80
rt 100,5,16384
mavv 40,24,84
dm 14,24,67
mavv 48,24,88
rt 102,5,18432
mavv 56,24,92
s_add_u32 m0, s32, 16384
mavv 4,28,64
rt 104,6,16384
mavv 12,28,68
rqv 24,1,32768
mavv 20,28,72
s_add_u32 s61, s28, s64
mavv 28,28,76
rt 106,6,18432
mavv 36,28,80
dm 12,20,61
mavv 44,28,84
rt 108,7,16384
mavv 52,28,88
rt 110,7,18432
mavv 60,28,92
s_add_u32 m0, s33, 1024
mavv 64,32,64
rt 112,8,16384
mavv 72,32,68
rqv 28,1,34816
mavv 80,32,72
s_add_u32 s67, s67, s31
mavv 88,32,76
rt 114,8,18432
mavv 96,32,80
dm 15,24,67
mavv 104,32,84
rt 116,9,16384
mavv 112,32,88
s_add_u32 m0, s32, 17408
mavv 120,32,92
rt 118,9,18432
mavv 68,36,64
s_add_u32 s61, s61, s30
mavv 76,36,68
rt 120,10,16384
mavv 84,36,72
rqv 32,1,36864
mavv 92,36,76
rt 122,10,18432
mavv 100,36,80
dm 13,20,61
mavv 108,36,84
rt 124,11,16384
mavv 116,36,88
s_add_u32 m0, s33, 2048
mavv 124,36,92
rt 126,11,18432
mavv 128,40,64
s_add_u32 s67, s67, s31
mavv 136,40,68
rqv 36,1,38912
mavv 144,40,72
rqv 56,1,45056
mavv 152,40,76
dm 14,24,67
mavv 160,40,80
s_add_u32 m0, s32, 18432
mavv 168,40,84
mavv 176,40,88
s_add_u32 s61, s61, s30
mavv 184,40,92
mavv 132,44,64
rqv 40,1,40960
mavv 140,44,68
dm 12,20,61
mavv 148,44,72
rqv 60,1,47104
mavv 156,44,76
s_add_u32 m0, s33, 3072
mavv 164,44,80
mavv 172,44,84
s_add_u32 s67, s67, s31
mavv 180,44,88
mavv 188,44,92
dm 15,24,67
mavv 192,48,64
rqv 44,1,43008
mavv 200,48,68
mavv 208,48,72
s_add_u32 m0, s32, 19456
mavv 216,48,76
mavv 224,48,80
s_add_u32 s61, s61, s30
mavv 232,48,84
mavv 240,48,88
dm 13,20,61
mavv 248,48,92
mavv 196,52,64
mavv 204,52,68
s_add_u32 s29, s29, s65
mavv 212,52,72
mavv 220,52,76
s_add_u32 s28, s28, 128
mavv 228,52,80
mavv 236,52,84
mavv 244,52,88
mavv 252,52,92
s_waitcnt vmcnt(16) lgkmcnt(0)
s_barrier
global_load_dwordx4 v[224:227], v16, s[4:5] sc1
mavv 0,24,96
rt 64,4,32768
mavv 8,24,100
s_add_u32 m0, s33, 16384
mavv 16,24,104
rt 66,4,34816
mavv 24,24,108
s_mov_b32 s67, s29
mavv 32,24,112
rt 68,5,32768
mavv 40,24,116
dm 14,24,67
mavv 48,24,120
rt 70,5,34816
mavv 56,24,124
s_add_u32 m0, s32, 32768
mavv 4,28,96
rt 72,6,32768
mavv 12,28,100
rqv 24,2,0
mavv 20,28,104
s_mov_b32 s61, s28
mavv 28,28,108
rt 74,6,34816
mavv 36,28,112
dm 12,20,61
mavv 44,28,116
rt 76,7,32768
mavv 52,28,120
s_add_u32 m0, s33, 17408
mavv 60,28,124
rt 78,7,34816
mavv 64,32,96
rt 80,8,32768
mavv 72,32,100
rqv 28,2,2048
mavv 80,32,104
s_add_u32 s67, s67, s31
mavv 88,32,108
rt 82,8,34816
mavv 96,32,112
dm 15,24,67
mavv 104,32,116
rt 84,9,32768
mavv 112,32,120
s_add_u32 m0, s32, 33792
mavv 120,32,124
rt 86,9,34816
mavv 68,36,96
s_add_u32 s61, s61, s30
mavv 76,36,100
rt 88,10,32768
mavv 84,36,104
rqv 32,2,4096
mavv 92,36,108
dm 13,20,61
mavv 100,36,112
rt 90,10,34816
mavv 108,36,116
s_add_u32 m0, s33, 18432
mavv 116,36,120
rt 92,11,32768
mavv 124,36,124
s_add_u32 s67, s67, s31
mavv 128,40,96
rt 94,11,34816
mavv 136,40,100
rqv 36,2,6144
mavv 144,40,104
dm 14,24,67
mavv 152,40,108
rqv 48,2,12288
mavv 160,40,112
s_add_u32 m0, s32, 34816
mavv 168,40,116
s_add_u32 s61, s61, s30
mavv 176,40,120
mavv 184,40,124
dm 12,20,61
mavv 132,44,96
rqv 40,2,8192
mavv 140,44,100
rqv 52,2,14336
mavv 148,44,104
s_add_u32 m0, s33, 19456
mavv 156,44,108
s_add_u32 s67, s67, s31
mavv 164,44,112
mavv 172,44,116
dm 15,24,67
mavv 180,44,120
mavv 188,44,124
mavv 192,56,96
rqv 44,2,10240
mavv 200,56,100
s_add_u32 m0, s32, 35840
mavv 208,56,104
s_add_u32 s61, s61, s30
mavv 216,56,108
mavv 224,56,112
dm 13,20,61
mavv 232,56,116
mavv 240,56,120
mavv 248,56,124
s_add_u32 s29, s29, s65
mavv 196,60,96
mavv 204,60,100
s_add_u32 s74, s78, .Lk1_BN+16384-.Lk1_base
s_addc_u32 s75, s79, 0
mavv 212,60,104
mavv 220,60,108
global_load_dword v19, v236, s[74:75]
mavv 228,60,112
mavv 236,60,116
mavv 244,60,120
mavv 252,60,124
s_waitcnt vmcnt(18) lgkmcnt(0)
s_barrier
mavv 0,24,64
rt 96,4,49152
mavv 8,24,68
s_add_u32 m0, s33, 32768
mavv 16,24,72
rt 98,4,51200
mavv 24,24,76
s_mov_b32 s67, s29
mavv 32,24,80
rt 100,5,49152
mavv 40,24,84
dm 14,24,67
mavv 48,24,88
rt 102,5,51200
mavv 56,24,92
s_add_u32 m0, s32, 49152
mavv 4,28,64
rt 104,6,49152
mavv 12,28,68
rqv 24,3,0
mavv 20,28,72
s_add_u32 s61, s28, s64
mavv 28,28,76
rt 106,6,51200
mavv 36,28,80
dm 12,20,61
mavv 44,28,84
rt 108,7,49152
mavv 52,28,88
s_add_u32 m0, s33, 33792
mavv 60,28,92
rt 110,7,51200
mavv 64,32,64
rt 112,8,49152
mavv 72,32,68
rqv 28,3,2048
mavv 80,32,72
s_add_u32 s67, s67, s31
mavv 88,32,76
rt 114,8,51200
mavv 96,32,80
dm 15,24,67
mavv 104,32,84
rt 116,9,49152
mavv 112,32,88
s_add_u32 m0, s32, 50176
mavv 120,32,92
rt 118,9,51200
mavv 68,36,64
s_add_u32 s61, s61, s30
mavv 76,36,68
rt 120,10,49152
mavv 84,36,72
rqv 32,3,4096
mavv 92,36,76
dm 13,20,61
mavv 100,36,80
rt 122,10,51200
mavv 108,36,84
s_add_u32 m0, s33, 34816
mavv 116,36,88
rt 124,11,49152
mavv 124,36,92
s_add_u32 s67, s67, s31
mavv 128,40,64
rt 126,11,51200
mavv 136,40,68
rqv 36,3,6144
mavv 144,40,72
dm 14,24,67
mavv 152,40,76
rqv 56,3,12288
mavv 160,40,80
s_add_u32 m0, s32, 51200
mavv 168,40,84
s_add_u32 s61, s61, s30
mavv 176,40,88
mavv 184,40,92
dm 12,20,61
mavv 132,44,64
rqv 40,3,8192
mavv 140,44,68
rqv 60,3,14336
mavv 148,44,72
s_add_u32 m0, s33, 35840
mavv 156,44,76
s_add_u32 s67, s67, s31
mavv 164,44,80
mavv 172,44,84
dm 15,24,67
mavv 180,44,88
mavv 188,44,92
mavv 192,48,64
rqv 44,3,10240
mavv 200,48,68
s_add_u32 m0, s32, 52224
mavv 208,48,72
s_add_u32 s61, s61, s30
mavv 216,48,76
mavv 224,48,80
dm 13,20,61
mavv 232,48,84
mavv 240,48,88
mavv 248,48,92
s_add_u32 s29, s29, s65
mavv 196,52,64
mavv 204,52,68
s_add_u32 s28, s28, 128
mavv 212,52,72
mavv 220,52,76
global_load_dword v19, v254, s[72:73] offset:1792
mavv 228,52,80
mavv 236,52,84
mavv 244,52,88
mavv 252,52,92
s_waitcnt vmcnt(19) lgkmcnt(0)
s_barrier
mavv 0,24,96
rt 64,4,0
mavv 8,24,100
s_add_u32 m0, s33, 49152
mavv 16,24,104
rt 66,4,2048
mavv 24,24,108
s_mov_b32 s67, s29
mavv 32,24,112
rt 68,5,0
mavv 40,24,116
dm 14,24,67
mavv 48,24,120
rt 70,5,2048
mavv 56,24,124
s_add_u32 m0, s32, 65536
mavv 4,28,96
rt 72,6,0
mavv 12,28,100
rqv 24,0,0
mavv 20,28,104
s_mov_b32 s61, s28
mavv 28,28,108
rt 74,6,2048
mavv 36,28,112
rt 76,7,0
mavv 44,28,116
dm 12,20,61
mavv 52,28,120
rt 78,7,2048
mavv 60,28,124
s_add_u32 m0, s33, 50176
mavv 64,32,96
rt 80,8,0
mavv 72,32,100
rqv 28,0,2048
mavv 80,32,104
s_add_u32 s67, s67, s31
mavv 88,32,108
rt 82,8,2048
mavv 96,32,112
dm 15,24,67
mavv 104,32,116
rt 84,9,0
mavv 112,32,120
rt 86,9,2048
mavv 120,32,124
s_add_u32 m0, s32, 66560
mavv 68,36,96
rt 88,10,0
mavv 76,36,100
rqv 32,0,4096
mavv 84,36,104
s_add_u32 s61, s61, s30
mavv 92,36,108
rt 90,10,2048
mavv 100,36,112
dm 13,20,61
mavv 108,36,116
rt 92,11,0
mavv 116,36,120
s_add_u32 m0, s33, 51200
mavv 124,36,124
rt 94,11,2048
mavv 128,40,96
rqv 36,0,6144
mavv 136,40,100
s_add_u32 s67, s67, s31
mavv 144,40,104
rqv 48,0,12288
mavv 152,40,108
dm 14,24,67
mavv 160,40,112
mavv 168,40,116
s_add_u32 m0, s32, 67584
mavv 176,40,120
mavv 184,40,124
s_add_u32 s61, s61, s30
mavv 132,44,96
rqv 40,0,8192
mavv 140,44,100
rqv 52,0,14336
mavv 148,44,104
dm 12,20,61
mavv 156,44,108
mavv 164,44,112
s_add_u32 m0, s33, 52224
mavv 172,44,116
mavv 180,44,120
mavv 188,44,124
s_add_u32 s67, s67, s31
mavv 192,56,96
rqv 44,0,10240
mavv 200,56,100
dm 15,24,67
mavv 208,56,104
mavv 216,56,108
mavv 224,56,112
s_add_u32 m0, s32, 68608
mavv 232,56,116
mavv 240,56,120
s_add_u32 s61, s61, s30
mavv 248,56,124
mavv 196,60,96
mavv 204,60,100
dm 13,20,61
mavv 212,60,104
mavv 220,60,108
s_add_u32 s29, s29, s65
mavv 228,60,112
mavv 236,60,116
mavv 244,60,120
mavv 252,60,124
s_cmp_lg_u32 s49, 0
s_cbranch_scc1 .Lk1_BN_acq
s_waitcnt vmcnt(16)
s_mov_b32 s63, 0x400000
.Lk1_BN_chk:
v_xor_b32 v224, s82, v224
v_xor_b32 v225, s82, v225
v_xor_b32 v226, s82, v226
v_xor_b32 v227, s82, v227
v_or3_b32 v224, v224, v225, v226
v_or_b32 v224, v224, v227
v_cmp_ne_u32 vcc, 0, v224
s_nop 4
s_cbranch_vccz .Lk1_BN_ok
s_sleep 8
global_load_dwordx4 v[224:227], v16, s[4:5] sc1
s_waitcnt vmcnt(0)
s_sub_u32 s63, s63, 1
s_cmp_lg_u32 s63, 0
s_cbranch_scc1 .Lk1_BN_chk
.Lk1_BN_ok:
s_cmp_lg_u32 s59, 0
s_cbranch_scc1 .Lk1_BN_acq
v_mov_b32 v20, 0
v_mov_b32 v21, s82
s_mov_b64 exec, 1
global_store_dword v20, v21, s[4:5] offset:1024 sc1
s_mov_b64 exec, -1
.Lk1_BN_acq:
s_waitcnt vmcnt(17) lgkmcnt(0)
s_barrier
mavv 0,24,64
rt 96,4,16384
mavv 8,24,68
s_add_u32 m0, s33, 0
mavv 16,24,72
rt 98,4,18432
mavv 24,24,76
s_mov_b32 s67, s29
mavv 32,24,80
rt 100,5,16384
mavv 40,24,84
dm 14,24,67
mavv 48,24,88
rt 102,5,18432
mavv 56,24,92
s_add_u32 m0, s32, 81920
mavv 4,28,64
rt 104,6,16384
mavv 12,28,68
rqv 24,1,0
mavv 20,28,72
s_add_u32 s61, s28, s64
mavv 28,28,76
rt 106,6,18432
mavv 36,28,80
dm 12,20,61
mavv 44,28,84
rt 108,7,16384
mavv 52,28,88
s_add_u32 m0, s33, 1024
mavv 60,28,92
rt 110,7,18432
mavv 64,32,64
s_add_u32 s67, s67, s31
mavv 72,32,68
rt 112,8,16384
mavv 80,32,72
rqv 28,1,2048
mavv 88,32,76
dm 15,24,67
mavv 96,32,80
rt 114,8,18432
mavv 104,32,84
s_add_u32 m0, s32, 82944
)ASM" ::: "memory");
  asm volatile(R"ASM(
mavv 112,32,88
rt 116,9,16384
mavv 120,32,92
rt 118,9,18432
mavv 68,36,64
s_add_u32 s61, s61, s30
rt 120,10,16384
mavv 76,36,68
rqv 32,1,4096
mavv 84,36,72
dm 13,20,61
mavv 92,36,76
rt 122,10,18432
mavv 100,36,80
s_add_u32 m0, s33, 2048
mavv 108,36,84
rt 124,11,16384
mavv 116,36,88
s_add_u32 s67, s67, s31
mavv 124,36,92
rt 126,11,18432
mavv 128,40,64
dm 14,24,67
mavv 136,40,68
rqv 36,1,6144
mavv 144,40,72
rqv 56,1,12288
mavv 152,40,76
s_add_u32 m0, s32, 83968
mavv 160,40,80
s_add_u32 s61, s61, s30
mavv 168,40,84
dm 12,20,61
mavv 176,40,88
mavv 184,40,92
s_add_u32 m0, s33, 3072
mavv 132,44,64
rqv 40,1,8192
mavv 140,44,68
rqv 60,1,14336
mavv 148,44,72
s_add_u32 s67, s67, s31
mavv 156,44,76
mavv 164,44,80
dm 15,24,67
mavv 172,44,84
mavv 180,44,88
s_add_u32 m0, s32, 84992
mavv 188,44,92
mavv 192,48,64
rqv 44,1,10240
mavv 200,48,68
s_add_u32 s61, s61, s30
mavv 208,48,72
dm 13,20,61
mavv 216,48,76
mavv 224,48,80
mavv 232,48,84
s_add_u32 s29, s29, s65
mavv 240,48,88
mavv 248,48,92
s_add_u32 s28, s28, 128
mavv 196,52,64
mavv 204,52,68
s_add_u32 s74, s78, .Lk1_BN+20480-.Lk1_base
s_addc_u32 s75, s79, 0
mavv 212,52,72
mavv 220,52,76
global_load_dword v19, v236, s[74:75]
mavv 228,52,80
mavv 236,52,84
mavv 244,52,88
mavv 252,52,92
s_waitcnt vmcnt(17) lgkmcnt(0)
s_barrier
mavv 0,24,96
rt 64,4,32768
mavv 8,24,100
s_add_u32 m0, s33, 16384
mavv 16,24,104
rt 66,4,34816
mavv 24,24,108
s_mov_b32 s67, s29
mavv 32,24,112
rt 68,5,32768
mavv 40,24,116
dm 14,24,67
mavv 48,24,120
rt 70,5,34816
mavv 56,24,124
s_add_u32 m0, s32, 0
mavv 4,28,96
rt 72,6,32768
mavv 12,28,100
rqv 24,0,32768
mavv 20,28,104
s_mov_b32 s61, s28
mavv 28,28,108
rt 74,6,34816
mavv 36,28,112
rt 76,7,32768
mavv 44,28,116
dm 12,20,61
mavv 52,28,120
rt 78,7,34816
mavv 60,28,124
s_add_u32 m0, s33, 17408
mavv 64,32,96
rt 80,8,32768
mavv 72,32,100
rqv 28,0,34816
mavv 80,32,104
s_add_u32 s67, s67, s31
mavv 88,32,108
rt 82,8,34816
mavv 96,32,112
dm 15,24,67
mavv 104,32,116
rt 84,9,32768
mavv 112,32,120
rt 86,9,34816
mavv 120,32,124
s_add_u32 m0, s32, 1024
mavv 68,36,96
rt 88,10,32768
mavv 76,36,100
rqv 32,0,36864
mavv 84,36,104
s_add_u32 s61, s61, s30
mavv 92,36,108
rt 90,10,34816
mavv 100,36,112
dm 13,20,61
mavv 108,36,116
rt 92,11,32768
mavv 116,36,120
s_add_u32 m0, s33, 18432
mavv 124,36,124
rt 94,11,34816
mavv 128,40,96
rqv 36,0,38912
mavv 136,40,100
s_add_u32 s67, s67, s31
mavv 144,40,104
rqv 48,0,45056
mavv 152,40,108
dm 14,24,67
mavv 160,40,112
mavv 168,40,116
s_add_u32 m0, s32, 2048
mavv 176,40,120
mavv 184,40,124
s_add_u32 s61, s61, s30
mavv 132,44,96
rqv 40,0,40960
mavv 140,44,100
rqv 52,0,47104
mavv 148,44,104
dm 12,20,61
mavv 156,44,108
mavv 164,44,112
s_add_u32 m0, s33, 19456
mavv 172,44,116
mavv 180,44,120
mavv 188,44,124
s_add_u32 s67, s67, s31
mavv 192,56,96
rqv 44,0,43008
mavv 200,56,100
dm 15,24,67
mavv 208,56,104
mavv 216,56,108
mavv 224,56,112
s_add_u32 m0, s32, 3072
mavv 232,56,116
mavv 240,56,120
s_add_u32 s61, s61, s30
mavv 248,56,124
mavv 196,60,96
mavv 204,60,100
dm 13,20,61
mavv 212,60,104
mavv 220,60,108
s_add_u32 s29, s29, s65
mavv 228,60,112
mavv 236,60,116
mavv 244,60,120
mavv 252,60,124
s_waitcnt vmcnt(17) lgkmcnt(0)
s_barrier
mavv 0,24,64
rt 96,4,49152
mavv 8,24,68
s_add_u32 m0, s33, 32768
mavv 16,24,72
rt 98,4,51200
mavv 24,24,76
s_mov_b32 s67, s29
mavv 32,24,80
rt 100,5,49152
mavv 40,24,84
dm 14,24,67
mavv 48,24,88
rt 102,5,51200
mavv 56,24,92
s_add_u32 m0, s32, 16384
mavv 4,28,64
rt 104,6,49152
mavv 12,28,68
rqv 24,1,32768
mavv 20,28,72
s_add_u32 s61, s28, s64
mavv 28,28,76
rt 106,6,51200
mavv 36,28,80
dm 12,20,61
mavv 44,28,84
rt 108,7,49152
mavv 52,28,88
s_add_u32 m0, s33, 33792
mavv 60,28,92
rt 110,7,51200
mavv 64,32,64
s_add_u32 s67, s67, s31
mavv 72,32,68
rt 112,8,49152
mavv 80,32,72
rqv 28,1,34816
mavv 88,32,76
dm 15,24,67
mavv 96,32,80
rt 114,8,51200
mavv 104,32,84
s_add_u32 m0, s32, 17408
mavv 112,32,88
rt 116,9,49152
mavv 120,32,92
rt 118,9,51200
mavv 68,36,64
s_add_u32 s61, s61, s30
rt 120,10,49152
mavv 76,36,68
rqv 32,1,36864
mavv 84,36,72
dm 13,20,61
mavv 92,36,76
rt 122,10,51200
mavv 100,36,80
s_add_u32 m0, s33, 34816
mavv 108,36,84
rt 124,11,49152
mavv 116,36,88
s_add_u32 s67, s67, s31
mavv 124,36,92
rt 126,11,51200
mavv 128,40,64
dm 14,24,67
mavv 136,40,68
rqv 36,1,38912
mavv 144,40,72
rqv 56,1,45056
mavv 152,40,76
s_add_u32 m0, s32, 18432
mavv 160,40,80
s_add_u32 s61, s61, s30
mavv 168,40,84
dm 12,20,61
mavv 176,40,88
mavv 184,40,92
s_add_u32 m0, s33, 35840
mavv 132,44,64
rqv 40,1,40960
mavv 140,44,68
rqv 60,1,47104
mavv 148,44,72
s_add_u32 s67, s67, s31
mavv 156,44,76
mavv 164,44,80
dm 15,24,67
mavv 172,44,84
mavv 180,44,88
s_add_u32 m0, s32, 19456
mavv 188,44,92
mavv 192,48,64
rqv 44,1,43008
mavv 200,48,68
s_add_u32 s61, s61, s30
mavv 208,48,72
dm 13,20,61
mavv 216,48,76
mavv 224,48,80
mavv 232,48,84
s_add_u32 s29, s29, s65
mavv 240,48,88
mavv 248,48,92
s_add_u32 s28, s28, 128
mavv 196,52,64
mavv 204,52,68
s_add_u32 s74, s78, .Lk1_packB+0-.Lk1_base
s_addc_u32 s75, s79, 0
mavv 212,52,72
mavv 220,52,76
global_load_dword v19, v236, s[74:75]
mavv 228,52,80
mavv 236,52,84
mavv 244,52,88
mavv 252,52,92
s_waitcnt vmcnt(17) lgkmcnt(0)
s_barrier
mavv 0,24,96
rt 64,4,0
mavv 8,24,100
s_add_u32 m0, s33, 49152
mavv 16,24,104
rt 66,4,2048
mavv 24,24,108
s_mov_b32 s67, s29
mavv 32,24,112
rt 68,5,0
mavv 40,24,116
dm 14,24,67
mavv 48,24,120
rt 70,5,2048
mavv 56,24,124
s_add_u32 m0, s32, 32768
mavv 4,28,96
rt 72,6,0
mavv 12,28,100
rqv 24,2,0
mavv 20,28,104
s_mov_b32 s61, s28
mavv 28,28,108
rt 74,6,2048
mavv 36,28,112
dm 12,20,61
mavv 44,28,116
rt 76,7,0
mavv 52,28,120
s_add_u32 m0, s33, 50176
mavv 60,28,124
rt 78,7,2048
mavv 64,32,96
rt 80,8,0
mavv 72,32,100
rqv 28,2,2048
mavv 80,32,104
s_add_u32 s67, s67, s31
mavv 88,32,108
rt 82,8,2048
mavv 96,32,112
dm 15,24,67
mavv 104,32,116
rt 84,9,0
mavv 112,32,120
s_add_u32 m0, s32, 33792
mavv 120,32,124
rt 86,9,2048
mavv 68,36,96
s_add_u32 s61, s61, s30
mavv 76,36,100
rt 88,10,0
mavv 84,36,104
rqv 32,2,4096
mavv 92,36,108
dm 13,20,61
mavv 100,36,112
rt 90,10,2048
mavv 108,36,116
s_add_u32 m0, s33, 51200
mavv 116,36,120
rt 92,11,0
mavv 124,36,124
s_add_u32 s67, s67, s31
mavv 128,40,96
rt 94,11,2048
mavv 136,40,100
rqv 36,2,6144
mavv 144,40,104
dm 14,24,67
mavv 152,40,108
rqv 48,2,12288
mavv 160,40,112
s_add_u32 m0, s32, 34816
mavv 168,40,116
s_add_u32 s61, s61, s30
mavv 176,40,120
mavv 184,40,124
dm 12,20,61
mavv 132,44,96
rqv 40,2,8192
mavv 140,44,100
rqv 52,2,14336
mavv 148,44,104
s_add_u32 m0, s33, 52224
mavv 156,44,108
s_add_u32 s67, s67, s31
mavv 164,44,112
mavv 172,44,116
dm 15,24,67
mavv 180,44,120
mavv 188,44,124
mavv 192,56,96
rqv 44,2,10240
mavv 200,56,100
s_add_u32 m0, s32, 35840
mavv 208,56,104
s_add_u32 s61, s61, s30
mavv 216,56,108
mavv 224,56,112
dm 13,20,61
mavv 232,56,116
mavv 240,56,120
mavv 248,56,124
s_add_u32 s29, s29, s65
mavv 196,60,96
mavv 204,60,100
s_add_u32 s74, s78, .Lk1_packB+4096-.Lk1_base
s_addc_u32 s75, s79, 0
mavv 212,60,104
mavv 220,60,108
global_load_dword v19, v236, s[74:75]
mavv 228,60,112
mavv 236,60,116
mavv 244,60,120
mavv 252,60,124
s_waitcnt vmcnt(18) lgkmcnt(0)
s_barrier
v_and_b32 v22, 0xf0, v16
v_and_b32 v21, 0x100, v16
v_lshl_add_u32 v22, v21, 2, v22
v_lshrrev_b32 v21, 1, v16
v_and_b32 v21, 0x100, v21
v_add_u32 v22, v22, v21
mavv 0,24,64
rt 96,4,16384
mavv 8,24,68
s_add_u32 m0, s33, 0
mavv 16,24,72
rt 98,4,18432
mavv 24,24,76
s_mov_b32 s67, s29
mavv 32,24,80
rt 100,5,16384
mavv 40,24,84
dm 14,24,67
mavv 48,24,88
rt 102,5,18432
mavv 56,24,92
s_add_u32 m0, s32, 49152
mavv 4,28,64
rt 104,6,16384
mavv 12,28,68
rqv 24,3,0
mavv 20,28,72
s_add_u32 s61, s28, s64
mavv 28,28,76
rt 106,6,18432
mavv 36,28,80
dm 12,20,61
mavv 44,28,84
rt 108,7,16384
mavv 52,28,88
s_add_u32 m0, s33, 1024
mavv 60,28,92
rt 110,7,18432
mavv 64,32,64
s_add_u32 s67, s67, s31
mavv 72,32,68
rt 112,8,16384
mavv 80,32,72
rqv 28,3,2048
mavv 88,32,76
dm 15,24,67
mavv 96,32,80
rt 114,8,18432
mavv 104,32,84
s_add_u32 m0, s32, 50176
mavv 112,32,88
rt 116,9,16384
mavv 120,32,92
rt 118,9,18432
mavv 68,36,64
s_add_u32 s61, s61, s30
rt 120,10,16384
mavv 76,36,68
rqv 32,3,4096
mavv 84,36,72
dm 13,20,61
mavv 92,36,76
rt 122,10,18432
mavv 100,36,80
s_add_u32 m0, s33, 2048
mavv 108,36,84
rt 124,11,16384
mavv 116,36,88
s_add_u32 s67, s67, s31
mavv 124,36,92
rt 126,11,18432
mavv 128,40,64
dm 14,24,67
mavv 136,40,68
rqv 36,3,6144
mavv 144,40,72
rqv 56,3,12288
mavv 152,40,76
s_add_u32 m0, s32, 51200
mavv 160,40,80
s_add_u32 s61, s61, s30
mavv 168,40,84
dm 12,20,61
mavv 176,40,88
mavv 184,40,92
s_add_u32 m0, s33, 3072
mavv 132,44,64
rqv 40,3,8192
mavv 140,44,68
rqv 60,3,14336
mavv 148,44,72
s_add_u32 s67, s67, s31
mavv 156,44,76
mavv 164,44,80
dm 15,24,67
mavv 172,44,84
mavv 180,44,88
s_add_u32 m0, s32, 52224
mavv 188,44,92
mavv 192,48,64
rqv 44,3,10240
mavv 200,48,68
s_add_u32 s61, s61, s30
mavv 208,48,72
dm 13,20,61
mavv 216,48,76
mavv 224,48,80
mavv 232,48,84
s_add_u32 s29, s29, s65
mavv 240,48,88
mavv 248,48,92
s_add_u32 s28, s28, 128
mavv 196,52,64
mavv 204,52,68
s_add_u32 s74, s78, .Lk1_packB+8192-.Lk1_base
s_addc_u32 s75, s79, 0
mavv 212,52,72
mavv 220,52,76
global_load_dword v19, v236, s[74:75]
mavv 228,52,80
mavv 236,52,84
mavv 244,52,88
mavv 252,52,92
s_waitcnt vmcnt(18) lgkmcnt(0)
s_barrier
mavv 0,24,96
rt 64,4,32768
mavv 8,24,100
s_add_u32 m0, s33, 16384
mavv 16,24,104
s_mov_b32 s67, s29
mavv 24,24,108
rt 66,4,34816
mavv 32,24,112
dm 14,24,67
mavv 40,24,116
rt 68,5,32768
mavv 48,24,120
s_add_u32 m0, s33, 17408
mavv 56,24,124
rt 70,5,34816
mavv 4,28,96
s_add_u32 s67, s67, s31
dm 15,24,67
mavv 12,28,100
rt 72,6,32768
mavv 20,28,104
rqv 24,0,0
mavv 28,28,108
s_add_u32 m0, s33, 18432
mavv 36,28,112
rt 74,6,34816
s_add_u32 s67, s67, s31
mavv 44,28,116
rt 76,7,32768
mavv 52,28,120
dm 14,24,67
mavv 60,28,124
rt 78,7,34816
mavv 64,32,96
s_add_u32 m0, s33, 19456
s_add_u32 s67, s67, s31
mavv 72,32,100
rt 80,8,32768
mavv 80,32,104
rqv 28,0,2048
mavv 88,32,108
dm 15,24,67
mavv 96,32,112
rt 82,8,34816
s_add_u32 s29, s29, s65
mavv 104,32,116
rt 84,9,32768
)ASM" ::: "memory");
  asm volatile(R"ASM(
mavv 112,32,120
s_add_u32 m0, s17, 65536
mavv 120,32,124
rt 86,9,34816
mavv 68,36,96
s_add_u32 s96, s96, s97
buffer_load_dwordx4 v16, s[92:95], s96 offen sc1 lds
mavv 76,36,100
rt 88,10,32768
mavv 84,36,104
rqv 32,0,4096
mavv 92,36,108
s_add_u32 m0, s17, 69632
mavv 100,36,112
rt 90,10,34816
s_add_u32 s96, s96, s97
mavv 108,36,116
rt 92,11,32768
mavv 116,36,120
buffer_load_dwordx4 v16, s[92:95], s96 offen sc1 lds
mavv 124,36,124
rt 94,11,34816
mavv 128,40,96
s_add_u32 m0, s17, 73728
mavv 136,40,100
s_add_u32 s96, s96, s97
rqv 36,0,6144
mavv 144,40,104
rqv 48,0,12288
mavv 152,40,108
buffer_load_dwordx4 v16, s[92:95], s96 offen sc1 lds
mavv 160,40,112
s_add_u32 m0, s17, 77824
mavv 168,40,116
s_add_u32 s96, s96, s97
mavv 176,40,120
buffer_load_dwordx4 v16, s[92:95], s96 offen sc1 lds
mavv 184,40,124
s_add_u32 m0, s17, 81920
mavv 132,44,96
rqv 40,0,8192
mavv 140,44,100
rqv 52,0,14336
mavv 148,44,104
s_add_u32 s96, s96, s97
mavv 156,44,108
buffer_load_dwordx4 v16, s[92:95], s96 offen sc1 lds
mavv 164,44,112
s_add_u32 m0, s17, 86016
mavv 172,44,116
mavv 180,44,120
s_add_u32 s96, s96, s97
mavv 188,44,124
buffer_load_dwordx4 v16, s[92:95], s96 offen sc1 lds
mavv 192,56,96
rqv 44,0,10240
mavv 200,56,100
s_add_u32 m0, s17, 90112
mavv 208,56,104
s_add_u32 s96, s96, s97
mavv 216,56,108
mavv 224,56,112
buffer_load_dwordx4 v16, s[92:95], s96 offen sc1 lds
mavv 232,56,116
mavv 240,56,120
s_add_u32 m0, s17, 94208
mavv 248,56,124
s_add_u32 s96, s96, s97
mavv 196,60,96
mavv 204,60,100
buffer_load_dwordx4 v16, s[92:95], s96 offen sc1 lds
mavv 212,60,104
s_add_u32 s74, s78, .Lk1_packB+12288-.Lk1_base
s_addc_u32 s75, s79, 0
mavv 220,60,108
mavv 228,60,112
global_load_dword v19, v236, s[74:75]
mavv 236,60,116
mavv 244,60,120
mavv 252,60,124
s_waitcnt vmcnt(22) lgkmcnt(0)
s_barrier
mavv 0,24,64
rt 96,4,49152
mavv 8,24,68
s_add_u32 m0, s17, 131072
mavv 16,24,72
rt 98,4,51200
mavv 24,24,76
mavv 32,24,80
rt 100,5,49152
mavv 40,24,84
s_add_u32 s96, s96, s97
mavv 48,24,88
rt 102,5,51200
mavv 56,24,92
mavv 4,28,64
rt 104,6,49152
mavv 12,28,68
rqv 24,1,0
mavv 20,28,72
buffer_load_dwordx4 v16, s[92:95], s96 offen sc1 lds
mavv 28,28,76
rt 106,6,51200
mavv 36,28,80
rt 108,7,49152
mavv 44,28,84
s_add_u32 m0, s17, 135168
mavv 52,28,88
rt 110,7,51200
mavv 60,28,92
mavv 64,32,64
rt 112,8,49152
mavv 72,32,68
rqv 28,1,2048
mavv 80,32,72
s_add_u32 s96, s96, s97
mavv 88,32,76
rt 114,8,51200
mavv 96,32,80
rt 116,9,49152
mavv 104,32,84
mavv 112,32,88
rt 118,9,51200
mavv 120,32,92
buffer_load_dwordx4 v16, s[92:95], s96 offen sc1 lds
mavv 68,36,64
rt 120,10,49152
mavv 76,36,68
rqv 32,1,4096
mavv 84,36,72
rt 122,10,51200
mavv 92,36,76
s_add_u32 m0, s17, 139264
mavv 100,36,80
rt 124,11,49152
mavv 108,36,84
mavv 116,36,88
rt 126,11,51200
mavv 124,36,92
s_add_u32 s96, s96, s97
mavv 128,40,64
rqv 36,1,6144
mavv 136,40,68
rqv 56,1,12288
mavv 144,40,72
mavv 152,40,76
buffer_load_dwordx4 v16, s[92:95], s96 offen sc1 lds
mavv 160,40,80
mavv 168,40,84
mavv 176,40,88
mavv 184,40,92
s_add_u32 m0, s17, 143360
mavv 132,44,64
rqv 40,1,8192
mavv 140,44,68
rqv 60,1,14336
mavv 148,44,72
mavv 156,44,76
mavv 164,44,80
s_add_u32 s96, s96, s97
mavv 172,44,84
mavv 180,44,88
mavv 188,44,92
mavv 192,48,64
rqv 44,1,10240
mavv 200,48,68
buffer_load_dwordx4 v16, s[92:95], s96 offen sc1 lds
mavv 208,48,72
mavv 216,48,76
mavv 224,48,80
mavv 232,48,84
s_add_u32 s74, s78, .Lk1_packB+16384-.Lk1_base
s_addc_u32 s75, s79, 0
mavv 240,48,88
mavv 248,48,92
mavv 196,52,64
mavv 204,52,68
global_load_dword v19, v236, s[74:75]
mavv 212,52,72
mavv 220,52,76
mavv 228,52,80
mavv 236,52,84
mavv 244,52,88
mavv 252,52,92
s_waitcnt vmcnt(18) lgkmcnt(0)
s_barrier
mavv 0,24,96
rt 64,4,0
mavv 8,24,100
s_add_u32 m0, s17, 0
mavv 16,24,104
rt 66,4,2048
mavv 24,24,108
s_add_u32 s96, s96, s97
mavv 32,24,112
rt 68,5,0
mavv 40,24,116
buffer_load_dwordx4 v16, s[92:95], s96 offen sc1 lds
mavv 48,24,120
rt 70,5,2048
mavv 56,24,124
s_add_u32 m0, s17, 4096
mavv 4,28,96
rt 72,6,0
mavv 12,28,100
rqv 24,0,32768
mavv 20,28,104
s_add_u32 s96, s96, s97
mavv 28,28,108
rt 74,6,2048
mavv 36,28,112
buffer_load_dwordx4 v16, s[92:95], s96 offen sc1 lds
mavv 44,28,116
rt 76,7,0
mavv 52,28,120
rt 78,7,2048
mavv 60,28,124
s_add_u32 m0, s17, 8192
mavv 64,32,96
rt 80,8,0
mavv 72,32,100
rqv 28,0,34816
mavv 80,32,104
s_add_u32 s96, s96, s97
mavv 88,32,108
rt 82,8,2048
mavv 96,32,112
buffer_load_dwordx4 v16, s[92:95], s96 offen sc1 lds
mavv 104,32,116
rt 84,9,0
mavv 112,32,120
s_add_u32 m0, s17, 12288
mavv 120,32,124
rt 86,9,2048
mavv 68,36,96
s_add_u32 s96, s96, s97
mavv 76,36,100
rt 88,10,0
mavv 84,36,104
rqv 32,0,36864
mavv 92,36,108
rt 90,10,2048
mavv 100,36,112
buffer_load_dwordx4 v16, s[92:95], s96 offen sc1 lds
mavv 108,36,116
rt 92,11,0
mavv 116,36,120
s_add_u32 m0, s17, 16384
mavv 124,36,124
rt 94,11,2048
mavv 128,40,96
s_add_u32 s96, s96, s97
mavv 136,40,100
rqv 36,0,38912
mavv 144,40,104
rqv 48,0,45056
mavv 152,40,108
buffer_load_dwordx4 v16, s[92:95], s96 offen sc1 lds
mavv 160,40,112
s_add_u32 m0, s17, 20480
mavv 168,40,116
mavv 176,40,120
s_add_u32 s96, s96, s97
mavv 184,40,124
mavv 132,44,96
rqv 40,0,40960
mavv 140,44,100
buffer_load_dwordx4 v16, s[92:95], s96 offen sc1 lds
mavv 148,44,104
rqv 52,0,47104
mavv 156,44,108
s_add_u32 m0, s17, 24576
mavv 164,44,112
mavv 172,44,116
s_add_u32 s96, s96, s97
mavv 180,44,120
mavv 188,44,124
buffer_load_dwordx4 v16, s[92:95], s96 offen sc1 lds
mavv 192,56,96
rqv 44,0,43008
mavv 200,56,100
mavv 208,56,104
s_add_u32 m0, s17, 28672
mavv 216,56,108
mavv 224,56,112
s_add_u32 s96, s96, s97
mavv 232,56,116
mavv 240,56,120
buffer_load_dwordx4 v16, s[92:95], s96 offen sc1 lds
mavv 248,56,124
mavv 196,60,96
mavv 204,60,100
s_add_u32 s74, s78, .Lk1_packB+20480-.Lk1_base
s_addc_u32 s75, s79, 0
mavv 212,60,104
mavv 220,60,108
global_load_dword v19, v236, s[74:75]
mavv 228,60,112
mavv 236,60,116
mavv 244,60,120
mavv 252,60,124
s_waitcnt vmcnt(14) lgkmcnt(0)
s_barrier
mavv 0,24,64
rt 96,4,16384
mavv 8,24,68
s_add_u32 m0, s17, 147456
mavv 16,24,72
rt 98,4,18432
mavv 24,24,76
s_add_u32 s96, s96, s97
mavv 32,24,80
rt 100,5,16384
mavv 40,24,84
buffer_load_dwordx4 v16, s[92:95], s96 offen sc1 lds
mavv 48,24,88
rt 102,5,18432
mavv 56,24,92
s_add_u32 m0, s17, 151552
mavv 4,28,64
rt 104,6,16384
mavv 12,28,68
rqv 24,1,32768
mavv 20,28,72
s_add_u32 s96, s96, s97
mavv 28,28,76
rt 106,6,18432
mavv 36,28,80
buffer_load_dwordx4 v16, s[92:95], s96 offen sc1 lds
mavv 44,28,84
rt 108,7,16384
mavv 52,28,88
rt 110,7,18432
mavv 60,28,92
s_add_u32 m0, s17, 155648
mavv 64,32,64
rt 112,8,16384
mavv 72,32,68
rqv 28,1,34816
mavv 80,32,72
s_add_u32 s96, s96, s97
mavv 88,32,76
rt 114,8,18432
mavv 96,32,80
buffer_load_dwordx4 v16, s[92:95], s96 offen sc1 lds
mavv 104,32,84
rt 116,9,16384
mavv 112,32,88
s_add_u32 m0, s17, 159744
mavv 120,32,92
rt 118,9,18432
mavv 68,36,64
s_add_u32 s96, s96, s97
mavv 76,36,68
rt 120,10,16384
mavv 84,36,72
rqv 32,1,36864
mavv 92,36,76
rt 122,10,18432
mavv 100,36,80
buffer_load_dwordx4 v16, s[92:95], s96 offen sc1 lds
mavv 108,36,84
rt 124,11,16384
mavv 116,36,88
s_add_u32 m0, s17, 98304
mavv 124,36,92
rt 126,11,18432
mavv 128,40,64
s_add_u32 s96, s96, s97
mavv 136,40,68
rqv 36,1,38912
mavv 144,40,72
rqv 56,1,45056
mavv 152,40,76
buffer_load_dwordx4 v16, s[92:95], s96 offen sc1 lds
mavv 160,40,80
s_add_u32 m0, s17, 102400
mavv 168,40,84
mavv 176,40,88
s_add_u32 s96, s96, s97
mavv 184,40,92
mavv 132,44,64
rqv 40,1,40960
mavv 140,44,68
buffer_load_dwordx4 v16, s[92:95], s96 offen sc1 lds
mavv 148,44,72
rqv 60,1,47104
mavv 156,44,76
s_add_u32 m0, s17, 106496
mavv 164,44,80
mavv 172,44,84
s_add_u32 s96, s96, s97
mavv 180,44,88
mavv 188,44,92
buffer_load_dwordx4 v16, s[92:95], s96 offen sc1 lds
mavv 192,48,64
rqv 44,1,43008
mavv 200,48,68
mavv 208,48,72
s_add_u32 m0, s17, 110592
mavv 216,48,76
mavv 224,48,80
s_add_u32 s96, s96, s97
mavv 232,48,84
mavv 240,48,88
buffer_load_dwordx4 v16, s[92:95], s96 offen sc1 lds
mavv 248,48,92
mavv 196,52,64
mavv 204,52,68
s_add_u32 s74, s78, .Lk1_packB+24576-.Lk1_base
s_addc_u32 s75, s79, 0
mavv 212,52,72
mavv 220,52,76
global_load_dword v19, v236, s[74:75]
mavv 228,52,80
mavv 236,52,84
mavv 244,52,88
mavv 252,52,92
s_waitcnt vmcnt(18) lgkmcnt(0)
s_barrier
mavv 0,24,96
s_add_u32 s74, s78, .Lk1_packB+28672-.Lk1_base
s_addc_u32 s75, s79, 0
mavv 8,24,100
mavv 16,24,104
mavv 24,24,108
global_load_dword v19, v236, s[74:75]
mavv 32,24,112
mavv 40,24,116
mavv 48,24,120
s_add_u32 m0, s17, 114688
mavv 56,24,124
mavv 4,28,96
mavv 12,28,100
mavv 20,28,104
s_add_u32 s96, s96, s97
mavv 28,28,108
mavv 36,28,112
ar 80,0
ar 81,1
ar 82,2
ar 83,3
ar 84,4
ar 85,5
mavv 44,28,116
buffer_load_dwordx4 v16, s[92:95], s96 offen sc1 lds
ar 86,6
ar 87,7
cv 128,80,81
cv 129,82,83
cv 130,84,85
cv 131,86,87
mavv 52,28,120
ar 88,8
ar 89,9
ar 90,10
ar 91,11
ar 92,12
ar 93,13
mavv 60,28,124
ar 94,14
ar 95,15
cv 132,88,89
cv 133,90,91
cv 134,92,93
cv 135,94,95
mavv 64,32,96
ar 80,16
ar 81,17
ar 82,18
ar 83,19
ar 84,20
ar 85,21
mavv 72,32,100
s_add_u32 m0, s17, 118784
ar 86,22
ar 87,23
cv 136,80,81
cv 137,82,83
cv 138,84,85
cv 139,86,87
mavv 80,32,104
ar 88,24
ar 89,25
ar 90,26
ar 91,27
ar 92,28
ar 93,29
mavv 88,32,108
ar 94,30
ar 95,31
cv 140,88,89
cv 141,90,91
cv 142,92,93
cv 143,94,95
mavv 96,32,112
s_add_u32 s96, s96, s97
ar 80,32
ar 81,33
ar 82,34
ar 83,35
ar 84,36
ar 85,37
mavv 104,32,116
ar 86,38
ar 87,39
cv 144,80,81
cv 145,82,83
cv 146,84,85
cv 147,86,87
mavv 112,32,120
ar 88,40
ar 89,41
ar 90,42
ar 91,43
ar 92,44
ar 93,45
mavv 120,32,124
ar 94,46
ar 95,47
cv 148,88,89
cv 149,90,91
cv 150,92,93
cv 151,94,95
mavv 68,36,96
buffer_load_dwordx4 v16, s[92:95], s96 offen sc1 lds
ar 80,48
ar 81,49
ar 82,50
ar 83,51
ar 84,52
ar 85,53
mavv 76,36,100
ar 86,54
ar 87,55
cv 152,80,81
cv 153,82,83
cv 154,84,85
cv 155,86,87
mavv 84,36,104
ar 88,56
ar 89,57
ar 90,58
ar 91,59
ar 92,60
ar 93,61
mavv 92,36,108
s_add_u32 m0, s17, 122880
ar 94,62
ar 95,63
cv 156,88,89
cv 157,90,91
cv 158,92,93
cv 159,94,95
mavv 100,36,112
ar 80,64
ar 81,65
ar 82,66
ar 83,67
ar 84,68
ar 85,69
mavv 108,36,116
ar 86,70
ar 87,71
cv 160,80,81
cv 161,82,83
cv 162,84,85
cv 163,86,87
mavv 116,36,120
ar 88,72
ar 89,73
ar 90,74
ar 91,75
ar 92,76
ar 93,77
mavv 124,36,124
s_add_u32 s96, s96, s97
ar 94,78
ar 95,79
cv 164,88,89
cv 165,90,91
cv 166,92,93
cv 167,94,95
mavv 128,40,96
ar 80,80
ar 81,81
ar 82,82
ar 83,83
ar 84,84
ar 85,85
mavv 136,40,100
ar 86,86
ar 87,87
cv 168,80,81
cv 169,82,83
cv 170,84,85
cv 171,86,87
mavv 144,40,104
buffer_load_dwordx4 v16, s[92:95], s96 offen sc1 lds
)ASM" ::: "memory");
  asm volatile(R"ASM(
ar 88,88
ar 89,89
ar 90,90
ar 91,91
ar 92,92
ar 93,93
mavv 152,40,108
ar 94,94
ar 95,95
cv 172,88,89
cv 173,90,91
cv 174,92,93
cv 175,94,95
mavv 160,40,112
ar 80,96
ar 81,97
ar 82,98
ar 83,99
ar 84,100
ar 85,101
mavv 168,40,116
s_add_u32 m0, s17, 126976
ar 86,102
ar 87,103
cv 176,80,81
cv 177,82,83
cv 178,84,85
cv 179,86,87
mavv 176,40,120
ar 88,104
ar 89,105
ar 90,106
ar 91,107
ar 92,108
ar 93,109
mavv 184,40,124
ar 94,110
ar 95,111
cv 180,88,89
cv 181,90,91
cv 182,92,93
cv 183,94,95
mavv 132,44,96
ar 80,112
ar 81,113
ar 82,114
ar 83,115
ar 84,116
ar 85,117
mavv 140,44,100
s_add_u32 s96, s96, s97
ar 86,118
ar 87,119
cv 184,80,81
cv 185,82,83
cv 186,84,85
cv 187,86,87
mavv 148,44,104
ar 88,120
ar 89,121
ar 90,122
ar 91,123
ar 92,124
ar 93,125
mavv 156,44,108
ar 94,126
ar 95,127
cv 188,88,89
cv 189,90,91
cv 190,92,93
cv 191,94,95
mavv 164,44,112
buffer_load_dwordx4 v16, s[92:95], s96 offen sc1 lds
ar 80,128
ar 81,129
ar 82,130
ar 83,131
ar 84,132
ar 85,133
mavv 172,44,116
ar 86,134
ar 87,135
cv 192,80,81
cv 193,82,83
cv 194,84,85
cv 195,86,87
mavv 180,44,120
ar 88,136
ar 89,137
ar 90,138
ar 91,139
ar 92,140
ar 93,141
mavv 188,44,124
ar 94,142
ar 95,143
cv 196,88,89
cv 197,90,91
cv 198,92,93
cv 199,94,95
mavv 192,56,96
global_load_dwordx4 v[64:67], v22, s[98:99] offset:0 sc1
ar 80,144
ar 81,145
ar 82,146
ar 83,147
ar 84,148
ar 85,149
mavv 200,56,100
ar 86,150
ar 87,151
cv 200,80,81
cv 201,82,83
cv 202,84,85
cv 203,86,87
mavv 208,56,104
ar 88,152
ar 89,153
ar 90,154
ar 91,155
ar 92,156
ar 93,157
mavv 216,56,108
global_load_dwordx4 v[68:71], v22, s[98:99] offset:512 sc1
ar 94,158
ar 95,159
cv 204,88,89
cv 205,90,91
cv 206,92,93
cv 207,94,95
mavv 224,56,112
ar 80,160
ar 81,161
ar 82,162
ar 83,163
ar 84,164
ar 85,165
mavv 232,56,116
ar 86,166
ar 87,167
cv 208,80,81
cv 209,82,83
cv 210,84,85
cv 211,86,87
mavv 240,56,120
ar 88,168
ar 89,169
ar 90,170
ar 91,171
ar 92,172
ar 93,173
mavv 248,56,124
global_load_dwordx4 v[72:75], v22, s[98:99] offset:2048 sc1
ar 94,174
ar 95,175
cv 212,88,89
cv 213,90,91
cv 214,92,93
cv 215,94,95
mavv 196,60,96
ar 80,176
ar 81,177
ar 82,178
ar 83,179
ar 84,180
ar 85,181
mavv 204,60,100
ar 86,182
ar 87,183
cv 216,80,81
cv 217,82,83
cv 218,84,85
cv 219,86,87
mavv 212,60,104
global_load_dwordx4 v[76:79], v22, s[98:99] offset:2560 sc1
ar 88,184
ar 89,185
ar 90,186
ar 91,187
ar 92,188
ar 93,189
mavv 220,60,108
ar 94,190
ar 95,191
cv 220,88,89
cv 221,90,91
cv 222,92,93
cv 223,94,95
mavv 228,60,112
ar 80,192
ar 81,193
ar 82,194
ar 83,195
ar 84,196
ar 85,197
mavv 236,60,116
ar 86,198
ar 87,199
cv 224,80,81
cv 225,82,83
cv 226,84,85
cv 227,86,87
mavv 244,60,120
ar 88,200
ar 89,201
ar 90,202
ar 91,203
ar 92,204
ar 93,205
mavv 252,60,124
ar 94,206
ar 95,207
cv 228,88,89
cv 229,90,91
cv 230,92,93
cv 231,94,95
s_nop 5
v_accvgpr_write_b32 a192, v237
v_accvgpr_write_b32 a193, v238
v_accvgpr_write_b32 a194, v239
v_accvgpr_write_b32 a195, v240
v_accvgpr_write_b32 a196, v241
v_accvgpr_write_b32 a197, v242
v_accvgpr_write_b32 a198, v243
v_accvgpr_write_b32 a199, v244
v_accvgpr_write_b32 a200, v245
v_accvgpr_write_b32 a201, v246
v_accvgpr_write_b32 a202, v247
v_accvgpr_write_b32 a203, v248
v_accvgpr_write_b32 a204, v249
v_accvgpr_write_b32 a205, v250
v_accvgpr_write_b32 a206, v251
ar 80,208
ar 81,209
ar 82,210
ar 83,211
ar 84,212
ar 85,213
ar 86,214
ar 87,215
cv 232,80,81
cv 233,82,83
cv 234,84,85
cv 235,86,87
ar 88,216
ar 89,217
ar 90,218
ar 91,219
ar 92,220
ar 93,221
ar 94,222
ar 95,223
cv 236,88,89
cv 237,90,91
cv 238,92,93
cv 239,94,95
ar 80,224
ar 81,225
ar 82,226
ar 83,227
ar 84,228
ar 85,229
ar 86,230
ar 87,231
cv 240,80,81
cv 241,82,83
cv 242,84,85
cv 243,86,87
ar 88,232
ar 89,233
ar 90,234
ar 91,235
ar 92,236
ar 93,237
ar 94,238
ar 95,239
cv 244,88,89
cv 245,90,91
cv 246,92,93
cv 247,94,95
ar 80,240
ar 81,241
ar 82,242
ar 83,243
ar 84,244
ar 85,245
ar 86,246
ar 87,247
cv 248,80,81
cv 249,82,83
cv 250,84,85
cv 251,86,87
ar 88,248
ar 89,249
ar 90,250
ar 91,251
ar 92,252
ar 93,253
ar 94,254
ar 95,255
cv 252,88,89
cv 253,90,91
cv 254,92,93
cv 255,94,95
.Lk1_packB:
v_mov_b32 v0, v16
v_add_u32 v1, s18, v22
v_add_u32 v2, 0x10000, v1
v_add_u32 v3, 0x20000, v1
v_mov_b32 v11, v1
s_mov_b32 s71, 0x4b804b80
s_mov_b32 s76, 0
s_mov_b32 s77, -1
s_waitcnt vmcnt(0)
s_barrier
rqa 96,2,0
rqa 100,2,512
rqa 104,2,4096
rqa 108,2,4608
v_add_u32 v6, 67584, v11
ds_read2_b64 a[112:115], v6 offset0:0 offset1:64
ds_read2_b64 a[116:119], v6 offset0:1 offset1:65
v_cndmask_b32_e64 v70, v70, 0, s[76:77]
v_cndmask_b32_e64 v71, v71, 0, s[76:77]
v_cndmask_b32_e64 v78, v78, 0, s[76:77]
v_cndmask_b32_e64 v79, v79, 0, s[76:77]
ar 8,205
s_nop 0
v_readlane_b32 s20, v8, 0
v_readlane_b32 s21, v8, 1
v_readlane_b32 s22, v8, 2
v_readlane_b32 s23, v8, 3
v_readlane_b32 s24, v8, 4
v_readlane_b32 s25, v8, 5
v_readlane_b32 s26, v8, 6
v_readlane_b32 s27, v8, 7
v_readlane_b32 s28, v8, 8
v_readlane_b32 s29, v8, 9
v_readlane_b32 s30, v8, 10
v_readlane_b32 s31, v8, 11
v_readlane_b32 s32, v8, 12
v_readlane_b32 s33, v8, 13
v_readlane_b32 s34, v8, 14
v_readlane_b32 s35, v8, 15
v_readlane_b32 s36, v8, 16
v_readlane_b32 s37, v8, 17
v_readlane_b32 s38, v8, 18
v_readlane_b32 s39, v8, 19
v_readlane_b32 s40, v8, 20
v_readlane_b32 s41, v8, 21
v_readlane_b32 s42, v8, 22
v_readlane_b32 s43, v8, 23
v_readlane_b32 s44, v8, 24
v_readlane_b32 s45, v8, 25
v_readlane_b32 s46, v8, 26
v_readlane_b32 s47, v8, 27
v_readlane_b32 s48, v8, 28
v_readlane_b32 s49, v8, 29
v_readlane_b32 s50, v8, 30
v_readlane_b32 s51, v8, 31
v_readlane_b32 s96, v8, 32
v_readlane_b32 s97, v8, 33
v_readlane_b32 s98, v8, 34
v_readlane_b32 s99, v8, 35
v_readlane_b32 s60, v8, 36
v_readlane_b32 s61, v8, 37
v_readlane_b32 s62, v8, 38
v_readlane_b32 s63, v8, 39
v_readlane_b32 s66, v8, 40
v_lshlrev_b32 v12, 2, v0
v_lshrrev_b32 v13, 2, v0
v_and_b32 v13, 60, v13
s_mov_b32 s65, 0x10000
s_mov_b32 s64, 0
s_waitcnt lgkmcnt(4)
v_mfma_f32_32x32x16_bf16 v[48:63], a[96:99], v[64:67], 0
v_mfma_f32_32x32x16_bf16 v[80:95], a[96:99], v[72:75], 0
v_mfma_f32_32x32x16_bf16 v[48:63], a[100:103], v[68:71], v[48:63]
v_mfma_f32_32x32x16_bf16 v[80:95], a[100:103], v[76:79], v[80:95]
s_nop 11
m3 8,48,49,50
m3 9,51,52,53
m3 9,9,54,55
m3 8,8,56,57
m3 9,9,58,59
m3 8,8,60,61
m3 9,9,62,63
v_max_f32 v8, v8, v9
v_mov_b32 v9, v8
s_nop 1
v_permlane32_swap_b32 v8, v9
s_nop 0
v_max_f32 v8, v8, v9
cv 24,8,8
v_and_b32 v24, 0xffff0000, v24
v_sub_f32 v8, v8, v24
cv 25,8,8
v_and_b32 v25, 0xffff0000, v25
v_sub_f32 v8, v25, v8
v_cvt_pk_bf16_f32 v26, -v24, -v25
v_cvt_pk_bf16_f32 v27, v8, 0
v_add_f32 v24, v24, v25
v_lshlrev_b32 v8, 16, v27
v_sub_f32 v8, v24, v8
v_mov_b32 v9, v8
v_mov_b32 v4, v8
v_sub_f32 v10, 0, v9
ex 10
v_cndmask_b32_e64 v70, v70, v26, s[76:77]
v_cndmask_b32_e64 v71, v71, v27, s[76:77]
v_sub_f32 v48, v48, v9
v_sub_f32 v49, v49, v9
v_sub_f32 v50, v50, v9
v_sub_f32 v51, v51, v9
v_sub_f32 v52, v52, v9
v_sub_f32 v53, v53, v9
v_sub_f32 v54, v54, v9
v_sub_f32 v55, v55, v9
v_sub_f32 v56, v56, v9
v_sub_f32 v57, v57, v9
v_sub_f32 v58, v58, v9
v_sub_f32 v59, v59, v9
v_sub_f32 v60, v60, v9
v_sub_f32 v61, v61, v9
v_sub_f32 v62, v62, v9
v_sub_f32 v63, v63, v9
m3 8,80,81,82
m3 9,83,84,85
m3 9,9,86,87
m3 8,8,88,89
m3 9,9,90,91
m3 8,8,92,93
m3 9,9,94,95
v_max_f32 v8, v8, v9
v_mov_b32 v9, v8
s_nop 1
v_permlane32_swap_b32 v8, v9
s_nop 0
v_max_f32 v8, v8, v9
cv 24,8,8
v_and_b32 v24, 0xffff0000, v24
v_sub_f32 v8, v8, v24
cv 25,8,8
v_and_b32 v25, 0xffff0000, v25
v_sub_f32 v8, v25, v8
v_cvt_pk_bf16_f32 v26, -v24, -v25
v_cvt_pk_bf16_f32 v27, v8, 0
v_add_f32 v24, v24, v25
v_lshlrev_b32 v8, 16, v27
v_sub_f32 v8, v24, v8
v_mov_b32 v9, v8
v_mov_b32 v5, v8
v_sub_f32 v10, 0, v9
ex 10
v_cndmask_b32_e64 v78, v78, v26, s[76:77]
v_cndmask_b32_e64 v79, v79, v27, s[76:77]
v_sub_f32 v80, v80, v9
v_sub_f32 v81, v81, v9
v_sub_f32 v82, v82, v9
v_sub_f32 v83, v83, v9
v_sub_f32 v84, v84, v9
v_sub_f32 v85, v85, v9
v_sub_f32 v86, v86, v9
v_sub_f32 v87, v87, v9
v_sub_f32 v88, v88, v9
v_sub_f32 v89, v89, v9
v_sub_f32 v90, v90, v9
v_sub_f32 v91, v91, v9
v_sub_f32 v92, v92, v9
v_sub_f32 v93, v93, v9
v_sub_f32 v94, v94, v9
v_sub_f32 v95, v95, v9
s_waitcnt lgkmcnt(0)
ex 48
ex 49
ex 50
ex 51
cv 16,48,49
rqa 96,2,8192
rqa 100,2,8704
ex 52
ex 53
cv 17,50,51
ex 54
ex 55
cv 18,52,53
v_pk_maximum3_f16 v8, v16, v17, v18
ex 56
ex 57
cv 19,54,55
ex 58
ex 59
cv 20,56,57
v_pk_maximum3_f16 v8, v8, v19, v20
ex 60
ex 61
cv 21,58,59
s_mov_b32 s68, s56
ex 62
ex 63
cv 22,60,61
v_pk_maximum3_f16 v8, v8, v21, v22
v_add_u32 v7, 71680, v11
ds_read2_b64 a[120:123], v7 offset0:0 offset1:64
ds_read2_b64 a[124:127], v7 offset0:1 offset1:65
v_mfma_f32_32x32x16_bf16 v[96:111], a[104:107], v[64:67], 0
ex 80
ex 81
cv 23,62,63
ex 82
ex 83
cv 24,80,81
v_pk_maximum3_f16 v8, v8, v23, v24
s_mov_b32 s69, s68
v_mfma_f32_32x32x16_bf16 v[112:127], a[104:107], v[72:75], 0
ex 84
ex 85
cv 25,82,83
ex 86
ex 87
cv 26,84,85
v_pk_maximum3_f16 v8, v8, v25, v26
v_mfma_f32_32x32x16_bf16 v[96:111], a[108:111], v[68:71], v[96:111]
ex 88
ex 89
cv 27,86,87
buffer_store_dwordx4 v[128:131], v0, s[52:55], s68 offen offset:0 sc1
ex 90
ex 91
cv 28,88,89
v_pk_maximum3_f16 v8, v8, v27, v28
v_mfma_f32_32x32x16_bf16 v[112:127], a[108:111], v[76:79], v[112:127]
ex 92
ex 93
cv 29,90,91
ex 94
ex 95
cv 30,92,93
v_pk_maximum3_f16 v8, v8, v29, v30
buffer_store_dwordx4 v[132:135], v0, s[52:55], s68 offen offset:1024 sc1
s_nop 0
cv 31,94,95
v_pk_max_u16 v8, v8, v31
v_pk_max_u16 v8, v8, v8 op_sel:[0,1] op_sel_hi:[1,0]
v_cmp_lt_u32 vcc, s71, v8
s_cbranch_vccnz .Lk1_atr0
.Lk1_aret0:
s_waitcnt lgkmcnt(0)
v_mfma_f32_32x32x16_bf16 a[64:79], a[112:115], v[16:19], 0
ex 96
ex 97
ex 98
ex 99
cv 32,96,97
rqa 104,2,12288
rqa 108,2,12800
v_mfma_f32_32x32x16_bf16 a[80:95], a[112:115], v[24:27], 0
ex 100
ex 101
cv 33,98,99
ex 102
ex 103
cv 34,100,101
v_pk_maximum3_f16 v8, v32, v33, v34
v_mfma_f32_32x32x16_bf16 a[64:79], a[116:119], v[20:23], a[64:79]
ex 104
ex 105
cv 35,102,103
ex 106
ex 107
cv 36,104,105
v_pk_maximum3_f16 v8, v8, v35, v36
v_mfma_f32_32x32x16_bf16 a[80:95], a[116:119], v[28:31], a[80:95]
ex 108
ex 109
cv 37,106,107
ex 110
ex 111
cv 38,108,109
v_pk_maximum3_f16 v8, v8, v37, v38
v_add_u32 v6, 75776, v11
ds_read2_b64 a[112:115], v6 offset0:0 offset1:64
ds_read2_b64 a[116:119], v6 offset0:1 offset1:65
v_mfma_f32_32x32x16_bf16 v[48:63], a[96:99], v[64:67], 0
ex 112
ex 113
cv 39,110,111
ex 114
ex 115
cv 40,112,113
v_pk_maximum3_f16 v8, v8, v39, v40
buffer_store_dwordx4 v[136:139], v0, s[52:55], s68 offen offset:2048 sc1
v_mfma_f32_32x32x16_bf16 v[80:95], a[96:99], v[72:75], 0
ex 116
ex 117
cv 41,114,115
ex 118
ex 119
cv 42,116,117
v_pk_maximum3_f16 v8, v8, v41, v42
)ASM" ::: "memory");
  asm volatile(R"ASM(
v_mfma_f32_32x32x16_bf16 v[48:63], a[100:103], v[68:71], v[48:63]
ex 120
ex 121
cv 43,118,119
ex 122
ex 123
cv 44,120,121
v_pk_maximum3_f16 v8, v8, v43, v44
v_mfma_f32_32x32x16_bf16 v[80:95], a[100:103], v[76:79], v[80:95]
ex 124
ex 125
cv 45,122,123
ex 126
ex 127
cv 46,124,125
v_pk_maximum3_f16 v8, v8, v45, v46
buffer_store_dwordx4 v[140:143], v0, s[52:55], s68 offen offset:3072 sc1
s_nop 0
cv 47,126,127
v_pk_max_u16 v8, v8, v47
v_pk_max_u16 v8, v8, v8 op_sel:[0,1] op_sel_hi:[1,0]
v_cmp_lt_u32 vcc, s71, v8
s_cbranch_vccnz .Lk1_atr1
.Lk1_aret1:
s_waitcnt lgkmcnt(0)
v_mfma_f32_32x32x16_bf16 a[64:79], a[120:123], v[32:35], a[64:79]
ex 48
ex 49
ex 50
ex 51
cv 16,48,49
rqa 96,2,16384
rqa 100,2,16896
v_mfma_f32_32x32x16_bf16 a[80:95], a[120:123], v[40:43], a[80:95]
ex 52
ex 53
cv 17,50,51
ex 54
ex 55
cv 18,52,53
v_pk_maximum3_f16 v8, v16, v17, v18
v_mfma_f32_32x32x16_bf16 a[64:79], a[124:127], v[36:39], a[64:79]
ex 56
ex 57
cv 19,54,55
ex 58
ex 59
cv 20,56,57
v_pk_maximum3_f16 v8, v8, v19, v20
v_mfma_f32_32x32x16_bf16 a[80:95], a[124:127], v[44:47], a[80:95]
ex 60
ex 61
cv 21,58,59
ex 62
ex 63
cv 22,60,61
v_pk_maximum3_f16 v8, v8, v21, v22
v_add_u32 v7, 79872, v11
ds_read2_b64 a[120:123], v7 offset0:0 offset1:64
ds_read2_b64 a[124:127], v7 offset0:1 offset1:65
s_add_u32 s68, s68, s58
v_mfma_f32_32x32x16_bf16 v[96:111], a[104:107], v[64:67], 0
ex 80
ex 81
cv 23,62,63
ex 82
ex 83
cv 24,80,81
v_pk_maximum3_f16 v8, v8, v23, v24
v_mfma_f32_32x32x16_bf16 v[112:127], a[104:107], v[72:75], 0
ex 84
ex 85
cv 25,82,83
ex 86
ex 87
cv 26,84,85
v_pk_maximum3_f16 v8, v8, v25, v26
buffer_store_dwordx4 v[144:147], v0, s[52:55], s68 offen offset:0 sc1
v_mfma_f32_32x32x16_bf16 v[96:111], a[108:111], v[68:71], v[96:111]
ex 88
ex 89
cv 27,86,87
ex 90
ex 91
cv 28,88,89
v_pk_maximum3_f16 v8, v8, v27, v28
v_mfma_f32_32x32x16_bf16 v[112:127], a[108:111], v[76:79], v[112:127]
ex 92
ex 93
cv 29,90,91
ex 94
ex 95
cv 30,92,93
v_pk_maximum3_f16 v8, v8, v29, v30
buffer_store_dwordx4 v[148:151], v0, s[52:55], s68 offen offset:1024 sc1
s_nop 0
cv 31,94,95
v_pk_max_u16 v8, v8, v31
v_pk_max_u16 v8, v8, v8 op_sel:[0,1] op_sel_hi:[1,0]
v_cmp_lt_u32 vcc, s71, v8
s_cbranch_vccnz .Lk1_atr2
.Lk1_aret2:
s_waitcnt lgkmcnt(0)
v_mfma_f32_32x32x16_bf16 a[64:79], a[112:115], v[16:19], a[64:79]
ex 96
ex 97
ex 98
ex 99
cv 32,96,97
rqa 104,2,20480
rqa 108,2,20992
v_mfma_f32_32x32x16_bf16 a[80:95], a[112:115], v[24:27], a[80:95]
ex 100
ex 101
cv 33,98,99
ex 102
ex 103
cv 34,100,101
v_pk_maximum3_f16 v8, v32, v33, v34
v_mfma_f32_32x32x16_bf16 a[64:79], a[116:119], v[20:23], a[64:79]
ex 104
ex 105
cv 35,102,103
ex 106
ex 107
cv 36,104,105
v_pk_maximum3_f16 v8, v8, v35, v36
v_mfma_f32_32x32x16_bf16 a[80:95], a[116:119], v[28:31], a[80:95]
ex 108
ex 109
cv 37,106,107
ex 110
ex 111
cv 38,108,109
v_pk_maximum3_f16 v8, v8, v37, v38
v_add_u32 v6, 83968, v11
ds_read2_b64 a[112:115], v6 offset0:0 offset1:64
ds_read2_b64 a[116:119], v6 offset0:1 offset1:65
v_mfma_f32_32x32x16_bf16 v[48:63], a[96:99], v[64:67], 0
ex 112
ex 113
cv 39,110,111
ex 114
ex 115
cv 40,112,113
v_pk_maximum3_f16 v8, v8, v39, v40
buffer_store_dwordx4 v[152:155], v0, s[52:55], s68 offen offset:2048 sc1
v_mfma_f32_32x32x16_bf16 v[80:95], a[96:99], v[72:75], 0
ex 116
ex 117
cv 41,114,115
ex 118
ex 119
cv 42,116,117
v_pk_maximum3_f16 v8, v8, v41, v42
v_mfma_f32_32x32x16_bf16 v[48:63], a[100:103], v[68:71], v[48:63]
ex 120
ex 121
cv 43,118,119
ex 122
ex 123
cv 44,120,121
v_pk_maximum3_f16 v8, v8, v43, v44
v_mfma_f32_32x32x16_bf16 v[80:95], a[100:103], v[76:79], v[80:95]
ex 124
ex 125
cv 45,122,123
ex 126
ex 127
cv 46,124,125
v_pk_maximum3_f16 v8, v8, v45, v46
buffer_store_dwordx4 v[156:159], v0, s[52:55], s68 offen offset:3072 sc1
s_nop 0
cv 47,126,127
v_pk_max_u16 v8, v8, v47
v_pk_max_u16 v8, v8, v8 op_sel:[0,1] op_sel_hi:[1,0]
v_cmp_lt_u32 vcc, s71, v8
s_cbranch_vccnz .Lk1_atr3
.Lk1_aret3:
s_waitcnt lgkmcnt(0)
v_mfma_f32_32x32x16_bf16 a[64:79], a[120:123], v[32:35], a[64:79]
ex 48
ex 49
ex 50
ex 51
cv 16,48,49
rqa 96,2,24576
rqa 100,2,25088
v_mfma_f32_32x32x16_bf16 a[80:95], a[120:123], v[40:43], a[80:95]
ex 52
ex 53
cv 17,50,51
ex 54
ex 55
cv 18,52,53
v_pk_maximum3_f16 v8, v16, v17, v18
v_mfma_f32_32x32x16_bf16 a[64:79], a[124:127], v[36:39], a[64:79]
ex 56
ex 57
cv 19,54,55
ex 58
ex 59
cv 20,56,57
v_pk_maximum3_f16 v8, v8, v19, v20
v_mfma_f32_32x32x16_bf16 a[80:95], a[124:127], v[44:47], a[80:95]
ex 60
ex 61
cv 21,58,59
s_add_u32 s68, s69, s57
ex 62
ex 63
cv 22,60,61
v_pk_maximum3_f16 v8, v8, v21, v22
v_add_u32 v7, 88064, v11
ds_read2_b64 a[120:123], v7 offset0:0 offset1:64
ds_read2_b64 a[124:127], v7 offset0:1 offset1:65
v_mfma_f32_32x32x16_bf16 v[96:111], a[104:107], v[64:67], 0
ex 80
ex 81
cv 23,62,63
ex 82
ex 83
cv 24,80,81
v_pk_maximum3_f16 v8, v8, v23, v24
s_mov_b32 s69, s68
v_mfma_f32_32x32x16_bf16 v[112:127], a[104:107], v[72:75], 0
ex 84
ex 85
cv 25,82,83
ex 86
ex 87
cv 26,84,85
v_pk_maximum3_f16 v8, v8, v25, v26
v_mfma_f32_32x32x16_bf16 v[96:111], a[108:111], v[68:71], v[96:111]
ex 88
ex 89
cv 27,86,87
buffer_store_dwordx4 v[160:163], v0, s[52:55], s68 offen offset:0 sc1
ex 90
ex 91
cv 28,88,89
v_pk_maximum3_f16 v8, v8, v27, v28
v_mfma_f32_32x32x16_bf16 v[112:127], a[108:111], v[76:79], v[112:127]
ex 92
ex 93
cv 29,90,91
ex 94
ex 95
cv 30,92,93
v_pk_maximum3_f16 v8, v8, v29, v30
buffer_store_dwordx4 v[164:167], v0, s[52:55], s68 offen offset:1024 sc1
s_nop 0
cv 31,94,95
v_pk_max_u16 v8, v8, v31
v_pk_max_u16 v8, v8, v8 op_sel:[0,1] op_sel_hi:[1,0]
v_cmp_lt_u32 vcc, s71, v8
s_cbranch_vccnz .Lk1_atr4
.Lk1_aret4:
s_waitcnt lgkmcnt(0)
v_mfma_f32_32x32x16_bf16 a[64:79], a[112:115], v[16:19], a[64:79]
ex 96
ex 97
ex 98
ex 99
cv 32,96,97
rqa 104,2,28672
rqa 108,2,29184
v_mfma_f32_32x32x16_bf16 a[80:95], a[112:115], v[24:27], a[80:95]
ex 100
ex 101
cv 33,98,99
ex 102
ex 103
cv 34,100,101
v_pk_maximum3_f16 v8, v32, v33, v34
v_mfma_f32_32x32x16_bf16 a[64:79], a[116:119], v[20:23], a[64:79]
ex 104
ex 105
cv 35,102,103
ex 106
ex 107
cv 36,104,105
v_pk_maximum3_f16 v8, v8, v35, v36
v_mfma_f32_32x32x16_bf16 a[80:95], a[116:119], v[28:31], a[80:95]
ex 108
ex 109
cv 37,106,107
ex 110
ex 111
cv 38,108,109
v_pk_maximum3_f16 v8, v8, v37, v38
v_add_u32 v6, 92160, v11
ds_read2_b64 a[112:115], v6 offset0:0 offset1:64
ds_read2_b64 a[116:119], v6 offset0:1 offset1:65
v_mfma_f32_32x32x16_bf16 v[48:63], a[96:99], v[64:67], 0
ex 112
ex 113
cv 39,110,111
ex 114
ex 115
cv 40,112,113
v_pk_maximum3_f16 v8, v8, v39, v40
buffer_store_dwordx4 v[168:171], v0, s[52:55], s68 offen offset:2048 sc1
v_mfma_f32_32x32x16_bf16 v[80:95], a[96:99], v[72:75], 0
ex 116
ex 117
cv 41,114,115
ex 118
ex 119
cv 42,116,117
v_pk_maximum3_f16 v8, v8, v41, v42
v_mfma_f32_32x32x16_bf16 v[48:63], a[100:103], v[68:71], v[48:63]
ex 120
ex 121
cv 43,118,119
ex 122
ex 123
cv 44,120,121
v_pk_maximum3_f16 v8, v8, v43, v44
v_mfma_f32_32x32x16_bf16 v[80:95], a[100:103], v[76:79], v[80:95]
ex 124
ex 125
cv 45,122,123
ex 126
ex 127
cv 46,124,125
v_pk_maximum3_f16 v8, v8, v45, v46
buffer_store_dwordx4 v[172:175], v0, s[52:55], s68 offen offset:3072 sc1
s_nop 0
cv 47,126,127
v_pk_max_u16 v8, v8, v47
v_pk_max_u16 v8, v8, v8 op_sel:[0,1] op_sel_hi:[1,0]
v_cmp_lt_u32 vcc, s71, v8
s_cbranch_vccnz .Lk1_atr5
.Lk1_aret5:
s_waitcnt lgkmcnt(0)
v_mfma_f32_32x32x16_bf16 a[64:79], a[120:123], v[32:35], a[64:79]
ex 48
ex 49
ex 50
ex 51
cv 16,48,49
rqa 96,3,0
rqa 100,3,512
v_mfma_f32_32x32x16_bf16 a[80:95], a[120:123], v[40:43], a[80:95]
ex 52
ex 53
cv 17,50,51
ex 54
ex 55
cv 18,52,53
v_pk_maximum3_f16 v8, v16, v17, v18
v_mfma_f32_32x32x16_bf16 a[64:79], a[124:127], v[36:39], a[64:79]
ex 56
ex 57
cv 19,54,55
ex 58
ex 59
cv 20,56,57
v_pk_maximum3_f16 v8, v8, v19, v20
v_mfma_f32_32x32x16_bf16 a[80:95], a[124:127], v[44:47], a[80:95]
ex 60
ex 61
cv 21,58,59
ex 62
ex 63
cv 22,60,61
v_pk_maximum3_f16 v8, v8, v21, v22
v_add_u32 v7, 96256, v11
ds_read2_b64 a[120:123], v7 offset0:0 offset1:64
ds_read2_b64 a[124:127], v7 offset0:1 offset1:65
s_add_u32 s68, s68, s58
v_mfma_f32_32x32x16_bf16 v[96:111], a[104:107], v[64:67], 0
ex 80
ex 81
cv 23,62,63
ex 82
ex 83
cv 24,80,81
v_pk_maximum3_f16 v8, v8, v23, v24
v_mfma_f32_32x32x16_bf16 v[112:127], a[104:107], v[72:75], 0
ex 84
ex 85
cv 25,82,83
ex 86
ex 87
cv 26,84,85
v_pk_maximum3_f16 v8, v8, v25, v26
buffer_store_dwordx4 v[176:179], v0, s[52:55], s68 offen offset:0 sc1
v_mfma_f32_32x32x16_bf16 v[96:111], a[108:111], v[68:71], v[96:111]
ex 88
ex 89
cv 27,86,87
ex 90
ex 91
cv 28,88,89
v_pk_maximum3_f16 v8, v8, v27, v28
v_mfma_f32_32x32x16_bf16 v[112:127], a[108:111], v[76:79], v[112:127]
ex 92
ex 93
cv 29,90,91
ex 94
ex 95
cv 30,92,93
v_pk_maximum3_f16 v8, v8, v29, v30
buffer_store_dwordx4 v[180:183], v0, s[52:55], s68 offen offset:1024 sc1
s_nop 0
cv 31,94,95
v_pk_max_u16 v8, v8, v31
v_pk_max_u16 v8, v8, v8 op_sel:[0,1] op_sel_hi:[1,0]
v_cmp_lt_u32 vcc, s71, v8
s_cbranch_vccnz .Lk1_atr6
.Lk1_aret6:
s_waitcnt lgkmcnt(0)
v_mfma_f32_32x32x16_bf16 a[64:79], a[112:115], v[16:19], a[64:79]
ex 96
ex 97
ex 98
ex 99
cv 32,96,97
rqa 104,3,4096
rqa 108,3,4608
v_mfma_f32_32x32x16_bf16 a[80:95], a[112:115], v[24:27], a[80:95]
ex 100
ex 101
cv 33,98,99
ex 102
ex 103
cv 34,100,101
v_pk_maximum3_f16 v8, v32, v33, v34
v_mfma_f32_32x32x16_bf16 a[64:79], a[116:119], v[20:23], a[64:79]
ex 104
ex 105
cv 35,102,103
ex 106
ex 107
cv 36,104,105
v_pk_maximum3_f16 v8, v8, v35, v36
v_mfma_f32_32x32x16_bf16 a[80:95], a[116:119], v[28:31], a[80:95]
ex 108
ex 109
cv 37,106,107
ex 110
ex 111
cv 38,108,109
v_pk_maximum3_f16 v8, v8, v37, v38
v_add_u32 v6, 133120, v11
ds_read2_b64 a[112:115], v6 offset0:0 offset1:64
ds_read2_b64 a[116:119], v6 offset0:1 offset1:65
v_mfma_f32_32x32x16_bf16 v[48:63], a[96:99], v[64:67], 0
ex 112
ex 113
cv 39,110,111
ex 114
ex 115
cv 40,112,113
v_pk_maximum3_f16 v8, v8, v39, v40
buffer_store_dwordx4 v[184:187], v0, s[52:55], s68 offen offset:2048 sc1
v_mfma_f32_32x32x16_bf16 v[80:95], a[96:99], v[72:75], 0
ex 116
ex 117
cv 41,114,115
ex 118
ex 119
cv 42,116,117
v_pk_maximum3_f16 v8, v8, v41, v42
v_mfma_f32_32x32x16_bf16 v[48:63], a[100:103], v[68:71], v[48:63]
ex 120
ex 121
cv 43,118,119
ex 122
ex 123
cv 44,120,121
v_pk_maximum3_f16 v8, v8, v43, v44
v_mfma_f32_32x32x16_bf16 v[80:95], a[100:103], v[76:79], v[80:95]
ex 124
ex 125
cv 45,122,123
ex 126
ex 127
cv 46,124,125
v_pk_maximum3_f16 v8, v8, v45, v46
buffer_store_dwordx4 v[188:191], v0, s[52:55], s68 offen offset:3072 sc1
s_nop 0
cv 47,126,127
v_pk_max_u16 v8, v8, v47
v_pk_max_u16 v8, v8, v8 op_sel:[0,1] op_sel_hi:[1,0]
v_cmp_lt_u32 vcc, s71, v8
s_cbranch_vccnz .Lk1_atr7
.Lk1_aret7:
s_waitcnt lgkmcnt(0)
v_mfma_f32_32x32x16_bf16 a[64:79], a[120:123], v[32:35], a[64:79]
ex 48
ex 49
ex 50
ex 51
cv 16,48,49
rqa 96,3,8192
rqa 100,3,8704
v_mfma_f32_32x32x16_bf16 a[80:95], a[120:123], v[40:43], a[80:95]
ex 52
ex 53
cv 17,50,51
ex 54
ex 55
cv 18,52,53
v_pk_maximum3_f16 v8, v16, v17, v18
v_mfma_f32_32x32x16_bf16 a[64:79], a[124:127], v[36:39], a[64:79]
ex 56
ex 57
cv 19,54,55
ex 58
ex 59
cv 20,56,57
v_pk_maximum3_f16 v8, v8, v19, v20
v_mfma_f32_32x32x16_bf16 a[80:95], a[124:127], v[44:47], a[80:95]
ex 60
ex 61
cv 21,58,59
s_add_u32 s68, s69, s57
ex 62
ex 63
cv 22,60,61
v_pk_maximum3_f16 v8, v8, v21, v22
v_add_u32 v7, 137216, v11
ds_read2_b64 a[120:123], v7 offset0:0 offset1:64
ds_read2_b64 a[124:127], v7 offset0:1 offset1:65
v_mfma_f32_32x32x16_bf16 v[96:111], a[104:107], v[64:67], 0
ex 80
ex 81
cv 23,62,63
ex 82
ex 83
cv 24,80,81
v_pk_maximum3_f16 v8, v8, v23, v24
s_mov_b32 s69, s68
v_mfma_f32_32x32x16_bf16 v[112:127], a[104:107], v[72:75], 0
ex 84
ex 85
cv 25,82,83
ex 86
ex 87
cv 26,84,85
v_pk_maximum3_f16 v8, v8, v25, v26
v_mfma_f32_32x32x16_bf16 v[96:111], a[108:111], v[68:71], v[96:111]
ex 88
ex 89
cv 27,86,87
buffer_store_dwordx4 v[192:195], v0, s[52:55], s68 offen offset:0 sc1
ex 90
ex 91
cv 28,88,89
v_pk_maximum3_f16 v8, v8, v27, v28
v_mfma_f32_32x32x16_bf16 v[112:127], a[108:111], v[76:79], v[112:127]
ex 92
ex 93
cv 29,90,91
ex 94
ex 95
cv 30,92,93
v_pk_maximum3_f16 v8, v8, v29, v30
buffer_store_dwordx4 v[196:199], v0, s[52:55], s68 offen offset:1024 sc1
s_nop 0
cv 31,94,95
v_pk_max_u16 v8, v8, v31
v_pk_max_u16 v8, v8, v8 op_sel:[0,1] op_sel_hi:[1,0]
v_cmp_lt_u32 vcc, s71, v8
s_cbranch_vccnz .Lk1_atr8
.Lk1_aret8:
s_waitcnt lgkmcnt(0)
v_mfma_f32_32x32x16_bf16 a[64:79], a[112:115], v[16:19], a[64:79]
ex 96
ex 97
ex 98
ex 99
cv 32,96,97
rqa 104,3,12288
rqa 108,3,12800
v_mfma_f32_32x32x16_bf16 a[80:95], a[112:115], v[24:27], a[80:95]
ex 100
ex 101
cv 33,98,99
ex 102
ex 103
cv 34,100,101
v_pk_maximum3_f16 v8, v32, v33, v34
v_mfma_f32_32x32x16_bf16 a[64:79], a[116:119], v[20:23], a[64:79]
ex 104
ex 105
cv 35,102,103
ex 106
ex 107
cv 36,104,105
v_pk_maximum3_f16 v8, v8, v35, v36
v_mfma_f32_32x32x16_bf16 a[80:95], a[116:119], v[28:31], a[80:95]
ex 108
ex 109
cv 37,106,107
ex 110
ex 111
)ASM" ::: "memory");
  asm volatile(R"ASM(
cv 38,108,109
v_pk_maximum3_f16 v8, v8, v37, v38
v_add_u32 v6, 141312, v11
ds_read2_b64 a[112:115], v6 offset0:0 offset1:64
ds_read2_b64 a[116:119], v6 offset0:1 offset1:65
v_mfma_f32_32x32x16_bf16 v[48:63], a[96:99], v[64:67], 0
ex 112
ex 113
cv 39,110,111
ex 114
ex 115
cv 40,112,113
v_pk_maximum3_f16 v8, v8, v39, v40
buffer_store_dwordx4 v[200:203], v0, s[52:55], s68 offen offset:2048 sc1
v_mfma_f32_32x32x16_bf16 v[80:95], a[96:99], v[72:75], 0
ex 116
ex 117
cv 41,114,115
ex 118
ex 119
cv 42,116,117
v_pk_maximum3_f16 v8, v8, v41, v42
v_mfma_f32_32x32x16_bf16 v[48:63], a[100:103], v[68:71], v[48:63]
ex 120
ex 121
cv 43,118,119
ex 122
ex 123
cv 44,120,121
v_pk_maximum3_f16 v8, v8, v43, v44
v_mfma_f32_32x32x16_bf16 v[80:95], a[100:103], v[76:79], v[80:95]
ex 124
ex 125
cv 45,122,123
ex 126
ex 127
cv 46,124,125
v_pk_maximum3_f16 v8, v8, v45, v46
buffer_store_dwordx4 v[204:207], v0, s[52:55], s68 offen offset:3072 sc1
s_nop 0
cv 47,126,127
v_pk_max_u16 v8, v8, v47
v_pk_max_u16 v8, v8, v8 op_sel:[0,1] op_sel_hi:[1,0]
v_cmp_lt_u32 vcc, s71, v8
s_cbranch_vccnz .Lk1_atr9
.Lk1_aret9:
s_waitcnt lgkmcnt(0)
v_mfma_f32_32x32x16_bf16 a[64:79], a[120:123], v[32:35], a[64:79]
ex 48
ex 49
ex 50
ex 51
cv 16,48,49
rqa 96,1,0
rqa 100,1,512
v_mfma_f32_32x32x16_bf16 a[80:95], a[120:123], v[40:43], a[80:95]
ex 52
ex 53
cv 17,50,51
ex 54
ex 55
cv 18,52,53
v_pk_maximum3_f16 v8, v16, v17, v18
v_mfma_f32_32x32x16_bf16 a[64:79], a[124:127], v[36:39], a[64:79]
ex 56
ex 57
cv 19,54,55
ex 58
ex 59
cv 20,56,57
v_pk_maximum3_f16 v8, v8, v19, v20
v_mfma_f32_32x32x16_bf16 a[80:95], a[124:127], v[44:47], a[80:95]
ex 60
ex 61
cv 21,58,59
ex 62
ex 63
cv 22,60,61
v_pk_maximum3_f16 v8, v8, v21, v22
v_add_u32 v7, 145408, v11
ds_read2_b64 a[120:123], v7 offset0:0 offset1:64
ds_read2_b64 a[124:127], v7 offset0:1 offset1:65
s_add_u32 s68, s68, s58
v_mfma_f32_32x32x16_bf16 v[96:111], a[104:107], v[64:67], 0
ex 80
ex 81
cv 23,62,63
ex 82
ex 83
cv 24,80,81
v_pk_maximum3_f16 v8, v8, v23, v24
v_mfma_f32_32x32x16_bf16 v[112:127], a[104:107], v[72:75], 0
ex 84
ex 85
cv 25,82,83
ex 86
ex 87
cv 26,84,85
v_pk_maximum3_f16 v8, v8, v25, v26
buffer_store_dwordx4 v[208:211], v0, s[52:55], s68 offen offset:0 sc1
v_mfma_f32_32x32x16_bf16 v[96:111], a[108:111], v[68:71], v[96:111]
ex 88
ex 89
cv 27,86,87
ex 90
ex 91
cv 28,88,89
v_pk_maximum3_f16 v8, v8, v27, v28
v_mfma_f32_32x32x16_bf16 v[112:127], a[108:111], v[76:79], v[112:127]
ex 92
ex 93
cv 29,90,91
ex 94
ex 95
cv 30,92,93
v_pk_maximum3_f16 v8, v8, v29, v30
buffer_store_dwordx4 v[212:215], v0, s[52:55], s68 offen offset:1024 sc1
s_nop 0
cv 31,94,95
v_pk_max_u16 v8, v8, v31
v_pk_max_u16 v8, v8, v8 op_sel:[0,1] op_sel_hi:[1,0]
v_cmp_lt_u32 vcc, s71, v8
s_cbranch_vccnz .Lk1_atr10
.Lk1_aret10:
s_waitcnt lgkmcnt(0)
v_mfma_f32_32x32x16_bf16 a[64:79], a[112:115], v[16:19], a[64:79]
ex 96
ex 97
ex 98
ex 99
cv 32,96,97
rqa 104,1,4096
rqa 108,1,4608
v_mfma_f32_32x32x16_bf16 a[80:95], a[112:115], v[24:27], a[80:95]
ex 100
ex 101
cv 33,98,99
ex 102
ex 103
cv 34,100,101
v_pk_maximum3_f16 v8, v32, v33, v34
v_mfma_f32_32x32x16_bf16 a[64:79], a[116:119], v[20:23], a[64:79]
ex 104
ex 105
cv 35,102,103
ex 106
ex 107
cv 36,104,105
v_pk_maximum3_f16 v8, v8, v35, v36
v_mfma_f32_32x32x16_bf16 a[80:95], a[116:119], v[28:31], a[80:95]
ex 108
ex 109
cv 37,106,107
ex 110
ex 111
cv 38,108,109
v_pk_maximum3_f16 v8, v8, v37, v38
v_add_u32 v6, 2048, v11
ds_read2_b64 a[112:115], v6 offset0:0 offset1:64
ds_read2_b64 a[116:119], v6 offset0:1 offset1:65
v_mfma_f32_32x32x16_bf16 v[48:63], a[96:99], v[64:67], 0
ex 112
ex 113
cv 39,110,111
ex 114
ex 115
cv 40,112,113
v_pk_maximum3_f16 v8, v8, v39, v40
buffer_store_dwordx4 v[216:219], v0, s[52:55], s68 offen offset:2048 sc1
v_mfma_f32_32x32x16_bf16 v[80:95], a[96:99], v[72:75], 0
ex 116
ex 117
cv 41,114,115
ex 118
ex 119
cv 42,116,117
v_pk_maximum3_f16 v8, v8, v41, v42
v_mfma_f32_32x32x16_bf16 v[48:63], a[100:103], v[68:71], v[48:63]
ex 120
ex 121
cv 43,118,119
ex 122
ex 123
cv 44,120,121
v_pk_maximum3_f16 v8, v8, v43, v44
v_mfma_f32_32x32x16_bf16 v[80:95], a[100:103], v[76:79], v[80:95]
ex 124
ex 125
cv 45,122,123
ex 126
ex 127
cv 46,124,125
v_pk_maximum3_f16 v8, v8, v45, v46
buffer_store_dwordx4 v[220:223], v0, s[52:55], s68 offen offset:3072 sc1
s_nop 0
cv 47,126,127
v_pk_max_u16 v8, v8, v47
v_pk_max_u16 v8, v8, v8 op_sel:[0,1] op_sel_hi:[1,0]
v_cmp_lt_u32 vcc, s71, v8
s_cbranch_vccnz .Lk1_atr11
.Lk1_aret11:
s_waitcnt lgkmcnt(0)
v_mfma_f32_32x32x16_bf16 a[64:79], a[120:123], v[32:35], a[64:79]
ex 48
ex 49
ex 50
ex 51
cv 16,48,49
rqa 96,1,8192
rqa 100,1,8704
v_mfma_f32_32x32x16_bf16 a[80:95], a[120:123], v[40:43], a[80:95]
ex 52
ex 53
cv 17,50,51
ex 54
ex 55
cv 18,52,53
v_pk_maximum3_f16 v8, v16, v17, v18
v_mfma_f32_32x32x16_bf16 a[64:79], a[124:127], v[36:39], a[64:79]
ex 56
ex 57
cv 19,54,55
ex 58
ex 59
cv 20,56,57
v_pk_maximum3_f16 v8, v8, v19, v20
v_mfma_f32_32x32x16_bf16 a[80:95], a[124:127], v[44:47], a[80:95]
ex 60
ex 61
cv 21,58,59
s_add_u32 s68, s69, s57
ex 62
ex 63
cv 22,60,61
v_pk_maximum3_f16 v8, v8, v21, v22
v_add_u32 v7, 6144, v11
ds_read2_b64 a[120:123], v7 offset0:0 offset1:64
ds_read2_b64 a[124:127], v7 offset0:1 offset1:65
v_mfma_f32_32x32x16_bf16 v[96:111], a[104:107], v[64:67], 0
ex 80
ex 81
cv 23,62,63
ex 82
ex 83
cv 24,80,81
v_pk_maximum3_f16 v8, v8, v23, v24
s_mov_b32 s69, s68
v_mfma_f32_32x32x16_bf16 v[112:127], a[104:107], v[72:75], 0
ex 84
ex 85
cv 25,82,83
ex 86
ex 87
cv 26,84,85
v_pk_maximum3_f16 v8, v8, v25, v26
v_mfma_f32_32x32x16_bf16 v[96:111], a[108:111], v[68:71], v[96:111]
ex 88
ex 89
cv 27,86,87
buffer_store_dwordx4 v[224:227], v0, s[52:55], s68 offen offset:0 sc1
ex 90
ex 91
cv 28,88,89
v_pk_maximum3_f16 v8, v8, v27, v28
v_mfma_f32_32x32x16_bf16 v[112:127], a[108:111], v[76:79], v[112:127]
ex 92
ex 93
cv 29,90,91
ex 94
ex 95
cv 30,92,93
v_pk_maximum3_f16 v8, v8, v29, v30
buffer_store_dwordx4 v[228:231], v0, s[52:55], s68 offen offset:1024 sc1
s_nop 0
cv 31,94,95
v_pk_max_u16 v8, v8, v31
v_pk_max_u16 v8, v8, v8 op_sel:[0,1] op_sel_hi:[1,0]
v_cmp_lt_u32 vcc, s71, v8
s_cbranch_vccnz .Lk1_atr12
.Lk1_aret12:
s_waitcnt lgkmcnt(0)
v_mfma_f32_32x32x16_bf16 a[64:79], a[112:115], v[16:19], a[64:79]
ex 96
ex 97
ex 98
ex 99
cv 32,96,97
rqa 104,1,12288
rqa 108,1,12800
v_mfma_f32_32x32x16_bf16 a[80:95], a[112:115], v[24:27], a[80:95]
ex 100
ex 101
cv 33,98,99
ex 102
ex 103
cv 34,100,101
v_pk_maximum3_f16 v8, v32, v33, v34
v_mfma_f32_32x32x16_bf16 a[64:79], a[116:119], v[20:23], a[64:79]
ex 104
ex 105
cv 35,102,103
ex 106
ex 107
cv 36,104,105
v_pk_maximum3_f16 v8, v8, v35, v36
v_mfma_f32_32x32x16_bf16 a[80:95], a[116:119], v[28:31], a[80:95]
ex 108
ex 109
cv 37,106,107
ex 110
ex 111
cv 38,108,109
v_pk_maximum3_f16 v8, v8, v37, v38
v_add_u32 v6, 10240, v11
ds_read2_b64 a[112:115], v6 offset0:0 offset1:64
ds_read2_b64 a[116:119], v6 offset0:1 offset1:65
v_mfma_f32_32x32x16_bf16 v[48:63], a[96:99], v[64:67], 0
ex 112
ex 113
cv 39,110,111
ex 114
ex 115
cv 40,112,113
v_pk_maximum3_f16 v8, v8, v39, v40
buffer_store_dwordx4 v[232:235], v0, s[52:55], s68 offen offset:2048 sc1
v_mfma_f32_32x32x16_bf16 v[80:95], a[96:99], v[72:75], 0
ex 116
ex 117
cv 41,114,115
ex 118
ex 119
cv 42,116,117
v_pk_maximum3_f16 v8, v8, v41, v42
v_mfma_f32_32x32x16_bf16 v[48:63], a[100:103], v[68:71], v[48:63]
ex 120
ex 121
cv 43,118,119
ex 122
ex 123
cv 44,120,121
v_pk_maximum3_f16 v8, v8, v43, v44
v_mfma_f32_32x32x16_bf16 v[80:95], a[100:103], v[76:79], v[80:95]
ex 124
ex 125
cv 45,122,123
ex 126
ex 127
cv 46,124,125
v_pk_maximum3_f16 v8, v8, v45, v46
buffer_store_dwordx4 v[236:239], v0, s[52:55], s68 offen offset:3072 sc1
s_nop 0
cv 47,126,127
v_pk_max_u16 v8, v8, v47
v_pk_max_u16 v8, v8, v8 op_sel:[0,1] op_sel_hi:[1,0]
v_cmp_lt_u32 vcc, s71, v8
s_cbranch_vccnz .Lk1_atr13
.Lk1_aret13:
s_waitcnt lgkmcnt(0)
v_mfma_f32_32x32x16_bf16 a[64:79], a[120:123], v[32:35], a[64:79]
ex 48
ex 49
ex 50
ex 51
cv 16,48,49
rqa 96,1,16384
rqa 100,1,16896
v_mfma_f32_32x32x16_bf16 a[80:95], a[120:123], v[40:43], a[80:95]
ex 52
ex 53
cv 17,50,51
ex 54
ex 55
cv 18,52,53
v_pk_maximum3_f16 v8, v16, v17, v18
v_mfma_f32_32x32x16_bf16 a[64:79], a[124:127], v[36:39], a[64:79]
ex 56
ex 57
cv 19,54,55
ex 58
ex 59
cv 20,56,57
v_pk_maximum3_f16 v8, v8, v19, v20
v_mfma_f32_32x32x16_bf16 a[80:95], a[124:127], v[44:47], a[80:95]
ex 60
ex 61
cv 21,58,59
ex 62
ex 63
cv 22,60,61
v_pk_maximum3_f16 v8, v8, v21, v22
v_add_u32 v7, 14336, v11
ds_read2_b64 a[120:123], v7 offset0:0 offset1:64
ds_read2_b64 a[124:127], v7 offset0:1 offset1:65
s_add_u32 s68, s68, s58
v_mfma_f32_32x32x16_bf16 v[96:111], a[104:107], v[64:67], 0
ex 80
ex 81
cv 23,62,63
ex 82
ex 83
cv 24,80,81
v_pk_maximum3_f16 v8, v8, v23, v24
v_mfma_f32_32x32x16_bf16 v[112:127], a[104:107], v[72:75], 0
ex 84
ex 85
cv 25,82,83
ex 86
ex 87
cv 26,84,85
v_pk_maximum3_f16 v8, v8, v25, v26
buffer_store_dwordx4 v[240:243], v0, s[52:55], s68 offen offset:0 sc1
v_mfma_f32_32x32x16_bf16 v[96:111], a[108:111], v[68:71], v[96:111]
ex 88
ex 89
cv 27,86,87
ex 90
ex 91
cv 28,88,89
v_pk_maximum3_f16 v8, v8, v27, v28
v_mfma_f32_32x32x16_bf16 v[112:127], a[108:111], v[76:79], v[112:127]
ex 92
ex 93
cv 29,90,91
ex 94
ex 95
cv 30,92,93
v_pk_maximum3_f16 v8, v8, v29, v30
buffer_store_dwordx4 v[244:247], v0, s[52:55], s68 offen offset:1024 sc1
s_nop 0
cv 31,94,95
v_pk_max_u16 v8, v8, v31
v_pk_max_u16 v8, v8, v8 op_sel:[0,1] op_sel_hi:[1,0]
v_cmp_lt_u32 vcc, s71, v8
s_cbranch_vccnz .Lk1_atr14
.Lk1_aret14:
s_waitcnt lgkmcnt(0)
v_mfma_f32_32x32x16_bf16 a[64:79], a[112:115], v[16:19], a[64:79]
ex 96
ex 97
ex 98
ex 99
cv 32,96,97
rqa 104,1,20480
rqa 108,1,20992
v_mfma_f32_32x32x16_bf16 a[80:95], a[112:115], v[24:27], a[80:95]
ex 100
ex 101
cv 33,98,99
ex 102
ex 103
cv 34,100,101
v_pk_maximum3_f16 v8, v32, v33, v34
v_mfma_f32_32x32x16_bf16 a[64:79], a[116:119], v[20:23], a[64:79]
ex 104
ex 105
cv 35,102,103
ex 106
ex 107
cv 36,104,105
v_pk_maximum3_f16 v8, v8, v35, v36
v_mfma_f32_32x32x16_bf16 a[80:95], a[116:119], v[28:31], a[80:95]
ex 108
ex 109
cv 37,106,107
ex 110
ex 111
cv 38,108,109
v_pk_maximum3_f16 v8, v8, v37, v38
v_add_u32 v6, 18432, v11
ds_read2_b64 a[112:115], v6 offset0:0 offset1:64
ds_read2_b64 a[116:119], v6 offset0:1 offset1:65
v_mfma_f32_32x32x16_bf16 v[48:63], a[96:99], v[64:67], 0
ex 112
ex 113
cv 39,110,111
ex 114
ex 115
cv 40,112,113
v_pk_maximum3_f16 v8, v8, v39, v40
buffer_store_dwordx4 v[248:251], v0, s[52:55], s68 offen offset:2048 sc1
v_mfma_f32_32x32x16_bf16 v[80:95], a[96:99], v[72:75], 0
ex 116
ex 117
cv 41,114,115
ex 118
ex 119
cv 42,116,117
v_pk_maximum3_f16 v8, v8, v41, v42
v_mfma_f32_32x32x16_bf16 v[48:63], a[100:103], v[68:71], v[48:63]
ex 120
ex 121
cv 43,118,119
ex 122
ex 123
cv 44,120,121
v_pk_maximum3_f16 v8, v8, v43, v44
v_mfma_f32_32x32x16_bf16 v[80:95], a[100:103], v[76:79], v[80:95]
ex 124
ex 125
cv 45,122,123
ex 126
ex 127
cv 46,124,125
v_pk_maximum3_f16 v8, v8, v45, v46
buffer_store_dwordx4 v[252:255], v0, s[52:55], s68 offen offset:3072 sc1
s_nop 0
cv 47,126,127
v_pk_max_u16 v8, v8, v47
v_pk_max_u16 v8, v8, v8 op_sel:[0,1] op_sel_hi:[1,0]
v_cmp_lt_u32 vcc, s71, v8
s_cbranch_vccnz .Lk1_atr15
.Lk1_aret15:
s_waitcnt lgkmcnt(0)
v_mfma_f32_32x32x16_bf16 a[64:79], a[120:123], v[32:35], a[64:79]
ex 48
ex 49
ex 50
ex 51
cv 16,48,49
rqa 96,1,24576
rqa 100,1,25088
v_mfma_f32_32x32x16_bf16 a[80:95], a[120:123], v[40:43], a[80:95]
ex 52
ex 53
cv 17,50,51
ex 54
ex 55
cv 18,52,53
v_pk_maximum3_f16 v8, v16, v17, v18
v_mfma_f32_32x32x16_bf16 a[64:79], a[124:127], v[36:39], a[64:79]
ex 56
ex 57
cv 19,54,55
ex 58
ex 59
cv 20,56,57
v_pk_maximum3_f16 v8, v8, v19, v20
global_load_dwordx4 a[128:131], v0, s[38:39] offset:0 sc1
v_mfma_f32_32x32x16_bf16 a[80:95], a[124:127], v[44:47], a[80:95]
ex 60
ex 61
cv 21,58,59
ex 62
ex 63
cv 22,60,61
v_pk_maximum3_f16 v8, v8, v21, v22
v_add_u32 v7, 22528, v11
ds_read2_b64 a[120:123], v7 offset0:0 offset1:64
ds_read2_b64 a[124:127], v7 offset0:1 offset1:65
global_load_dwordx4 a[132:135], v0, s[38:39] offset:1024 sc1
v_mfma_f32_32x32x16_bf16 v[96:111], a[104:107], v[64:67], 0
ex 80
ex 81
cv 23,62,63
ex 82
ex 83
cv 24,80,81
v_pk_maximum3_f16 v8, v8, v23, v24
s_add_u32 s38, s38, 0x20000
s_addc_u32 s39, s39, 0
v_mfma_f32_32x32x16_bf16 v[112:127], a[104:107], v[72:75], 0
ex 84
ex 85
cv 25,82,83
ex 86
ex 87
cv 26,84,85
v_pk_maximum3_f16 v8, v8, v25, v26
global_load_dwordx4 a[136:139], v0, s[38:39] offset:0 sc1
v_mfma_f32_32x32x16_bf16 v[96:111], a[108:111], v[68:71], v[96:111]
ex 88
ex 89
cv 27,86,87
ex 90
ex 91
cv 28,88,89
v_pk_maximum3_f16 v8, v8, v27, v28
global_load_dwordx4 a[140:143], v0, s[38:39] offset:1024 sc1
v_mfma_f32_32x32x16_bf16 v[112:127], a[108:111], v[76:79], v[112:127]
ex 92
ex 93
cv 29,90,91
ex 94
ex 95
cv 30,92,93
v_pk_maximum3_f16 v8, v8, v29, v30
s_add_u32 s38, s38, 0x20000
s_addc_u32 s39, s39, 0
s_nop 0
cv 31,94,95
v_pk_max_u16 v8, v8, v31
v_pk_max_u16 v8, v8, v8 op_sel:[0,1] op_sel_hi:[1,0]
v_cmp_lt_u32 vcc, s71, v8
s_cbranch_vccnz .Lk1_atr16
.Lk1_aret16:
s_waitcnt lgkmcnt(0)
v_mfma_f32_32x32x16_bf16 a[64:79], a[112:115], v[16:19], a[64:79]
ex 96
ex 97
ex 98
)ASM" ::: "memory");
  asm volatile(R"ASM(
ex 99
cv 32,96,97
rqa 104,1,28672
rqa 108,1,29184
v_mfma_f32_32x32x16_bf16 a[80:95], a[112:115], v[24:27], a[80:95]
ex 100
ex 101
cv 33,98,99
ex 102
ex 103
cv 34,100,101
v_pk_maximum3_f16 v8, v32, v33, v34
v_mfma_f32_32x32x16_bf16 a[64:79], a[116:119], v[20:23], a[64:79]
ex 104
ex 105
cv 35,102,103
ex 106
ex 107
cv 36,104,105
v_pk_maximum3_f16 v8, v8, v35, v36
global_load_dwordx4 a[144:147], v0, s[38:39] offset:0 sc1
v_mfma_f32_32x32x16_bf16 a[80:95], a[116:119], v[28:31], a[80:95]
ex 108
ex 109
cv 37,106,107
ex 110
ex 111
cv 38,108,109
v_pk_maximum3_f16 v8, v8, v37, v38
v_add_u32 v6, 26624, v11
ds_read2_b64 a[112:115], v6 offset0:0 offset1:64
ds_read2_b64 a[116:119], v6 offset0:1 offset1:65
global_load_dwordx4 a[148:151], v0, s[38:39] offset:1024 sc1
v_mfma_f32_32x32x16_bf16 v[48:63], a[96:99], v[64:67], 0
ex 112
ex 113
cv 39,110,111
ex 114
ex 115
cv 40,112,113
v_pk_maximum3_f16 v8, v8, v39, v40
s_add_u32 s38, s38, 0x20000
s_addc_u32 s39, s39, 0
v_mfma_f32_32x32x16_bf16 v[80:95], a[96:99], v[72:75], 0
ex 116
ex 117
cv 41,114,115
ex 118
ex 119
cv 42,116,117
v_pk_maximum3_f16 v8, v8, v41, v42
global_load_dwordx4 a[152:155], v0, s[38:39] offset:0 sc1
v_mfma_f32_32x32x16_bf16 v[48:63], a[100:103], v[68:71], v[48:63]
ex 120
ex 121
cv 43,118,119
ex 122
ex 123
cv 44,120,121
v_pk_maximum3_f16 v8, v8, v43, v44
global_load_dwordx4 a[156:159], v0, s[38:39] offset:1024 sc1
v_mfma_f32_32x32x16_bf16 v[80:95], a[100:103], v[76:79], v[80:95]
ex 124
ex 125
cv 45,122,123
ex 126
ex 127
cv 46,124,125
v_pk_maximum3_f16 v8, v8, v45, v46
s_add_u32 s38, s38, 0x20000
s_addc_u32 s39, s39, 0
s_nop 0
cv 47,126,127
v_pk_max_u16 v8, v8, v47
v_pk_max_u16 v8, v8, v8 op_sel:[0,1] op_sel_hi:[1,0]
v_cmp_lt_u32 vcc, s71, v8
s_cbranch_vccnz .Lk1_atr17
.Lk1_aret17:
s_waitcnt lgkmcnt(0)
v_mfma_f32_32x32x16_bf16 a[64:79], a[120:123], v[32:35], a[64:79]
ex 48
ex 49
ex 50
ex 51
cv 16,48,49
rqa 96,3,16384
rqa 100,3,16896
v_mfma_f32_32x32x16_bf16 a[80:95], a[120:123], v[40:43], a[80:95]
ex 52
ex 53
cv 17,50,51
ex 54
ex 55
cv 18,52,53
v_pk_maximum3_f16 v8, v16, v17, v18
v_mfma_f32_32x32x16_bf16 a[64:79], a[124:127], v[36:39], a[64:79]
ex 56
ex 57
cv 19,54,55
s_getpc_b64 s[68:69]
.Lf_w1:
s_add_u32 s68, s68, .Lf_A_epi-.Lf_w1
s_addc_u32 s69, s69, (.Lf_A_epi-.Lf_w1)>>32
ex 58
ex 59
cv 20,56,57
v_pk_maximum3_f16 v8, v8, v19, v20
v_mfma_f32_32x32x16_bf16 a[80:95], a[124:127], v[44:47], a[80:95]
ex 60
ex 61
cv 21,58,59
global_load_dword v254, v12, s[68:69]
ex 62
ex 63
cv 22,60,61
v_pk_maximum3_f16 v8, v8, v21, v22
v_add_u32 v7, 30720, v11
ds_read2_b64 a[120:123], v7 offset0:0 offset1:64
ds_read2_b64 a[124:127], v7 offset0:1 offset1:65
global_load_dwordx4 a[160:163], v0, s[38:39] offset:0 sc1
v_mfma_f32_32x32x16_bf16 v[96:111], a[104:107], v[64:67], 0
ex 80
ex 81
cv 23,62,63
ex 82
ex 83
cv 24,80,81
v_pk_maximum3_f16 v8, v8, v23, v24
global_load_dwordx4 a[164:167], v0, s[38:39] offset:1024 sc1
v_mfma_f32_32x32x16_bf16 v[112:127], a[104:107], v[72:75], 0
ex 84
ex 85
cv 25,82,83
ex 86
ex 87
cv 26,84,85
v_pk_maximum3_f16 v8, v8, v25, v26
s_add_u32 s38, s38, 0x20000
s_addc_u32 s39, s39, 0
v_mfma_f32_32x32x16_bf16 v[96:111], a[108:111], v[68:71], v[96:111]
ex 88
ex 89
cv 27,86,87
global_load_dwordx4 a[168:171], v0, s[38:39] offset:0 sc1
ex 90
ex 91
cv 28,88,89
v_pk_maximum3_f16 v8, v8, v27, v28
v_mfma_f32_32x32x16_bf16 v[112:127], a[108:111], v[76:79], v[112:127]
ex 92
ex 93
cv 29,90,91
global_load_dwordx4 a[172:175], v0, s[38:39] offset:1024 sc1
ex 94
ex 95
cv 30,92,93
v_pk_maximum3_f16 v8, v8, v29, v30
s_add_u32 s38, s38, 0x20000
s_addc_u32 s39, s39, 0
s_nop 0
cv 31,94,95
v_pk_max_u16 v8, v8, v31
v_pk_max_u16 v8, v8, v8 op_sel:[0,1] op_sel_hi:[1,0]
v_cmp_lt_u32 vcc, s71, v8
s_cbranch_vccnz .Lk1_atr18
.Lk1_aret18:
s_waitcnt lgkmcnt(0)
v_mfma_f32_32x32x16_bf16 a[64:79], a[112:115], v[16:19], a[64:79]
ex 96
ex 97
ex 98
ex 99
cv 32,96,97
rqa 104,3,20480
rqa 108,3,20992
v_mfma_f32_32x32x16_bf16 a[80:95], a[112:115], v[24:27], a[80:95]
ex 100
ex 101
cv 33,98,99
ex 102
ex 103
cv 34,100,101
v_pk_maximum3_f16 v8, v32, v33, v34
v_mfma_f32_32x32x16_bf16 a[64:79], a[116:119], v[20:23], a[64:79]
ex 104
ex 105
cv 35,102,103
s_getpc_b64 s[68:69]
.Lf_w2:
s_add_u32 s68, s68, .Lf_A_epi+4096-.Lf_w2
s_addc_u32 s69, s69, (.Lf_A_epi+4096-.Lf_w2)>>32
ex 106
ex 107
cv 36,104,105
v_pk_maximum3_f16 v8, v8, v35, v36
v_mfma_f32_32x32x16_bf16 a[80:95], a[116:119], v[28:31], a[80:95]
ex 108
ex 109
cv 37,106,107
global_load_dword v254, v12, s[68:69]
ex 110
ex 111
cv 38,108,109
v_pk_maximum3_f16 v8, v8, v37, v38
v_add_u32 v6, 149504, v11
ds_read2_b64 a[112:115], v6 offset0:0 offset1:64
ds_read2_b64 a[116:119], v6 offset0:1 offset1:65
global_load_dwordx4 a[176:179], v0, s[38:39] offset:0 sc1
v_mfma_f32_32x32x16_bf16 v[48:63], a[96:99], v[64:67], 0
ex 112
ex 113
cv 39,110,111
ex 114
ex 115
cv 40,112,113
v_pk_maximum3_f16 v8, v8, v39, v40
global_load_dwordx4 a[180:183], v0, s[38:39] offset:1024 sc1
v_mfma_f32_32x32x16_bf16 v[80:95], a[96:99], v[72:75], 0
ex 116
ex 117
cv 41,114,115
ex 118
ex 119
cv 42,116,117
v_pk_maximum3_f16 v8, v8, v41, v42
s_add_u32 s38, s38, 0x20000
s_addc_u32 s39, s39, 0
v_mfma_f32_32x32x16_bf16 v[48:63], a[100:103], v[68:71], v[48:63]
ex 120
ex 121
cv 43,118,119
global_load_dwordx4 a[184:187], v0, s[38:39] offset:0 sc1
ex 122
ex 123
cv 44,120,121
v_pk_maximum3_f16 v8, v8, v43, v44
v_mfma_f32_32x32x16_bf16 v[80:95], a[100:103], v[76:79], v[80:95]
ex 124
ex 125
cv 45,122,123
global_load_dwordx4 a[188:191], v0, s[38:39] offset:1024 sc1
ex 126
ex 127
cv 46,124,125
v_pk_maximum3_f16 v8, v8, v45, v46
s_add_u32 s38, s38, 0x20000
s_addc_u32 s39, s39, 0
s_nop 0
cv 47,126,127
v_pk_max_u16 v8, v8, v47
v_pk_max_u16 v8, v8, v8 op_sel:[0,1] op_sel_hi:[1,0]
v_cmp_lt_u32 vcc, s71, v8
s_cbranch_vccnz .Lk1_atr19
.Lk1_aret19:
s_cmp_lg_u32 s66, 0
s_cbranch_scc1 .Lf_noq20
global_load_dwordx4 a[0:3], v0, s[36:37] offset:0 sc1
global_load_dwordx4 a[4:7], v0, s[36:37] offset:1024 sc1
s_add_u32 s36, s36, 0x20000
s_addc_u32 s37, s37, 0
global_load_dwordx4 a[8:11], v0, s[36:37] offset:0 sc1
global_load_dwordx4 a[12:15], v0, s[36:37] offset:1024 sc1
s_add_u32 s36, s36, 0x20000
s_addc_u32 s37, s37, 0
.Lf_noq20:
s_waitcnt lgkmcnt(0)
v_mfma_f32_32x32x16_bf16 a[64:79], a[120:123], v[32:35], a[64:79]
ex 48
ex 49
ex 50
ex 51
cv 16,48,49
rqa 96,3,24576
rqa 100,3,25088
v_mfma_f32_32x32x16_bf16 a[80:95], a[120:123], v[40:43], a[80:95]
ex 52
ex 53
cv 17,50,51
ex 54
ex 55
cv 18,52,53
v_pk_maximum3_f16 v8, v16, v17, v18
v_mfma_f32_32x32x16_bf16 a[64:79], a[124:127], v[36:39], a[64:79]
ex 56
ex 57
cv 19,54,55
ex 58
ex 59
cv 20,56,57
v_pk_maximum3_f16 v8, v8, v19, v20
v_mfma_f32_32x32x16_bf16 a[80:95], a[124:127], v[44:47], a[80:95]
ex 60
ex 61
cv 21,58,59
ex 62
ex 63
cv 22,60,61
v_pk_maximum3_f16 v8, v8, v21, v22
v_add_u32 v7, 153600, v11
ds_read2_b64 a[120:123], v7 offset0:0 offset1:64
ds_read2_b64 a[124:127], v7 offset0:1 offset1:65
v_mfma_f32_32x32x16_bf16 v[96:111], a[104:107], v[64:67], 0
ex 80
ex 81
cv 23,62,63
ex 82
ex 83
cv 24,80,81
v_pk_maximum3_f16 v8, v8, v23, v24
s_getpc_b64 s[68:69]
.Lf_w3:
s_add_u32 s68, s68, .Lf_A_epi+8192-.Lf_w3
s_addc_u32 s69, s69, (.Lf_A_epi+8192-.Lf_w3)>>32
v_mfma_f32_32x32x16_bf16 v[112:127], a[104:107], v[72:75], 0
ex 84
ex 85
cv 25,82,83
ex 86
ex 87
cv 26,84,85
v_pk_maximum3_f16 v8, v8, v25, v26
v_mfma_f32_32x32x16_bf16 v[96:111], a[108:111], v[68:71], v[96:111]
ex 88
ex 89
cv 27,86,87
ex 90
ex 91
cv 28,88,89
v_pk_maximum3_f16 v8, v8, v27, v28
v_mfma_f32_32x32x16_bf16 v[112:127], a[108:111], v[76:79], v[112:127]
ex 92
ex 93
cv 29,90,91
ex 94
ex 95
cv 30,92,93
v_pk_maximum3_f16 v8, v8, v29, v30
global_load_dword v254, v12, s[68:69]
s_nop 0
cv 31,94,95
v_pk_max_u16 v8, v8, v31
v_pk_max_u16 v8, v8, v8 op_sel:[0,1] op_sel_hi:[1,0]
v_cmp_lt_u32 vcc, s71, v8
s_cbranch_vccnz .Lk1_atr20
.Lk1_aret20:
s_cmp_lg_u32 s66, 0
s_cbranch_scc1 .Lf_noq21
global_load_dwordx4 a[16:19], v0, s[36:37] offset:0 sc1
global_load_dwordx4 a[20:23], v0, s[36:37] offset:1024 sc1
s_add_u32 s36, s36, 0x20000
s_addc_u32 s37, s37, 0
global_load_dwordx4 a[24:27], v0, s[36:37] offset:0 sc1
global_load_dwordx4 a[28:31], v0, s[36:37] offset:1024 sc1
s_add_u32 s36, s36, 0x20000
s_addc_u32 s37, s37, 0
.Lf_noq21:
s_waitcnt lgkmcnt(0)
v_mfma_f32_32x32x16_bf16 a[64:79], a[112:115], v[16:19], a[64:79]
ex 96
ex 97
ex 98
ex 99
cv 32,96,97
rqa 104,3,28672
rqa 108,3,29184
v_mfma_f32_32x32x16_bf16 a[80:95], a[112:115], v[24:27], a[80:95]
ex 100
ex 101
cv 33,98,99
ex 102
ex 103
cv 34,100,101
v_pk_maximum3_f16 v8, v32, v33, v34
v_mfma_f32_32x32x16_bf16 a[64:79], a[116:119], v[20:23], a[64:79]
ex 104
ex 105
cv 35,102,103
ex 106
ex 107
cv 36,104,105
v_pk_maximum3_f16 v8, v8, v35, v36
v_mfma_f32_32x32x16_bf16 a[80:95], a[116:119], v[28:31], a[80:95]
ex 108
ex 109
cv 37,106,107
ex 110
ex 111
cv 38,108,109
v_pk_maximum3_f16 v8, v8, v37, v38
v_add_u32 v6, 157696, v11
ds_read2_b64 a[112:115], v6 offset0:0 offset1:64
ds_read2_b64 a[116:119], v6 offset0:1 offset1:65
v_mfma_f32_32x32x16_bf16 v[48:63], a[96:99], v[64:67], 0
ex 112
ex 113
cv 39,110,111
ex 114
ex 115
cv 40,112,113
v_pk_maximum3_f16 v8, v8, v39, v40
s_getpc_b64 s[68:69]
.Lf_w4:
s_add_u32 s68, s68, .Lk2_proA-.Lf_w4
s_addc_u32 s69, s69, (.Lk2_proA-.Lf_w4)>>32
v_mfma_f32_32x32x16_bf16 v[80:95], a[96:99], v[72:75], 0
ex 116
ex 117
cv 41,114,115
ex 118
ex 119
cv 42,116,117
v_pk_maximum3_f16 v8, v8, v41, v42
v_mfma_f32_32x32x16_bf16 v[48:63], a[100:103], v[68:71], v[48:63]
ex 120
ex 121
cv 43,118,119
ex 122
ex 123
cv 44,120,121
v_pk_maximum3_f16 v8, v8, v43, v44
v_mfma_f32_32x32x16_bf16 v[80:95], a[100:103], v[76:79], v[80:95]
ex 124
ex 125
cv 45,122,123
ex 126
ex 127
cv 46,124,125
v_pk_maximum3_f16 v8, v8, v45, v46
global_load_dword v254, v12, s[68:69]
s_nop 0
cv 47,126,127
v_pk_max_u16 v8, v8, v47
v_pk_max_u16 v8, v8, v8 op_sel:[0,1] op_sel_hi:[1,0]
v_cmp_lt_u32 vcc, s71, v8
s_cbranch_vccnz .Lk1_atr21
.Lk1_aret21:
s_cmp_lg_u32 s66, 0
s_cbranch_scc1 .Lf_noq22
global_load_dwordx4 a[32:35], v0, s[36:37] offset:0 sc1
global_load_dwordx4 a[36:39], v0, s[36:37] offset:1024 sc1
s_add_u32 s36, s36, 0x20000
s_addc_u32 s37, s37, 0
global_load_dwordx4 a[40:43], v0, s[36:37] offset:0 sc1
global_load_dwordx4 a[44:47], v0, s[36:37] offset:1024 sc1
s_add_u32 s36, s36, 0x20000
s_addc_u32 s37, s37, 0
.Lf_noq22:
s_waitcnt vmcnt(0)
s_barrier
s_cmp_lg_u32 s51, 3
s_cbranch_scc1 .Lf_nof2
v_mov_b32 v10, s82
v_mov_b32 v9, 0
s_mov_b64 exec, 1
global_store_dword v9, v10, s[62:63] sc1
s_mov_b64 exec, -1
.Lf_nof2:
s_waitcnt lgkmcnt(0)
v_mfma_f32_32x32x16_bf16 a[64:79], a[120:123], v[32:35], a[64:79]
ex 48
ex 49
ex 50
ex 51
cv 16,48,49
rqa 96,2,32768
rqa 100,2,33280
v_mfma_f32_32x32x16_bf16 a[80:95], a[120:123], v[40:43], a[80:95]
ex 52
ex 53
cv 17,50,51
ex 54
ex 55
cv 18,52,53
v_pk_maximum3_f16 v8, v16, v17, v18
v_mfma_f32_32x32x16_bf16 a[64:79], a[124:127], v[36:39], a[64:79]
ex 56
ex 57
cv 19,54,55
ex 58
ex 59
cv 20,56,57
v_pk_maximum3_f16 v8, v8, v19, v20
v_mfma_f32_32x32x16_bf16 a[80:95], a[124:127], v[44:47], a[80:95]
ex 60
ex 61
cv 21,58,59
ex 62
ex 63
cv 22,60,61
v_pk_maximum3_f16 v8, v8, v21, v22
v_add_u32 v7, 161792, v11
ds_read2_b64 a[120:123], v7 offset0:0 offset1:64
ds_read2_b64 a[124:127], v7 offset0:1 offset1:65
v_mfma_f32_32x32x16_bf16 v[96:111], a[104:107], v[64:67], 0
ex 80
ex 81
cv 23,62,63
ex 82
ex 83
cv 24,80,81
v_pk_maximum3_f16 v8, v8, v23, v24
s_getpc_b64 s[68:69]
.Lf_w5:
s_add_u32 s68, s68, .Lk2_reg20-.Lf_w5
s_addc_u32 s69, s69, (.Lk2_reg20-.Lf_w5)>>32
v_mfma_f32_32x32x16_bf16 v[112:127], a[104:107], v[72:75], 0
ex 84
ex 85
cv 25,82,83
ex 86
ex 87
cv 26,84,85
v_pk_maximum3_f16 v8, v8, v25, v26
v_mfma_f32_32x32x16_bf16 v[96:111], a[108:111], v[68:71], v[96:111]
ex 88
ex 89
cv 27,86,87
ex 90
ex 91
cv 28,88,89
v_pk_maximum3_f16 v8, v8, v27, v28
v_mfma_f32_32x32x16_bf16 v[112:127], a[108:111], v[76:79], v[112:127]
ex 92
ex 93
cv 29,90,91
ex 94
ex 95
cv 30,92,93
v_pk_maximum3_f16 v8, v8, v29, v30
global_load_dword v254, v12, s[68:69]
s_nop 0
cv 31,94,95
v_pk_max_u16 v8, v8, v31
v_pk_max_u16 v8, v8, v8 op_sel:[0,1] op_sel_hi:[1,0]
v_cmp_lt_u32 vcc, s71, v8
s_cbranch_vccnz .Lk1_atr22
.Lk1_aret22:
s_cmp_lg_u32 s66, 0
s_cbranch_scc1 .Lf_noq23
global_load_dwordx4 a[48:51], v0, s[36:37] offset:0 sc1
global_load_dwordx4 a[52:55], v0, s[36:37] offset:1024 sc1
s_add_u32 s36, s36, 0x20000
s_addc_u32 s37, s37, 0
global_load_dwordx4 a[56:59], v0, s[36:37] offset:0 sc1
global_load_dwordx4 a[60:63], v0, s[36:37] offset:1024 sc1
s_add_u32 s36, s36, 0x20000
s_addc_u32 s37, s37, 0
.Lf_noq23:
s_waitcnt lgkmcnt(0)
v_mfma_f32_32x32x16_bf16 a[64:79], a[112:115], v[16:19], a[64:79]
ex 96
ex 97
ex 98
ex 99
cv 32,96,97
rqa 104,2,36864
rqa 108,2,37376
v_mfma_f32_32x32x16_bf16 a[80:95], a[112:115], v[24:27], a[80:95]
ex 100
ex 101
cv 33,98,99
ex 102
ex 103
cv 34,100,101
v_pk_maximum3_f16 v8, v32, v33, v34
v_mfma_f32_32x32x16_bf16 a[64:79], a[116:119], v[20:23], a[64:79]
ex 104
ex 105
cv 35,102,103
ex 106
ex 107
cv 36,104,105
v_pk_maximum3_f16 v8, v8, v35, v36
v_mfma_f32_32x32x16_bf16 a[80:95], a[116:119], v[28:31], a[80:95]
ex 108
ex 109
cv 37,106,107
ex 110
ex 111
cv 38,108,109
v_pk_maximum3_f16 v8, v8, v37, v38
v_add_u32 v6, 100352, v11
ds_read2_b64 a[112:115], v6 offset0:0 offset1:64
ds_read2_b64 a[116:119], v6 offset0:1 offset1:65
v_mfma_f32_32x32x16_bf16 v[48:63], a[96:99], v[64:67], 0
ex 112
ex 113
cv 39,110,111
ex 114
ex 115
cv 40,112,113
v_pk_maximum3_f16 v8, v8, v39, v40
s_getpc_b64 s[68:69]
.Lf_w6:
s_add_u32 s68, s68, .Lk2_reg20+4096-.Lf_w6
s_addc_u32 s69, s69, (.Lk2_reg20+4096-.Lf_w6)>>32
v_mfma_f32_32x32x16_bf16 v[80:95], a[96:99], v[72:75], 0
ex 116
ex 117
cv 41,114,115
)ASM" ::: "memory");
  asm volatile(R"ASM(
ex 118
ex 119
cv 42,116,117
v_pk_maximum3_f16 v8, v8, v41, v42
v_mfma_f32_32x32x16_bf16 v[48:63], a[100:103], v[68:71], v[48:63]
ex 120
ex 121
cv 43,118,119
ex 122
ex 123
cv 44,120,121
v_pk_maximum3_f16 v8, v8, v43, v44
v_mfma_f32_32x32x16_bf16 v[80:95], a[100:103], v[76:79], v[80:95]
ex 124
ex 125
cv 45,122,123
ex 126
ex 127
cv 46,124,125
v_pk_maximum3_f16 v8, v8, v45, v46
global_load_dword v254, v12, s[68:69]
s_nop 0
cv 47,126,127
v_pk_max_u16 v8, v8, v47
v_pk_max_u16 v8, v8, v8 op_sel:[0,1] op_sel_hi:[1,0]
v_cmp_lt_u32 vcc, s71, v8
s_cbranch_vccnz .Lk1_atr23
.Lk1_aret23:
s_waitcnt lgkmcnt(0)
v_mfma_f32_32x32x16_bf16 a[64:79], a[120:123], v[32:35], a[64:79]
ex 48
ex 49
ex 50
ex 51
cv 16,48,49
rqa 96,2,40960
rqa 100,2,41472
v_mfma_f32_32x32x16_bf16 a[80:95], a[120:123], v[40:43], a[80:95]
ex 52
ex 53
cv 17,50,51
ex 54
ex 55
cv 18,52,53
v_pk_maximum3_f16 v8, v16, v17, v18
v_mfma_f32_32x32x16_bf16 a[64:79], a[124:127], v[36:39], a[64:79]
ex 56
ex 57
cv 19,54,55
ex 58
ex 59
cv 20,56,57
v_pk_maximum3_f16 v8, v8, v19, v20
v_mfma_f32_32x32x16_bf16 a[80:95], a[124:127], v[44:47], a[80:95]
ex 60
ex 61
cv 21,58,59
ex 62
ex 63
cv 22,60,61
v_pk_maximum3_f16 v8, v8, v21, v22
v_add_u32 v7, 104448, v11
ds_read2_b64 a[120:123], v7 offset0:0 offset1:64
ds_read2_b64 a[124:127], v7 offset0:1 offset1:65
v_mfma_f32_32x32x16_bf16 v[96:111], a[104:107], v[64:67], 0
ex 80
ex 81
cv 23,62,63
ex 82
ex 83
cv 24,80,81
v_pk_maximum3_f16 v8, v8, v23, v24
v_mfma_f32_32x32x16_bf16 v[112:127], a[104:107], v[72:75], 0
ex 84
ex 85
cv 25,82,83
ex 86
ex 87
cv 26,84,85
v_pk_maximum3_f16 v8, v8, v25, v26
v_mfma_f32_32x32x16_bf16 v[96:111], a[108:111], v[68:71], v[96:111]
ex 88
ex 89
cv 27,86,87
ex 90
ex 91
cv 28,88,89
v_pk_maximum3_f16 v8, v8, v27, v28
v_mfma_f32_32x32x16_bf16 v[112:127], a[108:111], v[76:79], v[112:127]
ex 92
ex 93
cv 29,90,91
ex 94
ex 95
cv 30,92,93
v_pk_maximum3_f16 v8, v8, v29, v30
s_nop 0
cv 31,94,95
v_pk_max_u16 v8, v8, v31
v_pk_max_u16 v8, v8, v8 op_sel:[0,1] op_sel_hi:[1,0]
v_cmp_lt_u32 vcc, s71, v8
s_cbranch_vccnz .Lk1_atr24
.Lk1_aret24:
s_waitcnt lgkmcnt(0)
v_mfma_f32_32x32x16_bf16 a[64:79], a[112:115], v[16:19], a[64:79]
ex 96
ex 97
ex 98
ex 99
cv 32,96,97
rqa 104,2,45056
rqa 108,2,45568
v_mfma_f32_32x32x16_bf16 a[80:95], a[112:115], v[24:27], a[80:95]
ex 100
ex 101
cv 33,98,99
ex 102
ex 103
cv 34,100,101
v_pk_maximum3_f16 v8, v32, v33, v34
v_mfma_f32_32x32x16_bf16 a[64:79], a[116:119], v[20:23], a[64:79]
ex 104
ex 105
cv 35,102,103
ex 106
ex 107
cv 36,104,105
v_pk_maximum3_f16 v8, v8, v35, v36
v_mfma_f32_32x32x16_bf16 a[80:95], a[116:119], v[28:31], a[80:95]
ex 108
ex 109
cv 37,106,107
ex 110
ex 111
cv 38,108,109
v_pk_maximum3_f16 v8, v8, v37, v38
v_add_u32 v6, 108544, v11
ds_read2_b64 a[112:115], v6 offset0:0 offset1:64
ds_read2_b64 a[116:119], v6 offset0:1 offset1:65
v_mfma_f32_32x32x16_bf16 v[48:63], a[96:99], v[64:67], 0
ex 112
ex 113
cv 39,110,111
ex 114
ex 115
cv 40,112,113
v_pk_maximum3_f16 v8, v8, v39, v40
v_mfma_f32_32x32x16_bf16 v[80:95], a[96:99], v[72:75], 0
ex 116
ex 117
cv 41,114,115
ex 118
ex 119
cv 42,116,117
v_pk_maximum3_f16 v8, v8, v41, v42
v_mfma_f32_32x32x16_bf16 v[48:63], a[100:103], v[68:71], v[48:63]
ex 120
ex 121
cv 43,118,119
ex 122
ex 123
cv 44,120,121
v_pk_maximum3_f16 v8, v8, v43, v44
v_mfma_f32_32x32x16_bf16 v[80:95], a[100:103], v[76:79], v[80:95]
ex 124
ex 125
cv 45,122,123
ex 126
ex 127
cv 46,124,125
v_pk_maximum3_f16 v8, v8, v45, v46
s_nop 0
cv 47,126,127
v_pk_max_u16 v8, v8, v47
v_pk_max_u16 v8, v8, v8 op_sel:[0,1] op_sel_hi:[1,0]
v_cmp_lt_u32 vcc, s71, v8
s_cbranch_vccnz .Lk1_atr25
.Lk1_aret25:
s_waitcnt lgkmcnt(0)
v_mfma_f32_32x32x16_bf16 a[64:79], a[120:123], v[32:35], a[64:79]
ex 48
ex 49
ex 50
ex 51
cv 16,48,49
rqa 96,2,49152
rqa 100,2,49664
v_mfma_f32_32x32x16_bf16 a[80:95], a[120:123], v[40:43], a[80:95]
ex 52
ex 53
cv 17,50,51
ex 54
ex 55
cv 18,52,53
v_pk_maximum3_f16 v8, v16, v17, v18
v_mfma_f32_32x32x16_bf16 a[64:79], a[124:127], v[36:39], a[64:79]
ex 56
ex 57
cv 19,54,55
ex 58
ex 59
cv 20,56,57
v_pk_maximum3_f16 v8, v8, v19, v20
v_mfma_f32_32x32x16_bf16 a[80:95], a[124:127], v[44:47], a[80:95]
ex 60
ex 61
cv 21,58,59
ex 62
ex 63
cv 22,60,61
v_pk_maximum3_f16 v8, v8, v21, v22
v_add_u32 v7, 112640, v11
ds_read2_b64 a[120:123], v7 offset0:0 offset1:64
ds_read2_b64 a[124:127], v7 offset0:1 offset1:65
v_mfma_f32_32x32x16_bf16 v[96:111], a[104:107], v[64:67], 0
ex 80
ex 81
cv 23,62,63
ex 82
ex 83
cv 24,80,81
v_pk_maximum3_f16 v8, v8, v23, v24
v_mfma_f32_32x32x16_bf16 v[112:127], a[104:107], v[72:75], 0
ex 84
ex 85
cv 25,82,83
ex 86
ex 87
cv 26,84,85
v_pk_maximum3_f16 v8, v8, v25, v26
v_mfma_f32_32x32x16_bf16 v[96:111], a[108:111], v[68:71], v[96:111]
ex 88
ex 89
cv 27,86,87
ex 90
ex 91
cv 28,88,89
v_pk_maximum3_f16 v8, v8, v27, v28
v_mfma_f32_32x32x16_bf16 v[112:127], a[108:111], v[76:79], v[112:127]
ex 92
ex 93
cv 29,90,91
ex 94
ex 95
cv 30,92,93
v_pk_maximum3_f16 v8, v8, v29, v30
global_load_dword v14, v13, s[60:61] sc1
s_nop 0
cv 31,94,95
v_pk_max_u16 v8, v8, v31
v_pk_max_u16 v8, v8, v8 op_sel:[0,1] op_sel_hi:[1,0]
v_cmp_lt_u32 vcc, s71, v8
s_cbranch_vccnz .Lk1_atr26
.Lk1_aret26:
s_waitcnt lgkmcnt(0)
v_mfma_f32_32x32x16_bf16 a[64:79], a[112:115], v[16:19], a[64:79]
ex 96
ex 97
ex 98
ex 99
cv 32,96,97
rqa 104,2,53248
rqa 108,2,53760
v_mfma_f32_32x32x16_bf16 a[80:95], a[112:115], v[24:27], a[80:95]
ex 100
ex 101
cv 33,98,99
ex 102
ex 103
cv 34,100,101
v_pk_maximum3_f16 v8, v32, v33, v34
v_mfma_f32_32x32x16_bf16 a[64:79], a[116:119], v[20:23], a[64:79]
ex 104
ex 105
cv 35,102,103
ex 106
ex 107
cv 36,104,105
v_pk_maximum3_f16 v8, v8, v35, v36
v_mfma_f32_32x32x16_bf16 a[80:95], a[116:119], v[28:31], a[80:95]
ex 108
ex 109
cv 37,106,107
ex 110
ex 111
cv 38,108,109
v_pk_maximum3_f16 v8, v8, v37, v38
v_add_u32 v6, 116736, v11
ds_read2_b64 a[112:115], v6 offset0:0 offset1:64
ds_read2_b64 a[116:119], v6 offset0:1 offset1:65
v_mfma_f32_32x32x16_bf16 v[48:63], a[96:99], v[64:67], 0
ex 112
ex 113
cv 39,110,111
ex 114
ex 115
cv 40,112,113
v_pk_maximum3_f16 v8, v8, v39, v40
v_mfma_f32_32x32x16_bf16 v[80:95], a[96:99], v[72:75], 0
ex 116
ex 117
cv 41,114,115
ex 118
ex 119
cv 42,116,117
v_pk_maximum3_f16 v8, v8, v41, v42
v_mfma_f32_32x32x16_bf16 v[48:63], a[100:103], v[68:71], v[48:63]
ex 120
ex 121
cv 43,118,119
ex 122
ex 123
cv 44,120,121
v_pk_maximum3_f16 v8, v8, v43, v44
v_mfma_f32_32x32x16_bf16 v[80:95], a[100:103], v[76:79], v[80:95]
ex 124
ex 125
cv 45,122,123
ex 126
ex 127
cv 46,124,125
v_pk_maximum3_f16 v8, v8, v45, v46
s_nop 0
cv 47,126,127
v_pk_max_u16 v8, v8, v47
v_pk_max_u16 v8, v8, v8 op_sel:[0,1] op_sel_hi:[1,0]
v_cmp_lt_u32 vcc, s71, v8
s_cbranch_vccnz .Lk1_atr27
.Lk1_aret27:
s_mov_b32 s67, 0x400000
s_waitcnt vmcnt(0)
.Lf_f2chk:
v_cmp_ne_u32 vcc, s82, v14
s_nop 4
s_cbranch_vccz .Lf_f2ok
s_sleep 4
global_load_dword v14, v13, s[60:61] sc1
s_waitcnt vmcnt(0)
s_sub_u32 s67, s67, 1
s_cmp_lg_u32 s67, 0
s_cbranch_scc1 .Lf_f2chk
.Lf_f2ok:
s_waitcnt lgkmcnt(0)
v_mfma_f32_32x32x16_bf16 a[64:79], a[120:123], v[32:35], a[64:79]
ex 48
ex 49
ex 50
ex 51
cv 16,48,49
rqa 96,2,57344
rqa 100,2,57856
v_mfma_f32_32x32x16_bf16 a[80:95], a[120:123], v[40:43], a[80:95]
ex 52
ex 53
cv 17,50,51
ex 54
ex 55
cv 18,52,53
v_pk_maximum3_f16 v8, v16, v17, v18
s_add_u32 m0, s99, 0
s_nop 0
v_mfma_f32_32x32x16_bf16 a[64:79], a[124:127], v[36:39], a[64:79]
ex 56
ex 57
cv 19,54,55
buffer_load_dwordx4 v0, s[20:23], s42 offen offset:0 sc1 lds
ex 58
ex 59
cv 20,56,57
v_pk_maximum3_f16 v8, v8, v19, v20
buffer_load_dwordx4 v0, s[20:23], s42 offen offset:1024 sc1 lds
v_mfma_f32_32x32x16_bf16 a[80:95], a[124:127], v[44:47], a[80:95]
ex 60
ex 61
cv 21,58,59
buffer_load_dwordx4 v0, s[20:23], s42 offen offset:2048 sc1 lds
ex 62
ex 63
cv 22,60,61
v_pk_maximum3_f16 v8, v8, v21, v22
v_add_u32 v7, 120832, v11
ds_read2_b64 a[120:123], v7 offset0:0 offset1:64
ds_read2_b64 a[124:127], v7 offset0:1 offset1:65
buffer_load_dwordx4 v0, s[20:23], s42 offen offset:3072 sc1 lds
v_mfma_f32_32x32x16_bf16 v[96:111], a[104:107], v[64:67], 0
ex 80
ex 81
cv 23,62,63
s_add_u32 s42, s42, 0x8000
ex 82
ex 83
cv 24,80,81
v_pk_maximum3_f16 v8, v8, v23, v24
s_cmp_eq_u32 s42, s44
s_cselect_b32 s42, s46, s42
s_cselect_b32 s22, s48, s22
s_cselect_b32 s44, -1, s44
v_mfma_f32_32x32x16_bf16 v[112:127], a[104:107], v[72:75], 0
ex 84
ex 85
cv 25,82,83
s_add_u32 m0, s99, 16384
s_nop 0
ex 86
ex 87
cv 26,84,85
v_pk_maximum3_f16 v8, v8, v25, v26
buffer_load_dwordx4 v0, s[20:23], s42 offen offset:0 sc1 lds
v_mfma_f32_32x32x16_bf16 v[96:111], a[108:111], v[68:71], v[96:111]
ex 88
ex 89
cv 27,86,87
buffer_load_dwordx4 v0, s[20:23], s42 offen offset:1024 sc1 lds
ex 90
ex 91
cv 28,88,89
v_pk_maximum3_f16 v8, v8, v27, v28
buffer_load_dwordx4 v0, s[20:23], s42 offen offset:2048 sc1 lds
v_mfma_f32_32x32x16_bf16 v[112:127], a[108:111], v[76:79], v[112:127]
ex 92
ex 93
cv 29,90,91
buffer_load_dwordx4 v0, s[20:23], s42 offen offset:3072 sc1 lds
ex 94
ex 95
cv 30,92,93
v_pk_maximum3_f16 v8, v8, v29, v30
s_add_u32 s42, s42, 0x8000
s_cmp_eq_u32 s42, s44
s_cselect_b32 s42, s46, s42
s_cselect_b32 s22, s48, s22
s_cselect_b32 s44, -1, s44
s_nop 0
cv 31,94,95
v_pk_max_u16 v8, v8, v31
v_pk_max_u16 v8, v8, v8 op_sel:[0,1] op_sel_hi:[1,0]
v_cmp_lt_u32 vcc, s71, v8
s_cbranch_vccnz .Lk1_atr28
.Lk1_aret28:
s_waitcnt lgkmcnt(0)
v_mfma_f32_32x32x16_bf16 a[64:79], a[112:115], v[16:19], a[64:79]
ex 96
ex 97
ex 98
ex 99
cv 32,96,97
rqa 104,2,61440
rqa 108,2,61952
v_mfma_f32_32x32x16_bf16 a[80:95], a[112:115], v[24:27], a[80:95]
ex 100
ex 101
cv 33,98,99
ex 102
ex 103
cv 34,100,101
v_pk_maximum3_f16 v8, v32, v33, v34
s_add_u32 m0, s40, s65
s_nop 0
v_mfma_f32_32x32x16_bf16 a[64:79], a[116:119], v[20:23], a[64:79]
ex 104
ex 105
cv 35,102,103
buffer_load_dwordx4 v0, s[24:27], s43 offen offset:0 sc1 lds
ex 106
ex 107
cv 36,104,105
v_pk_maximum3_f16 v8, v8, v35, v36
buffer_load_dwordx4 v0, s[24:27], s43 offen offset:1024 sc1 lds
buffer_load_dwordx4 v0, s[24:27], s43 offen offset:2048 sc1 lds
v_mfma_f32_32x32x16_bf16 a[80:95], a[116:119], v[28:31], a[80:95]
ex 108
ex 109
cv 37,106,107
buffer_load_dwordx4 v0, s[24:27], s43 offen offset:3072 sc1 lds
ex 110
ex 111
cv 38,108,109
v_pk_maximum3_f16 v8, v8, v37, v38
v_add_u32 v6, 124928, v11
ds_read2_b64 a[112:115], v6 offset0:0 offset1:64
ds_read2_b64 a[116:119], v6 offset0:1 offset1:65
s_add_u32 s43, s43, 0x8000
v_mfma_f32_32x32x16_bf16 v[48:63], a[96:99], v[64:67], 0
ex 112
ex 113
cv 39,110,111
s_add_u32 s65, s65, 0x4000
s_cmp_eq_u32 s65, 0x28000
s_cselect_b32 s65, 0x10000, s65
ex 114
ex 115
cv 40,112,113
v_pk_maximum3_f16 v8, v8, v39, v40
s_cmp_eq_u32 s43, s45
s_cselect_b32 s43, s47, s43
s_cselect_b32 s26, s48, s26
s_cselect_b32 s45, -1, s45
v_mfma_f32_32x32x16_bf16 v[80:95], a[96:99], v[72:75], 0
ex 116
ex 117
cv 41,114,115
s_add_u32 m0, s40, s65
s_nop 0
buffer_load_dwordx4 v0, s[24:27], s43 offen offset:0 sc1 lds
ex 118
ex 119
cv 42,116,117
v_pk_maximum3_f16 v8, v8, v41, v42
buffer_load_dwordx4 v0, s[24:27], s43 offen offset:1024 sc1 lds
v_mfma_f32_32x32x16_bf16 v[48:63], a[100:103], v[68:71], v[48:63]
ex 120
ex 121
cv 43,118,119
buffer_load_dwordx4 v0, s[24:27], s43 offen offset:2048 sc1 lds
ex 122
ex 123
cv 44,120,121
v_pk_maximum3_f16 v8, v8, v43, v44
buffer_load_dwordx4 v0, s[24:27], s43 offen offset:3072 sc1 lds
s_add_u32 s43, s43, 0x8000
v_mfma_f32_32x32x16_bf16 v[80:95], a[100:103], v[76:79], v[80:95]
ex 124
ex 125
cv 45,122,123
s_add_u32 s65, s65, 0x4000
ex 126
ex 127
cv 46,124,125
v_pk_maximum3_f16 v8, v8, v45, v46
s_cmp_eq_u32 s65, 0x28000
s_cselect_b32 s65, 0x10000, s65
s_cmp_eq_u32 s43, s45
s_cselect_b32 s43, s47, s43
s_cselect_b32 s26, s48, s26
s_cselect_b32 s45, -1, s45
s_nop 0
cv 47,126,127
v_pk_max_u16 v8, v8, v47
v_pk_max_u16 v8, v8, v8 op_sel:[0,1] op_sel_hi:[1,0]
v_cmp_lt_u32 vcc, s71, v8
s_cbranch_vccnz .Lk1_atr29
.Lk1_aret29:
s_waitcnt lgkmcnt(0)
v_mfma_f32_32x32x16_bf16 a[64:79], a[120:123], v[32:35], a[64:79]
ex 48
ex 49
ex 50
ex 51
cv 16,48,49
v_mfma_f32_32x32x16_bf16 a[80:95], a[120:123], v[40:43], a[80:95]
ex 52
ex 53
cv 17,50,51
ex 54
ex 55
cv 18,52,53
v_pk_maximum3_f16 v8, v16, v17, v18
v_mfma_f32_32x32x16_bf16 a[64:79], a[124:127], v[36:39], a[64:79]
ex 56
ex 57
cv 19,54,55
s_add_u32 m0, s99, 32768
s_nop 0
ex 58
ex 59
cv 20,56,57
v_pk_maximum3_f16 v8, v8, v19, v20
v_mfma_f32_32x32x16_bf16 a[80:95], a[124:127], v[44:47], a[80:95]
ex 60
ex 61
cv 21,58,59
buffer_load_dwordx4 v0, s[20:23], s42 offen offset:0 sc1 lds
ex 62
ex 63
cv 22,60,61
v_pk_maximum3_f16 v8, v8, v21, v22
v_add_u32 v7, 129024, v11
ds_read2_b64 a[120:123], v7 offset0:0 offset1:64
ds_read2_b64 a[124:127], v7 offset0:1 offset1:65
v_mfma_f32_32x32x16_bf16 v[96:111], a[104:107], v[64:67], 0
ex 80
ex 81
cv 23,62,63
buffer_load_dwordx4 v0, s[20:23], s42 offen offset:1024 sc1 lds
ex 82
ex 83
cv 24,80,81
v_pk_maximum3_f16 v8, v8, v23, v24
v_mfma_f32_32x32x16_bf16 v[112:127], a[104:107], v[72:75], 0
ex 84
ex 85
cv 25,82,83
buffer_load_dwordx4 v0, s[20:23], s42 offen offset:2048 sc1 lds
ex 86
ex 87
cv 26,84,85
v_pk_maximum3_f16 v8, v8, v25, v26
v_mfma_f32_32x32x16_bf16 v[96:111], a[108:111], v[68:71], v[96:111]
ex 88
ex 89
)ASM" ::: "memory");
  asm volatile(R"ASM(
cv 27,86,87
buffer_load_dwordx4 v0, s[20:23], s42 offen offset:3072 sc1 lds
ex 90
ex 91
cv 28,88,89
v_pk_maximum3_f16 v8, v8, v27, v28
v_mfma_f32_32x32x16_bf16 v[112:127], a[108:111], v[76:79], v[112:127]
ex 92
ex 93
cv 29,90,91
s_add_u32 s42, s42, 0x8000
ex 94
ex 95
cv 30,92,93
v_pk_maximum3_f16 v8, v8, v29, v30
s_cmp_eq_u32 s42, s44
s_cselect_b32 s42, s46, s42
s_cselect_b32 s22, s48, s22
s_cselect_b32 s44, -1, s44
s_nop 0
cv 31,94,95
v_pk_max_u16 v8, v8, v31
v_pk_max_u16 v8, v8, v8 op_sel:[0,1] op_sel_hi:[1,0]
v_cmp_lt_u32 vcc, s71, v8
s_cbranch_vccnz .Lk1_atr30
.Lk1_aret30:
s_waitcnt lgkmcnt(0)
v_mfma_f32_32x32x16_bf16 a[64:79], a[112:115], v[16:19], a[64:79]
ex 96
ex 97
ex 98
ex 99
cv 32,96,97
v_mfma_f32_32x32x16_bf16 a[80:95], a[112:115], v[24:27], a[80:95]
ex 100
ex 101
cv 33,98,99
ex 102
ex 103
cv 34,100,101
v_pk_maximum3_f16 v8, v32, v33, v34
v_mfma_f32_32x32x16_bf16 a[64:79], a[116:119], v[20:23], a[64:79]
ex 104
ex 105
cv 35,102,103
s_add_u32 m0, s99, 49152
s_nop 0
ex 106
ex 107
cv 36,104,105
v_pk_maximum3_f16 v8, v8, v35, v36
v_mfma_f32_32x32x16_bf16 a[80:95], a[116:119], v[28:31], a[80:95]
ex 108
ex 109
cv 37,106,107
buffer_load_dwordx4 v0, s[20:23], s42 offen offset:0 sc1 lds
ex 110
ex 111
cv 38,108,109
v_pk_maximum3_f16 v8, v8, v37, v38
ex 112
ex 113
cv 39,110,111
buffer_load_dwordx4 v0, s[20:23], s42 offen offset:1024 sc1 lds
ex 114
ex 115
cv 40,112,113
v_pk_maximum3_f16 v8, v8, v39, v40
ex 116
ex 117
cv 41,114,115
buffer_load_dwordx4 v0, s[20:23], s42 offen offset:2048 sc1 lds
ex 118
ex 119
cv 42,116,117
v_pk_maximum3_f16 v8, v8, v41, v42
ex 120
ex 121
cv 43,118,119
buffer_load_dwordx4 v0, s[20:23], s42 offen offset:3072 sc1 lds
ex 122
ex 123
cv 44,120,121
v_pk_maximum3_f16 v8, v8, v43, v44
ex 124
ex 125
cv 45,122,123
s_add_u32 s42, s42, 0x8000
ex 126
ex 127
cv 46,124,125
v_pk_maximum3_f16 v8, v8, v45, v46
s_cmp_eq_u32 s42, s44
s_cselect_b32 s42, s46, s42
s_cselect_b32 s22, s48, s22
s_cselect_b32 s44, -1, s44
s_nop 0
cv 47,126,127
v_pk_max_u16 v8, v8, v47
v_pk_max_u16 v8, v8, v8 op_sel:[0,1] op_sel_hi:[1,0]
v_cmp_lt_u32 vcc, s71, v8
s_cbranch_vccnz .Lk1_atr31
.Lk1_aret31:
.Lf_A_epi:
s_waitcnt vmcnt(12)
s_barrier
v_mfma_f32_32x32x16_bf16 a[64:79], a[120:123], v[32:35], a[64:79]
v_mfma_f32_32x32x16_bf16 a[80:95], a[120:123], v[40:43], a[80:95]
v_mfma_f32_32x32x16_bf16 a[64:79], a[124:127], v[36:39], a[64:79]
v_mfma_f32_32x32x16_bf16 a[80:95], a[124:127], v[44:47], a[80:95]
v_subrev_u32 v14, s41, v1
v_lshrrev_b32 v9, 2, v0
v_and_b32 v9, 0x7c, v9
v_add_u32 v1, s41, v0
s_add_u32 m0, s40, s65
s_nop 0
buffer_load_dwordx4 v0, s[24:27], s43 offen offset:0 sc1 lds
buffer_load_dwordx4 v0, s[24:27], s43 offen offset:1024 sc1 lds
buffer_load_dwordx4 v0, s[24:27], s43 offen offset:2048 sc1 lds
buffer_load_dwordx4 v0, s[24:27], s43 offen offset:3072 sc1 lds
s_add_u32 s43, s43, 0x8000
s_add_u32 s65, s65, 0x4000
s_cmp_eq_u32 s65, 0x28000
s_cselect_b32 s65, 0x10000, s65
s_cmp_eq_u32 s43, s45
s_cselect_b32 s43, s47, s43
s_cselect_b32 s26, s48, s26
s_cselect_b32 s45, -1, s45
s_add_u32 m0, s40, s65
s_nop 0
buffer_load_dwordx4 v0, s[24:27], s43 offen offset:0 sc1 lds
buffer_load_dwordx4 v0, s[24:27], s43 offen offset:1024 sc1 lds
buffer_load_dwordx4 v0, s[24:27], s43 offen offset:2048 sc1 lds
buffer_load_dwordx4 v0, s[24:27], s43 offen offset:3072 sc1 lds
s_add_u32 s43, s43, 0x8000
s_add_u32 s65, s65, 0x4000
s_cmp_eq_u32 s65, 0x28000
s_cselect_b32 s65, 0x10000, s65
s_cmp_eq_u32 s43, s45
s_cselect_b32 s43, s47, s43
s_cselect_b32 s26, s48, s26
s_cselect_b32 s45, -1, s45
ar 80,64
ar 81,65
ar 82,66
ar 83,67
ar 84,68
ar 85,69
ar 86,70
ar 87,71
ar 88,72
ar 89,73
ar 90,74
ar 91,75
ar 92,76
ar 93,77
ar 94,78
ar 95,79
ar 96,80
ar 97,81
ar 98,82
ar 99,83
ar 100,84
ar 101,85
ar 102,86
ar 103,87
ar 104,88
ar 105,89
ar 106,90
ar 107,91
ar 108,92
ar 109,93
ar 110,94
ar 111,95
rqa 64,1,0
rqa 68,1,1024
rqa 72,1,2048
rqa 76,1,3072
rqa 80,1,4096
rqa 84,1,5120
rqa 88,1,6144
rqa 92,1,7168
rqa 96,1,8192
rqa 100,1,9216
rqa 104,1,10240
rqa 108,1,11264
rqa 112,1,12288
rqa 116,1,13312
rqa 120,1,14336
rqa 124,1,15360
s_cmp_lg_u32 s66, 0
s_cbranch_scc1 .Lk2_proA
v_mov_b32 v8, v92
v_mov_b32 v10, v92
s_nop 1
v_permlane32_swap_b32 v8, v10
s_nop 0
v_rcp_f32 v8, v10
v_log_f32 v10, v10
s_nop 0
v_add_f32 v4, v4, v10
v_mul_f32 v80, v8, v80
v_mul_f32 v81, v8, v81
v_mul_f32 v82, v8, v82
v_mul_f32 v83, v8, v83
v_mul_f32 v84, v8, v84
s_waitcnt lgkmcnt(8)
zvaa 44,64,0
v_mul_f32 v85, v8, v85
v_mul_f32 v86, v8, v86
v_mul_f32 v87, v8, v87
v_mul_f32 v88, v8, v88
v_mul_f32 v89, v8, v89
v_mul_f32 v90, v8, v90
v_mul_f32 v91, v8, v91
mvaa 44,68,8
v_mul_f32 v92, v8, v92
v_mul_f32 v93, v8, v93
v_mul_f32 v94, v8, v94
v_mul_f32 v95, v8, v95
cv 16,80,81
cv 17,82,83
cv 18,84,85
mvaa 44,72,16
cv 19,86,87
cv 20,88,89
cv 21,90,91
cv 22,92,93
cv 23,94,95
global_store_dwordx2 v14, v[16:17], s[0:1] offset:0 sc1
global_store_dwordx2 v14, v[18:19], s[0:1] offset:512 sc1
mvaa 44,76,24
global_store_dwordx2 v14, v[20:21], s[0:1] offset:8 sc1
global_store_dwordx2 v14, v[22:23], s[0:1] offset:520 sc1
v_mov_b32 v8, v108
v_mov_b32 v10, v108
s_nop 1
v_permlane32_swap_b32 v8, v10
s_nop 0
mvaa 44,80,32
v_rcp_f32 v8, v10
v_log_f32 v10, v10
s_nop 0
v_add_f32 v5, v5, v10
v_mul_f32 v96, v8, v96
v_mul_f32 v97, v8, v97
v_mul_f32 v98, v8, v98
mvaa 44,84,40
v_mul_f32 v99, v8, v99
v_mul_f32 v100, v8, v100
v_mul_f32 v101, v8, v101
v_mul_f32 v102, v8, v102
v_mul_f32 v103, v8, v103
v_mul_f32 v104, v8, v104
v_mul_f32 v105, v8, v105
mvaa 44,88,48
v_mul_f32 v106, v8, v106
v_mul_f32 v107, v8, v107
v_mul_f32 v108, v8, v108
v_mul_f32 v109, v8, v109
v_mul_f32 v110, v8, v110
v_mul_f32 v111, v8, v111
cv 24,96,97
mvaa 44,92,56
cv 25,98,99
cv 26,100,101
cv 27,102,103
cv 28,104,105
cv 29,106,107
cv 30,108,109
cv 31,110,111
zvaa 48,64,4
global_store_dwordx2 v14, v[24:25], s[0:1] offset:2048 sc1
global_store_dwordx2 v14, v[26:27], s[0:1] offset:2560 sc1
global_store_dwordx2 v14, v[28:29], s[0:1] offset:2056 sc1
global_store_dwordx2 v14, v[30:31], s[0:1] offset:2568 sc1
s_mov_b32 exec_lo, 0
global_store_dword v9, v4, s[2:3] offset:0 sc1
global_store_dword v9, v5, s[2:3] offset:128 sc1
s_mov_b64 exec, -1
s_mov_b32 s71, 0x41c00000
ar 9,206
mvaa 48,68,12
s_nop 0
v_readlane_b32 s4, v9, 0
v_readlane_b32 s5, v9, 1
v_readlane_b32 s6, v9, 2
v_readlane_b32 s7, v9, 3
v_readlane_b32 s8, v9, 4
v_readlane_b32 s9, v9, 5
mvaa 48,72,20
v_readlane_b32 s10, v9, 6
v_readlane_b32 s11, v9, 7
v_readlane_b32 s12, v9, 8
v_readlane_b32 s13, v9, 9
v_readlane_b32 s14, v9, 10
v_readlane_b32 s15, v9, 11
v_readlane_b32 s16, v9, 12
mvaa 48,76,28
v_readlane_b32 s17, v9, 13
v_readlane_b32 s18, v9, 14
v_readlane_b32 s19, v9, 15
v_readlane_b32 s52, v9, 16
v_readlane_b32 s53, v9, 17
v_readlane_b32 s54, v9, 18
v_readlane_b32 s55, v9, 19
mvaa 48,80,36
v_readlane_b32 s56, v9, 20
v_readlane_b32 s57, v9, 21
v_readlane_b32 s58, v9, 22
v_readlane_b32 s59, v9, 23
v_readlane_b32 s84, v9, 24
v_readlane_b32 s85, v9, 25
v_readlane_b32 s86, v9, 26
mvaa 48,84,44
v_readlane_b32 s87, v9, 27
v_readlane_b32 s88, v9, 28
v_readlane_b32 s89, v9, 29
v_readlane_b32 s90, v9, 30
v_readlane_b32 s91, v9, 31
v_readlane_b32 s92, v9, 32
v_readlane_b32 s93, v9, 33
mvaa 48,88,52
v_readlane_b32 s94, v9, 34
v_readlane_b32 s95, v9, 35
v_readlane_b32 s0, v9, 36
v_readlane_b32 s1, v9, 37
v_readlane_b32 s2, v9, 38
v_readlane_b32 s3, v9, 39
ar 4,192
mvaa 48,92,60
ar 76,193
ar 78,194
ar 13,195
ar 244,196
ar 245,197
ar 246,198
ar 247,199
s_waitcnt lgkmcnt(0)
zvaa 52,96,0
ar 248,200
ar 249,201
ar 250,202
ar 251,203
ar 255,204
v_mov_b32 v79, 0
s_nop 1
mvaa 52,100,8
global_load_dword v6, v76, s[32:33] sc1
global_load_dword v7, v76, s[32:33] offset:64 sc1
s_mov_b32 s60, 0
s_mov_b32 s61, 0xffff0000
s_mov_b32 s70, 0
s_mov_b32 s66, 0
s_mov_b32 s62, 0
mvaa 52,104,16
s_mov_b32 s63, 2
s_mov_b32 s83, 1
s_mov_b32 s32, 0
s_mov_b32 s33, 0
v_mov_b32 v16, 0x3f803f80
v_mov_b32 v17, 0x3f803f80
v_mov_b32 v18, 0x3f803f80
mvaa 52,108,24
v_mov_b32 v19, 0x3f803f80
v_mov_b32 v15, 0xff800000
v_add_u32 v2, 0x10000, v1
v_mov_b32 v14, v2
v_cmp_gt_i32_e64 s[72:73], 0, v4
v_cmp_gt_i32_e64 s[74:75], 1, v4
v_cmp_gt_i32_e64 s[76:77], 2, v4
mvaa 52,112,32
v_cmp_gt_i32_e64 s[78:79], 3, v4
v_mov_b64 v[36:37], 0
v_mov_b64 v[38:39], 0
v_mov_b64 v[40:41], 0
v_mov_b64 v[42:43], 0
v_mov_b64 v[80:81], 0
v_mov_b64 v[82:83], 0
mvaa 52,116,40
v_mov_b64 v[84:85], 0
v_mov_b64 v[86:87], 0
v_mov_b64 v[88:89], 0
v_mov_b64 v[90:91], 0
v_mov_b64 v[92:93], 0
v_mov_b64 v[94:95], 0
v_mov_b64 v[96:97], 0
mvaa 52,120,48
v_mov_b64 v[98:99], 0
v_mov_b64 v[100:101], 0
v_mov_b64 v[102:103], 0
v_mov_b64 v[104:105], 0
v_mov_b64 v[106:107], 0
v_mov_b64 v[108:109], 0
v_mov_b64 v[110:111], 0
mvaa 52,124,56
v_mov_b64 v[112:113], 0
v_mov_b64 v[114:115], 0
v_mov_b64 v[116:117], 0
v_mov_b64 v[118:119], 0
v_mov_b64 v[120:121], 0
v_mov_b64 v[122:123], 0
v_mov_b64 v[124:125], 0
zvaa 56,96,4
v_mov_b64 v[126:127], 0
v_mov_b64 v[128:129], 0
v_mov_b64 v[130:131], 0
v_mov_b64 v[132:133], 0
v_mov_b64 v[134:135], 0
v_mov_b64 v[136:137], 0
v_mov_b64 v[138:139], 0
mvaa 56,100,12
v_mov_b64 v[140:141], 0
v_mov_b64 v[142:143], 0
v_mov_b64 v[144:145], 0
v_mov_b64 v[146:147], 0
v_mov_b64 v[148:149], 0
v_mov_b64 v[150:151], 0
v_mov_b64 v[152:153], 0
mvaa 56,104,20
v_mov_b64 v[154:155], 0
v_mov_b64 v[156:157], 0
v_mov_b64 v[158:159], 0
v_mov_b64 v[160:161], 0
v_mov_b64 v[162:163], 0
v_mov_b64 v[164:165], 0
v_mov_b64 v[166:167], 0
mvaa 56,108,28
v_mov_b64 v[168:169], 0
v_mov_b64 v[170:171], 0
v_mov_b64 v[172:173], 0
v_mov_b64 v[174:175], 0
v_mov_b64 v[176:177], 0
v_mov_b64 v[178:179], 0
v_mov_b64 v[180:181], 0
mvaa 56,112,36
v_mov_b64 v[182:183], 0
v_mov_b64 v[184:185], 0
v_mov_b64 v[186:187], 0
v_mov_b64 v[188:189], 0
v_mov_b64 v[190:191], 0
v_mov_b64 v[192:193], 0
v_mov_b64 v[194:195], 0
mvaa 56,116,44
v_mov_b64 v[196:197], 0
v_mov_b64 v[198:199], 0
v_mov_b64 v[200:201], 0
v_mov_b64 v[202:203], 0
v_mov_b64 v[204:205], 0
v_mov_b64 v[206:207], 0
mvaa 56,120,52
mvaa 56,124,60
s_barrier
s_add_u32 m0, s40, s65
s_nop 0
buffer_load_dwordx4 v0, s[24:27], s43 offen offset:0 sc1 lds
buffer_load_dwordx4 v0, s[24:27], s43 offen offset:1024 sc1 lds
buffer_load_dwordx4 v0, s[24:27], s43 offen offset:2048 sc1 lds
buffer_load_dwordx4 v0, s[24:27], s43 offen offset:3072 sc1 lds
s_add_u32 s43, s43, 0x8000
s_add_u32 s65, s65, 0x4000
s_cmp_eq_u32 s65, 0x28000
s_cselect_b32 s65, 0x10000, s65
s_cmp_eq_u32 s43, s45
s_cselect_b32 s43, s47, s43
s_cselect_b32 s26, s48, s26
s_cselect_b32 s45, -1, s45
rqa 64,1,16384
rqa 68,1,17408
rqa 72,1,18432
rqa 76,1,19456
rqa 80,1,20480
rqa 84,1,21504
rqa 88,1,22528
rqa 92,1,23552
rqa 96,1,24576
rqa 100,1,25600
rqa 104,1,26624
s_mov_b32 s67, s52
s_mov_b32 s52, 0
s_cmp_lg_u32 s52, -1
s_cbranch_scc1 .Lk2_pB_nomask
v_cndmask_b32_e64 v44, v44, v15, s[72:73]
v_cndmask_b32_e64 v56, v56, v15, s[72:73]
v_mov_b32 v52, v15
v_cndmask_b32_e64 v45, v45, v15, s[74:75]
v_cndmask_b32_e64 v57, v57, v15, s[74:75]
v_mov_b32 v53, v15
v_cndmask_b32_e64 v46, v46, v15, s[76:77]
v_cndmask_b32_e64 v58, v58, v15, s[76:77]
v_mov_b32 v54, v15
v_cndmask_b32_e64 v47, v47, v15, s[78:79]
v_cndmask_b32_e64 v59, v59, v15, s[78:79]
v_mov_b32 v55, v15
.Lk2_pB_nomask:
m3 208,44,45,46
m3 209,47,52,53
m3 208,208,54,55
v_max_f32 v208, v208, v209
v_mov_b32 v209, v208
s_nop 1
v_permlane32_swap_b32 v208, v209
s_nop 0
v_max_f32 v208, v208, v209
v_mov_b32 v209, v208
s_nop 1
v_permlane16_swap_b32 v208, v209
s_nop 0
v_max_f32 v208, v208, v209
v_mov_b32 v4, v208
v_sub_f32 v44, v44, v208
v_sub_f32 v45, v45, v208
v_sub_f32 v46, v46, v208
v_sub_f32 v47, v47, v208
v_sub_f32 v52, v52, v208
v_sub_f32 v53, v53, v208
v_sub_f32 v54, v54, v208
v_sub_f32 v55, v55, v208
v_sub_f32 v20, 0, v208
v_sub_f32 v21, 0, v208
v_sub_f32 v22, 0, v208
v_sub_f32 v23, 0, v208
m3 212,48,49,50
m3 213,51,56,57
m3 212,212,58,59
v_max_f32 v212, v212, v213
v_mov_b32 v213, v212
s_nop 1
v_permlane32_swap_b32 v212, v213
s_nop 0
v_max_f32 v212, v212, v213
v_mov_b32 v213, v212
s_nop 1
v_permlane16_swap_b32 v212, v213
s_nop 0
v_max_f32 v212, v212, v213
v_mov_b32 v5, v212
v_sub_f32 v48, v48, v212
v_sub_f32 v49, v49, v212
v_sub_f32 v50, v50, v212
v_sub_f32 v51, v51, v212
v_sub_f32 v56, v56, v212
v_sub_f32 v57, v57, v212
v_sub_f32 v58, v58, v212
v_sub_f32 v59, v59, v212
v_sub_f32 v24, 0, v212
v_sub_f32 v25, 0, v212
v_sub_f32 v26, 0, v212
v_sub_f32 v27, 0, v212
s_mov_b32 s52, s67
s_min_u32 s97, s49, 2
s_cmp_eq_u32 s49, 0
s_cbranch_scc1 .Lk2_seam10
.Lk2_reg10:
s_waitcnt lgkmcnt(3)
s_add_u32 m0, s99, 0
v_mfma_f32_16x16x32_bf16 v[60:63], a[64:67], a[0:3], v[20:23]
rqa 108,1,27648
mvaa 60,68,8
ex 44
mvaa 60,72,16
rqa 112,1,28672
mvaa 60,76,24
ex 45
mvaa 60,80,32
rqa 116,1,29696
cv 28,44,45
mvaa 60,84,40
ex 46
mvaa 60,88,48
rqa 120,1,30720
mvaa 60,92,56
ex 47
buffer_load_dwordx4 v0, s[20:23], s42 offen offset:0 sc1 lds
v_mfma_f32_16x16x32_bf16 v[64:67], a[64:67], a[4:7], v[24:27]
rqa 124,1,31744
cv 29,46,47
mvaa 64,68,12
ex 52
mvaa 64,72,20
rqa 64,2,0
mvaa 64,76,28
ex 53
mvaa 64,80,36
rqa 68,2,1024
cv 30,52,53
mvaa 64,84,44
ex 54
mvaa 64,88,52
rqa 72,2,2048
mvaa 64,92,60
ex 55
buffer_load_dwordx4 v0, s[20:23], s42 offen offset:1024 sc1 lds
s_waitcnt lgkmcnt(3)
v_mfma_f32_16x16x32_bf16 v[68:71], a[96:99], a[0:3], v[20:23]
rqa 76,2,3072
cv 31,54,55
mvaa 68,100,8
ex 48
mvaa 68,104,16
)ASM" ::: "memory");
  asm volatile(R"ASM(
rqa 80,2,4096
mvaa 68,108,24
ex 49
mvaa 68,112,32
rqa 84,2,5120
cv 32,48,49
mvaa 68,116,40
ex 50
mvaa 68,120,48
rqa 88,2,6144
mvaa 68,124,56
ex 51
ex 59
buffer_load_dwordx4 v0, s[20:23], s42 offen offset:2048 sc1 lds
v_mfma_f32_16x16x32_bf16 v[72:75], a[96:99], a[4:7], v[24:27]
rqa 92,2,7168
cv 33,50,51
mvaa 72,100,12
ex 56
mvaa 72,104,20
rqa 96,2,8192
mvaa 72,108,28
ex 57
mvaa 72,112,36
rqa 100,2,9216
cv 34,56,57
mvaa 72,116,44
ex 58
mvaa 72,120,52
rqa 104,2,10240
cv 35,58,59
mvaa 72,124,60
buffer_load_dwordx4 v0, s[20:23], s42 offen offset:3072 sc1 lds
s_waitcnt lgkmcnt(7)
mvav 80,64,28
rqa 108,2,11264
mvav 88,64,32
s_waitcnt vmcnt(16)
s_barrier
mvav 84,68,28
rqa 112,2,12288
mvav 92,68,32
s_add_u32 m0, s40, s65
mvav 96,72,28
rqa 116,2,13312
mvvv 36,16,28
mvav 104,72,32
buffer_load_dwordx4 v0, s[24:27], s43 offen offset:0 sc1 lds
mvav 100,76,28
rqa 120,2,14336
m3 8,60,61,62
mvav 108,76,32
s_add_u32 s42, s42, 0x8000
s_waitcnt lgkmcnt(7)
mvav 112,80,28
rqa 124,2,15360
m3 9,63,64,65
mvav 120,80,32
s_cmp_eq_u32 s42, s44
s_cselect_b32 s42, s46, s42
s_cselect_b32 s22, s48, s22
s_cselect_b32 s44, -1, s44
mvav 116,84,28
rqa 64,1,32768
m3 8,8,66,67
mvav 124,84,32
buffer_load_dwordx4 v0, s[24:27], s43 offen offset:1024 sc1 lds
mvav 128,88,28
rqa 68,1,33792
m3 9,9,68,69
mvav 136,88,32
s_add_u32 s64, s64, 0x4000
mvav 132,92,28
rqa 72,1,34816
m3 8,8,70,71
mvav 140,92,32
s_cmp_eq_u32 s64, 0x18000
s_cselect_b32 s64, 0, s64
s_waitcnt lgkmcnt(7)
mvav 144,96,28
rqa 76,1,35840
mvav 152,96,32
v_add_u32 v2, s64, v14
mvav 148,100,28
rqa 80,1,36864
m3 9,9,72,73
mvav 156,100,32
buffer_load_dwordx4 v0, s[24:27], s43 offen offset:2048 sc1 lds
mvav 160,104,28
rqa 84,1,37888
m3 8,8,74,75
mvav 168,104,32
s_add_u32 s66, s66, 1
mvav 164,108,28
rqa 88,1,38912
v_max_f32 v8, v8, v9
mvav 172,108,32
s_waitcnt lgkmcnt(7)
mvav 176,112,28
rqa 92,1,39936
v_cmp_lt_f32 vcc, s71, v8
mvav 184,112,32
buffer_load_dwordx4 v0, s[24:27], s43 offen offset:3072 sc1 lds
mvav 180,116,28
rqa 96,1,40960
mvav 188,116,32
s_add_u32 s43, s43, 0x8000
mvav 192,120,28
rqa 100,1,41984
s_cmp_eq_u32 s66, s97
s_cselect_b64 vcc, -1, vcc
mvav 200,120,32
s_add_u32 s65, s65, 0x4000
mvav 196,124,28
rqa 104,1,43008
s_cmp_eq_u32 s43, s45
s_cselect_b32 s43, s47, s43
s_cselect_b32 s26, s48, s26
s_cselect_b32 s45, -1, s45
mvvv 40,16,32
mvav 204,124,32
s_cmp_eq_u32 s65, 0x28000
s_cselect_b32 s65, 0x10000, s65
s_cbranch_vccnz .Lk2_ev10
.Lk2_reg11:
s_waitcnt lgkmcnt(3)
s_add_u32 m0, s99, 16384
v_mfma_f32_16x16x32_bf16 v[44:47], a[64:67], a[0:3], v[20:23]
rqa 108,1,44032
mvaa 44,68,8
ex 60
mvaa 44,72,16
rqa 112,1,45056
mvaa 44,76,24
ex 61
mvaa 44,80,32
rqa 116,1,46080
cv 28,60,61
mvaa 44,84,40
ex 62
mvaa 44,88,48
rqa 120,1,47104
mvaa 44,92,56
ex 63
buffer_load_dwordx4 v0, s[20:23], s42 offen offset:0 sc1 lds
v_mfma_f32_16x16x32_bf16 v[48:51], a[64:67], a[4:7], v[24:27]
rqa 124,1,48128
cv 29,62,63
mvaa 48,68,12
ex 68
mvaa 48,72,20
rqa 64,2,0
mvaa 48,76,28
ex 69
mvaa 48,80,36
rqa 68,2,1024
cv 30,68,69
mvaa 48,84,44
ex 70
mvaa 48,88,52
rqa 72,2,2048
mvaa 48,92,60
ex 71
buffer_load_dwordx4 v0, s[20:23], s42 offen offset:1024 sc1 lds
s_waitcnt lgkmcnt(3)
v_mfma_f32_16x16x32_bf16 v[52:55], a[96:99], a[0:3], v[20:23]
rqa 76,2,3072
cv 31,70,71
mvaa 52,100,8
ex 64
mvaa 52,104,16
rqa 80,2,4096
mvaa 52,108,24
ex 65
mvaa 52,112,32
rqa 84,2,5120
cv 32,64,65
mvaa 52,116,40
ex 66
mvaa 52,120,48
rqa 88,2,6144
mvaa 52,124,56
ex 67
ex 75
buffer_load_dwordx4 v0, s[20:23], s42 offen offset:2048 sc1 lds
v_mfma_f32_16x16x32_bf16 v[56:59], a[96:99], a[4:7], v[24:27]
rqa 92,2,7168
cv 33,66,67
mvaa 56,100,12
ex 72
mvaa 56,104,20
rqa 96,2,8192
mvaa 56,108,28
ex 73
mvaa 56,112,36
rqa 100,2,9216
cv 34,72,73
mvaa 56,116,44
ex 74
mvaa 56,120,52
rqa 104,2,10240
cv 35,74,75
mvaa 56,124,60
buffer_load_dwordx4 v0, s[20:23], s42 offen offset:3072 sc1 lds
s_waitcnt lgkmcnt(7)
mvav 80,64,28
rqa 108,2,11264
mvav 88,64,32
s_waitcnt vmcnt(16)
s_barrier
mvav 84,68,28
rqa 112,2,12288
mvav 92,68,32
s_add_u32 m0, s40, s65
mvav 96,72,28
rqa 116,2,13312
mvvv 36,16,28
mvav 104,72,32
buffer_load_dwordx4 v0, s[24:27], s43 offen offset:0 sc1 lds
mvav 100,76,28
rqa 120,2,14336
m3 8,44,45,46
mvav 108,76,32
s_add_u32 s42, s42, 0x8000
s_waitcnt lgkmcnt(7)
mvav 112,80,28
rqa 124,2,15360
m3 9,47,48,49
mvav 120,80,32
s_cmp_eq_u32 s42, s44
s_cselect_b32 s42, s46, s42
s_cselect_b32 s22, s48, s22
s_cselect_b32 s44, -1, s44
mvav 116,84,28
rqa 64,1,49152
m3 8,8,50,51
mvav 124,84,32
buffer_load_dwordx4 v0, s[24:27], s43 offen offset:1024 sc1 lds
mvav 128,88,28
rqa 68,1,50176
m3 9,9,52,53
mvav 136,88,32
s_add_u32 s64, s64, 0x4000
mvav 132,92,28
rqa 72,1,51200
m3 8,8,54,55
mvav 140,92,32
s_cmp_eq_u32 s64, 0x18000
s_cselect_b32 s64, 0, s64
s_waitcnt lgkmcnt(7)
mvav 144,96,28
rqa 76,1,52224
mvav 152,96,32
v_add_u32 v2, s64, v14
mvav 148,100,28
rqa 80,1,53248
m3 9,9,56,57
mvav 156,100,32
buffer_load_dwordx4 v0, s[24:27], s43 offen offset:2048 sc1 lds
mvav 160,104,28
rqa 84,1,54272
m3 8,8,58,59
mvav 168,104,32
s_add_u32 s66, s66, 1
mvav 164,108,28
rqa 88,1,55296
v_max_f32 v8, v8, v9
mvav 172,108,32
s_waitcnt lgkmcnt(7)
mvav 176,112,28
rqa 92,1,56320
v_cmp_lt_f32 vcc, s71, v8
mvav 184,112,32
buffer_load_dwordx4 v0, s[24:27], s43 offen offset:3072 sc1 lds
mvav 180,116,28
rqa 96,1,57344
mvav 188,116,32
s_add_u32 s43, s43, 0x8000
mvav 192,120,28
rqa 100,1,58368
s_cmp_eq_u32 s66, s97
s_cselect_b64 vcc, -1, vcc
mvav 200,120,32
s_add_u32 s65, s65, 0x4000
mvav 196,124,28
rqa 104,1,59392
s_cmp_eq_u32 s43, s45
s_cselect_b32 s43, s47, s43
s_cselect_b32 s26, s48, s26
s_cselect_b32 s45, -1, s45
mvvv 40,16,32
mvav 204,124,32
s_cmp_eq_u32 s65, 0x28000
s_cselect_b32 s65, 0x10000, s65
s_cbranch_vccnz .Lk2_ev11
.Lk2_reg12:
s_waitcnt lgkmcnt(3)
s_add_u32 m0, s99, 32768
v_mfma_f32_16x16x32_bf16 v[60:63], a[64:67], a[0:3], v[20:23]
rqa 108,1,60416
mvaa 60,68,8
ex 44
mvaa 60,72,16
rqa 112,1,61440
mvaa 60,76,24
ex 45
mvaa 60,80,32
rqa 116,1,62464
cv 28,44,45
mvaa 60,84,40
ex 46
mvaa 60,88,48
rqa 120,1,63488
mvaa 60,92,56
ex 47
buffer_load_dwordx4 v0, s[20:23], s42 offen offset:0 sc1 lds
v_mfma_f32_16x16x32_bf16 v[64:67], a[64:67], a[4:7], v[24:27]
rqa 124,1,64512
cv 29,46,47
mvaa 64,68,12
ex 52
mvaa 64,72,20
rqa 64,2,0
mvaa 64,76,28
ex 53
mvaa 64,80,36
rqa 68,2,1024
cv 30,52,53
mvaa 64,84,44
ex 54
mvaa 64,88,52
rqa 72,2,2048
mvaa 64,92,60
ex 55
buffer_load_dwordx4 v0, s[20:23], s42 offen offset:1024 sc1 lds
s_waitcnt lgkmcnt(3)
v_mfma_f32_16x16x32_bf16 v[68:71], a[96:99], a[0:3], v[20:23]
rqa 76,2,3072
cv 31,54,55
mvaa 68,100,8
ex 48
mvaa 68,104,16
rqa 80,2,4096
mvaa 68,108,24
ex 49
mvaa 68,112,32
rqa 84,2,5120
cv 32,48,49
mvaa 68,116,40
ex 50
mvaa 68,120,48
rqa 88,2,6144
mvaa 68,124,56
ex 51
ex 59
buffer_load_dwordx4 v0, s[20:23], s42 offen offset:2048 sc1 lds
v_mfma_f32_16x16x32_bf16 v[72:75], a[96:99], a[4:7], v[24:27]
rqa 92,2,7168
cv 33,50,51
mvaa 72,100,12
ex 56
mvaa 72,104,20
rqa 96,2,8192
mvaa 72,108,28
ex 57
mvaa 72,112,36
rqa 100,2,9216
cv 34,56,57
mvaa 72,116,44
ex 58
mvaa 72,120,52
rqa 104,2,10240
cv 35,58,59
mvaa 72,124,60
buffer_load_dwordx4 v0, s[20:23], s42 offen offset:3072 sc1 lds
s_waitcnt lgkmcnt(7)
mvav 80,64,28
rqa 108,2,11264
mvav 88,64,32
s_waitcnt vmcnt(16)
s_barrier
mvav 84,68,28
rqa 112,2,12288
mvav 92,68,32
s_add_u32 m0, s40, s65
mvav 96,72,28
rqa 116,2,13312
mvvv 36,16,28
mvav 104,72,32
buffer_load_dwordx4 v0, s[24:27], s43 offen offset:0 sc1 lds
mvav 100,76,28
rqa 120,2,14336
m3 8,60,61,62
mvav 108,76,32
s_add_u32 s42, s42, 0x8000
s_waitcnt lgkmcnt(7)
mvav 112,80,28
rqa 124,2,15360
m3 9,63,64,65
mvav 120,80,32
s_cmp_eq_u32 s42, s44
s_cselect_b32 s42, s46, s42
s_cselect_b32 s22, s48, s22
s_cselect_b32 s44, -1, s44
mvav 116,84,28
rqa 64,1,0
m3 8,8,66,67
mvav 124,84,32
buffer_load_dwordx4 v0, s[24:27], s43 offen offset:1024 sc1 lds
mvav 128,88,28
rqa 68,1,1024
m3 9,9,68,69
mvav 136,88,32
s_add_u32 s64, s64, 0x4000
mvav 132,92,28
rqa 72,1,2048
m3 8,8,70,71
mvav 140,92,32
s_cmp_eq_u32 s64, 0x18000
s_cselect_b32 s64, 0, s64
s_waitcnt lgkmcnt(7)
mvav 144,96,28
rqa 76,1,3072
mvav 152,96,32
v_add_u32 v2, s64, v14
mvav 148,100,28
rqa 80,1,4096
m3 9,9,72,73
mvav 156,100,32
buffer_load_dwordx4 v0, s[24:27], s43 offen offset:2048 sc1 lds
mvav 160,104,28
rqa 84,1,5120
m3 8,8,74,75
mvav 168,104,32
s_add_u32 s66, s66, 1
mvav 164,108,28
rqa 88,1,6144
v_max_f32 v8, v8, v9
mvav 172,108,32
s_waitcnt lgkmcnt(7)
mvav 176,112,28
rqa 92,1,7168
v_cmp_lt_f32 vcc, s71, v8
mvav 184,112,32
buffer_load_dwordx4 v0, s[24:27], s43 offen offset:3072 sc1 lds
mvav 180,116,28
rqa 96,1,8192
mvav 188,116,32
s_add_u32 s43, s43, 0x8000
mvav 192,120,28
rqa 100,1,9216
s_cmp_eq_u32 s66, s97
s_cselect_b64 vcc, -1, vcc
mvav 200,120,32
s_add_u32 s65, s65, 0x4000
mvav 196,124,28
rqa 104,1,10240
s_cmp_eq_u32 s43, s45
s_cselect_b32 s43, s47, s43
s_cselect_b32 s26, s48, s26
s_cselect_b32 s45, -1, s45
mvvv 40,16,32
mvav 204,124,32
s_cmp_eq_u32 s65, 0x28000
s_cselect_b32 s65, 0x10000, s65
s_cbranch_vccnz .Lk2_ev12
.Lk2_reg13:
s_waitcnt lgkmcnt(3)
s_add_u32 m0, s99, 49152
v_mfma_f32_16x16x32_bf16 v[44:47], a[64:67], a[0:3], v[20:23]
rqa 108,1,11264
mvaa 44,68,8
ex 60
mvaa 44,72,16
rqa 112,1,12288
mvaa 44,76,24
ex 61
mvaa 44,80,32
rqa 116,1,13312
cv 28,60,61
mvaa 44,84,40
ex 62
mvaa 44,88,48
rqa 120,1,14336
mvaa 44,92,56
ex 63
buffer_load_dwordx4 v0, s[20:23], s42 offen offset:0 sc1 lds
v_mfma_f32_16x16x32_bf16 v[48:51], a[64:67], a[4:7], v[24:27]
rqa 124,1,15360
cv 29,62,63
mvaa 48,68,12
ex 68
mvaa 48,72,20
rqa 64,2,0
mvaa 48,76,28
ex 69
mvaa 48,80,36
rqa 68,2,1024
cv 30,68,69
mvaa 48,84,44
ex 70
mvaa 48,88,52
rqa 72,2,2048
mvaa 48,92,60
ex 71
buffer_load_dwordx4 v0, s[20:23], s42 offen offset:1024 sc1 lds
s_waitcnt lgkmcnt(3)
v_mfma_f32_16x16x32_bf16 v[52:55], a[96:99], a[0:3], v[20:23]
rqa 76,2,3072
cv 31,70,71
mvaa 52,100,8
ex 64
mvaa 52,104,16
rqa 80,2,4096
mvaa 52,108,24
ex 65
mvaa 52,112,32
rqa 84,2,5120
cv 32,64,65
mvaa 52,116,40
ex 66
mvaa 52,120,48
rqa 88,2,6144
mvaa 52,124,56
ex 67
ex 75
buffer_load_dwordx4 v0, s[20:23], s42 offen offset:2048 sc1 lds
v_mfma_f32_16x16x32_bf16 v[56:59], a[96:99], a[4:7], v[24:27]
rqa 92,2,7168
cv 33,66,67
mvaa 56,100,12
ex 72
mvaa 56,104,20
rqa 96,2,8192
mvaa 56,108,28
ex 73
mvaa 56,112,36
rqa 100,2,9216
cv 34,72,73
mvaa 56,116,44
ex 74
mvaa 56,120,52
rqa 104,2,10240
cv 35,74,75
mvaa 56,124,60
buffer_load_dwordx4 v0, s[20:23], s42 offen offset:3072 sc1 lds
s_waitcnt lgkmcnt(7)
mvav 80,64,28
rqa 108,2,11264
mvav 88,64,32
s_waitcnt vmcnt(16)
s_barrier
mvav 84,68,28
rqa 112,2,12288
mvav 92,68,32
s_add_u32 m0, s40, s65
mvav 96,72,28
rqa 116,2,13312
mvvv 36,16,28
mvav 104,72,32
buffer_load_dwordx4 v0, s[24:27], s43 offen offset:0 sc1 lds
mvav 100,76,28
rqa 120,2,14336
m3 8,44,45,46
mvav 108,76,32
s_add_u32 s42, s42, 0x8000
s_waitcnt lgkmcnt(7)
mvav 112,80,28
rqa 124,2,15360
m3 9,47,48,49
mvav 120,80,32
s_cmp_eq_u32 s42, s44
s_cselect_b32 s42, s46, s42
s_cselect_b32 s22, s48, s22
s_cselect_b32 s44, -1, s44
mvav 116,84,28
rqa 64,1,16384
m3 8,8,50,51
mvav 124,84,32
buffer_load_dwordx4 v0, s[24:27], s43 offen offset:1024 sc1 lds
mvav 128,88,28
rqa 68,1,17408
m3 9,9,52,53
mvav 136,88,32
s_add_u32 s64, s64, 0x4000
mvav 132,92,28
rqa 72,1,18432
m3 8,8,54,55
mvav 140,92,32
s_cmp_eq_u32 s64, 0x18000
s_cselect_b32 s64, 0, s64
s_waitcnt lgkmcnt(7)
mvav 144,96,28
rqa 76,1,19456
mvav 152,96,32
v_add_u32 v2, s64, v14
mvav 148,100,28
rqa 80,1,20480
m3 9,9,56,57
mvav 156,100,32
)ASM" ::: "memory");
  asm volatile(R"ASM(
buffer_load_dwordx4 v0, s[24:27], s43 offen offset:2048 sc1 lds
mvav 160,104,28
rqa 84,1,21504
m3 8,8,58,59
mvav 168,104,32
s_add_u32 s66, s66, 1
mvav 164,108,28
rqa 88,1,22528
v_max_f32 v8, v8, v9
mvav 172,108,32
s_waitcnt lgkmcnt(7)
mvav 176,112,28
rqa 92,1,23552
v_cmp_lt_f32 vcc, s71, v8
mvav 184,112,32
buffer_load_dwordx4 v0, s[24:27], s43 offen offset:3072 sc1 lds
mvav 180,116,28
rqa 96,1,24576
mvav 188,116,32
s_add_u32 s43, s43, 0x8000
mvav 192,120,28
rqa 100,1,25600
s_cmp_eq_u32 s66, s97
s_cselect_b64 vcc, -1, vcc
mvav 200,120,32
s_add_u32 s65, s65, 0x4000
mvav 196,124,28
rqa 104,1,26624
s_cmp_eq_u32 s43, s45
s_cselect_b32 s43, s47, s43
s_cselect_b32 s26, s48, s26
s_cselect_b32 s45, -1, s45
mvvv 40,16,32
mvav 204,124,32
s_cmp_eq_u32 s65, 0x28000
s_cselect_b32 s65, 0x10000, s65
s_cbranch_vccnz .Lk2_ev13
s_branch .Lk2_reg10
.Lk2_spc12:
s_waitcnt lgkmcnt(3)
s_add_u32 m0, s99, 32768
v_mfma_f32_16x16x32_bf16 v[60:63], a[64:67], a[0:3], v[20:23]
rqa 108,1,60416
mvaa 60,68,8
ex 44
mvaa 60,72,16
rqa 112,1,61440
mvaa 60,76,24
ex 45
mvaa 60,80,32
rqa 116,1,62464
cv 28,44,45
mvaa 60,84,40
ex 46
mvaa 60,88,48
rqa 120,1,63488
mvaa 60,92,56
ex 47
buffer_load_dwordx4 v0, s[20:23], s42 offen offset:0 sc1 lds
v_mfma_f32_16x16x32_bf16 v[64:67], a[64:67], a[4:7], v[24:27]
rqa 124,1,64512
cv 29,46,47
mvaa 64,68,12
ex 52
mvaa 64,72,20
rqa 64,2,0
mvaa 64,76,28
ex 53
mvaa 64,80,36
rqa 68,2,1024
cv 30,52,53
mvaa 64,84,44
ex 54
mvaa 64,88,52
rqa 72,2,2048
mvaa 64,92,60
ex 55
buffer_load_dwordx4 v0, s[20:23], s42 offen offset:1024 sc1 lds
s_waitcnt lgkmcnt(3)
v_mfma_f32_16x16x32_bf16 v[68:71], a[96:99], a[0:3], v[20:23]
rqa 76,2,3072
cv 31,54,55
mvaa 68,100,8
ex 48
mvaa 68,104,16
rqa 80,2,4096
mvaa 68,108,24
ex 49
mvaa 68,112,32
rqa 84,2,5120
cv 32,48,49
mvaa 68,116,40
ex 50
mvaa 68,120,48
rqa 88,2,6144
mvaa 68,124,56
ex 51
ex 59
buffer_load_dwordx4 v0, s[20:23], s42 offen offset:2048 sc1 lds
v_mfma_f32_16x16x32_bf16 v[72:75], a[96:99], a[4:7], v[24:27]
rqa 92,2,7168
cv 33,50,51
mvaa 72,100,12
ex 56
mvaa 72,104,20
rqa 96,2,8192
mvaa 72,108,28
ex 57
mvaa 72,112,36
rqa 100,2,9216
cv 34,56,57
mvaa 72,116,44
ex 58
mvaa 72,120,52
rqa 104,2,10240
cv 35,58,59
mvaa 72,124,60
buffer_load_dwordx4 v0, s[20:23], s42 offen offset:3072 sc1 lds
s_waitcnt lgkmcnt(7)
mvav 80,64,28
rqa 108,2,11264
mvav 88,64,32
s_mov_b32 s63, -1
s_mov_b32 s83, 0
s_waitcnt vmcnt(16)
s_barrier
s_getpc_b64 s[68:69]
.Lf_w13:
s_add_u32 s68, s68, .Lk2_seam10-.Lf_w13
s_addc_u32 s69, s69, (.Lk2_seam10-.Lf_w13)>>32
global_load_dword v254, v12, s[68:69]
s_getpc_b64 s[68:69]
.Lf_w14:
s_add_u32 s68, s68, .Lk2_seam10+4096-.Lf_w14
s_addc_u32 s69, s69, (.Lk2_seam10+4096-.Lf_w14)>>32
global_load_dword v254, v12, s[68:69]
s_getpc_b64 s[68:69]
.Lf_w15:
s_add_u32 s68, s68, .Lk2_publish0-.Lf_w15
s_addc_u32 s69, s69, (.Lk2_publish0-.Lf_w15)>>32
global_load_dword v254, v12, s[68:69]
s_cmp_lg_u32 s51, 3
s_cbranch_scc1 .Lk2_spc12_ret
v_mov_b32 v10, s82
v_mov_b32 v9, 0
s_mov_b64 exec, 1
global_store_dword v9, v10, s[0:1] sc1
s_mov_b64 exec, -1
.Lk2_spc12_ret:
s_min_u32 s97, s63, s49
mvav 84,68,28
rqa 112,2,12288
mvav 92,68,32
s_add_u32 m0, s40, s65
mvav 96,72,28
rqa 116,2,13312
mvvv 36,16,28
mvav 104,72,32
buffer_load_dwordx4 v0, s[24:27], s43 offen offset:0 sc1 lds
mvav 100,76,28
rqa 120,2,14336
m3 8,60,61,62
mvav 108,76,32
s_add_u32 s42, s42, 0x8000
s_waitcnt lgkmcnt(7)
mvav 112,80,28
rqa 124,2,15360
m3 9,63,64,65
mvav 120,80,32
s_cmp_eq_u32 s42, s44
s_cselect_b32 s42, s46, s42
s_cselect_b32 s22, s48, s22
s_cselect_b32 s44, -1, s44
mvav 116,84,28
rqa 64,1,0
m3 8,8,66,67
mvav 124,84,32
buffer_load_dwordx4 v0, s[24:27], s43 offen offset:1024 sc1 lds
mvav 128,88,28
rqa 68,1,1024
m3 9,9,68,69
mvav 136,88,32
s_add_u32 s64, s64, 0x4000
mvav 132,92,28
rqa 72,1,2048
m3 8,8,70,71
mvav 140,92,32
s_cmp_eq_u32 s64, 0x18000
s_cselect_b32 s64, 0, s64
s_waitcnt lgkmcnt(7)
mvav 144,96,28
rqa 76,1,3072
mvav 152,96,32
v_add_u32 v2, s64, v14
mvav 148,100,28
rqa 80,1,4096
m3 9,9,72,73
mvav 156,100,32
buffer_load_dwordx4 v0, s[24:27], s43 offen offset:2048 sc1 lds
mvav 160,104,28
rqa 84,1,5120
m3 8,8,74,75
mvav 168,104,32
s_add_u32 s66, s66, 1
mvav 164,108,28
rqa 88,1,6144
v_max_f32 v8, v8, v9
mvav 172,108,32
s_waitcnt lgkmcnt(7)
mvav 176,112,28
rqa 92,1,7168
v_cmp_lt_f32 vcc, s71, v8
mvav 184,112,32
buffer_load_dwordx4 v0, s[24:27], s43 offen offset:3072 sc1 lds
mvav 180,116,28
rqa 96,1,8192
mvav 188,116,32
s_add_u32 s43, s43, 0x8000
mvav 192,120,28
rqa 100,1,9216
s_cmp_eq_u32 s66, s97
s_cselect_b64 vcc, -1, vcc
mvav 200,120,32
s_add_u32 s65, s65, 0x4000
mvav 196,124,28
rqa 104,1,10240
s_cmp_eq_u32 s43, s45
s_cselect_b32 s43, s47, s43
s_cselect_b32 s26, s48, s26
s_cselect_b32 s45, -1, s45
mvvv 40,16,32
mvav 204,124,32
s_cmp_eq_u32 s65, 0x28000
s_cselect_b32 s65, 0x10000, s65
s_cbranch_vccnz .Lk2_ev12
s_branch .Lk2_reg13
.Lk2_seam10:
s_waitcnt lgkmcnt(3)
s_add_u32 m0, s99, 0
zvaa 60,64,128
rqa 108,1,27648
mvaa 60,68,136
ex 44
mvaa 60,72,144
rqa 112,1,28672
mvaa 60,76,152
ex 45
mvaa 60,80,160
rqa 116,1,29696
cv 28,44,45
mvaa 60,84,168
ex 46
mvaa 60,88,176
rqa 120,1,30720
mvaa 60,92,184
ex 47
buffer_load_dwordx4 v0, s[20:23], s42 offen offset:0 sc1 lds
zvaa 64,64,132
rqa 124,1,31744
cv 29,46,47
mvaa 64,68,140
ex 52
mvaa 64,72,148
rqa 64,2,0
mvaa 64,76,156
ex 53
mvaa 64,80,164
rqa 68,2,1024
cv 30,52,53
mvaa 64,84,172
ex 54
mvaa 64,88,180
rqa 72,2,2048
mvaa 64,92,188
ex 55
buffer_load_dwordx4 v0, s[20:23], s42 offen offset:1024 sc1 lds
s_waitcnt lgkmcnt(3)
zvaa 68,96,128
rqa 76,2,3072
cv 31,54,55
mvaa 68,100,136
ex 48
mvaa 68,104,144
rqa 80,2,4096
mvaa 68,108,152
ex 49
mvaa 68,112,160
rqa 84,2,5120
cv 32,48,49
mvaa 68,116,168
ex 50
mvaa 68,120,176
rqa 88,2,6144
mvaa 68,124,184
ex 51
ex 59
buffer_load_dwordx4 v0, s[20:23], s42 offen offset:2048 sc1 lds
zvaa 72,96,132
rqa 92,2,7168
cv 33,50,51
mvaa 72,100,140
ex 56
mvaa 72,104,148
rqa 96,2,8192
mvaa 72,108,156
ex 57
mvaa 72,112,164
rqa 100,2,9216
cv 34,56,57
mvaa 72,116,172
ex 58
mvaa 72,120,180
rqa 104,2,10240
cv 35,58,59
mvaa 72,124,188
buffer_load_dwordx4 v0, s[20:23], s42 offen offset:3072 sc1 lds
s_waitcnt lgkmcnt(7)
mvav 80,64,28
rqa 108,2,11264
mvav 88,64,32
s_waitcnt vmcnt(16)
s_barrier
mvav 84,68,28
rqa 112,2,12288
mvav 92,68,32
s_add_u32 m0, s40, s65
mvav 96,72,28
rqa 116,2,13312
mvvv 36,16,28
mvav 104,72,32
buffer_load_dwordx4 v0, s[24:27], s43 offen offset:0 sc1 lds
mvav 100,76,28
rqa 120,2,14336
mvav 108,76,32
s_add_u32 s42, s42, 0x8000
s_waitcnt lgkmcnt(7)
mvav 112,80,28
rqa 124,2,15360
mvav 120,80,32
s_cmp_eq_u32 s42, s44
s_cselect_b32 s42, s46, s42
s_cselect_b32 s22, s48, s22
s_cselect_b32 s44, -1, s44
mvav 116,84,28
rqa 64,1,32768
mvav 124,84,32
buffer_load_dwordx4 v0, s[24:27], s43 offen offset:1024 sc1 lds
mvav 128,88,28
rqa 68,1,33792
mvav 136,88,32
s_add_u32 s64, s64, 0x4000
mvav 132,92,28
rqa 72,1,34816
mvav 140,92,32
s_cmp_eq_u32 s64, 0x18000
s_cselect_b32 s64, 0, s64
s_waitcnt lgkmcnt(7)
mvav 144,96,28
rqa 76,1,35840
mvav 152,96,32
v_add_u32 v2, s64, v14
mvav 148,100,28
rqa 80,1,36864
mvav 156,100,32
buffer_load_dwordx4 v0, s[24:27], s43 offen offset:2048 sc1 lds
mvav 160,104,28
rqa 84,1,37888
mvav 168,104,32
mvav 164,108,28
rqa 88,1,38912
mvav 172,108,32
s_waitcnt lgkmcnt(7)
mvav 176,112,28
rqa 92,1,39936
mvav 184,112,32
buffer_load_dwordx4 v0, s[24:27], s43 offen offset:3072 sc1 lds
mvav 180,116,28
rqa 96,1,40960
mvav 188,116,32
s_add_u32 s43, s43, 0x8000
mvav 192,120,28
rqa 100,1,41984
mvav 200,120,32
s_add_u32 s65, s65, 0x4000
mvav 196,124,28
rqa 104,1,43008
s_cmp_eq_u32 s43, s45
s_cselect_b32 s43, s47, s43
s_cselect_b32 s26, s48, s26
s_cselect_b32 s45, -1, s45
mvvv 40,16,32
mvav 204,124,32
s_cmp_eq_u32 s65, 0x28000
s_cselect_b32 s65, 0x10000, s65
s_mov_b32 s67, 0
s_branch .Lk2_publish0
.Lk2_seam11:
s_waitcnt lgkmcnt(3)
s_add_u32 m0, s99, 16384
zvaa 44,64,128
rqa 108,1,44032
mvaa 44,68,136
ex 60
mvaa 44,72,144
rqa 112,1,45056
mvaa 44,76,152
ex 61
mvaa 44,80,160
rqa 116,1,46080
cv 28,60,61
mvaa 44,84,168
ex 62
mvaa 44,88,176
rqa 120,1,47104
mvaa 44,92,184
ex 63
buffer_load_dwordx4 v0, s[20:23], s42 offen offset:0 sc1 lds
zvaa 48,64,132
rqa 124,1,48128
cv 29,62,63
mvaa 48,68,140
ex 68
mvaa 48,72,148
rqa 64,2,0
mvaa 48,76,156
ex 69
mvaa 48,80,164
rqa 68,2,1024
cv 30,68,69
mvaa 48,84,172
ex 70
mvaa 48,88,180
rqa 72,2,2048
mvaa 48,92,188
ex 71
buffer_load_dwordx4 v0, s[20:23], s42 offen offset:1024 sc1 lds
s_waitcnt lgkmcnt(3)
zvaa 52,96,128
rqa 76,2,3072
cv 31,70,71
mvaa 52,100,136
ex 64
mvaa 52,104,144
rqa 80,2,4096
mvaa 52,108,152
ex 65
mvaa 52,112,160
rqa 84,2,5120
cv 32,64,65
mvaa 52,116,168
ex 66
mvaa 52,120,176
rqa 88,2,6144
mvaa 52,124,184
ex 67
ex 75
buffer_load_dwordx4 v0, s[20:23], s42 offen offset:2048 sc1 lds
zvaa 56,96,132
rqa 92,2,7168
cv 33,66,67
mvaa 56,100,140
ex 72
mvaa 56,104,148
rqa 96,2,8192
mvaa 56,108,156
ex 73
mvaa 56,112,164
rqa 100,2,9216
cv 34,72,73
mvaa 56,116,172
ex 74
mvaa 56,120,180
rqa 104,2,10240
cv 35,74,75
mvaa 56,124,188
buffer_load_dwordx4 v0, s[20:23], s42 offen offset:3072 sc1 lds
s_waitcnt lgkmcnt(7)
mvav 80,64,28
rqa 108,2,11264
mvav 88,64,32
s_waitcnt vmcnt(16)
s_barrier
mvav 84,68,28
rqa 112,2,12288
mvav 92,68,32
s_add_u32 m0, s40, s65
mvav 96,72,28
rqa 116,2,13312
mvvv 36,16,28
mvav 104,72,32
buffer_load_dwordx4 v0, s[24:27], s43 offen offset:0 sc1 lds
mvav 100,76,28
rqa 120,2,14336
mvav 108,76,32
s_add_u32 s42, s42, 0x8000
s_waitcnt lgkmcnt(7)
mvav 112,80,28
rqa 124,2,15360
mvav 120,80,32
s_cmp_eq_u32 s42, s44
s_cselect_b32 s42, s46, s42
s_cselect_b32 s22, s48, s22
s_cselect_b32 s44, -1, s44
mvav 116,84,28
rqa 64,1,49152
mvav 124,84,32
buffer_load_dwordx4 v0, s[24:27], s43 offen offset:1024 sc1 lds
mvav 128,88,28
rqa 68,1,50176
mvav 136,88,32
s_add_u32 s64, s64, 0x4000
mvav 132,92,28
rqa 72,1,51200
mvav 140,92,32
s_cmp_eq_u32 s64, 0x18000
s_cselect_b32 s64, 0, s64
s_waitcnt lgkmcnt(7)
mvav 144,96,28
rqa 76,1,52224
mvav 152,96,32
v_add_u32 v2, s64, v14
mvav 148,100,28
rqa 80,1,53248
mvav 156,100,32
buffer_load_dwordx4 v0, s[24:27], s43 offen offset:2048 sc1 lds
mvav 160,104,28
rqa 84,1,54272
mvav 168,104,32
mvav 164,108,28
rqa 88,1,55296
mvav 172,108,32
s_waitcnt lgkmcnt(7)
mvav 176,112,28
rqa 92,1,56320
mvav 184,112,32
buffer_load_dwordx4 v0, s[24:27], s43 offen offset:3072 sc1 lds
mvav 180,116,28
rqa 96,1,57344
mvav 188,116,32
s_add_u32 s43, s43, 0x8000
mvav 192,120,28
rqa 100,1,58368
mvav 200,120,32
s_add_u32 s65, s65, 0x4000
mvav 196,124,28
rqa 104,1,59392
s_cmp_eq_u32 s43, s45
s_cselect_b32 s43, s47, s43
s_cselect_b32 s26, s48, s26
s_cselect_b32 s45, -1, s45
mvvv 40,16,32
mvav 204,124,32
s_cmp_eq_u32 s65, 0x28000
s_cselect_b32 s65, 0x10000, s65
s_mov_b32 s67, 1
s_branch .Lk2_publish1
.Lk2_seam12:
s_waitcnt lgkmcnt(3)
s_add_u32 m0, s99, 32768
zvaa 60,64,128
rqa 108,1,60416
mvaa 60,68,136
ex 44
mvaa 60,72,144
rqa 112,1,61440
mvaa 60,76,152
ex 45
mvaa 60,80,160
rqa 116,1,62464
cv 28,44,45
mvaa 60,84,168
ex 46
mvaa 60,88,176
rqa 120,1,63488
mvaa 60,92,184
ex 47
buffer_load_dwordx4 v0, s[20:23], s42 offen offset:0 sc1 lds
zvaa 64,64,132
rqa 124,1,64512
cv 29,46,47
mvaa 64,68,140
ex 52
mvaa 64,72,148
rqa 64,2,0
mvaa 64,76,156
ex 53
mvaa 64,80,164
rqa 68,2,1024
cv 30,52,53
mvaa 64,84,172
ex 54
mvaa 64,88,180
rqa 72,2,2048
mvaa 64,92,188
ex 55
buffer_load_dwordx4 v0, s[20:23], s42 offen offset:1024 sc1 lds
s_waitcnt lgkmcnt(3)
zvaa 68,96,128
rqa 76,2,3072
cv 31,54,55
)ASM" ::: "memory");
  asm volatile(R"ASM(
mvaa 68,100,136
ex 48
mvaa 68,104,144
rqa 80,2,4096
mvaa 68,108,152
ex 49
mvaa 68,112,160
rqa 84,2,5120
cv 32,48,49
mvaa 68,116,168
ex 50
mvaa 68,120,176
rqa 88,2,6144
mvaa 68,124,184
ex 51
ex 59
buffer_load_dwordx4 v0, s[20:23], s42 offen offset:2048 sc1 lds
zvaa 72,96,132
rqa 92,2,7168
cv 33,50,51
mvaa 72,100,140
ex 56
mvaa 72,104,148
rqa 96,2,8192
mvaa 72,108,156
ex 57
mvaa 72,112,164
rqa 100,2,9216
cv 34,56,57
mvaa 72,116,172
ex 58
mvaa 72,120,180
rqa 104,2,10240
cv 35,58,59
mvaa 72,124,188
buffer_load_dwordx4 v0, s[20:23], s42 offen offset:3072 sc1 lds
s_waitcnt lgkmcnt(7)
mvav 80,64,28
rqa 108,2,11264
mvav 88,64,32
s_waitcnt vmcnt(16)
s_barrier
mvav 84,68,28
rqa 112,2,12288
mvav 92,68,32
s_add_u32 m0, s40, s65
mvav 96,72,28
rqa 116,2,13312
mvvv 36,16,28
mvav 104,72,32
buffer_load_dwordx4 v0, s[24:27], s43 offen offset:0 sc1 lds
mvav 100,76,28
rqa 120,2,14336
mvav 108,76,32
s_add_u32 s42, s42, 0x8000
s_waitcnt lgkmcnt(7)
mvav 112,80,28
rqa 124,2,15360
mvav 120,80,32
s_cmp_eq_u32 s42, s44
s_cselect_b32 s42, s46, s42
s_cselect_b32 s22, s48, s22
s_cselect_b32 s44, -1, s44
mvav 116,84,28
rqa 64,1,0
mvav 124,84,32
buffer_load_dwordx4 v0, s[24:27], s43 offen offset:1024 sc1 lds
mvav 128,88,28
rqa 68,1,1024
mvav 136,88,32
s_add_u32 s64, s64, 0x4000
mvav 132,92,28
rqa 72,1,2048
mvav 140,92,32
s_cmp_eq_u32 s64, 0x18000
s_cselect_b32 s64, 0, s64
s_waitcnt lgkmcnt(7)
mvav 144,96,28
rqa 76,1,3072
mvav 152,96,32
v_add_u32 v2, s64, v14
mvav 148,100,28
rqa 80,1,4096
mvav 156,100,32
buffer_load_dwordx4 v0, s[24:27], s43 offen offset:2048 sc1 lds
mvav 160,104,28
rqa 84,1,5120
mvav 168,104,32
mvav 164,108,28
rqa 88,1,6144
mvav 172,108,32
s_waitcnt lgkmcnt(7)
mvav 176,112,28
rqa 92,1,7168
mvav 184,112,32
buffer_load_dwordx4 v0, s[24:27], s43 offen offset:3072 sc1 lds
mvav 180,116,28
rqa 96,1,8192
mvav 188,116,32
s_add_u32 s43, s43, 0x8000
mvav 192,120,28
rqa 100,1,9216
mvav 200,120,32
s_add_u32 s65, s65, 0x4000
mvav 196,124,28
rqa 104,1,10240
s_cmp_eq_u32 s43, s45
s_cselect_b32 s43, s47, s43
s_cselect_b32 s26, s48, s26
s_cselect_b32 s45, -1, s45
mvvv 40,16,32
mvav 204,124,32
s_cmp_eq_u32 s65, 0x28000
s_cselect_b32 s65, 0x10000, s65
s_mov_b32 s67, 2
s_branch .Lk2_publish0
.Lk2_seam13:
s_waitcnt lgkmcnt(3)
s_add_u32 m0, s99, 49152
zvaa 44,64,128
rqa 108,1,11264
mvaa 44,68,136
ex 60
mvaa 44,72,144
rqa 112,1,12288
mvaa 44,76,152
ex 61
mvaa 44,80,160
rqa 116,1,13312
cv 28,60,61
mvaa 44,84,168
ex 62
mvaa 44,88,176
rqa 120,1,14336
mvaa 44,92,184
ex 63
buffer_load_dwordx4 v0, s[20:23], s42 offen offset:0 sc1 lds
zvaa 48,64,132
rqa 124,1,15360
cv 29,62,63
mvaa 48,68,140
ex 68
mvaa 48,72,148
rqa 64,2,0
mvaa 48,76,156
ex 69
mvaa 48,80,164
rqa 68,2,1024
cv 30,68,69
mvaa 48,84,172
ex 70
mvaa 48,88,180
rqa 72,2,2048
mvaa 48,92,188
ex 71
buffer_load_dwordx4 v0, s[20:23], s42 offen offset:1024 sc1 lds
s_waitcnt lgkmcnt(3)
zvaa 52,96,128
rqa 76,2,3072
cv 31,70,71
mvaa 52,100,136
ex 64
mvaa 52,104,144
rqa 80,2,4096
mvaa 52,108,152
ex 65
mvaa 52,112,160
rqa 84,2,5120
cv 32,64,65
mvaa 52,116,168
ex 66
mvaa 52,120,176
rqa 88,2,6144
mvaa 52,124,184
ex 67
ex 75
buffer_load_dwordx4 v0, s[20:23], s42 offen offset:2048 sc1 lds
zvaa 56,96,132
rqa 92,2,7168
cv 33,66,67
mvaa 56,100,140
ex 72
mvaa 56,104,148
rqa 96,2,8192
mvaa 56,108,156
ex 73
mvaa 56,112,164
rqa 100,2,9216
cv 34,72,73
mvaa 56,116,172
ex 74
mvaa 56,120,180
rqa 104,2,10240
cv 35,74,75
mvaa 56,124,188
buffer_load_dwordx4 v0, s[20:23], s42 offen offset:3072 sc1 lds
s_waitcnt lgkmcnt(7)
mvav 80,64,28
rqa 108,2,11264
mvav 88,64,32
s_waitcnt vmcnt(16)
s_barrier
mvav 84,68,28
rqa 112,2,12288
mvav 92,68,32
s_add_u32 m0, s40, s65
mvav 96,72,28
rqa 116,2,13312
mvvv 36,16,28
mvav 104,72,32
buffer_load_dwordx4 v0, s[24:27], s43 offen offset:0 sc1 lds
mvav 100,76,28
rqa 120,2,14336
mvav 108,76,32
s_add_u32 s42, s42, 0x8000
s_waitcnt lgkmcnt(7)
mvav 112,80,28
rqa 124,2,15360
mvav 120,80,32
s_cmp_eq_u32 s42, s44
s_cselect_b32 s42, s46, s42
s_cselect_b32 s22, s48, s22
s_cselect_b32 s44, -1, s44
mvav 116,84,28
rqa 64,1,16384
mvav 124,84,32
buffer_load_dwordx4 v0, s[24:27], s43 offen offset:1024 sc1 lds
mvav 128,88,28
rqa 68,1,17408
mvav 136,88,32
s_add_u32 s64, s64, 0x4000
mvav 132,92,28
rqa 72,1,18432
mvav 140,92,32
s_cmp_eq_u32 s64, 0x18000
s_cselect_b32 s64, 0, s64
s_waitcnt lgkmcnt(7)
mvav 144,96,28
rqa 76,1,19456
mvav 152,96,32
v_add_u32 v2, s64, v14
mvav 148,100,28
rqa 80,1,20480
mvav 156,100,32
buffer_load_dwordx4 v0, s[24:27], s43 offen offset:2048 sc1 lds
mvav 160,104,28
rqa 84,1,21504
mvav 168,104,32
mvav 164,108,28
rqa 88,1,22528
mvav 172,108,32
s_waitcnt lgkmcnt(7)
mvav 176,112,28
rqa 92,1,23552
mvav 184,112,32
buffer_load_dwordx4 v0, s[24:27], s43 offen offset:3072 sc1 lds
mvav 180,116,28
rqa 96,1,24576
mvav 188,116,32
s_add_u32 s43, s43, 0x8000
mvav 192,120,28
rqa 100,1,25600
mvav 200,120,32
s_add_u32 s65, s65, 0x4000
mvav 196,124,28
rqa 104,1,26624
s_cmp_eq_u32 s43, s45
s_cselect_b32 s43, s47, s43
s_cselect_b32 s26, s48, s26
s_cselect_b32 s45, -1, s45
mvvv 40,16,32
mvav 204,124,32
s_cmp_eq_u32 s65, 0x28000
s_cselect_b32 s65, 0x10000, s65
s_mov_b32 s67, 3
s_branch .Lk2_publish1
.Lk2_publish0:
s_cmp_eq_u32 s83, 0
s_cbranch_scc1 .Lk2_pub0_f3
s_mov_b32 s83, 0
s_waitcnt vmcnt(0)
s_barrier
s_cmp_lg_u32 s51, 3
s_cbranch_scc1 .Lk2_pub0_f3
v_mov_b32 v10, s82
v_mov_b32 v9, 0
s_mov_b64 exec, 1
global_store_dword v9, v10, s[0:1] sc1
s_mov_b64 exec, -1
.Lk2_pub0_f3:
s_getpc_b64 s[68:69]
.Lf_w39:
s_add_u32 s68, s68, .Lk2_fin20-.Lf_w39
s_addc_u32 s69, s69, (.Lk2_fin20-.Lf_w39)>>32
global_load_dword v254, v12, s[68:69]
s_getpc_b64 s[68:69]
.Lf_w40:
s_add_u32 s68, s68, .Lk2_fin20+4096-.Lf_w40
s_addc_u32 s69, s69, (.Lk2_fin20+4096-.Lf_w40)>>32
global_load_dword v254, v12, s[68:69]
s_getpc_b64 s[68:69]
.Lf_w41:
s_add_u32 s68, s68, .Lk2_fin20+8192-.Lf_w41
s_addc_u32 s69, s69, (.Lk2_fin20+8192-.Lf_w41)>>32
global_load_dword v254, v12, s[68:69]
s_nop 3
v_rcp_f32 v8, v36
v_log_f32 v232, v36
v_rcp_f32 v10, v40
v_log_f32 v233, v40
s_nop 0
v_add_f32 v232, v232, v4
v_add_f32 v233, v233, v5
s_mov_b64 s[68:69], s[28:29]
v_pk_mul_f32 v[208:209], v[80:81], v[8:9] op_sel_hi:[1,0]
v_pk_mul_f32 v[210:211], v[82:83], v[8:9] op_sel_hi:[1,0]
v_pk_mul_f32 v[212:213], v[84:85], v[8:9] op_sel_hi:[1,0]
v_pk_mul_f32 v[214:215], v[86:87], v[8:9] op_sel_hi:[1,0]
cv 216,208,209
cv 217,210,211
cv 218,212,213
cv 219,214,215
global_store_dwordx4 v0, v[216:219], s[68:69] offset:0 sc1
v_pk_mul_f32 v[220:221], v[88:89], v[10:11] op_sel_hi:[1,0]
v_pk_mul_f32 v[222:223], v[90:91], v[10:11] op_sel_hi:[1,0]
v_pk_mul_f32 v[224:225], v[92:93], v[10:11] op_sel_hi:[1,0]
v_pk_mul_f32 v[226:227], v[94:95], v[10:11] op_sel_hi:[1,0]
cv 228,220,221
cv 229,222,223
cv 230,224,225
cv 231,226,227
global_store_dwordx4 v0, v[228:231], s[68:69] offset:1024 sc1
v_pk_mul_f32 v[208:209], v[96:97], v[8:9] op_sel_hi:[1,0]
v_pk_mul_f32 v[210:211], v[98:99], v[8:9] op_sel_hi:[1,0]
v_pk_mul_f32 v[212:213], v[100:101], v[8:9] op_sel_hi:[1,0]
v_pk_mul_f32 v[214:215], v[102:103], v[8:9] op_sel_hi:[1,0]
cv 216,208,209
cv 217,210,211
cv 218,212,213
cv 219,214,215
global_store_dwordx4 v0, v[216:219], s[68:69] offset:2048 sc1
v_pk_mul_f32 v[220:221], v[104:105], v[10:11] op_sel_hi:[1,0]
v_pk_mul_f32 v[222:223], v[106:107], v[10:11] op_sel_hi:[1,0]
v_pk_mul_f32 v[224:225], v[108:109], v[10:11] op_sel_hi:[1,0]
v_pk_mul_f32 v[226:227], v[110:111], v[10:11] op_sel_hi:[1,0]
cv 228,220,221
cv 229,222,223
cv 230,224,225
cv 231,226,227
global_store_dwordx4 v0, v[228:231], s[68:69] offset:3072 sc1
s_add_u32 s68, s68, 0x1000
s_addc_u32 s69, s69, 0
v_pk_mul_f32 v[208:209], v[112:113], v[8:9] op_sel_hi:[1,0]
v_pk_mul_f32 v[210:211], v[114:115], v[8:9] op_sel_hi:[1,0]
v_pk_mul_f32 v[212:213], v[116:117], v[8:9] op_sel_hi:[1,0]
v_pk_mul_f32 v[214:215], v[118:119], v[8:9] op_sel_hi:[1,0]
cv 216,208,209
cv 217,210,211
cv 218,212,213
cv 219,214,215
global_store_dwordx4 v0, v[216:219], s[68:69] offset:0 sc1
v_pk_mul_f32 v[220:221], v[120:121], v[10:11] op_sel_hi:[1,0]
v_pk_mul_f32 v[222:223], v[122:123], v[10:11] op_sel_hi:[1,0]
v_pk_mul_f32 v[224:225], v[124:125], v[10:11] op_sel_hi:[1,0]
v_pk_mul_f32 v[226:227], v[126:127], v[10:11] op_sel_hi:[1,0]
cv 228,220,221
cv 229,222,223
cv 230,224,225
cv 231,226,227
global_store_dwordx4 v0, v[228:231], s[68:69] offset:1024 sc1
v_pk_mul_f32 v[208:209], v[128:129], v[8:9] op_sel_hi:[1,0]
v_pk_mul_f32 v[210:211], v[130:131], v[8:9] op_sel_hi:[1,0]
v_pk_mul_f32 v[212:213], v[132:133], v[8:9] op_sel_hi:[1,0]
v_pk_mul_f32 v[214:215], v[134:135], v[8:9] op_sel_hi:[1,0]
cv 216,208,209
cv 217,210,211
cv 218,212,213
cv 219,214,215
global_store_dwordx4 v0, v[216:219], s[68:69] offset:2048 sc1
v_pk_mul_f32 v[220:221], v[136:137], v[10:11] op_sel_hi:[1,0]
v_pk_mul_f32 v[222:223], v[138:139], v[10:11] op_sel_hi:[1,0]
v_pk_mul_f32 v[224:225], v[140:141], v[10:11] op_sel_hi:[1,0]
v_pk_mul_f32 v[226:227], v[142:143], v[10:11] op_sel_hi:[1,0]
cv 228,220,221
cv 229,222,223
cv 230,224,225
cv 231,226,227
global_store_dwordx4 v0, v[228:231], s[68:69] offset:3072 sc1
s_add_u32 s68, s68, 0x1000
s_addc_u32 s69, s69, 0
v_pk_mul_f32 v[208:209], v[144:145], v[8:9] op_sel_hi:[1,0]
v_pk_mul_f32 v[210:211], v[146:147], v[8:9] op_sel_hi:[1,0]
v_pk_mul_f32 v[212:213], v[148:149], v[8:9] op_sel_hi:[1,0]
v_pk_mul_f32 v[214:215], v[150:151], v[8:9] op_sel_hi:[1,0]
cv 216,208,209
cv 217,210,211
cv 218,212,213
cv 219,214,215
global_store_dwordx4 v0, v[216:219], s[68:69] offset:0 sc1
v_pk_mul_f32 v[220:221], v[152:153], v[10:11] op_sel_hi:[1,0]
v_pk_mul_f32 v[222:223], v[154:155], v[10:11] op_sel_hi:[1,0]
v_pk_mul_f32 v[224:225], v[156:157], v[10:11] op_sel_hi:[1,0]
v_pk_mul_f32 v[226:227], v[158:159], v[10:11] op_sel_hi:[1,0]
cv 228,220,221
cv 229,222,223
cv 230,224,225
cv 231,226,227
global_store_dwordx4 v0, v[228:231], s[68:69] offset:1024 sc1
v_pk_mul_f32 v[208:209], v[160:161], v[8:9] op_sel_hi:[1,0]
v_pk_mul_f32 v[210:211], v[162:163], v[8:9] op_sel_hi:[1,0]
v_pk_mul_f32 v[212:213], v[164:165], v[8:9] op_sel_hi:[1,0]
v_pk_mul_f32 v[214:215], v[166:167], v[8:9] op_sel_hi:[1,0]
cv 216,208,209
cv 217,210,211
cv 218,212,213
cv 219,214,215
global_store_dwordx4 v0, v[216:219], s[68:69] offset:2048 sc1
v_pk_mul_f32 v[220:221], v[168:169], v[10:11] op_sel_hi:[1,0]
v_pk_mul_f32 v[222:223], v[170:171], v[10:11] op_sel_hi:[1,0]
v_pk_mul_f32 v[224:225], v[172:173], v[10:11] op_sel_hi:[1,0]
v_pk_mul_f32 v[226:227], v[174:175], v[10:11] op_sel_hi:[1,0]
cv 228,220,221
cv 229,222,223
cv 230,224,225
cv 231,226,227
global_store_dwordx4 v0, v[228:231], s[68:69] offset:3072 sc1
s_add_u32 s68, s68, 0x1000
s_addc_u32 s69, s69, 0
v_pk_mul_f32 v[208:209], v[176:177], v[8:9] op_sel_hi:[1,0]
v_pk_mul_f32 v[210:211], v[178:179], v[8:9] op_sel_hi:[1,0]
v_pk_mul_f32 v[212:213], v[180:181], v[8:9] op_sel_hi:[1,0]
v_pk_mul_f32 v[214:215], v[182:183], v[8:9] op_sel_hi:[1,0]
cv 216,208,209
cv 217,210,211
cv 218,212,213
cv 219,214,215
global_store_dwordx4 v0, v[216:219], s[68:69] offset:0 sc1
v_pk_mul_f32 v[220:221], v[184:185], v[10:11] op_sel_hi:[1,0]
v_pk_mul_f32 v[222:223], v[186:187], v[10:11] op_sel_hi:[1,0]
v_pk_mul_f32 v[224:225], v[188:189], v[10:11] op_sel_hi:[1,0]
v_pk_mul_f32 v[226:227], v[190:191], v[10:11] op_sel_hi:[1,0]
cv 228,220,221
cv 229,222,223
cv 230,224,225
cv 231,226,227
global_store_dwordx4 v0, v[228:231], s[68:69] offset:1024 sc1
v_pk_mul_f32 v[208:209], v[192:193], v[8:9] op_sel_hi:[1,0]
v_pk_mul_f32 v[210:211], v[194:195], v[8:9] op_sel_hi:[1,0]
v_pk_mul_f32 v[212:213], v[196:197], v[8:9] op_sel_hi:[1,0]
v_pk_mul_f32 v[214:215], v[198:199], v[8:9] op_sel_hi:[1,0]
cv 216,208,209
cv 217,210,211
cv 218,212,213
cv 219,214,215
global_store_dwordx4 v0, v[216:219], s[68:69] offset:2048 sc1
v_pk_mul_f32 v[220:221], v[200:201], v[10:11] op_sel_hi:[1,0]
v_pk_mul_f32 v[222:223], v[202:203], v[10:11] op_sel_hi:[1,0]
v_pk_mul_f32 v[224:225], v[204:205], v[10:11] op_sel_hi:[1,0]
v_pk_mul_f32 v[226:227], v[206:207], v[10:11] op_sel_hi:[1,0]
cv 228,220,221
cv 229,222,223
cv 230,224,225
cv 231,226,227
global_store_dwordx4 v0, v[228:231], s[68:69] offset:3072 sc1
s_add_u32 s68, s68, 0x1000
s_addc_u32 s69, s69, 0
v_lshrrev_b32 v9, 1, v0
global_store_dwordx2 v9, v[232:233], s[68:69] sc1
v_mov_b64 v[36:37], 0
v_mov_b64 v[38:39], 0
v_mov_b64 v[40:41], 0
v_mov_b64 v[42:43], 0
v_mov_b64 v[80:81], 0
v_mov_b64 v[82:83], 0
v_mov_b64 v[84:85], 0
v_mov_b64 v[86:87], 0
v_mov_b64 v[88:89], 0
v_mov_b64 v[90:91], 0
v_mov_b64 v[92:93], 0
v_mov_b64 v[94:95], 0
v_mov_b64 v[96:97], 0
v_mov_b64 v[98:99], 0
v_mov_b64 v[100:101], 0
v_mov_b64 v[102:103], 0
v_mov_b64 v[104:105], 0
v_mov_b64 v[106:107], 0
v_mov_b64 v[108:109], 0
v_mov_b64 v[110:111], 0
v_mov_b64 v[112:113], 0
v_mov_b64 v[114:115], 0
v_mov_b64 v[116:117], 0
v_mov_b64 v[118:119], 0
v_mov_b64 v[120:121], 0
v_mov_b64 v[122:123], 0
v_mov_b64 v[124:125], 0
v_mov_b64 v[126:127], 0
v_mov_b64 v[128:129], 0
v_mov_b64 v[130:131], 0
v_mov_b64 v[132:133], 0
v_mov_b64 v[134:135], 0
v_mov_b64 v[136:137], 0
v_mov_b64 v[138:139], 0
v_mov_b64 v[140:141], 0
v_mov_b64 v[142:143], 0
v_mov_b64 v[144:145], 0
v_mov_b64 v[146:147], 0
v_mov_b64 v[148:149], 0
v_mov_b64 v[150:151], 0
v_mov_b64 v[152:153], 0
v_mov_b64 v[154:155], 0
v_mov_b64 v[156:157], 0
v_mov_b64 v[158:159], 0
v_mov_b64 v[160:161], 0
v_mov_b64 v[162:163], 0
v_mov_b64 v[164:165], 0
v_mov_b64 v[166:167], 0
v_mov_b64 v[168:169], 0
v_mov_b64 v[170:171], 0
v_mov_b64 v[172:173], 0
v_mov_b64 v[174:175], 0
v_mov_b64 v[176:177], 0
v_mov_b64 v[178:179], 0
v_mov_b64 v[180:181], 0
v_mov_b64 v[182:183], 0
v_mov_b64 v[184:185], 0
v_mov_b64 v[186:187], 0
v_mov_b64 v[188:189], 0
v_mov_b64 v[190:191], 0
v_mov_b64 v[192:193], 0
v_mov_b64 v[194:195], 0
v_mov_b64 v[196:197], 0
v_mov_b64 v[198:199], 0
v_mov_b64 v[200:201], 0
v_mov_b64 v[202:203], 0
v_mov_b64 v[204:205], 0
v_mov_b64 v[206:207], 0
s_cmp_lg_u32 s52, -1
s_cbranch_scc1 .Lk2_pub0_nomask
v_cndmask_b32_e64 v60, v60, v15, s[72:73]
v_cndmask_b32_e64 v72, v72, v15, s[72:73]
v_mov_b32 v68, v15
v_cndmask_b32_e64 v61, v61, v15, s[74:75]
v_cndmask_b32_e64 v73, v73, v15, s[74:75]
v_mov_b32 v69, v15
v_cndmask_b32_e64 v62, v62, v15, s[76:77]
v_cndmask_b32_e64 v74, v74, v15, s[76:77]
v_mov_b32 v70, v15
v_cndmask_b32_e64 v63, v63, v15, s[78:79]
v_cndmask_b32_e64 v75, v75, v15, s[78:79]
v_mov_b32 v71, v15
.Lk2_pub0_nomask:
m3 208,60,61,62
m3 209,63,68,69
m3 208,208,70,71
v_max_f32 v208, v208, v209
v_mov_b32 v209, v208
s_nop 1
v_permlane32_swap_b32 v208, v209
s_nop 0
v_max_f32 v208, v208, v209
v_mov_b32 v209, v208
s_nop 1
v_permlane16_swap_b32 v208, v209
s_nop 0
v_max_f32 v208, v208, v209
v_mov_b32 v4, v208
v_sub_f32 v60, v60, v208
v_sub_f32 v61, v61, v208
v_sub_f32 v62, v62, v208
v_sub_f32 v63, v63, v208
v_sub_f32 v68, v68, v208
v_sub_f32 v69, v69, v208
v_sub_f32 v70, v70, v208
v_sub_f32 v71, v71, v208
v_sub_f32 v20, 0, v208
v_sub_f32 v21, 0, v208
v_sub_f32 v22, 0, v208
v_sub_f32 v23, 0, v208
m3 212,64,65,66
m3 213,67,72,73
m3 212,212,74,75
v_max_f32 v212, v212, v213
v_mov_b32 v213, v212
s_nop 1
v_permlane32_swap_b32 v212, v213
s_nop 0
v_max_f32 v212, v212, v213
v_mov_b32 v213, v212
s_nop 1
v_permlane16_swap_b32 v212, v213
s_nop 0
v_max_f32 v212, v212, v213
v_mov_b32 v5, v212
v_sub_f32 v64, v64, v212
v_sub_f32 v65, v65, v212
v_sub_f32 v66, v66, v212
v_sub_f32 v67, v67, v212
v_sub_f32 v72, v72, v212
v_sub_f32 v73, v73, v212
v_sub_f32 v74, v74, v212
)ASM" ::: "memory");
  asm volatile(R"ASM(
v_sub_f32 v75, v75, v212
v_sub_f32 v24, 0, v212
v_sub_f32 v25, 0, v212
v_sub_f32 v26, 0, v212
v_sub_f32 v27, 0, v212
s_mov_b32 s66, 0
s_mov_b32 s70, 3
s_mov_b32 s63, 0
s_cmp_gt_u32 s50, 3
s_cbranch_scc1 .Lk2_pub0_late
s_mov_b32 s70, 0
s_mov_b32 s63, s52
s_waitcnt vmcnt(0)
s_barrier
s_cmp_lg_u32 s51, 3
s_cbranch_scc1 .Lk2_pub0_late
v_mov_b32 v10, s82
v_mov_b32 v9, 0
s_mov_b64 exec, 1
global_store_dword v9, v10, s[30:31] sc1
s_mov_b64 exec, -1
.Lk2_pub0_late:
s_cmp_eq_u32 s50, 0
s_cbranch_scc1 .Lk2_pub0_f
s_cmp_eq_u32 s67, 0
s_cbranch_scc1 .Lk2_spc21
s_branch .Lk2_spc23
.Lk2_pub0_f:
s_cmp_eq_u32 s67, 0
s_cbranch_scc1 .Lk2_fin21
s_branch .Lk2_fin23
.Lk2_publish1:
s_cmp_eq_u32 s83, 0
s_cbranch_scc1 .Lk2_pub1_f3
s_mov_b32 s83, 0
s_waitcnt vmcnt(0)
s_barrier
s_cmp_lg_u32 s51, 3
s_cbranch_scc1 .Lk2_pub1_f3
v_mov_b32 v10, s82
v_mov_b32 v9, 0
s_mov_b64 exec, 1
global_store_dword v9, v10, s[0:1] sc1
s_mov_b64 exec, -1
.Lk2_pub1_f3:
s_getpc_b64 s[68:69]
.Lf_w42:
s_add_u32 s68, s68, .Lk2_fin20-.Lf_w42
s_addc_u32 s69, s69, (.Lk2_fin20-.Lf_w42)>>32
global_load_dword v254, v12, s[68:69]
s_getpc_b64 s[68:69]
.Lf_w43:
s_add_u32 s68, s68, .Lk2_fin20+4096-.Lf_w43
s_addc_u32 s69, s69, (.Lk2_fin20+4096-.Lf_w43)>>32
global_load_dword v254, v12, s[68:69]
s_getpc_b64 s[68:69]
.Lf_w44:
s_add_u32 s68, s68, .Lk2_fin20+8192-.Lf_w44
s_addc_u32 s69, s69, (.Lk2_fin20+8192-.Lf_w44)>>32
global_load_dword v254, v12, s[68:69]
s_nop 3
v_rcp_f32 v8, v36
v_log_f32 v232, v36
v_rcp_f32 v10, v40
v_log_f32 v233, v40
s_nop 0
v_add_f32 v232, v232, v4
v_add_f32 v233, v233, v5
s_mov_b64 s[68:69], s[28:29]
v_pk_mul_f32 v[208:209], v[80:81], v[8:9] op_sel_hi:[1,0]
v_pk_mul_f32 v[210:211], v[82:83], v[8:9] op_sel_hi:[1,0]
v_pk_mul_f32 v[212:213], v[84:85], v[8:9] op_sel_hi:[1,0]
v_pk_mul_f32 v[214:215], v[86:87], v[8:9] op_sel_hi:[1,0]
cv 216,208,209
cv 217,210,211
cv 218,212,213
cv 219,214,215
global_store_dwordx4 v0, v[216:219], s[68:69] offset:0 sc1
v_pk_mul_f32 v[220:221], v[88:89], v[10:11] op_sel_hi:[1,0]
v_pk_mul_f32 v[222:223], v[90:91], v[10:11] op_sel_hi:[1,0]
v_pk_mul_f32 v[224:225], v[92:93], v[10:11] op_sel_hi:[1,0]
v_pk_mul_f32 v[226:227], v[94:95], v[10:11] op_sel_hi:[1,0]
cv 228,220,221
cv 229,222,223
cv 230,224,225
cv 231,226,227
global_store_dwordx4 v0, v[228:231], s[68:69] offset:1024 sc1
v_pk_mul_f32 v[208:209], v[96:97], v[8:9] op_sel_hi:[1,0]
v_pk_mul_f32 v[210:211], v[98:99], v[8:9] op_sel_hi:[1,0]
v_pk_mul_f32 v[212:213], v[100:101], v[8:9] op_sel_hi:[1,0]
v_pk_mul_f32 v[214:215], v[102:103], v[8:9] op_sel_hi:[1,0]
cv 216,208,209
cv 217,210,211
cv 218,212,213
cv 219,214,215
global_store_dwordx4 v0, v[216:219], s[68:69] offset:2048 sc1
v_pk_mul_f32 v[220:221], v[104:105], v[10:11] op_sel_hi:[1,0]
v_pk_mul_f32 v[222:223], v[106:107], v[10:11] op_sel_hi:[1,0]
v_pk_mul_f32 v[224:225], v[108:109], v[10:11] op_sel_hi:[1,0]
v_pk_mul_f32 v[226:227], v[110:111], v[10:11] op_sel_hi:[1,0]
cv 228,220,221
cv 229,222,223
cv 230,224,225
cv 231,226,227
global_store_dwordx4 v0, v[228:231], s[68:69] offset:3072 sc1
s_add_u32 s68, s68, 0x1000
s_addc_u32 s69, s69, 0
v_pk_mul_f32 v[208:209], v[112:113], v[8:9] op_sel_hi:[1,0]
v_pk_mul_f32 v[210:211], v[114:115], v[8:9] op_sel_hi:[1,0]
v_pk_mul_f32 v[212:213], v[116:117], v[8:9] op_sel_hi:[1,0]
v_pk_mul_f32 v[214:215], v[118:119], v[8:9] op_sel_hi:[1,0]
cv 216,208,209
cv 217,210,211
cv 218,212,213
cv 219,214,215
global_store_dwordx4 v0, v[216:219], s[68:69] offset:0 sc1
v_pk_mul_f32 v[220:221], v[120:121], v[10:11] op_sel_hi:[1,0]
v_pk_mul_f32 v[222:223], v[122:123], v[10:11] op_sel_hi:[1,0]
v_pk_mul_f32 v[224:225], v[124:125], v[10:11] op_sel_hi:[1,0]
v_pk_mul_f32 v[226:227], v[126:127], v[10:11] op_sel_hi:[1,0]
cv 228,220,221
cv 229,222,223
cv 230,224,225
cv 231,226,227
global_store_dwordx4 v0, v[228:231], s[68:69] offset:1024 sc1
v_pk_mul_f32 v[208:209], v[128:129], v[8:9] op_sel_hi:[1,0]
v_pk_mul_f32 v[210:211], v[130:131], v[8:9] op_sel_hi:[1,0]
v_pk_mul_f32 v[212:213], v[132:133], v[8:9] op_sel_hi:[1,0]
v_pk_mul_f32 v[214:215], v[134:135], v[8:9] op_sel_hi:[1,0]
cv 216,208,209
cv 217,210,211
cv 218,212,213
cv 219,214,215
global_store_dwordx4 v0, v[216:219], s[68:69] offset:2048 sc1
v_pk_mul_f32 v[220:221], v[136:137], v[10:11] op_sel_hi:[1,0]
v_pk_mul_f32 v[222:223], v[138:139], v[10:11] op_sel_hi:[1,0]
v_pk_mul_f32 v[224:225], v[140:141], v[10:11] op_sel_hi:[1,0]
v_pk_mul_f32 v[226:227], v[142:143], v[10:11] op_sel_hi:[1,0]
cv 228,220,221
cv 229,222,223
cv 230,224,225
cv 231,226,227
global_store_dwordx4 v0, v[228:231], s[68:69] offset:3072 sc1
s_add_u32 s68, s68, 0x1000
s_addc_u32 s69, s69, 0
v_pk_mul_f32 v[208:209], v[144:145], v[8:9] op_sel_hi:[1,0]
v_pk_mul_f32 v[210:211], v[146:147], v[8:9] op_sel_hi:[1,0]
v_pk_mul_f32 v[212:213], v[148:149], v[8:9] op_sel_hi:[1,0]
v_pk_mul_f32 v[214:215], v[150:151], v[8:9] op_sel_hi:[1,0]
cv 216,208,209
cv 217,210,211
cv 218,212,213
cv 219,214,215
global_store_dwordx4 v0, v[216:219], s[68:69] offset:0 sc1
v_pk_mul_f32 v[220:221], v[152:153], v[10:11] op_sel_hi:[1,0]
v_pk_mul_f32 v[222:223], v[154:155], v[10:11] op_sel_hi:[1,0]
v_pk_mul_f32 v[224:225], v[156:157], v[10:11] op_sel_hi:[1,0]
v_pk_mul_f32 v[226:227], v[158:159], v[10:11] op_sel_hi:[1,0]
cv 228,220,221
cv 229,222,223
cv 230,224,225
cv 231,226,227
global_store_dwordx4 v0, v[228:231], s[68:69] offset:1024 sc1
v_pk_mul_f32 v[208:209], v[160:161], v[8:9] op_sel_hi:[1,0]
v_pk_mul_f32 v[210:211], v[162:163], v[8:9] op_sel_hi:[1,0]
v_pk_mul_f32 v[212:213], v[164:165], v[8:9] op_sel_hi:[1,0]
v_pk_mul_f32 v[214:215], v[166:167], v[8:9] op_sel_hi:[1,0]
cv 216,208,209
cv 217,210,211
cv 218,212,213
cv 219,214,215
global_store_dwordx4 v0, v[216:219], s[68:69] offset:2048 sc1
v_pk_mul_f32 v[220:221], v[168:169], v[10:11] op_sel_hi:[1,0]
v_pk_mul_f32 v[222:223], v[170:171], v[10:11] op_sel_hi:[1,0]
v_pk_mul_f32 v[224:225], v[172:173], v[10:11] op_sel_hi:[1,0]
v_pk_mul_f32 v[226:227], v[174:175], v[10:11] op_sel_hi:[1,0]
cv 228,220,221
cv 229,222,223
cv 230,224,225
cv 231,226,227
global_store_dwordx4 v0, v[228:231], s[68:69] offset:3072 sc1
s_add_u32 s68, s68, 0x1000
s_addc_u32 s69, s69, 0
v_pk_mul_f32 v[208:209], v[176:177], v[8:9] op_sel_hi:[1,0]
v_pk_mul_f32 v[210:211], v[178:179], v[8:9] op_sel_hi:[1,0]
v_pk_mul_f32 v[212:213], v[180:181], v[8:9] op_sel_hi:[1,0]
v_pk_mul_f32 v[214:215], v[182:183], v[8:9] op_sel_hi:[1,0]
cv 216,208,209
cv 217,210,211
cv 218,212,213
cv 219,214,215
global_store_dwordx4 v0, v[216:219], s[68:69] offset:0 sc1
v_pk_mul_f32 v[220:221], v[184:185], v[10:11] op_sel_hi:[1,0]
v_pk_mul_f32 v[222:223], v[186:187], v[10:11] op_sel_hi:[1,0]
v_pk_mul_f32 v[224:225], v[188:189], v[10:11] op_sel_hi:[1,0]
v_pk_mul_f32 v[226:227], v[190:191], v[10:11] op_sel_hi:[1,0]
cv 228,220,221
cv 229,222,223
cv 230,224,225
cv 231,226,227
global_store_dwordx4 v0, v[228:231], s[68:69] offset:1024 sc1
v_pk_mul_f32 v[208:209], v[192:193], v[8:9] op_sel_hi:[1,0]
v_pk_mul_f32 v[210:211], v[194:195], v[8:9] op_sel_hi:[1,0]
v_pk_mul_f32 v[212:213], v[196:197], v[8:9] op_sel_hi:[1,0]
v_pk_mul_f32 v[214:215], v[198:199], v[8:9] op_sel_hi:[1,0]
cv 216,208,209
cv 217,210,211
cv 218,212,213
cv 219,214,215
global_store_dwordx4 v0, v[216:219], s[68:69] offset:2048 sc1
v_pk_mul_f32 v[220:221], v[200:201], v[10:11] op_sel_hi:[1,0]
v_pk_mul_f32 v[222:223], v[202:203], v[10:11] op_sel_hi:[1,0]
v_pk_mul_f32 v[224:225], v[204:205], v[10:11] op_sel_hi:[1,0]
v_pk_mul_f32 v[226:227], v[206:207], v[10:11] op_sel_hi:[1,0]
cv 228,220,221
cv 229,222,223
cv 230,224,225
cv 231,226,227
global_store_dwordx4 v0, v[228:231], s[68:69] offset:3072 sc1
s_add_u32 s68, s68, 0x1000
s_addc_u32 s69, s69, 0
v_lshrrev_b32 v9, 1, v0
global_store_dwordx2 v9, v[232:233], s[68:69] sc1
v_mov_b64 v[36:37], 0
v_mov_b64 v[38:39], 0
v_mov_b64 v[40:41], 0
v_mov_b64 v[42:43], 0
v_mov_b64 v[80:81], 0
v_mov_b64 v[82:83], 0
v_mov_b64 v[84:85], 0
v_mov_b64 v[86:87], 0
v_mov_b64 v[88:89], 0
v_mov_b64 v[90:91], 0
v_mov_b64 v[92:93], 0
v_mov_b64 v[94:95], 0
v_mov_b64 v[96:97], 0
v_mov_b64 v[98:99], 0
v_mov_b64 v[100:101], 0
v_mov_b64 v[102:103], 0
v_mov_b64 v[104:105], 0
v_mov_b64 v[106:107], 0
v_mov_b64 v[108:109], 0
v_mov_b64 v[110:111], 0
v_mov_b64 v[112:113], 0
v_mov_b64 v[114:115], 0
v_mov_b64 v[116:117], 0
v_mov_b64 v[118:119], 0
v_mov_b64 v[120:121], 0
v_mov_b64 v[122:123], 0
v_mov_b64 v[124:125], 0
v_mov_b64 v[126:127], 0
v_mov_b64 v[128:129], 0
v_mov_b64 v[130:131], 0
v_mov_b64 v[132:133], 0
v_mov_b64 v[134:135], 0
v_mov_b64 v[136:137], 0
v_mov_b64 v[138:139], 0
v_mov_b64 v[140:141], 0
v_mov_b64 v[142:143], 0
v_mov_b64 v[144:145], 0
v_mov_b64 v[146:147], 0
v_mov_b64 v[148:149], 0
v_mov_b64 v[150:151], 0
v_mov_b64 v[152:153], 0
v_mov_b64 v[154:155], 0
v_mov_b64 v[156:157], 0
v_mov_b64 v[158:159], 0
v_mov_b64 v[160:161], 0
v_mov_b64 v[162:163], 0
v_mov_b64 v[164:165], 0
v_mov_b64 v[166:167], 0
v_mov_b64 v[168:169], 0
v_mov_b64 v[170:171], 0
v_mov_b64 v[172:173], 0
v_mov_b64 v[174:175], 0
v_mov_b64 v[176:177], 0
v_mov_b64 v[178:179], 0
v_mov_b64 v[180:181], 0
v_mov_b64 v[182:183], 0
v_mov_b64 v[184:185], 0
v_mov_b64 v[186:187], 0
v_mov_b64 v[188:189], 0
v_mov_b64 v[190:191], 0
v_mov_b64 v[192:193], 0
v_mov_b64 v[194:195], 0
v_mov_b64 v[196:197], 0
v_mov_b64 v[198:199], 0
v_mov_b64 v[200:201], 0
v_mov_b64 v[202:203], 0
v_mov_b64 v[204:205], 0
v_mov_b64 v[206:207], 0
s_cmp_lg_u32 s52, -1
s_cbranch_scc1 .Lk2_pub1_nomask
v_cndmask_b32_e64 v44, v44, v15, s[72:73]
v_cndmask_b32_e64 v56, v56, v15, s[72:73]
v_mov_b32 v52, v15
v_cndmask_b32_e64 v45, v45, v15, s[74:75]
v_cndmask_b32_e64 v57, v57, v15, s[74:75]
v_mov_b32 v53, v15
v_cndmask_b32_e64 v46, v46, v15, s[76:77]
v_cndmask_b32_e64 v58, v58, v15, s[76:77]
v_mov_b32 v54, v15
v_cndmask_b32_e64 v47, v47, v15, s[78:79]
v_cndmask_b32_e64 v59, v59, v15, s[78:79]
v_mov_b32 v55, v15
.Lk2_pub1_nomask:
m3 208,44,45,46
m3 209,47,52,53
m3 208,208,54,55
v_max_f32 v208, v208, v209
v_mov_b32 v209, v208
s_nop 1
v_permlane32_swap_b32 v208, v209
s_nop 0
v_max_f32 v208, v208, v209
v_mov_b32 v209, v208
s_nop 1
v_permlane16_swap_b32 v208, v209
s_nop 0
v_max_f32 v208, v208, v209
v_mov_b32 v4, v208
v_sub_f32 v44, v44, v208
v_sub_f32 v45, v45, v208
v_sub_f32 v46, v46, v208
v_sub_f32 v47, v47, v208
v_sub_f32 v52, v52, v208
v_sub_f32 v53, v53, v208
v_sub_f32 v54, v54, v208
v_sub_f32 v55, v55, v208
v_sub_f32 v20, 0, v208
v_sub_f32 v21, 0, v208
v_sub_f32 v22, 0, v208
v_sub_f32 v23, 0, v208
m3 212,48,49,50
m3 213,51,56,57
m3 212,212,58,59
v_max_f32 v212, v212, v213
v_mov_b32 v213, v212
s_nop 1
v_permlane32_swap_b32 v212, v213
s_nop 0
v_max_f32 v212, v212, v213
v_mov_b32 v213, v212
s_nop 1
v_permlane16_swap_b32 v212, v213
s_nop 0
v_max_f32 v212, v212, v213
v_mov_b32 v5, v212
v_sub_f32 v48, v48, v212
v_sub_f32 v49, v49, v212
v_sub_f32 v50, v50, v212
v_sub_f32 v51, v51, v212
v_sub_f32 v56, v56, v212
v_sub_f32 v57, v57, v212
v_sub_f32 v58, v58, v212
v_sub_f32 v59, v59, v212
v_sub_f32 v24, 0, v212
v_sub_f32 v25, 0, v212
v_sub_f32 v26, 0, v212
v_sub_f32 v27, 0, v212
s_mov_b32 s66, 0
s_mov_b32 s70, 3
s_mov_b32 s63, 0
s_cmp_gt_u32 s50, 3
s_cbranch_scc1 .Lk2_pub1_late
s_mov_b32 s70, 0
s_mov_b32 s63, s52
s_waitcnt vmcnt(0)
s_barrier
s_cmp_lg_u32 s51, 3
s_cbranch_scc1 .Lk2_pub1_late
v_mov_b32 v10, s82
v_mov_b32 v9, 0
s_mov_b64 exec, 1
global_store_dword v9, v10, s[30:31] sc1
s_mov_b64 exec, -1
.Lk2_pub1_late:
s_cmp_eq_u32 s50, 0
s_cbranch_scc1 .Lk2_pub1_f
s_cmp_eq_u32 s67, 1
s_cbranch_scc1 .Lk2_spc22
s_branch .Lk2_spc20
.Lk2_pub1_f:
s_cmp_eq_u32 s67, 1
s_cbranch_scc1 .Lk2_fin22
s_branch .Lk2_fin20
.Lk2_proA:
v_mov_b32 v8, v92
v_mov_b32 v10, v92
s_nop 1
v_permlane32_swap_b32 v8, v10
s_nop 0
v_rcp_f32 v8, v10
v_log_f32 v10, v10
s_nop 0
v_add_f32 v4, v4, v10
v_mul_f32 v80, v8, v80
v_mul_f32 v81, v8, v81
v_mul_f32 v82, v8, v82
v_mul_f32 v83, v8, v83
v_mul_f32 v84, v8, v84
s_waitcnt lgkmcnt(8)
zvaa 44,64,128
v_mul_f32 v85, v8, v85
v_mul_f32 v86, v8, v86
v_mul_f32 v87, v8, v87
v_mul_f32 v88, v8, v88
v_mul_f32 v89, v8, v89
v_mul_f32 v90, v8, v90
v_mul_f32 v91, v8, v91
mvaa 44,68,136
v_mul_f32 v92, v8, v92
v_mul_f32 v93, v8, v93
v_mul_f32 v94, v8, v94
v_mul_f32 v95, v8, v95
cv 16,80,81
cv 17,82,83
cv 18,84,85
mvaa 44,72,144
cv 19,86,87
cv 20,88,89
cv 21,90,91
cv 22,92,93
cv 23,94,95
global_store_dwordx2 v14, v[16:17], s[0:1] offset:0 sc1
global_store_dwordx2 v14, v[18:19], s[0:1] offset:512 sc1
mvaa 44,76,152
global_store_dwordx2 v14, v[20:21], s[0:1] offset:8 sc1
global_store_dwordx2 v14, v[22:23], s[0:1] offset:520 sc1
v_mov_b32 v8, v108
v_mov_b32 v10, v108
s_nop 1
v_permlane32_swap_b32 v8, v10
s_nop 0
mvaa 44,80,160
v_rcp_f32 v8, v10
v_log_f32 v10, v10
s_nop 0
v_add_f32 v5, v5, v10
v_mul_f32 v96, v8, v96
v_mul_f32 v97, v8, v97
v_mul_f32 v98, v8, v98
mvaa 44,84,168
v_mul_f32 v99, v8, v99
v_mul_f32 v100, v8, v100
v_mul_f32 v101, v8, v101
v_mul_f32 v102, v8, v102
v_mul_f32 v103, v8, v103
v_mul_f32 v104, v8, v104
v_mul_f32 v105, v8, v105
mvaa 44,88,176
v_mul_f32 v106, v8, v106
v_mul_f32 v107, v8, v107
v_mul_f32 v108, v8, v108
v_mul_f32 v109, v8, v109
v_mul_f32 v110, v8, v110
v_mul_f32 v111, v8, v111
cv 24,96,97
mvaa 44,92,184
cv 25,98,99
cv 26,100,101
cv 27,102,103
cv 28,104,105
cv 29,106,107
cv 30,108,109
cv 31,110,111
zvaa 48,64,132
global_store_dwordx2 v14, v[24:25], s[0:1] offset:2048 sc1
global_store_dwordx2 v14, v[26:27], s[0:1] offset:2560 sc1
global_store_dwordx2 v14, v[28:29], s[0:1] offset:2056 sc1
global_store_dwordx2 v14, v[30:31], s[0:1] offset:2568 sc1
s_mov_b32 exec_lo, 0
global_store_dword v9, v4, s[2:3] offset:0 sc1
global_store_dword v9, v5, s[2:3] offset:128 sc1
s_mov_b64 exec, -1
s_mov_b32 s71, 0x41c00000
ar 9,206
mvaa 48,68,140
s_nop 0
v_readlane_b32 s4, v9, 0
v_readlane_b32 s5, v9, 1
v_readlane_b32 s6, v9, 2
v_readlane_b32 s7, v9, 3
v_readlane_b32 s8, v9, 4
v_readlane_b32 s9, v9, 5
mvaa 48,72,148
v_readlane_b32 s10, v9, 6
v_readlane_b32 s11, v9, 7
v_readlane_b32 s12, v9, 8
v_readlane_b32 s13, v9, 9
v_readlane_b32 s14, v9, 10
v_readlane_b32 s15, v9, 11
v_readlane_b32 s16, v9, 12
mvaa 48,76,156
v_readlane_b32 s17, v9, 13
v_readlane_b32 s18, v9, 14
v_readlane_b32 s19, v9, 15
v_readlane_b32 s52, v9, 16
v_readlane_b32 s53, v9, 17
v_readlane_b32 s54, v9, 18
v_readlane_b32 s55, v9, 19
mvaa 48,80,164
v_readlane_b32 s56, v9, 20
v_readlane_b32 s57, v9, 21
v_readlane_b32 s58, v9, 22
v_readlane_b32 s59, v9, 23
v_readlane_b32 s84, v9, 24
v_readlane_b32 s85, v9, 25
v_readlane_b32 s86, v9, 26
mvaa 48,84,172
v_readlane_b32 s87, v9, 27
v_readlane_b32 s88, v9, 28
v_readlane_b32 s89, v9, 29
v_readlane_b32 s90, v9, 30
v_readlane_b32 s91, v9, 31
v_readlane_b32 s92, v9, 32
v_readlane_b32 s93, v9, 33
mvaa 48,88,180
v_readlane_b32 s94, v9, 34
v_readlane_b32 s95, v9, 35
v_readlane_b32 s0, v9, 36
v_readlane_b32 s1, v9, 37
v_readlane_b32 s2, v9, 38
v_readlane_b32 s3, v9, 39
ar 4,192
mvaa 48,92,188
ar 76,193
ar 78,194
ar 13,195
ar 244,196
ar 245,197
ar 246,198
ar 247,199
s_waitcnt lgkmcnt(0)
zvaa 52,96,128
ar 248,200
ar 249,201
ar 250,202
ar 251,203
ar 255,204
v_mov_b32 v79, 0
s_nop 1
mvaa 52,100,136
global_load_dword v6, v76, s[32:33] sc1
global_load_dword v7, v76, s[32:33] offset:64 sc1
s_mov_b32 s60, 0
s_mov_b32 s61, 0xffff0000
s_mov_b32 s70, 0
s_mov_b32 s66, 0
s_mov_b32 s62, 0
mvaa 52,104,144
s_mov_b32 s63, 2
s_mov_b32 s83, 1
s_mov_b32 s32, 0
s_mov_b32 s33, 0
v_mov_b32 v16, 0x3f803f80
v_mov_b32 v17, 0x3f803f80
v_mov_b32 v18, 0x3f803f80
mvaa 52,108,152
v_mov_b32 v19, 0x3f803f80
v_mov_b32 v15, 0xff800000
v_add_u32 v2, 0x10000, v1
v_mov_b32 v14, v2
v_cmp_gt_i32_e64 s[72:73], 0, v4
v_cmp_gt_i32_e64 s[74:75], 1, v4
v_cmp_gt_i32_e64 s[76:77], 2, v4
mvaa 52,112,160
v_cmp_gt_i32_e64 s[78:79], 3, v4
v_mov_b64 v[36:37], 0
v_mov_b64 v[38:39], 0
v_mov_b64 v[40:41], 0
v_mov_b64 v[42:43], 0
v_mov_b64 v[80:81], 0
v_mov_b64 v[82:83], 0
mvaa 52,116,168
v_mov_b64 v[84:85], 0
v_mov_b64 v[86:87], 0
v_mov_b64 v[88:89], 0
v_mov_b64 v[90:91], 0
v_mov_b64 v[92:93], 0
v_mov_b64 v[94:95], 0
v_mov_b64 v[96:97], 0
mvaa 52,120,176
v_mov_b64 v[98:99], 0
v_mov_b64 v[100:101], 0
v_mov_b64 v[102:103], 0
v_mov_b64 v[104:105], 0
v_mov_b64 v[106:107], 0
v_mov_b64 v[108:109], 0
v_mov_b64 v[110:111], 0
mvaa 52,124,184
v_mov_b64 v[112:113], 0
v_mov_b64 v[114:115], 0
v_mov_b64 v[116:117], 0
v_mov_b64 v[118:119], 0
v_mov_b64 v[120:121], 0
v_mov_b64 v[122:123], 0
v_mov_b64 v[124:125], 0
zvaa 56,96,132
v_mov_b64 v[126:127], 0
)ASM" ::: "memory");
  asm volatile(R"ASM(
v_mov_b64 v[128:129], 0
v_mov_b64 v[130:131], 0
v_mov_b64 v[132:133], 0
v_mov_b64 v[134:135], 0
v_mov_b64 v[136:137], 0
v_mov_b64 v[138:139], 0
mvaa 56,100,140
v_mov_b64 v[140:141], 0
v_mov_b64 v[142:143], 0
v_mov_b64 v[144:145], 0
v_mov_b64 v[146:147], 0
v_mov_b64 v[148:149], 0
v_mov_b64 v[150:151], 0
v_mov_b64 v[152:153], 0
mvaa 56,104,148
v_mov_b64 v[154:155], 0
v_mov_b64 v[156:157], 0
v_mov_b64 v[158:159], 0
v_mov_b64 v[160:161], 0
v_mov_b64 v[162:163], 0
v_mov_b64 v[164:165], 0
v_mov_b64 v[166:167], 0
mvaa 56,108,156
v_mov_b64 v[168:169], 0
v_mov_b64 v[170:171], 0
v_mov_b64 v[172:173], 0
v_mov_b64 v[174:175], 0
v_mov_b64 v[176:177], 0
v_mov_b64 v[178:179], 0
v_mov_b64 v[180:181], 0
mvaa 56,112,164
v_mov_b64 v[182:183], 0
v_mov_b64 v[184:185], 0
v_mov_b64 v[186:187], 0
v_mov_b64 v[188:189], 0
v_mov_b64 v[190:191], 0
v_mov_b64 v[192:193], 0
v_mov_b64 v[194:195], 0
mvaa 56,116,172
v_mov_b64 v[196:197], 0
v_mov_b64 v[198:199], 0
v_mov_b64 v[200:201], 0
v_mov_b64 v[202:203], 0
v_mov_b64 v[204:205], 0
v_mov_b64 v[206:207], 0
mvaa 56,120,180
mvaa 56,124,188
s_barrier
s_add_u32 m0, s40, s65
s_nop 0
buffer_load_dwordx4 v0, s[24:27], s43 offen offset:0 sc1 lds
buffer_load_dwordx4 v0, s[24:27], s43 offen offset:1024 sc1 lds
buffer_load_dwordx4 v0, s[24:27], s43 offen offset:2048 sc1 lds
buffer_load_dwordx4 v0, s[24:27], s43 offen offset:3072 sc1 lds
s_add_u32 s43, s43, 0x8000
s_add_u32 s65, s65, 0x4000
s_cmp_eq_u32 s65, 0x28000
s_cselect_b32 s65, 0x10000, s65
s_cmp_eq_u32 s43, s45
s_cselect_b32 s43, s47, s43
s_cselect_b32 s26, s48, s26
s_cselect_b32 s45, -1, s45
rqa 64,1,16384
rqa 68,1,17408
rqa 72,1,18432
rqa 76,1,19456
rqa 80,1,20480
rqa 84,1,21504
rqa 88,1,22528
rqa 92,1,23552
rqa 96,1,24576
rqa 100,1,25600
rqa 104,1,26624
s_cmp_lg_u32 s52, -1
s_cbranch_scc1 .Lk2_pA_nomask
v_cndmask_b32_e64 v44, v44, v15, s[72:73]
v_cndmask_b32_e64 v56, v56, v15, s[72:73]
v_mov_b32 v52, v15
v_cndmask_b32_e64 v45, v45, v15, s[74:75]
v_cndmask_b32_e64 v57, v57, v15, s[74:75]
v_mov_b32 v53, v15
v_cndmask_b32_e64 v46, v46, v15, s[76:77]
v_cndmask_b32_e64 v58, v58, v15, s[76:77]
v_mov_b32 v54, v15
v_cndmask_b32_e64 v47, v47, v15, s[78:79]
v_cndmask_b32_e64 v59, v59, v15, s[78:79]
v_mov_b32 v55, v15
.Lk2_pA_nomask:
m3 208,44,45,46
m3 209,47,52,53
m3 208,208,54,55
v_max_f32 v208, v208, v209
v_mov_b32 v209, v208
s_nop 1
v_permlane32_swap_b32 v208, v209
s_nop 0
v_max_f32 v208, v208, v209
v_mov_b32 v209, v208
s_nop 1
v_permlane16_swap_b32 v208, v209
s_nop 0
v_max_f32 v208, v208, v209
v_mov_b32 v4, v208
v_sub_f32 v44, v44, v208
v_sub_f32 v45, v45, v208
v_sub_f32 v46, v46, v208
v_sub_f32 v47, v47, v208
v_sub_f32 v52, v52, v208
v_sub_f32 v53, v53, v208
v_sub_f32 v54, v54, v208
v_sub_f32 v55, v55, v208
v_sub_f32 v20, 0, v208
v_sub_f32 v21, 0, v208
v_sub_f32 v22, 0, v208
v_sub_f32 v23, 0, v208
m3 212,48,49,50
m3 213,51,56,57
m3 212,212,58,59
v_max_f32 v212, v212, v213
v_mov_b32 v213, v212
s_nop 1
v_permlane32_swap_b32 v212, v213
s_nop 0
v_max_f32 v212, v212, v213
v_mov_b32 v213, v212
s_nop 1
v_permlane16_swap_b32 v212, v213
s_nop 0
v_max_f32 v212, v212, v213
v_mov_b32 v5, v212
v_sub_f32 v48, v48, v212
v_sub_f32 v49, v49, v212
v_sub_f32 v50, v50, v212
v_sub_f32 v51, v51, v212
v_sub_f32 v56, v56, v212
v_sub_f32 v57, v57, v212
v_sub_f32 v58, v58, v212
v_sub_f32 v59, v59, v212
v_sub_f32 v24, 0, v212
v_sub_f32 v25, 0, v212
v_sub_f32 v26, 0, v212
v_sub_f32 v27, 0, v212
s_mov_b32 s97, 2
s_branch .Lk2_reg20
.Lk2_spc20:
s_waitcnt lgkmcnt(3)
s_cmp_ge_u32 s42, s22
s_cselect_b32 s99, 0x29000, s99
s_add_u32 m0, s99, 0
v_mfma_f32_16x16x32_bf16 v[60:63], a[64:67], a[128:131], v[20:23]
rqa 108,1,27648
mvaa 60,68,136
ex 44
mvaa 60,72,144
rqa 112,1,28672
mvaa 60,76,152
ex 45
mvaa 60,80,160
rqa 116,1,29696
cv 28,44,45
mvaa 60,84,168
ex 46
mvaa 60,88,176
rqa 120,1,30720
mvaa 60,92,184
ex 47
buffer_load_dwordx4 v0, s[20:23], s42 offen offset:0 sc1 lds
v_mfma_f32_16x16x32_bf16 v[64:67], a[64:67], a[132:135], v[24:27]
rqa 124,1,31744
cv 29,46,47
mvaa 64,68,140
ex 52
mvaa 64,72,148
rqa 64,2,0
mvaa 64,76,156
ex 53
mvaa 64,80,164
rqa 68,2,1024
cv 30,52,53
mvaa 64,84,172
ex 54
mvaa 64,88,180
rqa 72,2,2048
mvaa 64,92,188
ex 55
buffer_load_dwordx4 v0, s[20:23], s42 offen offset:1024 sc1 lds
s_waitcnt lgkmcnt(3)
v_mfma_f32_16x16x32_bf16 v[68:71], a[96:99], a[128:131], v[20:23]
rqa 76,2,3072
cv 31,54,55
mvaa 68,100,136
ex 48
mvaa 68,104,144
rqa 80,2,4096
mvaa 68,108,152
ex 49
mvaa 68,112,160
rqa 84,2,5120
cv 32,48,49
mvaa 68,116,168
ex 50
mvaa 68,120,176
rqa 88,2,6144
mvaa 68,124,184
ex 51
ex 59
buffer_load_dwordx4 v0, s[20:23], s42 offen offset:2048 sc1 lds
v_mfma_f32_16x16x32_bf16 v[72:75], a[96:99], a[132:135], v[24:27]
rqa 92,2,7168
cv 33,50,51
mvaa 72,100,140
ex 56
mvaa 72,104,148
rqa 96,2,8192
mvaa 72,108,156
ex 57
mvaa 72,112,164
rqa 100,2,9216
cv 34,56,57
mvaa 72,116,172
ex 58
mvaa 72,120,180
rqa 104,2,10240
cv 35,58,59
mvaa 72,124,188
buffer_load_dwordx4 v0, s[20:23], s42 offen offset:3072 sc1 lds
s_waitcnt lgkmcnt(7)
mvav 80,64,28
rqa 108,2,11264
mvav 88,64,32
s_cmp_eq_u32 s51, 0
s_cselect_b32 s67, 2, 0
s_sub_u32 s67, s52, s67
s_mov_b32 s63, s67
s_cmp_lt_i32 s66, s67
s_cbranch_scc1 .Lk2_spc20_1
s_add_u32 s63, s66, 1
s_cmp_eq_u32 s66, s52
s_cbranch_scc1 .Lk2_spc20_m
s_cmp_lg_u32 s66, s67
s_cbranch_scc1 .Lk2_spc20_a
.Lk2_spc20_E1:
global_load_dword v77, v79, s[30:31] sc1
global_load_dwordx2 v[234:235], v79, s[2:3] sc1
s_mov_b32 s32, 1
s_branch .Lk2_spc20_1
.Lk2_spc20_a:
s_cmp_eq_u32 s32, 0
s_cbranch_scc1 .Lk2_spc20_1
.Lk2_spc20_E2:
s_waitcnt vmcnt(8)
s_mov_b32 s67, 0x400000
.Lk2_spc20_f3:
v_xor_b32 v234, s82, v234
v_xor_b32 v235, s82, v235
v_or_b32 v234, v234, v235
v_cmp_ne_u32 vcc, 0, v234
s_nop 4
s_cbranch_vccz .Lk2_spc20_f3ok
s_sleep 4
global_load_dwordx2 v[234:235], v79, s[2:3] sc1
s_waitcnt vmcnt(0)
s_sub_u32 s67, s67, 1
s_cmp_lg_u32 s67, 0
s_cbranch_scc1 .Lk2_spc20_f3
v_mov_b32 v6, 0x7fc00000
.Lk2_spc20_f3ok:
s_mov_b32 s33, 1
s_cmp_eq_u32 s53, 0
s_cbranch_scc1 .Lk2_spc20_go
v_readfirstlane_b32 s62, v77
s_nop 1
s_cmp_lg_u32 s62, s82
s_cselect_b32 s62, 0, 1
s_cbranch_scc1 .Lk2_spc20_go
s_mov_b64 s[68:69], s[28:29]
global_load_dwordx4 a[192:195], v0, s[68:69] offset:0 sc1
global_load_dwordx4 a[196:199], v0, s[68:69] offset:1024 sc1
global_load_dwordx4 a[200:203], v0, s[68:69] offset:2048 sc1
global_load_dwordx4 a[204:207], v0, s[68:69] offset:3072 sc1
s_add_u32 s68, s68, 0x1000
s_addc_u32 s69, s69, 0
global_load_dwordx4 a[208:211], v0, s[68:69] offset:0 sc1
global_load_dwordx4 a[212:215], v0, s[68:69] offset:1024 sc1
global_load_dwordx4 a[216:219], v0, s[68:69] offset:2048 sc1
global_load_dwordx4 a[220:223], v0, s[68:69] offset:3072 sc1
s_add_u32 s68, s68, 0x1000
s_addc_u32 s69, s69, 0
global_load_dwordx4 a[224:227], v0, s[68:69] offset:0 sc1
global_load_dwordx4 a[228:231], v0, s[68:69] offset:1024 sc1
global_load_dwordx4 a[232:235], v0, s[68:69] offset:2048 sc1
global_load_dwordx4 a[236:239], v0, s[68:69] offset:3072 sc1
s_add_u32 s68, s68, 0x1000
s_addc_u32 s69, s69, 0
global_load_dwordx4 a[240:243], v0, s[68:69] offset:0 sc1
global_load_dwordx4 a[244:247], v0, s[68:69] offset:1024 sc1
global_load_dwordx4 a[248:251], v0, s[68:69] offset:2048 sc1
global_load_dwordx4 a[252:255], v0, s[68:69] offset:3072 sc1
s_add_u32 s68, s68, 0x1000
s_addc_u32 s69, s69, 0
v_lshrrev_b32 v9, 1, v0
global_load_dwordx2 v[242:243], v9, s[68:69] sc1
.Lk2_spc20_go:
global_load_dwordx4 v[216:219], v0, s[54:55] offset:0 sc1
global_load_dwordx4 v[220:223], v0, s[54:55] offset:1024 sc1
s_add_u32 s54, s54, 0x200000
s_addc_u32 s55, s55, 0
global_load_dwordx4 v[224:227], v0, s[54:55] offset:0 sc1
global_load_dwordx4 v[228:231], v0, s[54:55] offset:1024 sc1
global_load_dword v232, v76, s[56:57] offset:0 sc1
global_load_dword v233, v76, s[56:57] offset:64 sc1
s_add_u32 s56, s56, 0x20000
s_addc_u32 s57, s57, 0
global_load_dword v234, v76, s[56:57] offset:0 sc1
global_load_dword v235, v76, s[56:57] offset:64 sc1
s_branch .Lk2_spc20_x
.Lk2_spc20_m:
s_mov_b32 s63, -1
s_nop 1
v_cndmask_b32_e64 v60, v60, v15, s[72:73]
v_cndmask_b32_e64 v72, v72, v15, s[72:73]
v_mov_b32 v68, v15
v_cndmask_b32_e64 v61, v61, v15, s[74:75]
v_cndmask_b32_e64 v73, v73, v15, s[74:75]
v_mov_b32 v69, v15
v_cndmask_b32_e64 v62, v62, v15, s[76:77]
v_cndmask_b32_e64 v74, v74, v15, s[76:77]
v_mov_b32 v70, v15
v_cndmask_b32_e64 v63, v63, v15, s[78:79]
v_cndmask_b32_e64 v75, v75, v15, s[78:79]
v_mov_b32 v71, v15
s_cmp_eq_u32 s33, 0
s_cbranch_scc1 .Lk2_spc20_1
.Lk2_spc20_x:
s_cmp_lg_u32 s70, 0
s_cbranch_scc1 .Lk2_spc20_1
s_cmp_lg_u32 s62, 0
s_cbranch_scc1 .Lk2_spc20_x2
s_waitcnt vmcnt(24)
.Lk2_spc20_x2:
s_waitcnt vmcnt(41)
s_barrier
s_branch .Lk2_spc20_ret
.Lk2_spc20_1:
s_cmp_lg_u32 s83, 0
s_cbranch_scc1 .Lk2_spc20_4
s_cmp_eq_u32 s70, 0
s_cbranch_scc1 .Lk2_spc20_2
s_sub_u32 s70, s70, 1
s_cmp_eq_u32 s70, 0
s_cbranch_scc1 .Lk2_spc20_3
s_add_u32 s63, s66, 1
s_waitcnt vmcnt(33)
s_barrier
s_branch .Lk2_spc20_ret
.Lk2_spc20_3:
s_waitcnt vmcnt(16)
s_barrier
s_cmp_lg_u32 s51, 3
s_cbranch_scc1 .Lk2_spc20_ret
v_mov_b32 v10, s82
v_mov_b32 v9, 0
s_mov_b64 exec, 1
global_store_dword v9, v10, s[30:31] sc1
s_mov_b64 exec, -1
s_branch .Lk2_spc20_ret
.Lk2_spc20_4:
s_mov_b32 s83, 0
s_waitcnt vmcnt(16)
s_barrier
s_getpc_b64 s[68:69]
.Lf_w19:
s_add_u32 s68, s68, .Lk2_fin20-.Lf_w19
s_addc_u32 s69, s69, (.Lk2_fin20-.Lf_w19)>>32
global_load_dword v254, v12, s[68:69]
s_getpc_b64 s[68:69]
.Lf_w20:
s_add_u32 s68, s68, .Lk2_fin20+4096-.Lf_w20
s_addc_u32 s69, s69, (.Lk2_fin20+4096-.Lf_w20)>>32
global_load_dword v254, v12, s[68:69]
s_getpc_b64 s[68:69]
.Lf_w21:
s_add_u32 s68, s68, .Lk2_fin20+8192-.Lf_w21
s_addc_u32 s69, s69, (.Lk2_fin20+8192-.Lf_w21)>>32
global_load_dword v254, v12, s[68:69]
s_getpc_b64 s[68:69]
.Lf_w22:
s_add_u32 s68, s68, .Lk2_spc20-.Lf_w22
s_addc_u32 s69, s69, (.Lk2_spc20-.Lf_w22)>>32
global_load_dword v254, v12, s[68:69]
s_getpc_b64 s[68:69]
.Lf_w23:
s_add_u32 s68, s68, .Lk2_spc20+4096-.Lf_w23
s_addc_u32 s69, s69, (.Lk2_spc20+4096-.Lf_w23)>>32
global_load_dword v254, v12, s[68:69]
s_cmp_lg_u32 s51, 3
s_cbranch_scc1 .Lk2_spc20_ret
v_mov_b32 v10, s82
v_mov_b32 v9, 0
s_mov_b64 exec, 1
global_store_dword v9, v10, s[0:1] sc1
s_mov_b64 exec, -1
s_branch .Lk2_spc20_ret
.Lk2_spc20_2:
s_waitcnt vmcnt(16)
s_barrier
.Lk2_spc20_ret:
s_min_u32 s97, s63, s50
mvav 84,68,28
rqa 112,2,12288
mvav 92,68,32
s_add_u32 m0, s40, s65
mvav 96,72,28
rqa 116,2,13312
mvvv 36,16,28
mvav 104,72,32
buffer_load_dwordx4 v0, s[24:27], s43 offen offset:0 sc1 lds
mvav 100,76,28
rqa 120,2,14336
m3 8,60,61,62
mvav 108,76,32
s_add_u32 s42, s42, 0x8000
s_waitcnt lgkmcnt(7)
mvav 112,80,28
rqa 124,2,15360
m3 9,63,64,65
mvav 120,80,32
mvav 116,84,28
rqa 64,1,32768
m3 8,8,66,67
mvav 124,84,32
buffer_load_dwordx4 v0, s[24:27], s43 offen offset:1024 sc1 lds
mvav 128,88,28
rqa 68,1,33792
m3 9,9,68,69
mvav 136,88,32
s_add_u32 s64, s64, 0x4000
mvav 132,92,28
rqa 72,1,34816
m3 8,8,70,71
mvav 140,92,32
s_cmp_eq_u32 s64, 0x18000
s_cselect_b32 s64, 0, s64
s_waitcnt lgkmcnt(7)
mvav 144,96,28
rqa 76,1,35840
mvav 152,96,32
v_add_u32 v2, s64, v14
mvav 148,100,28
rqa 80,1,36864
m3 9,9,72,73
mvav 156,100,32
buffer_load_dwordx4 v0, s[24:27], s43 offen offset:2048 sc1 lds
mvav 160,104,28
rqa 84,1,37888
m3 8,8,74,75
mvav 168,104,32
s_add_u32 s66, s66, 1
mvav 164,108,28
rqa 88,1,38912
v_max_f32 v8, v8, v9
mvav 172,108,32
s_waitcnt lgkmcnt(7)
mvav 176,112,28
rqa 92,1,39936
v_cmp_lt_f32 vcc, s71, v8
mvav 184,112,32
buffer_load_dwordx4 v0, s[24:27], s43 offen offset:3072 sc1 lds
mvav 180,116,28
rqa 96,1,40960
mvav 188,116,32
s_add_u32 s43, s43, 0x8000
mvav 192,120,28
rqa 100,1,41984
s_cmp_eq_u32 s66, s97
s_cselect_b64 vcc, -1, vcc
mvav 200,120,32
s_add_u32 s65, s65, 0x4000
mvav 196,124,28
rqa 104,1,43008
mvvv 40,16,32
mvav 204,124,32
s_cmp_eq_u32 s65, 0x28000
s_cselect_b32 s65, 0x10000, s65
s_cbranch_vccnz .Lk2_ev20
s_branch .Lk2_reg21
.Lk2_spc21:
s_waitcnt lgkmcnt(3)
s_cmp_ge_u32 s42, s22
s_cselect_b32 s99, 0x29000, s99
s_add_u32 m0, s99, 16384
v_mfma_f32_16x16x32_bf16 v[44:47], a[64:67], a[128:131], v[20:23]
rqa 108,1,44032
mvaa 44,68,136
ex 60
mvaa 44,72,144
rqa 112,1,45056
mvaa 44,76,152
ex 61
mvaa 44,80,160
rqa 116,1,46080
cv 28,60,61
mvaa 44,84,168
ex 62
mvaa 44,88,176
rqa 120,1,47104
mvaa 44,92,184
ex 63
buffer_load_dwordx4 v0, s[20:23], s42 offen offset:0 sc1 lds
v_mfma_f32_16x16x32_bf16 v[48:51], a[64:67], a[132:135], v[24:27]
rqa 124,1,48128
cv 29,62,63
mvaa 48,68,140
ex 68
mvaa 48,72,148
rqa 64,2,0
mvaa 48,76,156
ex 69
mvaa 48,80,164
rqa 68,2,1024
cv 30,68,69
mvaa 48,84,172
ex 70
mvaa 48,88,180
rqa 72,2,2048
mvaa 48,92,188
ex 71
buffer_load_dwordx4 v0, s[20:23], s42 offen offset:1024 sc1 lds
s_waitcnt lgkmcnt(3)
v_mfma_f32_16x16x32_bf16 v[52:55], a[96:99], a[128:131], v[20:23]
rqa 76,2,3072
cv 31,70,71
mvaa 52,100,136
ex 64
mvaa 52,104,144
rqa 80,2,4096
mvaa 52,108,152
ex 65
mvaa 52,112,160
rqa 84,2,5120
cv 32,64,65
mvaa 52,116,168
ex 66
mvaa 52,120,176
rqa 88,2,6144
mvaa 52,124,184
ex 67
ex 75
buffer_load_dwordx4 v0, s[20:23], s42 offen offset:2048 sc1 lds
v_mfma_f32_16x16x32_bf16 v[56:59], a[96:99], a[132:135], v[24:27]
rqa 92,2,7168
cv 33,66,67
mvaa 56,100,140
ex 72
mvaa 56,104,148
rqa 96,2,8192
mvaa 56,108,156
ex 73
mvaa 56,112,164
rqa 100,2,9216
cv 34,72,73
mvaa 56,116,172
ex 74
mvaa 56,120,180
rqa 104,2,10240
cv 35,74,75
mvaa 56,124,188
buffer_load_dwordx4 v0, s[20:23], s42 offen offset:3072 sc1 lds
s_waitcnt lgkmcnt(7)
mvav 80,64,28
rqa 108,2,11264
mvav 88,64,32
s_cmp_eq_u32 s51, 0
s_cselect_b32 s67, 2, 0
s_sub_u32 s67, s52, s67
s_mov_b32 s63, s67
s_cmp_lt_i32 s66, s67
s_cbranch_scc1 .Lk2_spc21_1
s_add_u32 s63, s66, 1
s_cmp_eq_u32 s66, s52
s_cbranch_scc1 .Lk2_spc21_m
s_cmp_lg_u32 s66, s67
s_cbranch_scc1 .Lk2_spc21_a
.Lk2_spc21_E1:
global_load_dword v77, v79, s[30:31] sc1
global_load_dwordx2 v[234:235], v79, s[2:3] sc1
s_mov_b32 s32, 1
s_branch .Lk2_spc21_1
.Lk2_spc21_a:
s_cmp_eq_u32 s32, 0
s_cbranch_scc1 .Lk2_spc21_1
.Lk2_spc21_E2:
s_waitcnt vmcnt(8)
s_mov_b32 s67, 0x400000
.Lk2_spc21_f3:
v_xor_b32 v234, s82, v234
v_xor_b32 v235, s82, v235
v_or_b32 v234, v234, v235
v_cmp_ne_u32 vcc, 0, v234
s_nop 4
)ASM" ::: "memory");
  asm volatile(R"ASM(
s_cbranch_vccz .Lk2_spc21_f3ok
s_sleep 4
global_load_dwordx2 v[234:235], v79, s[2:3] sc1
s_waitcnt vmcnt(0)
s_sub_u32 s67, s67, 1
s_cmp_lg_u32 s67, 0
s_cbranch_scc1 .Lk2_spc21_f3
v_mov_b32 v6, 0x7fc00000
.Lk2_spc21_f3ok:
s_mov_b32 s33, 1
s_cmp_eq_u32 s53, 0
s_cbranch_scc1 .Lk2_spc21_go
v_readfirstlane_b32 s62, v77
s_nop 1
s_cmp_lg_u32 s62, s82
s_cselect_b32 s62, 0, 1
s_cbranch_scc1 .Lk2_spc21_go
s_mov_b64 s[68:69], s[28:29]
global_load_dwordx4 a[192:195], v0, s[68:69] offset:0 sc1
global_load_dwordx4 a[196:199], v0, s[68:69] offset:1024 sc1
global_load_dwordx4 a[200:203], v0, s[68:69] offset:2048 sc1
global_load_dwordx4 a[204:207], v0, s[68:69] offset:3072 sc1
s_add_u32 s68, s68, 0x1000
s_addc_u32 s69, s69, 0
global_load_dwordx4 a[208:211], v0, s[68:69] offset:0 sc1
global_load_dwordx4 a[212:215], v0, s[68:69] offset:1024 sc1
global_load_dwordx4 a[216:219], v0, s[68:69] offset:2048 sc1
global_load_dwordx4 a[220:223], v0, s[68:69] offset:3072 sc1
s_add_u32 s68, s68, 0x1000
s_addc_u32 s69, s69, 0
global_load_dwordx4 a[224:227], v0, s[68:69] offset:0 sc1
global_load_dwordx4 a[228:231], v0, s[68:69] offset:1024 sc1
global_load_dwordx4 a[232:235], v0, s[68:69] offset:2048 sc1
global_load_dwordx4 a[236:239], v0, s[68:69] offset:3072 sc1
s_add_u32 s68, s68, 0x1000
s_addc_u32 s69, s69, 0
global_load_dwordx4 a[240:243], v0, s[68:69] offset:0 sc1
global_load_dwordx4 a[244:247], v0, s[68:69] offset:1024 sc1
global_load_dwordx4 a[248:251], v0, s[68:69] offset:2048 sc1
global_load_dwordx4 a[252:255], v0, s[68:69] offset:3072 sc1
s_add_u32 s68, s68, 0x1000
s_addc_u32 s69, s69, 0
v_lshrrev_b32 v9, 1, v0
global_load_dwordx2 v[242:243], v9, s[68:69] sc1
.Lk2_spc21_go:
global_load_dwordx4 v[216:219], v0, s[54:55] offset:0 sc1
global_load_dwordx4 v[220:223], v0, s[54:55] offset:1024 sc1
s_add_u32 s54, s54, 0x200000
s_addc_u32 s55, s55, 0
global_load_dwordx4 v[224:227], v0, s[54:55] offset:0 sc1
global_load_dwordx4 v[228:231], v0, s[54:55] offset:1024 sc1
global_load_dword v232, v76, s[56:57] offset:0 sc1
global_load_dword v233, v76, s[56:57] offset:64 sc1
s_add_u32 s56, s56, 0x20000
s_addc_u32 s57, s57, 0
global_load_dword v234, v76, s[56:57] offset:0 sc1
global_load_dword v235, v76, s[56:57] offset:64 sc1
s_branch .Lk2_spc21_x
.Lk2_spc21_m:
s_mov_b32 s63, -1
s_nop 1
v_cndmask_b32_e64 v44, v44, v15, s[72:73]
v_cndmask_b32_e64 v56, v56, v15, s[72:73]
v_mov_b32 v52, v15
v_cndmask_b32_e64 v45, v45, v15, s[74:75]
v_cndmask_b32_e64 v57, v57, v15, s[74:75]
v_mov_b32 v53, v15
v_cndmask_b32_e64 v46, v46, v15, s[76:77]
v_cndmask_b32_e64 v58, v58, v15, s[76:77]
v_mov_b32 v54, v15
v_cndmask_b32_e64 v47, v47, v15, s[78:79]
v_cndmask_b32_e64 v59, v59, v15, s[78:79]
v_mov_b32 v55, v15
s_cmp_eq_u32 s33, 0
s_cbranch_scc1 .Lk2_spc21_1
.Lk2_spc21_x:
s_cmp_lg_u32 s70, 0
s_cbranch_scc1 .Lk2_spc21_1
s_cmp_lg_u32 s62, 0
s_cbranch_scc1 .Lk2_spc21_x2
s_waitcnt vmcnt(24)
.Lk2_spc21_x2:
s_waitcnt vmcnt(41)
s_barrier
s_branch .Lk2_spc21_ret
.Lk2_spc21_1:
s_cmp_lg_u32 s83, 0
s_cbranch_scc1 .Lk2_spc21_4
s_cmp_eq_u32 s70, 0
s_cbranch_scc1 .Lk2_spc21_2
s_sub_u32 s70, s70, 1
s_cmp_eq_u32 s70, 0
s_cbranch_scc1 .Lk2_spc21_3
s_add_u32 s63, s66, 1
s_waitcnt vmcnt(33)
s_barrier
s_branch .Lk2_spc21_ret
.Lk2_spc21_3:
s_waitcnt vmcnt(16)
s_barrier
s_cmp_lg_u32 s51, 3
s_cbranch_scc1 .Lk2_spc21_ret
v_mov_b32 v10, s82
v_mov_b32 v9, 0
s_mov_b64 exec, 1
global_store_dword v9, v10, s[30:31] sc1
s_mov_b64 exec, -1
s_branch .Lk2_spc21_ret
.Lk2_spc21_4:
s_mov_b32 s83, 0
s_waitcnt vmcnt(16)
s_barrier
s_getpc_b64 s[68:69]
.Lf_w24:
s_add_u32 s68, s68, .Lk2_fin20-.Lf_w24
s_addc_u32 s69, s69, (.Lk2_fin20-.Lf_w24)>>32
global_load_dword v254, v12, s[68:69]
s_getpc_b64 s[68:69]
.Lf_w25:
s_add_u32 s68, s68, .Lk2_fin20+4096-.Lf_w25
s_addc_u32 s69, s69, (.Lk2_fin20+4096-.Lf_w25)>>32
global_load_dword v254, v12, s[68:69]
s_getpc_b64 s[68:69]
.Lf_w26:
s_add_u32 s68, s68, .Lk2_fin20+8192-.Lf_w26
s_addc_u32 s69, s69, (.Lk2_fin20+8192-.Lf_w26)>>32
global_load_dword v254, v12, s[68:69]
s_getpc_b64 s[68:69]
.Lf_w27:
s_add_u32 s68, s68, .Lk2_spc20-.Lf_w27
s_addc_u32 s69, s69, (.Lk2_spc20-.Lf_w27)>>32
global_load_dword v254, v12, s[68:69]
s_getpc_b64 s[68:69]
.Lf_w28:
s_add_u32 s68, s68, .Lk2_spc20+4096-.Lf_w28
s_addc_u32 s69, s69, (.Lk2_spc20+4096-.Lf_w28)>>32
global_load_dword v254, v12, s[68:69]
s_cmp_lg_u32 s51, 3
s_cbranch_scc1 .Lk2_spc21_ret
v_mov_b32 v10, s82
v_mov_b32 v9, 0
s_mov_b64 exec, 1
global_store_dword v9, v10, s[0:1] sc1
s_mov_b64 exec, -1
s_branch .Lk2_spc21_ret
.Lk2_spc21_2:
s_waitcnt vmcnt(16)
s_barrier
.Lk2_spc21_ret:
s_min_u32 s97, s63, s50
mvav 84,68,28
rqa 112,2,12288
mvav 92,68,32
s_add_u32 m0, s40, s65
mvav 96,72,28
rqa 116,2,13312
mvvv 36,16,28
mvav 104,72,32
buffer_load_dwordx4 v0, s[24:27], s43 offen offset:0 sc1 lds
mvav 100,76,28
rqa 120,2,14336
m3 8,44,45,46
mvav 108,76,32
s_add_u32 s42, s42, 0x8000
s_waitcnt lgkmcnt(7)
mvav 112,80,28
rqa 124,2,15360
m3 9,47,48,49
mvav 120,80,32
mvav 116,84,28
rqa 64,1,49152
m3 8,8,50,51
mvav 124,84,32
buffer_load_dwordx4 v0, s[24:27], s43 offen offset:1024 sc1 lds
mvav 128,88,28
rqa 68,1,50176
m3 9,9,52,53
mvav 136,88,32
s_add_u32 s64, s64, 0x4000
mvav 132,92,28
rqa 72,1,51200
m3 8,8,54,55
mvav 140,92,32
s_cmp_eq_u32 s64, 0x18000
s_cselect_b32 s64, 0, s64
s_waitcnt lgkmcnt(7)
mvav 144,96,28
rqa 76,1,52224
mvav 152,96,32
v_add_u32 v2, s64, v14
mvav 148,100,28
rqa 80,1,53248
m3 9,9,56,57
mvav 156,100,32
buffer_load_dwordx4 v0, s[24:27], s43 offen offset:2048 sc1 lds
mvav 160,104,28
rqa 84,1,54272
m3 8,8,58,59
mvav 168,104,32
s_add_u32 s66, s66, 1
mvav 164,108,28
rqa 88,1,55296
v_max_f32 v8, v8, v9
mvav 172,108,32
s_waitcnt lgkmcnt(7)
mvav 176,112,28
rqa 92,1,56320
v_cmp_lt_f32 vcc, s71, v8
mvav 184,112,32
buffer_load_dwordx4 v0, s[24:27], s43 offen offset:3072 sc1 lds
mvav 180,116,28
rqa 96,1,57344
mvav 188,116,32
s_add_u32 s43, s43, 0x8000
mvav 192,120,28
rqa 100,1,58368
s_cmp_eq_u32 s66, s97
s_cselect_b64 vcc, -1, vcc
mvav 200,120,32
s_add_u32 s65, s65, 0x4000
mvav 196,124,28
rqa 104,1,59392
mvvv 40,16,32
mvav 204,124,32
s_cmp_eq_u32 s65, 0x28000
s_cselect_b32 s65, 0x10000, s65
s_cbranch_vccnz .Lk2_ev21
s_branch .Lk2_reg22
.Lk2_spc22:
s_waitcnt lgkmcnt(3)
s_cmp_ge_u32 s42, s22
s_cselect_b32 s99, 0x29000, s99
s_add_u32 m0, s99, 32768
v_mfma_f32_16x16x32_bf16 v[60:63], a[64:67], a[128:131], v[20:23]
rqa 108,1,60416
mvaa 60,68,136
ex 44
mvaa 60,72,144
rqa 112,1,61440
mvaa 60,76,152
ex 45
mvaa 60,80,160
rqa 116,1,62464
cv 28,44,45
mvaa 60,84,168
ex 46
mvaa 60,88,176
rqa 120,1,63488
mvaa 60,92,184
ex 47
buffer_load_dwordx4 v0, s[20:23], s42 offen offset:0 sc1 lds
v_mfma_f32_16x16x32_bf16 v[64:67], a[64:67], a[132:135], v[24:27]
rqa 124,1,64512
cv 29,46,47
mvaa 64,68,140
ex 52
mvaa 64,72,148
rqa 64,2,0
mvaa 64,76,156
ex 53
mvaa 64,80,164
rqa 68,2,1024
cv 30,52,53
mvaa 64,84,172
ex 54
mvaa 64,88,180
rqa 72,2,2048
mvaa 64,92,188
ex 55
buffer_load_dwordx4 v0, s[20:23], s42 offen offset:1024 sc1 lds
s_waitcnt lgkmcnt(3)
v_mfma_f32_16x16x32_bf16 v[68:71], a[96:99], a[128:131], v[20:23]
rqa 76,2,3072
cv 31,54,55
mvaa 68,100,136
ex 48
mvaa 68,104,144
rqa 80,2,4096
mvaa 68,108,152
ex 49
mvaa 68,112,160
rqa 84,2,5120
cv 32,48,49
mvaa 68,116,168
ex 50
mvaa 68,120,176
rqa 88,2,6144
mvaa 68,124,184
ex 51
ex 59
buffer_load_dwordx4 v0, s[20:23], s42 offen offset:2048 sc1 lds
v_mfma_f32_16x16x32_bf16 v[72:75], a[96:99], a[132:135], v[24:27]
rqa 92,2,7168
cv 33,50,51
mvaa 72,100,140
ex 56
mvaa 72,104,148
rqa 96,2,8192
mvaa 72,108,156
ex 57
mvaa 72,112,164
rqa 100,2,9216
cv 34,56,57
mvaa 72,116,172
ex 58
mvaa 72,120,180
rqa 104,2,10240
cv 35,58,59
mvaa 72,124,188
buffer_load_dwordx4 v0, s[20:23], s42 offen offset:3072 sc1 lds
s_waitcnt lgkmcnt(7)
mvav 80,64,28
rqa 108,2,11264
mvav 88,64,32
s_cmp_eq_u32 s51, 0
s_cselect_b32 s67, 2, 0
s_sub_u32 s67, s52, s67
s_mov_b32 s63, s67
s_cmp_lt_i32 s66, s67
s_cbranch_scc1 .Lk2_spc22_1
s_add_u32 s63, s66, 1
s_cmp_eq_u32 s66, s52
s_cbranch_scc1 .Lk2_spc22_m
s_cmp_lg_u32 s66, s67
s_cbranch_scc1 .Lk2_spc22_a
.Lk2_spc22_E1:
global_load_dword v77, v79, s[30:31] sc1
global_load_dwordx2 v[234:235], v79, s[2:3] sc1
s_mov_b32 s32, 1
s_branch .Lk2_spc22_1
.Lk2_spc22_a:
s_cmp_eq_u32 s32, 0
s_cbranch_scc1 .Lk2_spc22_1
.Lk2_spc22_E2:
s_waitcnt vmcnt(8)
s_mov_b32 s67, 0x400000
.Lk2_spc22_f3:
v_xor_b32 v234, s82, v234
v_xor_b32 v235, s82, v235
v_or_b32 v234, v234, v235
v_cmp_ne_u32 vcc, 0, v234
s_nop 4
s_cbranch_vccz .Lk2_spc22_f3ok
s_sleep 4
global_load_dwordx2 v[234:235], v79, s[2:3] sc1
s_waitcnt vmcnt(0)
s_sub_u32 s67, s67, 1
s_cmp_lg_u32 s67, 0
s_cbranch_scc1 .Lk2_spc22_f3
v_mov_b32 v6, 0x7fc00000
.Lk2_spc22_f3ok:
s_mov_b32 s33, 1
s_cmp_eq_u32 s53, 0
s_cbranch_scc1 .Lk2_spc22_go
v_readfirstlane_b32 s62, v77
s_nop 1
s_cmp_lg_u32 s62, s82
s_cselect_b32 s62, 0, 1
s_cbranch_scc1 .Lk2_spc22_go
s_mov_b64 s[68:69], s[28:29]
global_load_dwordx4 a[192:195], v0, s[68:69] offset:0 sc1
global_load_dwordx4 a[196:199], v0, s[68:69] offset:1024 sc1
global_load_dwordx4 a[200:203], v0, s[68:69] offset:2048 sc1
global_load_dwordx4 a[204:207], v0, s[68:69] offset:3072 sc1
s_add_u32 s68, s68, 0x1000
s_addc_u32 s69, s69, 0
global_load_dwordx4 a[208:211], v0, s[68:69] offset:0 sc1
global_load_dwordx4 a[212:215], v0, s[68:69] offset:1024 sc1
global_load_dwordx4 a[216:219], v0, s[68:69] offset:2048 sc1
global_load_dwordx4 a[220:223], v0, s[68:69] offset:3072 sc1
s_add_u32 s68, s68, 0x1000
s_addc_u32 s69, s69, 0
global_load_dwordx4 a[224:227], v0, s[68:69] offset:0 sc1
global_load_dwordx4 a[228:231], v0, s[68:69] offset:1024 sc1
global_load_dwordx4 a[232:235], v0, s[68:69] offset:2048 sc1
global_load_dwordx4 a[236:239], v0, s[68:69] offset:3072 sc1
s_add_u32 s68, s68, 0x1000
s_addc_u32 s69, s69, 0
global_load_dwordx4 a[240:243], v0, s[68:69] offset:0 sc1
global_load_dwordx4 a[244:247], v0, s[68:69] offset:1024 sc1
global_load_dwordx4 a[248:251], v0, s[68:69] offset:2048 sc1
global_load_dwordx4 a[252:255], v0, s[68:69] offset:3072 sc1
s_add_u32 s68, s68, 0x1000
s_addc_u32 s69, s69, 0
v_lshrrev_b32 v9, 1, v0
global_load_dwordx2 v[242:243], v9, s[68:69] sc1
.Lk2_spc22_go:
global_load_dwordx4 v[216:219], v0, s[54:55] offset:0 sc1
global_load_dwordx4 v[220:223], v0, s[54:55] offset:1024 sc1
s_add_u32 s54, s54, 0x200000
s_addc_u32 s55, s55, 0
global_load_dwordx4 v[224:227], v0, s[54:55] offset:0 sc1
global_load_dwordx4 v[228:231], v0, s[54:55] offset:1024 sc1
global_load_dword v232, v76, s[56:57] offset:0 sc1
global_load_dword v233, v76, s[56:57] offset:64 sc1
s_add_u32 s56, s56, 0x20000
s_addc_u32 s57, s57, 0
global_load_dword v234, v76, s[56:57] offset:0 sc1
global_load_dword v235, v76, s[56:57] offset:64 sc1
s_branch .Lk2_spc22_x
.Lk2_spc22_m:
s_mov_b32 s63, -1
s_nop 1
v_cndmask_b32_e64 v60, v60, v15, s[72:73]
v_cndmask_b32_e64 v72, v72, v15, s[72:73]
v_mov_b32 v68, v15
v_cndmask_b32_e64 v61, v61, v15, s[74:75]
v_cndmask_b32_e64 v73, v73, v15, s[74:75]
v_mov_b32 v69, v15
v_cndmask_b32_e64 v62, v62, v15, s[76:77]
v_cndmask_b32_e64 v74, v74, v15, s[76:77]
v_mov_b32 v70, v15
v_cndmask_b32_e64 v63, v63, v15, s[78:79]
v_cndmask_b32_e64 v75, v75, v15, s[78:79]
v_mov_b32 v71, v15
s_cmp_eq_u32 s33, 0
s_cbranch_scc1 .Lk2_spc22_1
.Lk2_spc22_x:
s_cmp_lg_u32 s70, 0
s_cbranch_scc1 .Lk2_spc22_1
s_cmp_lg_u32 s62, 0
s_cbranch_scc1 .Lk2_spc22_x2
s_waitcnt vmcnt(24)
.Lk2_spc22_x2:
s_waitcnt vmcnt(41)
s_barrier
s_branch .Lk2_spc22_ret
.Lk2_spc22_1:
s_cmp_lg_u32 s83, 0
s_cbranch_scc1 .Lk2_spc22_4
s_cmp_eq_u32 s70, 0
s_cbranch_scc1 .Lk2_spc22_2
s_sub_u32 s70, s70, 1
s_cmp_eq_u32 s70, 0
s_cbranch_scc1 .Lk2_spc22_3
s_add_u32 s63, s66, 1
s_waitcnt vmcnt(33)
s_barrier
s_branch .Lk2_spc22_ret
.Lk2_spc22_3:
s_waitcnt vmcnt(16)
s_barrier
s_cmp_lg_u32 s51, 3
s_cbranch_scc1 .Lk2_spc22_ret
v_mov_b32 v10, s82
v_mov_b32 v9, 0
s_mov_b64 exec, 1
global_store_dword v9, v10, s[30:31] sc1
s_mov_b64 exec, -1
s_branch .Lk2_spc22_ret
.Lk2_spc22_4:
s_mov_b32 s83, 0
s_waitcnt vmcnt(16)
s_barrier
s_getpc_b64 s[68:69]
.Lf_w29:
s_add_u32 s68, s68, .Lk2_fin20-.Lf_w29
s_addc_u32 s69, s69, (.Lk2_fin20-.Lf_w29)>>32
global_load_dword v254, v12, s[68:69]
s_getpc_b64 s[68:69]
.Lf_w30:
s_add_u32 s68, s68, .Lk2_fin20+4096-.Lf_w30
s_addc_u32 s69, s69, (.Lk2_fin20+4096-.Lf_w30)>>32
global_load_dword v254, v12, s[68:69]
s_getpc_b64 s[68:69]
.Lf_w31:
s_add_u32 s68, s68, .Lk2_fin20+8192-.Lf_w31
s_addc_u32 s69, s69, (.Lk2_fin20+8192-.Lf_w31)>>32
global_load_dword v254, v12, s[68:69]
s_getpc_b64 s[68:69]
.Lf_w32:
s_add_u32 s68, s68, .Lk2_spc20-.Lf_w32
s_addc_u32 s69, s69, (.Lk2_spc20-.Lf_w32)>>32
global_load_dword v254, v12, s[68:69]
s_getpc_b64 s[68:69]
.Lf_w33:
s_add_u32 s68, s68, .Lk2_spc20+4096-.Lf_w33
s_addc_u32 s69, s69, (.Lk2_spc20+4096-.Lf_w33)>>32
global_load_dword v254, v12, s[68:69]
s_cmp_lg_u32 s51, 3
s_cbranch_scc1 .Lk2_spc22_ret
v_mov_b32 v10, s82
v_mov_b32 v9, 0
s_mov_b64 exec, 1
global_store_dword v9, v10, s[0:1] sc1
s_mov_b64 exec, -1
s_branch .Lk2_spc22_ret
.Lk2_spc22_2:
s_waitcnt vmcnt(16)
s_barrier
.Lk2_spc22_ret:
s_min_u32 s97, s63, s50
mvav 84,68,28
rqa 112,2,12288
mvav 92,68,32
s_add_u32 m0, s40, s65
mvav 96,72,28
rqa 116,2,13312
mvvv 36,16,28
mvav 104,72,32
buffer_load_dwordx4 v0, s[24:27], s43 offen offset:0 sc1 lds
mvav 100,76,28
rqa 120,2,14336
m3 8,60,61,62
mvav 108,76,32
s_add_u32 s42, s42, 0x8000
s_waitcnt lgkmcnt(7)
mvav 112,80,28
rqa 124,2,15360
m3 9,63,64,65
mvav 120,80,32
mvav 116,84,28
rqa 64,1,0
m3 8,8,66,67
mvav 124,84,32
buffer_load_dwordx4 v0, s[24:27], s43 offen offset:1024 sc1 lds
mvav 128,88,28
rqa 68,1,1024
m3 9,9,68,69
mvav 136,88,32
s_add_u32 s64, s64, 0x4000
mvav 132,92,28
rqa 72,1,2048
m3 8,8,70,71
mvav 140,92,32
s_cmp_eq_u32 s64, 0x18000
s_cselect_b32 s64, 0, s64
s_waitcnt lgkmcnt(7)
mvav 144,96,28
rqa 76,1,3072
mvav 152,96,32
v_add_u32 v2, s64, v14
mvav 148,100,28
rqa 80,1,4096
m3 9,9,72,73
mvav 156,100,32
buffer_load_dwordx4 v0, s[24:27], s43 offen offset:2048 sc1 lds
mvav 160,104,28
rqa 84,1,5120
m3 8,8,74,75
mvav 168,104,32
s_add_u32 s66, s66, 1
mvav 164,108,28
rqa 88,1,6144
v_max_f32 v8, v8, v9
mvav 172,108,32
s_waitcnt lgkmcnt(7)
mvav 176,112,28
rqa 92,1,7168
v_cmp_lt_f32 vcc, s71, v8
mvav 184,112,32
buffer_load_dwordx4 v0, s[24:27], s43 offen offset:3072 sc1 lds
mvav 180,116,28
rqa 96,1,8192
mvav 188,116,32
s_add_u32 s43, s43, 0x8000
mvav 192,120,28
rqa 100,1,9216
s_cmp_eq_u32 s66, s97
s_cselect_b64 vcc, -1, vcc
mvav 200,120,32
s_add_u32 s65, s65, 0x4000
mvav 196,124,28
rqa 104,1,10240
mvvv 40,16,32
mvav 204,124,32
s_cmp_eq_u32 s65, 0x28000
s_cselect_b32 s65, 0x10000, s65
s_cbranch_vccnz .Lk2_ev22
s_branch .Lk2_reg23
.Lk2_spc23:
s_waitcnt lgkmcnt(3)
s_cmp_ge_u32 s42, s22
s_cselect_b32 s99, 0x29000, s99
s_add_u32 m0, s99, 49152
v_mfma_f32_16x16x32_bf16 v[44:47], a[64:67], a[128:131], v[20:23]
rqa 108,1,11264
mvaa 44,68,136
ex 60
mvaa 44,72,144
rqa 112,1,12288
mvaa 44,76,152
ex 61
mvaa 44,80,160
rqa 116,1,13312
cv 28,60,61
mvaa 44,84,168
ex 62
mvaa 44,88,176
rqa 120,1,14336
mvaa 44,92,184
ex 63
buffer_load_dwordx4 v0, s[20:23], s42 offen offset:0 sc1 lds
v_mfma_f32_16x16x32_bf16 v[48:51], a[64:67], a[132:135], v[24:27]
rqa 124,1,15360
cv 29,62,63
mvaa 48,68,140
ex 68
mvaa 48,72,148
rqa 64,2,0
)ASM" ::: "memory");
  asm volatile(R"ASM(
mvaa 48,76,156
ex 69
mvaa 48,80,164
rqa 68,2,1024
cv 30,68,69
mvaa 48,84,172
ex 70
mvaa 48,88,180
rqa 72,2,2048
mvaa 48,92,188
ex 71
buffer_load_dwordx4 v0, s[20:23], s42 offen offset:1024 sc1 lds
s_waitcnt lgkmcnt(3)
v_mfma_f32_16x16x32_bf16 v[52:55], a[96:99], a[128:131], v[20:23]
rqa 76,2,3072
cv 31,70,71
mvaa 52,100,136
ex 64
mvaa 52,104,144
rqa 80,2,4096
mvaa 52,108,152
ex 65
mvaa 52,112,160
rqa 84,2,5120
cv 32,64,65
mvaa 52,116,168
ex 66
mvaa 52,120,176
rqa 88,2,6144
mvaa 52,124,184
ex 67
ex 75
buffer_load_dwordx4 v0, s[20:23], s42 offen offset:2048 sc1 lds
v_mfma_f32_16x16x32_bf16 v[56:59], a[96:99], a[132:135], v[24:27]
rqa 92,2,7168
cv 33,66,67
mvaa 56,100,140
ex 72
mvaa 56,104,148
rqa 96,2,8192
mvaa 56,108,156
ex 73
mvaa 56,112,164
rqa 100,2,9216
cv 34,72,73
mvaa 56,116,172
ex 74
mvaa 56,120,180
rqa 104,2,10240
cv 35,74,75
mvaa 56,124,188
buffer_load_dwordx4 v0, s[20:23], s42 offen offset:3072 sc1 lds
s_waitcnt lgkmcnt(7)
mvav 80,64,28
rqa 108,2,11264
mvav 88,64,32
s_cmp_eq_u32 s51, 0
s_cselect_b32 s67, 2, 0
s_sub_u32 s67, s52, s67
s_mov_b32 s63, s67
s_cmp_lt_i32 s66, s67
s_cbranch_scc1 .Lk2_spc23_1
s_add_u32 s63, s66, 1
s_cmp_eq_u32 s66, s52
s_cbranch_scc1 .Lk2_spc23_m
s_cmp_lg_u32 s66, s67
s_cbranch_scc1 .Lk2_spc23_a
.Lk2_spc23_E1:
global_load_dword v77, v79, s[30:31] sc1
global_load_dwordx2 v[234:235], v79, s[2:3] sc1
s_mov_b32 s32, 1
s_branch .Lk2_spc23_1
.Lk2_spc23_a:
s_cmp_eq_u32 s32, 0
s_cbranch_scc1 .Lk2_spc23_1
.Lk2_spc23_E2:
s_waitcnt vmcnt(8)
s_mov_b32 s67, 0x400000
.Lk2_spc23_f3:
v_xor_b32 v234, s82, v234
v_xor_b32 v235, s82, v235
v_or_b32 v234, v234, v235
v_cmp_ne_u32 vcc, 0, v234
s_nop 4
s_cbranch_vccz .Lk2_spc23_f3ok
s_sleep 4
global_load_dwordx2 v[234:235], v79, s[2:3] sc1
s_waitcnt vmcnt(0)
s_sub_u32 s67, s67, 1
s_cmp_lg_u32 s67, 0
s_cbranch_scc1 .Lk2_spc23_f3
v_mov_b32 v6, 0x7fc00000
.Lk2_spc23_f3ok:
s_mov_b32 s33, 1
s_cmp_eq_u32 s53, 0
s_cbranch_scc1 .Lk2_spc23_go
v_readfirstlane_b32 s62, v77
s_nop 1
s_cmp_lg_u32 s62, s82
s_cselect_b32 s62, 0, 1
s_cbranch_scc1 .Lk2_spc23_go
s_mov_b64 s[68:69], s[28:29]
global_load_dwordx4 a[192:195], v0, s[68:69] offset:0 sc1
global_load_dwordx4 a[196:199], v0, s[68:69] offset:1024 sc1
global_load_dwordx4 a[200:203], v0, s[68:69] offset:2048 sc1
global_load_dwordx4 a[204:207], v0, s[68:69] offset:3072 sc1
s_add_u32 s68, s68, 0x1000
s_addc_u32 s69, s69, 0
global_load_dwordx4 a[208:211], v0, s[68:69] offset:0 sc1
global_load_dwordx4 a[212:215], v0, s[68:69] offset:1024 sc1
global_load_dwordx4 a[216:219], v0, s[68:69] offset:2048 sc1
global_load_dwordx4 a[220:223], v0, s[68:69] offset:3072 sc1
s_add_u32 s68, s68, 0x1000
s_addc_u32 s69, s69, 0
global_load_dwordx4 a[224:227], v0, s[68:69] offset:0 sc1
global_load_dwordx4 a[228:231], v0, s[68:69] offset:1024 sc1
global_load_dwordx4 a[232:235], v0, s[68:69] offset:2048 sc1
global_load_dwordx4 a[236:239], v0, s[68:69] offset:3072 sc1
s_add_u32 s68, s68, 0x1000
s_addc_u32 s69, s69, 0
global_load_dwordx4 a[240:243], v0, s[68:69] offset:0 sc1
global_load_dwordx4 a[244:247], v0, s[68:69] offset:1024 sc1
global_load_dwordx4 a[248:251], v0, s[68:69] offset:2048 sc1
global_load_dwordx4 a[252:255], v0, s[68:69] offset:3072 sc1
s_add_u32 s68, s68, 0x1000
s_addc_u32 s69, s69, 0
v_lshrrev_b32 v9, 1, v0
global_load_dwordx2 v[242:243], v9, s[68:69] sc1
.Lk2_spc23_go:
global_load_dwordx4 v[216:219], v0, s[54:55] offset:0 sc1
global_load_dwordx4 v[220:223], v0, s[54:55] offset:1024 sc1
s_add_u32 s54, s54, 0x200000
s_addc_u32 s55, s55, 0
global_load_dwordx4 v[224:227], v0, s[54:55] offset:0 sc1
global_load_dwordx4 v[228:231], v0, s[54:55] offset:1024 sc1
global_load_dword v232, v76, s[56:57] offset:0 sc1
global_load_dword v233, v76, s[56:57] offset:64 sc1
s_add_u32 s56, s56, 0x20000
s_addc_u32 s57, s57, 0
global_load_dword v234, v76, s[56:57] offset:0 sc1
global_load_dword v235, v76, s[56:57] offset:64 sc1
s_branch .Lk2_spc23_x
.Lk2_spc23_m:
s_mov_b32 s63, -1
s_nop 1
v_cndmask_b32_e64 v44, v44, v15, s[72:73]
v_cndmask_b32_e64 v56, v56, v15, s[72:73]
v_mov_b32 v52, v15
v_cndmask_b32_e64 v45, v45, v15, s[74:75]
v_cndmask_b32_e64 v57, v57, v15, s[74:75]
v_mov_b32 v53, v15
v_cndmask_b32_e64 v46, v46, v15, s[76:77]
v_cndmask_b32_e64 v58, v58, v15, s[76:77]
v_mov_b32 v54, v15
v_cndmask_b32_e64 v47, v47, v15, s[78:79]
v_cndmask_b32_e64 v59, v59, v15, s[78:79]
v_mov_b32 v55, v15
s_cmp_eq_u32 s33, 0
s_cbranch_scc1 .Lk2_spc23_1
.Lk2_spc23_x:
s_cmp_lg_u32 s70, 0
s_cbranch_scc1 .Lk2_spc23_1
s_cmp_lg_u32 s62, 0
s_cbranch_scc1 .Lk2_spc23_x2
s_waitcnt vmcnt(24)
.Lk2_spc23_x2:
s_waitcnt vmcnt(41)
s_barrier
s_branch .Lk2_spc23_ret
.Lk2_spc23_1:
s_cmp_lg_u32 s83, 0
s_cbranch_scc1 .Lk2_spc23_4
s_cmp_eq_u32 s70, 0
s_cbranch_scc1 .Lk2_spc23_2
s_sub_u32 s70, s70, 1
s_cmp_eq_u32 s70, 0
s_cbranch_scc1 .Lk2_spc23_3
s_add_u32 s63, s66, 1
s_waitcnt vmcnt(33)
s_barrier
s_branch .Lk2_spc23_ret
.Lk2_spc23_3:
s_waitcnt vmcnt(16)
s_barrier
s_cmp_lg_u32 s51, 3
s_cbranch_scc1 .Lk2_spc23_ret
v_mov_b32 v10, s82
v_mov_b32 v9, 0
s_mov_b64 exec, 1
global_store_dword v9, v10, s[30:31] sc1
s_mov_b64 exec, -1
s_branch .Lk2_spc23_ret
.Lk2_spc23_4:
s_mov_b32 s83, 0
s_waitcnt vmcnt(16)
s_barrier
s_getpc_b64 s[68:69]
.Lf_w34:
s_add_u32 s68, s68, .Lk2_fin20-.Lf_w34
s_addc_u32 s69, s69, (.Lk2_fin20-.Lf_w34)>>32
global_load_dword v254, v12, s[68:69]
s_getpc_b64 s[68:69]
.Lf_w35:
s_add_u32 s68, s68, .Lk2_fin20+4096-.Lf_w35
s_addc_u32 s69, s69, (.Lk2_fin20+4096-.Lf_w35)>>32
global_load_dword v254, v12, s[68:69]
s_getpc_b64 s[68:69]
.Lf_w36:
s_add_u32 s68, s68, .Lk2_fin20+8192-.Lf_w36
s_addc_u32 s69, s69, (.Lk2_fin20+8192-.Lf_w36)>>32
global_load_dword v254, v12, s[68:69]
s_getpc_b64 s[68:69]
.Lf_w37:
s_add_u32 s68, s68, .Lk2_spc20-.Lf_w37
s_addc_u32 s69, s69, (.Lk2_spc20-.Lf_w37)>>32
global_load_dword v254, v12, s[68:69]
s_getpc_b64 s[68:69]
.Lf_w38:
s_add_u32 s68, s68, .Lk2_spc20+4096-.Lf_w38
s_addc_u32 s69, s69, (.Lk2_spc20+4096-.Lf_w38)>>32
global_load_dword v254, v12, s[68:69]
s_cmp_lg_u32 s51, 3
s_cbranch_scc1 .Lk2_spc23_ret
v_mov_b32 v10, s82
v_mov_b32 v9, 0
s_mov_b64 exec, 1
global_store_dword v9, v10, s[0:1] sc1
s_mov_b64 exec, -1
s_branch .Lk2_spc23_ret
.Lk2_spc23_2:
s_waitcnt vmcnt(16)
s_barrier
.Lk2_spc23_ret:
s_min_u32 s97, s63, s50
mvav 84,68,28
rqa 112,2,12288
mvav 92,68,32
s_add_u32 m0, s40, s65
mvav 96,72,28
rqa 116,2,13312
mvvv 36,16,28
mvav 104,72,32
buffer_load_dwordx4 v0, s[24:27], s43 offen offset:0 sc1 lds
mvav 100,76,28
rqa 120,2,14336
m3 8,44,45,46
mvav 108,76,32
s_add_u32 s42, s42, 0x8000
s_waitcnt lgkmcnt(7)
mvav 112,80,28
rqa 124,2,15360
m3 9,47,48,49
mvav 120,80,32
mvav 116,84,28
rqa 64,1,16384
m3 8,8,50,51
mvav 124,84,32
buffer_load_dwordx4 v0, s[24:27], s43 offen offset:1024 sc1 lds
mvav 128,88,28
rqa 68,1,17408
m3 9,9,52,53
mvav 136,88,32
s_add_u32 s64, s64, 0x4000
mvav 132,92,28
rqa 72,1,18432
m3 8,8,54,55
mvav 140,92,32
s_cmp_eq_u32 s64, 0x18000
s_cselect_b32 s64, 0, s64
s_waitcnt lgkmcnt(7)
mvav 144,96,28
rqa 76,1,19456
mvav 152,96,32
v_add_u32 v2, s64, v14
mvav 148,100,28
rqa 80,1,20480
m3 9,9,56,57
mvav 156,100,32
buffer_load_dwordx4 v0, s[24:27], s43 offen offset:2048 sc1 lds
mvav 160,104,28
rqa 84,1,21504
m3 8,8,58,59
mvav 168,104,32
s_add_u32 s66, s66, 1
mvav 164,108,28
rqa 88,1,22528
v_max_f32 v8, v8, v9
mvav 172,108,32
s_waitcnt lgkmcnt(7)
mvav 176,112,28
rqa 92,1,23552
v_cmp_lt_f32 vcc, s71, v8
mvav 184,112,32
buffer_load_dwordx4 v0, s[24:27], s43 offen offset:3072 sc1 lds
mvav 180,116,28
rqa 96,1,24576
mvav 188,116,32
s_add_u32 s43, s43, 0x8000
mvav 192,120,28
rqa 100,1,25600
s_cmp_eq_u32 s66, s97
s_cselect_b64 vcc, -1, vcc
mvav 200,120,32
s_add_u32 s65, s65, 0x4000
mvav 196,124,28
rqa 104,1,26624
mvvv 40,16,32
mvav 204,124,32
s_cmp_eq_u32 s65, 0x28000
s_cselect_b32 s65, 0x10000, s65
s_cbranch_vccnz .Lk2_ev23
s_branch .Lk2_reg20
.Lk2_reg20:
s_waitcnt lgkmcnt(3)
s_cmp_ge_u32 s42, s22
s_cselect_b32 s99, 0x29000, s99
s_add_u32 m0, s99, 0
v_mfma_f32_16x16x32_bf16 v[60:63], a[64:67], a[128:131], v[20:23]
rqa 108,1,27648
mvaa 60,68,136
ex 44
mvaa 60,72,144
rqa 112,1,28672
mvaa 60,76,152
ex 45
mvaa 60,80,160
rqa 116,1,29696
cv 28,44,45
mvaa 60,84,168
ex 46
mvaa 60,88,176
rqa 120,1,30720
mvaa 60,92,184
ex 47
buffer_load_dwordx4 v0, s[20:23], s42 offen offset:0 sc1 lds
v_mfma_f32_16x16x32_bf16 v[64:67], a[64:67], a[132:135], v[24:27]
rqa 124,1,31744
cv 29,46,47
mvaa 64,68,140
ex 52
mvaa 64,72,148
rqa 64,2,0
mvaa 64,76,156
ex 53
mvaa 64,80,164
rqa 68,2,1024
cv 30,52,53
mvaa 64,84,172
ex 54
mvaa 64,88,180
rqa 72,2,2048
mvaa 64,92,188
ex 55
buffer_load_dwordx4 v0, s[20:23], s42 offen offset:1024 sc1 lds
s_waitcnt lgkmcnt(3)
v_mfma_f32_16x16x32_bf16 v[68:71], a[96:99], a[128:131], v[20:23]
rqa 76,2,3072
cv 31,54,55
mvaa 68,100,136
ex 48
mvaa 68,104,144
rqa 80,2,4096
mvaa 68,108,152
ex 49
mvaa 68,112,160
rqa 84,2,5120
cv 32,48,49
mvaa 68,116,168
ex 50
mvaa 68,120,176
rqa 88,2,6144
mvaa 68,124,184
ex 51
ex 59
buffer_load_dwordx4 v0, s[20:23], s42 offen offset:2048 sc1 lds
v_mfma_f32_16x16x32_bf16 v[72:75], a[96:99], a[132:135], v[24:27]
rqa 92,2,7168
cv 33,50,51
mvaa 72,100,140
ex 56
mvaa 72,104,148
rqa 96,2,8192
mvaa 72,108,156
ex 57
mvaa 72,112,164
rqa 100,2,9216
cv 34,56,57
mvaa 72,116,172
ex 58
mvaa 72,120,180
rqa 104,2,10240
cv 35,58,59
mvaa 72,124,188
buffer_load_dwordx4 v0, s[20:23], s42 offen offset:3072 sc1 lds
s_waitcnt lgkmcnt(7)
mvav 80,64,28
rqa 108,2,11264
mvav 88,64,32
s_waitcnt vmcnt(16)
s_barrier
mvav 84,68,28
rqa 112,2,12288
mvav 92,68,32
s_add_u32 m0, s40, s65
mvav 96,72,28
rqa 116,2,13312
mvvv 36,16,28
mvav 104,72,32
buffer_load_dwordx4 v0, s[24:27], s43 offen offset:0 sc1 lds
mvav 100,76,28
rqa 120,2,14336
m3 8,60,61,62
mvav 108,76,32
s_add_u32 s42, s42, 0x8000
s_waitcnt lgkmcnt(7)
mvav 112,80,28
rqa 124,2,15360
m3 9,63,64,65
mvav 120,80,32
mvav 116,84,28
rqa 64,1,32768
m3 8,8,66,67
mvav 124,84,32
buffer_load_dwordx4 v0, s[24:27], s43 offen offset:1024 sc1 lds
mvav 128,88,28
rqa 68,1,33792
m3 9,9,68,69
mvav 136,88,32
s_add_u32 s64, s64, 0x4000
mvav 132,92,28
rqa 72,1,34816
m3 8,8,70,71
mvav 140,92,32
s_cmp_eq_u32 s64, 0x18000
s_cselect_b32 s64, 0, s64
s_waitcnt lgkmcnt(7)
mvav 144,96,28
rqa 76,1,35840
mvav 152,96,32
v_add_u32 v2, s64, v14
mvav 148,100,28
rqa 80,1,36864
m3 9,9,72,73
mvav 156,100,32
buffer_load_dwordx4 v0, s[24:27], s43 offen offset:2048 sc1 lds
mvav 160,104,28
rqa 84,1,37888
m3 8,8,74,75
mvav 168,104,32
s_add_u32 s66, s66, 1
mvav 164,108,28
rqa 88,1,38912
v_max_f32 v8, v8, v9
mvav 172,108,32
s_waitcnt lgkmcnt(7)
mvav 176,112,28
rqa 92,1,39936
v_cmp_lt_f32 vcc, s71, v8
mvav 184,112,32
buffer_load_dwordx4 v0, s[24:27], s43 offen offset:3072 sc1 lds
mvav 180,116,28
rqa 96,1,40960
mvav 188,116,32
s_add_u32 s43, s43, 0x8000
mvav 192,120,28
rqa 100,1,41984
s_cmp_eq_u32 s66, s97
s_cselect_b64 vcc, -1, vcc
mvav 200,120,32
s_add_u32 s65, s65, 0x4000
mvav 196,124,28
rqa 104,1,43008
mvvv 40,16,32
mvav 204,124,32
s_cmp_eq_u32 s65, 0x28000
s_cselect_b32 s65, 0x10000, s65
s_cbranch_vccnz .Lk2_ev20
.Lk2_reg21:
s_waitcnt lgkmcnt(3)
s_cmp_ge_u32 s42, s22
s_cselect_b32 s99, 0x29000, s99
s_add_u32 m0, s99, 16384
v_mfma_f32_16x16x32_bf16 v[44:47], a[64:67], a[128:131], v[20:23]
rqa 108,1,44032
mvaa 44,68,136
ex 60
mvaa 44,72,144
rqa 112,1,45056
mvaa 44,76,152
ex 61
mvaa 44,80,160
rqa 116,1,46080
cv 28,60,61
mvaa 44,84,168
ex 62
mvaa 44,88,176
rqa 120,1,47104
mvaa 44,92,184
ex 63
buffer_load_dwordx4 v0, s[20:23], s42 offen offset:0 sc1 lds
v_mfma_f32_16x16x32_bf16 v[48:51], a[64:67], a[132:135], v[24:27]
rqa 124,1,48128
cv 29,62,63
mvaa 48,68,140
ex 68
mvaa 48,72,148
rqa 64,2,0
mvaa 48,76,156
ex 69
mvaa 48,80,164
rqa 68,2,1024
cv 30,68,69
mvaa 48,84,172
ex 70
mvaa 48,88,180
rqa 72,2,2048
mvaa 48,92,188
ex 71
buffer_load_dwordx4 v0, s[20:23], s42 offen offset:1024 sc1 lds
s_waitcnt lgkmcnt(3)
v_mfma_f32_16x16x32_bf16 v[52:55], a[96:99], a[128:131], v[20:23]
rqa 76,2,3072
cv 31,70,71
mvaa 52,100,136
ex 64
mvaa 52,104,144
rqa 80,2,4096
mvaa 52,108,152
ex 65
mvaa 52,112,160
rqa 84,2,5120
cv 32,64,65
mvaa 52,116,168
ex 66
mvaa 52,120,176
rqa 88,2,6144
mvaa 52,124,184
ex 67
ex 75
buffer_load_dwordx4 v0, s[20:23], s42 offen offset:2048 sc1 lds
v_mfma_f32_16x16x32_bf16 v[56:59], a[96:99], a[132:135], v[24:27]
rqa 92,2,7168
cv 33,66,67
mvaa 56,100,140
ex 72
mvaa 56,104,148
rqa 96,2,8192
mvaa 56,108,156
ex 73
mvaa 56,112,164
rqa 100,2,9216
cv 34,72,73
mvaa 56,116,172
ex 74
mvaa 56,120,180
rqa 104,2,10240
cv 35,74,75
mvaa 56,124,188
buffer_load_dwordx4 v0, s[20:23], s42 offen offset:3072 sc1 lds
s_waitcnt lgkmcnt(7)
mvav 80,64,28
rqa 108,2,11264
mvav 88,64,32
s_waitcnt vmcnt(16)
s_barrier
mvav 84,68,28
rqa 112,2,12288
mvav 92,68,32
s_add_u32 m0, s40, s65
mvav 96,72,28
rqa 116,2,13312
mvvv 36,16,28
mvav 104,72,32
buffer_load_dwordx4 v0, s[24:27], s43 offen offset:0 sc1 lds
mvav 100,76,28
rqa 120,2,14336
m3 8,44,45,46
mvav 108,76,32
s_add_u32 s42, s42, 0x8000
s_waitcnt lgkmcnt(7)
mvav 112,80,28
rqa 124,2,15360
m3 9,47,48,49
mvav 120,80,32
mvav 116,84,28
rqa 64,1,49152
m3 8,8,50,51
mvav 124,84,32
buffer_load_dwordx4 v0, s[24:27], s43 offen offset:1024 sc1 lds
mvav 128,88,28
rqa 68,1,50176
m3 9,9,52,53
mvav 136,88,32
s_add_u32 s64, s64, 0x4000
mvav 132,92,28
rqa 72,1,51200
m3 8,8,54,55
mvav 140,92,32
s_cmp_eq_u32 s64, 0x18000
s_cselect_b32 s64, 0, s64
)ASM" ::: "memory");
  asm volatile(R"ASM(
s_waitcnt lgkmcnt(7)
mvav 144,96,28
rqa 76,1,52224
mvav 152,96,32
v_add_u32 v2, s64, v14
mvav 148,100,28
rqa 80,1,53248
m3 9,9,56,57
mvav 156,100,32
buffer_load_dwordx4 v0, s[24:27], s43 offen offset:2048 sc1 lds
mvav 160,104,28
rqa 84,1,54272
m3 8,8,58,59
mvav 168,104,32
s_add_u32 s66, s66, 1
mvav 164,108,28
rqa 88,1,55296
v_max_f32 v8, v8, v9
mvav 172,108,32
s_waitcnt lgkmcnt(7)
mvav 176,112,28
rqa 92,1,56320
v_cmp_lt_f32 vcc, s71, v8
mvav 184,112,32
buffer_load_dwordx4 v0, s[24:27], s43 offen offset:3072 sc1 lds
mvav 180,116,28
rqa 96,1,57344
mvav 188,116,32
s_add_u32 s43, s43, 0x8000
mvav 192,120,28
rqa 100,1,58368
s_cmp_eq_u32 s66, s97
s_cselect_b64 vcc, -1, vcc
mvav 200,120,32
s_add_u32 s65, s65, 0x4000
mvav 196,124,28
rqa 104,1,59392
mvvv 40,16,32
mvav 204,124,32
s_cmp_eq_u32 s65, 0x28000
s_cselect_b32 s65, 0x10000, s65
s_cbranch_vccnz .Lk2_ev21
.Lk2_reg22:
s_waitcnt lgkmcnt(3)
s_cmp_ge_u32 s42, s22
s_cselect_b32 s99, 0x29000, s99
s_add_u32 m0, s99, 32768
v_mfma_f32_16x16x32_bf16 v[60:63], a[64:67], a[128:131], v[20:23]
rqa 108,1,60416
mvaa 60,68,136
ex 44
mvaa 60,72,144
rqa 112,1,61440
mvaa 60,76,152
ex 45
mvaa 60,80,160
rqa 116,1,62464
cv 28,44,45
mvaa 60,84,168
ex 46
mvaa 60,88,176
rqa 120,1,63488
mvaa 60,92,184
ex 47
buffer_load_dwordx4 v0, s[20:23], s42 offen offset:0 sc1 lds
v_mfma_f32_16x16x32_bf16 v[64:67], a[64:67], a[132:135], v[24:27]
rqa 124,1,64512
cv 29,46,47
mvaa 64,68,140
ex 52
mvaa 64,72,148
rqa 64,2,0
mvaa 64,76,156
ex 53
mvaa 64,80,164
rqa 68,2,1024
cv 30,52,53
mvaa 64,84,172
ex 54
mvaa 64,88,180
rqa 72,2,2048
mvaa 64,92,188
ex 55
buffer_load_dwordx4 v0, s[20:23], s42 offen offset:1024 sc1 lds
s_waitcnt lgkmcnt(3)
v_mfma_f32_16x16x32_bf16 v[68:71], a[96:99], a[128:131], v[20:23]
rqa 76,2,3072
cv 31,54,55
mvaa 68,100,136
ex 48
mvaa 68,104,144
rqa 80,2,4096
mvaa 68,108,152
ex 49
mvaa 68,112,160
rqa 84,2,5120
cv 32,48,49
mvaa 68,116,168
ex 50
mvaa 68,120,176
rqa 88,2,6144
mvaa 68,124,184
ex 51
ex 59
buffer_load_dwordx4 v0, s[20:23], s42 offen offset:2048 sc1 lds
v_mfma_f32_16x16x32_bf16 v[72:75], a[96:99], a[132:135], v[24:27]
rqa 92,2,7168
cv 33,50,51
mvaa 72,100,140
ex 56
mvaa 72,104,148
rqa 96,2,8192
mvaa 72,108,156
ex 57
mvaa 72,112,164
rqa 100,2,9216
cv 34,56,57
mvaa 72,116,172
ex 58
mvaa 72,120,180
rqa 104,2,10240
cv 35,58,59
mvaa 72,124,188
buffer_load_dwordx4 v0, s[20:23], s42 offen offset:3072 sc1 lds
s_waitcnt lgkmcnt(7)
mvav 80,64,28
rqa 108,2,11264
mvav 88,64,32
s_waitcnt vmcnt(16)
s_barrier
mvav 84,68,28
rqa 112,2,12288
mvav 92,68,32
s_add_u32 m0, s40, s65
mvav 96,72,28
rqa 116,2,13312
mvvv 36,16,28
mvav 104,72,32
buffer_load_dwordx4 v0, s[24:27], s43 offen offset:0 sc1 lds
mvav 100,76,28
rqa 120,2,14336
m3 8,60,61,62
mvav 108,76,32
s_add_u32 s42, s42, 0x8000
s_waitcnt lgkmcnt(7)
mvav 112,80,28
rqa 124,2,15360
m3 9,63,64,65
mvav 120,80,32
mvav 116,84,28
rqa 64,1,0
m3 8,8,66,67
mvav 124,84,32
buffer_load_dwordx4 v0, s[24:27], s43 offen offset:1024 sc1 lds
mvav 128,88,28
rqa 68,1,1024
m3 9,9,68,69
mvav 136,88,32
s_add_u32 s64, s64, 0x4000
mvav 132,92,28
rqa 72,1,2048
m3 8,8,70,71
mvav 140,92,32
s_cmp_eq_u32 s64, 0x18000
s_cselect_b32 s64, 0, s64
s_waitcnt lgkmcnt(7)
mvav 144,96,28
rqa 76,1,3072
mvav 152,96,32
v_add_u32 v2, s64, v14
mvav 148,100,28
rqa 80,1,4096
m3 9,9,72,73
mvav 156,100,32
buffer_load_dwordx4 v0, s[24:27], s43 offen offset:2048 sc1 lds
mvav 160,104,28
rqa 84,1,5120
m3 8,8,74,75
mvav 168,104,32
s_add_u32 s66, s66, 1
mvav 164,108,28
rqa 88,1,6144
v_max_f32 v8, v8, v9
mvav 172,108,32
s_waitcnt lgkmcnt(7)
mvav 176,112,28
rqa 92,1,7168
v_cmp_lt_f32 vcc, s71, v8
mvav 184,112,32
buffer_load_dwordx4 v0, s[24:27], s43 offen offset:3072 sc1 lds
mvav 180,116,28
rqa 96,1,8192
mvav 188,116,32
s_add_u32 s43, s43, 0x8000
mvav 192,120,28
rqa 100,1,9216
s_cmp_eq_u32 s66, s97
s_cselect_b64 vcc, -1, vcc
mvav 200,120,32
s_add_u32 s65, s65, 0x4000
mvav 196,124,28
rqa 104,1,10240
mvvv 40,16,32
mvav 204,124,32
s_cmp_eq_u32 s65, 0x28000
s_cselect_b32 s65, 0x10000, s65
s_cbranch_vccnz .Lk2_ev22
.Lk2_reg23:
s_waitcnt lgkmcnt(3)
s_cmp_ge_u32 s42, s22
s_cselect_b32 s99, 0x29000, s99
s_add_u32 m0, s99, 49152
v_mfma_f32_16x16x32_bf16 v[44:47], a[64:67], a[128:131], v[20:23]
rqa 108,1,11264
mvaa 44,68,136
ex 60
mvaa 44,72,144
rqa 112,1,12288
mvaa 44,76,152
ex 61
mvaa 44,80,160
rqa 116,1,13312
cv 28,60,61
mvaa 44,84,168
ex 62
mvaa 44,88,176
rqa 120,1,14336
mvaa 44,92,184
ex 63
buffer_load_dwordx4 v0, s[20:23], s42 offen offset:0 sc1 lds
v_mfma_f32_16x16x32_bf16 v[48:51], a[64:67], a[132:135], v[24:27]
rqa 124,1,15360
cv 29,62,63
mvaa 48,68,140
ex 68
mvaa 48,72,148
rqa 64,2,0
mvaa 48,76,156
ex 69
mvaa 48,80,164
rqa 68,2,1024
cv 30,68,69
mvaa 48,84,172
ex 70
mvaa 48,88,180
rqa 72,2,2048
mvaa 48,92,188
ex 71
buffer_load_dwordx4 v0, s[20:23], s42 offen offset:1024 sc1 lds
s_waitcnt lgkmcnt(3)
v_mfma_f32_16x16x32_bf16 v[52:55], a[96:99], a[128:131], v[20:23]
rqa 76,2,3072
cv 31,70,71
mvaa 52,100,136
ex 64
mvaa 52,104,144
rqa 80,2,4096
mvaa 52,108,152
ex 65
mvaa 52,112,160
rqa 84,2,5120
cv 32,64,65
mvaa 52,116,168
ex 66
mvaa 52,120,176
rqa 88,2,6144
mvaa 52,124,184
ex 67
ex 75
buffer_load_dwordx4 v0, s[20:23], s42 offen offset:2048 sc1 lds
v_mfma_f32_16x16x32_bf16 v[56:59], a[96:99], a[132:135], v[24:27]
rqa 92,2,7168
cv 33,66,67
mvaa 56,100,140
ex 72
mvaa 56,104,148
rqa 96,2,8192
mvaa 56,108,156
ex 73
mvaa 56,112,164
rqa 100,2,9216
cv 34,72,73
mvaa 56,116,172
ex 74
mvaa 56,120,180
rqa 104,2,10240
cv 35,74,75
mvaa 56,124,188
buffer_load_dwordx4 v0, s[20:23], s42 offen offset:3072 sc1 lds
s_waitcnt lgkmcnt(7)
mvav 80,64,28
rqa 108,2,11264
mvav 88,64,32
s_waitcnt vmcnt(16)
s_barrier
mvav 84,68,28
rqa 112,2,12288
mvav 92,68,32
s_add_u32 m0, s40, s65
mvav 96,72,28
rqa 116,2,13312
mvvv 36,16,28
mvav 104,72,32
buffer_load_dwordx4 v0, s[24:27], s43 offen offset:0 sc1 lds
mvav 100,76,28
rqa 120,2,14336
m3 8,44,45,46
mvav 108,76,32
s_add_u32 s42, s42, 0x8000
s_waitcnt lgkmcnt(7)
mvav 112,80,28
rqa 124,2,15360
m3 9,47,48,49
mvav 120,80,32
mvav 116,84,28
rqa 64,1,16384
m3 8,8,50,51
mvav 124,84,32
buffer_load_dwordx4 v0, s[24:27], s43 offen offset:1024 sc1 lds
mvav 128,88,28
rqa 68,1,17408
m3 9,9,52,53
mvav 136,88,32
s_add_u32 s64, s64, 0x4000
mvav 132,92,28
rqa 72,1,18432
m3 8,8,54,55
mvav 140,92,32
s_cmp_eq_u32 s64, 0x18000
s_cselect_b32 s64, 0, s64
s_waitcnt lgkmcnt(7)
mvav 144,96,28
rqa 76,1,19456
mvav 152,96,32
v_add_u32 v2, s64, v14
mvav 148,100,28
rqa 80,1,20480
m3 9,9,56,57
mvav 156,100,32
buffer_load_dwordx4 v0, s[24:27], s43 offen offset:2048 sc1 lds
mvav 160,104,28
rqa 84,1,21504
m3 8,8,58,59
mvav 168,104,32
s_add_u32 s66, s66, 1
mvav 164,108,28
rqa 88,1,22528
v_max_f32 v8, v8, v9
mvav 172,108,32
s_waitcnt lgkmcnt(7)
mvav 176,112,28
rqa 92,1,23552
v_cmp_lt_f32 vcc, s71, v8
mvav 184,112,32
buffer_load_dwordx4 v0, s[24:27], s43 offen offset:3072 sc1 lds
mvav 180,116,28
rqa 96,1,24576
mvav 188,116,32
s_add_u32 s43, s43, 0x8000
mvav 192,120,28
rqa 100,1,25600
s_cmp_eq_u32 s66, s97
s_cselect_b64 vcc, -1, vcc
mvav 200,120,32
s_add_u32 s65, s65, 0x4000
mvav 196,124,28
rqa 104,1,26624
mvvv 40,16,32
mvav 204,124,32
s_cmp_eq_u32 s65, 0x28000
s_cselect_b32 s65, 0x10000, s65
s_cbranch_vccnz .Lk2_ev23
s_branch .Lk2_reg20
.Lk2_fin20:
.Lk2_fin22:
s_cmp_lg_u32 s51, 0
s_cbranch_scc1 .Lk2_fin0_g
s_cmp_eq_u32 s53, 0
s_cbranch_scc1 .Lk2_fin0_fB
s_cmp_lg_u32 s62, 0
s_cbranch_scc1 .Lk2_fin0_fA
s_add_u32 m0, s96, 0
s_nop 0
dm 244,88,8
s_add_u32 m0, s96, 1024
s_nop 0
dm 245,88,8
s_add_u32 m0, s96, 2048
s_nop 0
dm 246,88,8
s_add_u32 m0, s96, 3072
s_nop 0
dm 247,88,8
s_add_u32 m0, s96, 4096
s_nop 0
dm 248,88,8
s_add_u32 m0, s96, 5120
s_nop 0
dm 249,88,8
s_add_u32 m0, s96, 6144
s_nop 0
dm 250,88,8
s_add_u32 m0, s96, 28672
s_nop 0
dm 244,88,9
s_add_u32 m0, s96, 29696
s_nop 0
dm 245,88,9
s_add_u32 m0, s96, 30720
s_nop 0
dm 246,88,9
s_add_u32 m0, s96, 31744
s_nop 0
dm 247,88,9
s_add_u32 m0, s96, 32768
s_nop 0
dm 248,88,9
s_add_u32 m0, s96, 33792
s_nop 0
dm 249,88,9
s_add_u32 m0, s96, 34816
s_nop 0
dm 250,88,9
s_branch .Lk2_fin0_go
.Lk2_fin0_g:
global_load_dword v77, v79, s[30:31] sc1
global_load_dwordx2 v[234:235], v79, s[2:3] sc1
s_waitcnt vmcnt(14)
s_barrier
s_cmp_lg_u32 s51, 1
s_cbranch_scc1 .Lk2_fin0_go
.Lk2_fin0_F1:
s_waitcnt vmcnt(0)
s_mov_b32 s67, 0x400000
.Lk2_fin0x_f3:
v_xor_b32 v234, s82, v234
v_xor_b32 v235, s82, v235
v_or_b32 v234, v234, v235
v_cmp_ne_u32 vcc, 0, v234
s_nop 4
s_cbranch_vccz .Lk2_fin0x_f3ok
s_sleep 4
global_load_dwordx2 v[234:235], v79, s[2:3] sc1
s_waitcnt vmcnt(0)
s_sub_u32 s67, s67, 1
s_cmp_lg_u32 s67, 0
s_cbranch_scc1 .Lk2_fin0x_f3
v_mov_b32 v6, 0x7fc00000
.Lk2_fin0x_f3ok:
s_cmp_eq_u32 s53, 0
s_cbranch_scc1 .Lk2_fin0x_np
v_readfirstlane_b32 s62, v77
s_nop 1
s_cmp_lg_u32 s62, s82
s_cselect_b32 s62, 0, 1
s_cbranch_scc1 .Lk2_fin0x_np
s_mov_b64 s[68:69], s[28:29]
global_load_dwordx4 a[192:195], v0, s[68:69] offset:0 sc1
global_load_dwordx4 a[196:199], v0, s[68:69] offset:1024 sc1
global_load_dwordx4 a[200:203], v0, s[68:69] offset:2048 sc1
global_load_dwordx4 a[204:207], v0, s[68:69] offset:3072 sc1
s_add_u32 s68, s68, 0x1000
s_addc_u32 s69, s69, 0
global_load_dwordx4 a[208:211], v0, s[68:69] offset:0 sc1
global_load_dwordx4 a[212:215], v0, s[68:69] offset:1024 sc1
global_load_dwordx4 a[216:219], v0, s[68:69] offset:2048 sc1
global_load_dwordx4 a[220:223], v0, s[68:69] offset:3072 sc1
s_add_u32 s68, s68, 0x1000
s_addc_u32 s69, s69, 0
global_load_dwordx4 a[224:227], v0, s[68:69] offset:0 sc1
global_load_dwordx4 a[228:231], v0, s[68:69] offset:1024 sc1
global_load_dwordx4 a[232:235], v0, s[68:69] offset:2048 sc1
global_load_dwordx4 a[236:239], v0, s[68:69] offset:3072 sc1
s_add_u32 s68, s68, 0x1000
s_addc_u32 s69, s69, 0
global_load_dwordx4 a[240:243], v0, s[68:69] offset:0 sc1
global_load_dwordx4 a[244:247], v0, s[68:69] offset:1024 sc1
global_load_dwordx4 a[248:251], v0, s[68:69] offset:2048 sc1
global_load_dwordx4 a[252:255], v0, s[68:69] offset:3072 sc1
s_add_u32 s68, s68, 0x1000
s_addc_u32 s69, s69, 0
v_lshrrev_b32 v9, 1, v0
global_load_dwordx2 v[242:243], v9, s[68:69] sc1
.Lk2_fin0x_np:
global_load_dwordx4 v[216:219], v0, s[54:55] offset:0 sc1
global_load_dwordx4 v[220:223], v0, s[54:55] offset:1024 sc1
s_add_u32 s54, s54, 0x200000
s_addc_u32 s55, s55, 0
global_load_dwordx4 v[224:227], v0, s[54:55] offset:0 sc1
global_load_dwordx4 v[228:231], v0, s[54:55] offset:1024 sc1
global_load_dword v232, v76, s[56:57] offset:0 sc1
global_load_dword v233, v76, s[56:57] offset:64 sc1
s_add_u32 s56, s56, 0x20000
s_addc_u32 s57, s57, 0
global_load_dword v234, v76, s[56:57] offset:0 sc1
global_load_dword v235, v76, s[56:57] offset:64 sc1
.Lk2_fin0_go:
s_waitcnt lgkmcnt(0)
rqa 64,2,0
rqa 68,2,1024
rqa 72,2,2048
rqa 76,2,3072
rqa 80,2,4096
rqa 84,2,5120
rqa 88,2,6144
rqa 92,2,7168
rqa 96,2,8192
rqa 100,2,9216
rqa 104,2,10240
rqa 108,2,11264
rqa 112,2,12288
rqa 116,2,13312
rqa 120,2,14336
rqa 124,2,15360
ex 44
ex 45
ex 46
ex 47
ex 52
ex 53
ex 54
ex 55
ex 48
ex 49
ex 50
ex 51
ex 56
ex 57
ex 58
ex 59
cv 28,44,45
cv 29,46,47
cv 30,52,53
cv 31,54,55
cv 32,48,49
cv 33,50,51
cv 34,56,57
cv 35,58,59
s_waitcnt lgkmcnt(8)
mvav 80,64,28
mvav 88,64,32
mvav 84,68,28
mvav 92,68,32
mvvv 36,16,28
mvav 96,72,28
mvav 104,72,32
mvav 100,76,28
mvav 108,76,32
mvvv 40,16,32
mvav 112,80,28
mvav 120,80,32
mvav 116,84,28
mvav 124,84,32
mvav 128,88,28
mvav 136,88,32
mvav 132,92,28
mvav 140,92,32
s_waitcnt lgkmcnt(0)
mvav 144,96,28
mvav 152,96,32
mvav 148,100,28
mvav 156,100,32
mvav 160,104,28
mvav 168,104,32
mvav 164,108,28
mvav 172,108,32
mvav 176,112,28
mvav 184,112,32
mvav 180,116,28
mvav 188,116,32
mvav 192,120,28
mvav 200,120,32
mvav 196,124,28
mvav 204,124,32
s_cmp_eq_u32 s51, 0
s_cbranch_scc1 .Lk2_epi
s_cmp_eq_u32 s51, 1
s_cbranch_scc1 .Lk2_epi0
.Lk2_fin0_F2:
s_waitcnt vmcnt(6)
s_barrier
s_waitcnt vmcnt(0)
s_mov_b32 s67, 0x400000
.Lk2_fin0y_f3:
v_xor_b32 v234, s82, v234
v_xor_b32 v235, s82, v235
v_or_b32 v234, v234, v235
v_cmp_ne_u32 vcc, 0, v234
s_nop 4
s_cbranch_vccz .Lk2_fin0y_f3ok
s_sleep 4
global_load_dwordx2 v[234:235], v79, s[2:3] sc1
s_waitcnt vmcnt(0)
s_sub_u32 s67, s67, 1
s_cmp_lg_u32 s67, 0
s_cbranch_scc1 .Lk2_fin0y_f3
v_mov_b32 v6, 0x7fc00000
.Lk2_fin0y_f3ok:
s_cmp_eq_u32 s53, 0
s_cbranch_scc1 .Lk2_fin0y_np
)ASM" ::: "memory");
  asm volatile(R"ASM(
v_readfirstlane_b32 s62, v77
s_nop 1
s_cmp_lg_u32 s62, s82
s_cselect_b32 s62, 0, 1
s_cbranch_scc1 .Lk2_fin0y_np
s_mov_b64 s[68:69], s[28:29]
global_load_dwordx4 a[192:195], v0, s[68:69] offset:0 sc1
global_load_dwordx4 a[196:199], v0, s[68:69] offset:1024 sc1
global_load_dwordx4 a[200:203], v0, s[68:69] offset:2048 sc1
global_load_dwordx4 a[204:207], v0, s[68:69] offset:3072 sc1
s_add_u32 s68, s68, 0x1000
s_addc_u32 s69, s69, 0
global_load_dwordx4 a[208:211], v0, s[68:69] offset:0 sc1
global_load_dwordx4 a[212:215], v0, s[68:69] offset:1024 sc1
global_load_dwordx4 a[216:219], v0, s[68:69] offset:2048 sc1
global_load_dwordx4 a[220:223], v0, s[68:69] offset:3072 sc1
s_add_u32 s68, s68, 0x1000
s_addc_u32 s69, s69, 0
global_load_dwordx4 a[224:227], v0, s[68:69] offset:0 sc1
global_load_dwordx4 a[228:231], v0, s[68:69] offset:1024 sc1
global_load_dwordx4 a[232:235], v0, s[68:69] offset:2048 sc1
global_load_dwordx4 a[236:239], v0, s[68:69] offset:3072 sc1
s_add_u32 s68, s68, 0x1000
s_addc_u32 s69, s69, 0
global_load_dwordx4 a[240:243], v0, s[68:69] offset:0 sc1
global_load_dwordx4 a[244:247], v0, s[68:69] offset:1024 sc1
global_load_dwordx4 a[248:251], v0, s[68:69] offset:2048 sc1
global_load_dwordx4 a[252:255], v0, s[68:69] offset:3072 sc1
s_add_u32 s68, s68, 0x1000
s_addc_u32 s69, s69, 0
v_lshrrev_b32 v9, 1, v0
global_load_dwordx2 v[242:243], v9, s[68:69] sc1
.Lk2_fin0y_np:
global_load_dwordx4 v[216:219], v0, s[54:55] offset:0 sc1
global_load_dwordx4 v[220:223], v0, s[54:55] offset:1024 sc1
s_add_u32 s54, s54, 0x200000
s_addc_u32 s55, s55, 0
global_load_dwordx4 v[224:227], v0, s[54:55] offset:0 sc1
global_load_dwordx4 v[228:231], v0, s[54:55] offset:1024 sc1
global_load_dword v232, v76, s[56:57] offset:0 sc1
global_load_dword v233, v76, s[56:57] offset:64 sc1
s_add_u32 s56, s56, 0x20000
s_addc_u32 s57, s57, 0
global_load_dword v234, v76, s[56:57] offset:0 sc1
global_load_dword v235, v76, s[56:57] offset:64 sc1
s_cmp_eq_u32 s51, 2
s_cbranch_scc1 .Lk2_epi0
s_getpc_b64 s[68:69]
.Lf_w45:
s_add_u32 s68, s68, .Lk2_end-20480-.Lf_w45
s_addc_u32 s69, s69, (.Lk2_end-20480-.Lf_w45)>>32
global_load_dword v254, v12, s[68:69]
s_getpc_b64 s[68:69]
.Lf_w46:
s_add_u32 s68, s68, .Lk2_end-16384-.Lf_w46
s_addc_u32 s69, s69, (.Lk2_end-16384-.Lf_w46)>>32
global_load_dword v254, v12, s[68:69]
s_getpc_b64 s[68:69]
.Lf_w47:
s_add_u32 s68, s68, .Lk2_end-12288-.Lf_w47
s_addc_u32 s69, s69, (.Lk2_end-12288-.Lf_w47)>>32
global_load_dword v254, v12, s[68:69]
s_getpc_b64 s[68:69]
.Lf_w48:
s_add_u32 s68, s68, .Lk2_end-8192-.Lf_w48
s_addc_u32 s69, s69, (.Lk2_end-8192-.Lf_w48)>>32
global_load_dword v254, v12, s[68:69]
s_getpc_b64 s[68:69]
.Lf_w49:
s_add_u32 s68, s68, .Lk2_end-4096-.Lf_w49
s_addc_u32 s69, s69, (.Lk2_end-4096-.Lf_w49)>>32
global_load_dword v254, v12, s[68:69]
s_barrier
s_branch .Lk2_epi0
.Lk2_fin21:
.Lk2_fin23:
s_cmp_lg_u32 s51, 0
s_cbranch_scc1 .Lk2_fin1_g
s_cmp_eq_u32 s53, 0
s_cbranch_scc1 .Lk2_fin1_fB
s_cmp_lg_u32 s62, 0
s_cbranch_scc1 .Lk2_fin1_fA
s_add_u32 m0, s96, 0
s_nop 0
dm 244,88,8
s_add_u32 m0, s96, 1024
s_nop 0
dm 245,88,8
s_add_u32 m0, s96, 2048
s_nop 0
dm 246,88,8
s_add_u32 m0, s96, 3072
s_nop 0
dm 247,88,8
s_add_u32 m0, s96, 4096
s_nop 0
dm 248,88,8
s_add_u32 m0, s96, 5120
s_nop 0
dm 249,88,8
s_add_u32 m0, s96, 6144
s_nop 0
dm 250,88,8
s_add_u32 m0, s96, 28672
s_nop 0
dm 244,88,9
s_add_u32 m0, s96, 29696
s_nop 0
dm 245,88,9
s_add_u32 m0, s96, 30720
s_nop 0
dm 246,88,9
s_add_u32 m0, s96, 31744
s_nop 0
dm 247,88,9
s_add_u32 m0, s96, 32768
s_nop 0
dm 248,88,9
s_add_u32 m0, s96, 33792
s_nop 0
dm 249,88,9
s_add_u32 m0, s96, 34816
s_nop 0
dm 250,88,9
s_branch .Lk2_fin1_go
.Lk2_fin1_g:
global_load_dword v77, v79, s[30:31] sc1
global_load_dwordx2 v[234:235], v79, s[2:3] sc1
s_waitcnt vmcnt(14)
s_barrier
s_cmp_lg_u32 s51, 1
s_cbranch_scc1 .Lk2_fin1_go
.Lk2_fin1_F1:
s_waitcnt vmcnt(0)
s_mov_b32 s67, 0x400000
.Lk2_fin1x_f3:
v_xor_b32 v234, s82, v234
v_xor_b32 v235, s82, v235
v_or_b32 v234, v234, v235
v_cmp_ne_u32 vcc, 0, v234
s_nop 4
s_cbranch_vccz .Lk2_fin1x_f3ok
s_sleep 4
global_load_dwordx2 v[234:235], v79, s[2:3] sc1
s_waitcnt vmcnt(0)
s_sub_u32 s67, s67, 1
s_cmp_lg_u32 s67, 0
s_cbranch_scc1 .Lk2_fin1x_f3
v_mov_b32 v6, 0x7fc00000
.Lk2_fin1x_f3ok:
s_cmp_eq_u32 s53, 0
s_cbranch_scc1 .Lk2_fin1x_np
v_readfirstlane_b32 s62, v77
s_nop 1
s_cmp_lg_u32 s62, s82
s_cselect_b32 s62, 0, 1
s_cbranch_scc1 .Lk2_fin1x_np
s_mov_b64 s[68:69], s[28:29]
global_load_dwordx4 a[192:195], v0, s[68:69] offset:0 sc1
global_load_dwordx4 a[196:199], v0, s[68:69] offset:1024 sc1
global_load_dwordx4 a[200:203], v0, s[68:69] offset:2048 sc1
global_load_dwordx4 a[204:207], v0, s[68:69] offset:3072 sc1
s_add_u32 s68, s68, 0x1000
s_addc_u32 s69, s69, 0
global_load_dwordx4 a[208:211], v0, s[68:69] offset:0 sc1
global_load_dwordx4 a[212:215], v0, s[68:69] offset:1024 sc1
global_load_dwordx4 a[216:219], v0, s[68:69] offset:2048 sc1
global_load_dwordx4 a[220:223], v0, s[68:69] offset:3072 sc1
s_add_u32 s68, s68, 0x1000
s_addc_u32 s69, s69, 0
global_load_dwordx4 a[224:227], v0, s[68:69] offset:0 sc1
global_load_dwordx4 a[228:231], v0, s[68:69] offset:1024 sc1
global_load_dwordx4 a[232:235], v0, s[68:69] offset:2048 sc1
global_load_dwordx4 a[236:239], v0, s[68:69] offset:3072 sc1
s_add_u32 s68, s68, 0x1000
s_addc_u32 s69, s69, 0
global_load_dwordx4 a[240:243], v0, s[68:69] offset:0 sc1
global_load_dwordx4 a[244:247], v0, s[68:69] offset:1024 sc1
global_load_dwordx4 a[248:251], v0, s[68:69] offset:2048 sc1
global_load_dwordx4 a[252:255], v0, s[68:69] offset:3072 sc1
s_add_u32 s68, s68, 0x1000
s_addc_u32 s69, s69, 0
v_lshrrev_b32 v9, 1, v0
global_load_dwordx2 v[242:243], v9, s[68:69] sc1
.Lk2_fin1x_np:
global_load_dwordx4 v[216:219], v0, s[54:55] offset:0 sc1
global_load_dwordx4 v[220:223], v0, s[54:55] offset:1024 sc1
s_add_u32 s54, s54, 0x200000
s_addc_u32 s55, s55, 0
global_load_dwordx4 v[224:227], v0, s[54:55] offset:0 sc1
global_load_dwordx4 v[228:231], v0, s[54:55] offset:1024 sc1
global_load_dword v232, v76, s[56:57] offset:0 sc1
global_load_dword v233, v76, s[56:57] offset:64 sc1
s_add_u32 s56, s56, 0x20000
s_addc_u32 s57, s57, 0
global_load_dword v234, v76, s[56:57] offset:0 sc1
global_load_dword v235, v76, s[56:57] offset:64 sc1
.Lk2_fin1_go:
s_waitcnt lgkmcnt(0)
rqa 64,2,0
rqa 68,2,1024
rqa 72,2,2048
rqa 76,2,3072
rqa 80,2,4096
rqa 84,2,5120
rqa 88,2,6144
rqa 92,2,7168
rqa 96,2,8192
rqa 100,2,9216
rqa 104,2,10240
rqa 108,2,11264
rqa 112,2,12288
rqa 116,2,13312
rqa 120,2,14336
rqa 124,2,15360
ex 60
ex 61
ex 62
ex 63
ex 68
ex 69
ex 70
ex 71
ex 64
ex 65
ex 66
ex 67
ex 72
ex 73
ex 74
ex 75
cv 28,60,61
cv 29,62,63
cv 30,68,69
cv 31,70,71
cv 32,64,65
cv 33,66,67
cv 34,72,73
cv 35,74,75
s_waitcnt lgkmcnt(8)
mvav 80,64,28
mvav 88,64,32
mvav 84,68,28
mvav 92,68,32
mvvv 36,16,28
mvav 96,72,28
mvav 104,72,32
mvav 100,76,28
mvav 108,76,32
mvvv 40,16,32
mvav 112,80,28
mvav 120,80,32
mvav 116,84,28
mvav 124,84,32
mvav 128,88,28
mvav 136,88,32
mvav 132,92,28
mvav 140,92,32
s_waitcnt lgkmcnt(0)
mvav 144,96,28
mvav 152,96,32
mvav 148,100,28
mvav 156,100,32
mvav 160,104,28
mvav 168,104,32
mvav 164,108,28
mvav 172,108,32
mvav 176,112,28
mvav 184,112,32
mvav 180,116,28
mvav 188,116,32
mvav 192,120,28
mvav 200,120,32
mvav 196,124,28
mvav 204,124,32
s_cmp_eq_u32 s51, 0
s_cbranch_scc1 .Lk2_epi
s_cmp_eq_u32 s51, 1
s_cbranch_scc1 .Lk2_epi0
.Lk2_fin1_F2:
s_waitcnt vmcnt(6)
s_barrier
s_waitcnt vmcnt(0)
s_mov_b32 s67, 0x400000
.Lk2_fin1y_f3:
v_xor_b32 v234, s82, v234
v_xor_b32 v235, s82, v235
v_or_b32 v234, v234, v235
v_cmp_ne_u32 vcc, 0, v234
s_nop 4
s_cbranch_vccz .Lk2_fin1y_f3ok
s_sleep 4
global_load_dwordx2 v[234:235], v79, s[2:3] sc1
s_waitcnt vmcnt(0)
s_sub_u32 s67, s67, 1
s_cmp_lg_u32 s67, 0
s_cbranch_scc1 .Lk2_fin1y_f3
v_mov_b32 v6, 0x7fc00000
.Lk2_fin1y_f3ok:
s_cmp_eq_u32 s53, 0
s_cbranch_scc1 .Lk2_fin1y_np
v_readfirstlane_b32 s62, v77
s_nop 1
s_cmp_lg_u32 s62, s82
s_cselect_b32 s62, 0, 1
s_cbranch_scc1 .Lk2_fin1y_np
s_mov_b64 s[68:69], s[28:29]
global_load_dwordx4 a[192:195], v0, s[68:69] offset:0 sc1
global_load_dwordx4 a[196:199], v0, s[68:69] offset:1024 sc1
global_load_dwordx4 a[200:203], v0, s[68:69] offset:2048 sc1
global_load_dwordx4 a[204:207], v0, s[68:69] offset:3072 sc1
s_add_u32 s68, s68, 0x1000
s_addc_u32 s69, s69, 0
global_load_dwordx4 a[208:211], v0, s[68:69] offset:0 sc1
global_load_dwordx4 a[212:215], v0, s[68:69] offset:1024 sc1
global_load_dwordx4 a[216:219], v0, s[68:69] offset:2048 sc1
global_load_dwordx4 a[220:223], v0, s[68:69] offset:3072 sc1
s_add_u32 s68, s68, 0x1000
s_addc_u32 s69, s69, 0
global_load_dwordx4 a[224:227], v0, s[68:69] offset:0 sc1
global_load_dwordx4 a[228:231], v0, s[68:69] offset:1024 sc1
global_load_dwordx4 a[232:235], v0, s[68:69] offset:2048 sc1
global_load_dwordx4 a[236:239], v0, s[68:69] offset:3072 sc1
s_add_u32 s68, s68, 0x1000
s_addc_u32 s69, s69, 0
global_load_dwordx4 a[240:243], v0, s[68:69] offset:0 sc1
global_load_dwordx4 a[244:247], v0, s[68:69] offset:1024 sc1
global_load_dwordx4 a[248:251], v0, s[68:69] offset:2048 sc1
global_load_dwordx4 a[252:255], v0, s[68:69] offset:3072 sc1
s_add_u32 s68, s68, 0x1000
s_addc_u32 s69, s69, 0
v_lshrrev_b32 v9, 1, v0
global_load_dwordx2 v[242:243], v9, s[68:69] sc1
.Lk2_fin1y_np:
global_load_dwordx4 v[216:219], v0, s[54:55] offset:0 sc1
global_load_dwordx4 v[220:223], v0, s[54:55] offset:1024 sc1
s_add_u32 s54, s54, 0x200000
s_addc_u32 s55, s55, 0
global_load_dwordx4 v[224:227], v0, s[54:55] offset:0 sc1
global_load_dwordx4 v[228:231], v0, s[54:55] offset:1024 sc1
global_load_dword v232, v76, s[56:57] offset:0 sc1
global_load_dword v233, v76, s[56:57] offset:64 sc1
s_add_u32 s56, s56, 0x20000
s_addc_u32 s57, s57, 0
global_load_dword v234, v76, s[56:57] offset:0 sc1
global_load_dword v235, v76, s[56:57] offset:64 sc1
s_cmp_eq_u32 s51, 2
s_cbranch_scc1 .Lk2_epi0
s_getpc_b64 s[68:69]
.Lf_w50:
s_add_u32 s68, s68, .Lk2_end-20480-.Lf_w50
s_addc_u32 s69, s69, (.Lk2_end-20480-.Lf_w50)>>32
global_load_dword v254, v12, s[68:69]
s_getpc_b64 s[68:69]
.Lf_w51:
s_add_u32 s68, s68, .Lk2_end-16384-.Lf_w51
s_addc_u32 s69, s69, (.Lk2_end-16384-.Lf_w51)>>32
global_load_dword v254, v12, s[68:69]
s_getpc_b64 s[68:69]
.Lf_w52:
s_add_u32 s68, s68, .Lk2_end-12288-.Lf_w52
s_addc_u32 s69, s69, (.Lk2_end-12288-.Lf_w52)>>32
global_load_dword v254, v12, s[68:69]
s_getpc_b64 s[68:69]
.Lf_w53:
s_add_u32 s68, s68, .Lk2_end-8192-.Lf_w53
s_addc_u32 s69, s69, (.Lk2_end-8192-.Lf_w53)>>32
global_load_dword v254, v12, s[68:69]
s_getpc_b64 s[68:69]
.Lf_w54:
s_add_u32 s68, s68, .Lk2_end-4096-.Lf_w54
s_addc_u32 s69, s69, (.Lk2_end-4096-.Lf_w54)>>32
global_load_dword v254, v12, s[68:69]
s_barrier
s_branch .Lk2_epi0
.Lk2_fin0_fA:
s_waitcnt lgkmcnt(0)
rqa 64,2,0
rqa 68,2,1024
rqa 72,2,2048
rqa 76,2,3072
rqa 80,2,4096
rqa 84,2,5120
rqa 88,2,6144
rqa 92,2,7168
rqa 96,2,8192
rqa 100,2,9216
rqa 104,2,10240
rqa 108,2,11264
rqa 112,2,12288
rqa 116,2,13312
rqa 120,2,14336
rqa 124,2,15360
ex 44
ex 45
ex 46
ex 47
ex 52
ex 53
ex 54
ex 55
ex 48
ex 49
ex 50
ex 51
ex 56
ex 57
ex 58
ex 59
cv 28,44,45
cv 29,46,47
cv 30,52,53
cv 31,54,55
cv 32,48,49
cv 33,50,51
cv 34,56,57
cv 35,58,59
v_mov_b64 v[236:237], 0
v_mov_b64 v[240:241], 0
v_mov_b32 v238, v78
v_mov_b32 v239, v13
s_waitcnt vmcnt(12) lgkmcnt(8)
mvav 80,64,28
s_add_u32 m0, s96, 0
v_sub_f32 v8, v4, v242
v_min_f32 v8, 0x42a00000, v8
mvav 88,64,32
dm 244,88,8
v_max_f32 v8, 0xc2a00000, v8
ex 8
s_nop 0
mvav 84,68,28
s_add_u32 m0, s96, 1024
v_sub_f32 v10, v5, v243
v_min_f32 v10, 0x42a00000, v10
mvav 92,68,32
mvvv 36,16,28
dm 245,88,8
v_max_f32 v10, 0xc2a00000, v10
ex 10
s_nop 0
mvav 96,72,28
s_add_u32 m0, s96, 2048
v_sub_f32 v252, 1.0, v6
cv 252,252,252
mvav 104,72,32
dm 246,88,8
v_and_b32 v252, 0xffff0000, v252
v_sub_f32 v253, 1.0, v7
mvav 100,76,28
s_add_u32 m0, s96, 3072
cv 253,253,253
v_and_b32 v253, 0xffff0000, v253
mvav 108,76,32
mvvv 40,16,32
dm 247,88,8
v_max_f32 v60, v232, v234
v_sub_f32 v61, v232, v60
mvav 112,80,28
s_add_u32 m0, s96, 4096
v_pk_mul_f32 v[80:81], v[80:81], v[8:9] op_sel_hi:[1,0]
v_pk_mul_f32 v[82:83], v[82:83], v[8:9] op_sel_hi:[1,0]
v_sub_f32 v62, v234, v60
mvav 120,80,32
dm 248,88,8
v_pk_mul_f32 v[88:89], v[88:89], v[10:11] op_sel_hi:[1,0]
v_pk_mul_f32 v[90:91], v[90:91], v[10:11] op_sel_hi:[1,0]
ex 61
mvav 116,84,28
s_add_u32 m0, s96, 5120
v_pk_mul_f32 v[84:85], v[84:85], v[8:9] op_sel_hi:[1,0]
v_pk_mul_f32 v[86:87], v[86:87], v[8:9] op_sel_hi:[1,0]
ex 62
s_nop 0
mvav 124,84,32
dm 249,88,8
v_pk_mul_f32 v[92:93], v[92:93], v[10:11] op_sel_hi:[1,0]
v_pk_mul_f32 v[94:95], v[94:95], v[10:11] op_sel_hi:[1,0]
v_add_f32 v60, v61, v62
mvav 128,88,28
s_add_u32 m0, s96, 6144
v_pk_mul_f32 v[96:97], v[96:97], v[8:9] op_sel_hi:[1,0]
v_pk_mul_f32 v[98:99], v[98:99], v[8:9] op_sel_hi:[1,0]
mvva 80,238,192
v_rcp_f32 v60, v60
s_nop 0
mvav 136,88,32
dm 250,88,8
v_pk_mul_f32 v[104:105], v[104:105], v[10:11] op_sel_hi:[1,0]
v_pk_mul_f32 v[106:107], v[106:107], v[10:11] op_sel_hi:[1,0]
mvva 88,238,196
v_mul_f32 v60, v60, v6
mvav 132,92,28
s_add_u32 m0, s96, 28672
v_pk_mul_f32 v[100:101], v[100:101], v[8:9] op_sel_hi:[1,0]
v_pk_mul_f32 v[102:103], v[102:103], v[8:9] op_sel_hi:[1,0]
mvva 84,236,192
v_mul_f32 v61, v61, v60
mvav 140,92,32
dm 244,88,9
v_pk_mul_f32 v[108:109], v[108:109], v[10:11] op_sel_hi:[1,0]
v_pk_mul_f32 v[110:111], v[110:111], v[10:11] op_sel_hi:[1,0]
mvva 92,236,196
v_mul_f32 v62, v62, v60
s_waitcnt lgkmcnt(0)
mvav 144,96,28
s_add_u32 m0, s96, 29696
v_pk_mul_f32 v[112:113], v[112:113], v[8:9] op_sel_hi:[1,0]
v_pk_mul_f32 v[114:115], v[114:115], v[8:9] op_sel_hi:[1,0]
mvva 96,238,200
v_lshlrev_b32 v64, 16, v216
mvav 152,96,32
dm 245,88,9
v_pk_mul_f32 v[120:121], v[120:121], v[10:11] op_sel_hi:[1,0]
v_pk_mul_f32 v[122:123], v[122:123], v[10:11] op_sel_hi:[1,0]
mvva 104,238,204
v_and_b32 v65, 0xffff0000, v216
mvav 148,100,28
s_add_u32 m0, s96, 30720
v_pk_mul_f32 v[116:117], v[116:117], v[8:9] op_sel_hi:[1,0]
v_pk_mul_f32 v[118:119], v[118:119], v[8:9] op_sel_hi:[1,0]
mvva 100,236,200
v_lshlrev_b32 v66, 16, v224
mvav 156,100,32
dm 246,88,9
v_pk_mul_f32 v[124:125], v[124:125], v[10:11] op_sel_hi:[1,0]
v_pk_mul_f32 v[126:127], v[126:127], v[10:11] op_sel_hi:[1,0]
mvva 108,236,204
v_and_b32 v67, 0xffff0000, v224
mvav 160,104,28
s_add_u32 m0, s96, 31744
v_pk_mul_f32 v[128:129], v[128:129], v[8:9] op_sel_hi:[1,0]
v_pk_mul_f32 v[130:131], v[130:131], v[8:9] op_sel_hi:[1,0]
mvva 112,238,208
v_mul_f32 v64, v61, v64
mvav 168,104,32
dm 247,88,9
v_pk_mul_f32 v[136:137], v[136:137], v[10:11] op_sel_hi:[1,0]
v_pk_mul_f32 v[138:139], v[138:139], v[10:11] op_sel_hi:[1,0]
mvva 120,238,212
v_mul_f32 v65, v61, v65
mvav 164,108,28
s_add_u32 m0, s96, 32768
v_pk_mul_f32 v[132:133], v[132:133], v[8:9] op_sel_hi:[1,0]
v_pk_mul_f32 v[134:135], v[134:135], v[8:9] op_sel_hi:[1,0]
mvva 116,236,208
v_fmac_f32 v64, v62, v66
mvav 172,108,32
dm 248,88,9
v_pk_mul_f32 v[140:141], v[140:141], v[10:11] op_sel_hi:[1,0]
v_pk_mul_f32 v[142:143], v[142:143], v[10:11] op_sel_hi:[1,0]
mvva 124,236,212
v_fmac_f32 v65, v62, v67
mvav 176,112,28
s_add_u32 m0, s96, 33792
v_pk_mul_f32 v[144:145], v[144:145], v[8:9] op_sel_hi:[1,0]
v_pk_mul_f32 v[146:147], v[146:147], v[8:9] op_sel_hi:[1,0]
mvva 128,238,216
cv 208,64,65
mvav 184,112,32
dm 249,88,9
v_pk_mul_f32 v[152:153], v[152:153], v[10:11] op_sel_hi:[1,0]
v_pk_mul_f32 v[154:155], v[154:155], v[10:11] op_sel_hi:[1,0]
mvva 136,238,220
v_lshlrev_b32 v64, 16, v217
mvav 180,116,28
s_add_u32 m0, s96, 34816
v_pk_mul_f32 v[148:149], v[148:149], v[8:9] op_sel_hi:[1,0]
v_pk_mul_f32 v[150:151], v[150:151], v[8:9] op_sel_hi:[1,0]
mvva 132,236,216
v_and_b32 v65, 0xffff0000, v217
mvav 188,116,32
dm 250,88,9
v_pk_mul_f32 v[156:157], v[156:157], v[10:11] op_sel_hi:[1,0]
v_pk_mul_f32 v[158:159], v[158:159], v[10:11] op_sel_hi:[1,0]
mvva 140,236,220
v_lshlrev_b32 v66, 16, v225
mvav 192,120,28
v_pk_mul_f32 v[160:161], v[160:161], v[8:9] op_sel_hi:[1,0]
v_pk_mul_f32 v[162:163], v[162:163], v[8:9] op_sel_hi:[1,0]
mvva 144,238,224
v_and_b32 v67, 0xffff0000, v225
mvav 200,120,32
v_pk_mul_f32 v[168:169], v[168:169], v[10:11] op_sel_hi:[1,0]
v_pk_mul_f32 v[170:171], v[170:171], v[10:11] op_sel_hi:[1,0]
mvva 152,238,228
v_mul_f32 v64, v61, v64
mvav 196,124,28
v_pk_mul_f32 v[164:165], v[164:165], v[8:9] op_sel_hi:[1,0]
v_pk_mul_f32 v[166:167], v[166:167], v[8:9] op_sel_hi:[1,0]
mvva 148,236,224
v_mul_f32 v65, v61, v65
mvav 204,124,32
v_pk_mul_f32 v[172:173], v[172:173], v[10:11] op_sel_hi:[1,0]
v_pk_mul_f32 v[174:175], v[174:175], v[10:11] op_sel_hi:[1,0]
mvva 156,236,228
v_fmac_f32 v64, v62, v66
v_pk_mul_f32 v[176:177], v[176:177], v[8:9] op_sel_hi:[1,0]
v_pk_mul_f32 v[178:179], v[178:179], v[8:9] op_sel_hi:[1,0]
v_fmac_f32 v65, v62, v67
)ASM" ::: "memory");
  asm volatile(R"ASM(
mvva 160,238,232
v_pk_mul_f32 v[184:185], v[184:185], v[10:11] op_sel_hi:[1,0]
v_pk_mul_f32 v[186:187], v[186:187], v[10:11] op_sel_hi:[1,0]
cv 209,64,65
mvva 168,238,236
v_pk_mul_f32 v[180:181], v[180:181], v[8:9] op_sel_hi:[1,0]
v_pk_mul_f32 v[182:183], v[182:183], v[8:9] op_sel_hi:[1,0]
v_lshlrev_b32 v64, 16, v218
mvva 164,236,232
v_pk_mul_f32 v[188:189], v[188:189], v[10:11] op_sel_hi:[1,0]
v_pk_mul_f32 v[190:191], v[190:191], v[10:11] op_sel_hi:[1,0]
v_and_b32 v65, 0xffff0000, v218
mvva 172,236,236
v_pk_mul_f32 v[192:193], v[192:193], v[8:9] op_sel_hi:[1,0]
v_pk_mul_f32 v[194:195], v[194:195], v[8:9] op_sel_hi:[1,0]
v_lshlrev_b32 v66, 16, v226
mvva 176,238,240
v_pk_mul_f32 v[200:201], v[200:201], v[10:11] op_sel_hi:[1,0]
v_pk_mul_f32 v[202:203], v[202:203], v[10:11] op_sel_hi:[1,0]
v_and_b32 v67, 0xffff0000, v226
mvva 184,238,244
v_pk_mul_f32 v[196:197], v[196:197], v[8:9] op_sel_hi:[1,0]
v_pk_mul_f32 v[198:199], v[198:199], v[8:9] op_sel_hi:[1,0]
v_mul_f32 v64, v61, v64
mvva 180,236,240
v_pk_mul_f32 v[204:205], v[204:205], v[10:11] op_sel_hi:[1,0]
v_pk_mul_f32 v[206:207], v[206:207], v[10:11] op_sel_hi:[1,0]
v_mul_f32 v65, v61, v65
mvva 188,236,244
v_fmac_f32 v64, v62, v66
mvva 192,238,248
v_fmac_f32 v65, v62, v67
mvva 200,238,252
cv 210,64,65
mvva 196,236,248
v_lshlrev_b32 v64, 16, v219
mvva 204,236,252
v_and_b32 v65, 0xffff0000, v219
v_lshlrev_b32 v66, 16, v227
v_and_b32 v67, 0xffff0000, v227
v_mul_f32 v64, v61, v64
v_mul_f32 v65, v61, v65
v_fmac_f32 v64, v62, v66
v_fmac_f32 v65, v62, v67
cv 211,64,65
s_mov_b64 exec, s[60:61]
v_mov_b32 v210, 0
v_mov_b32 v211, 0
s_mov_b64 exec, -1
s_nop 2
v_max_f32 v60, v233, v235
v_sub_f32 v61, v233, v60
v_sub_f32 v62, v235, v60
ex 61
ex 62
s_nop 0
v_add_f32 v60, v61, v62
v_rcp_f32 v60, v60
s_nop 0
v_mul_f32 v60, v60, v7
v_mul_f32 v61, v61, v60
v_mul_f32 v62, v62, v60
v_lshlrev_b32 v64, 16, v220
v_and_b32 v65, 0xffff0000, v220
v_lshlrev_b32 v66, 16, v228
v_and_b32 v67, 0xffff0000, v228
v_mul_f32 v64, v61, v64
v_mul_f32 v65, v61, v65
v_fmac_f32 v64, v62, v66
v_fmac_f32 v65, v62, v67
cv 212,64,65
v_lshlrev_b32 v64, 16, v221
v_and_b32 v65, 0xffff0000, v221
v_lshlrev_b32 v66, 16, v229
v_and_b32 v67, 0xffff0000, v229
v_mul_f32 v64, v61, v64
v_mul_f32 v65, v61, v65
v_fmac_f32 v64, v62, v66
v_fmac_f32 v65, v62, v67
cv 213,64,65
v_lshlrev_b32 v64, 16, v222
v_and_b32 v65, 0xffff0000, v222
v_lshlrev_b32 v66, 16, v230
v_and_b32 v67, 0xffff0000, v230
v_mul_f32 v64, v61, v64
v_mul_f32 v65, v61, v65
v_fmac_f32 v64, v62, v66
v_fmac_f32 v65, v62, v67
cv 214,64,65
v_lshlrev_b32 v64, 16, v223
v_and_b32 v65, 0xffff0000, v223
v_lshlrev_b32 v66, 16, v231
v_and_b32 v67, 0xffff0000, v231
v_mul_f32 v64, v61, v64
v_mul_f32 v65, v61, v65
v_fmac_f32 v64, v62, v66
v_fmac_f32 v65, v62, v67
cv 215,64,65
s_mov_b64 exec, s[60:61]
v_mov_b32 v214, 0
v_mov_b32 v215, 0
s_mov_b64 exec, -1
s_nop 2
v_fma_f32 v36, v36, v8, 1.0
v_fma_f32 v40, v40, v10, 1.0
v_mov_b32 v24, v251
v_add_u32 v25, 57344, v251
v_add_u32 v26, 114688, v251
s_mov_b32 s99, s98
s_and_b32 s98, s98, 0xfc
s_mov_b32 s3, 0x20000
s_nop 3
v_rcp_f32 v8, v36
v_rcp_f32 v10, v40
s_nop 0
v_mul_f32 v8, v8, v252
v_mul_f32 v10, v10, v253
v_pk_mul_f32 v[80:81], v[80:81], v[8:9] op_sel_hi:[1,0]
v_pk_mul_f32 v[82:83], v[82:83], v[8:9] op_sel_hi:[1,0]
v_pk_mul_f32 v[84:85], v[84:85], v[8:9] op_sel_hi:[1,0]
v_pk_mul_f32 v[86:87], v[86:87], v[8:9] op_sel_hi:[1,0]
cv 80,80,81
cv 81,82,83
cv 82,84,85
cv 83,86,87
v_pk_mul_f32 v[88:89], v[88:89], v[10:11] op_sel_hi:[1,0]
v_pk_mul_f32 v[90:91], v[90:91], v[10:11] op_sel_hi:[1,0]
v_pk_mul_f32 v[92:93], v[92:93], v[10:11] op_sel_hi:[1,0]
v_pk_mul_f32 v[94:95], v[94:95], v[10:11] op_sel_hi:[1,0]
cv 84,88,89
cv 85,90,91
cv 86,92,93
cv 87,94,95
v_pk_mul_f32 v[96:97], v[96:97], v[8:9] op_sel_hi:[1,0]
v_pk_mul_f32 v[98:99], v[98:99], v[8:9] op_sel_hi:[1,0]
v_pk_mul_f32 v[100:101], v[100:101], v[8:9] op_sel_hi:[1,0]
v_pk_mul_f32 v[102:103], v[102:103], v[8:9] op_sel_hi:[1,0]
cv 88,96,97
cv 89,98,99
cv 90,100,101
cv 91,102,103
v_pk_mul_f32 v[104:105], v[104:105], v[10:11] op_sel_hi:[1,0]
v_pk_mul_f32 v[106:107], v[106:107], v[10:11] op_sel_hi:[1,0]
v_pk_mul_f32 v[108:109], v[108:109], v[10:11] op_sel_hi:[1,0]
v_pk_mul_f32 v[110:111], v[110:111], v[10:11] op_sel_hi:[1,0]
cv 92,104,105
cv 93,106,107
cv 94,108,109
cv 95,110,111
v_pk_mul_f32 v[112:113], v[112:113], v[8:9] op_sel_hi:[1,0]
v_pk_mul_f32 v[114:115], v[114:115], v[8:9] op_sel_hi:[1,0]
v_pk_mul_f32 v[116:117], v[116:117], v[8:9] op_sel_hi:[1,0]
v_pk_mul_f32 v[118:119], v[118:119], v[8:9] op_sel_hi:[1,0]
cv 96,112,113
cv 97,114,115
cv 98,116,117
cv 99,118,119
v_pk_mul_f32 v[120:121], v[120:121], v[10:11] op_sel_hi:[1,0]
v_pk_mul_f32 v[122:123], v[122:123], v[10:11] op_sel_hi:[1,0]
v_pk_mul_f32 v[124:125], v[124:125], v[10:11] op_sel_hi:[1,0]
v_pk_mul_f32 v[126:127], v[126:127], v[10:11] op_sel_hi:[1,0]
cv 100,120,121
cv 101,122,123
cv 102,124,125
cv 103,126,127
v_pk_mul_f32 v[128:129], v[128:129], v[8:9] op_sel_hi:[1,0]
v_pk_mul_f32 v[130:131], v[130:131], v[8:9] op_sel_hi:[1,0]
v_pk_mul_f32 v[132:133], v[132:133], v[8:9] op_sel_hi:[1,0]
v_pk_mul_f32 v[134:135], v[134:135], v[8:9] op_sel_hi:[1,0]
cv 104,128,129
cv 105,130,131
cv 106,132,133
cv 107,134,135
v_pk_mul_f32 v[136:137], v[136:137], v[10:11] op_sel_hi:[1,0]
v_pk_mul_f32 v[138:139], v[138:139], v[10:11] op_sel_hi:[1,0]
v_pk_mul_f32 v[140:141], v[140:141], v[10:11] op_sel_hi:[1,0]
v_pk_mul_f32 v[142:143], v[142:143], v[10:11] op_sel_hi:[1,0]
cv 108,136,137
cv 109,138,139
cv 110,140,141
cv 111,142,143
v_pk_mul_f32 v[144:145], v[144:145], v[8:9] op_sel_hi:[1,0]
v_pk_mul_f32 v[146:147], v[146:147], v[8:9] op_sel_hi:[1,0]
v_pk_mul_f32 v[148:149], v[148:149], v[8:9] op_sel_hi:[1,0]
v_pk_mul_f32 v[150:151], v[150:151], v[8:9] op_sel_hi:[1,0]
cv 112,144,145
cv 113,146,147
cv 114,148,149
cv 115,150,151
v_pk_mul_f32 v[152:153], v[152:153], v[10:11] op_sel_hi:[1,0]
v_pk_mul_f32 v[154:155], v[154:155], v[10:11] op_sel_hi:[1,0]
v_pk_mul_f32 v[156:157], v[156:157], v[10:11] op_sel_hi:[1,0]
v_pk_mul_f32 v[158:159], v[158:159], v[10:11] op_sel_hi:[1,0]
cv 116,152,153
cv 117,154,155
cv 118,156,157
cv 119,158,159
v_pk_mul_f32 v[160:161], v[160:161], v[8:9] op_sel_hi:[1,0]
v_pk_mul_f32 v[162:163], v[162:163], v[8:9] op_sel_hi:[1,0]
v_pk_mul_f32 v[164:165], v[164:165], v[8:9] op_sel_hi:[1,0]
v_pk_mul_f32 v[166:167], v[166:167], v[8:9] op_sel_hi:[1,0]
cv 120,160,161
cv 121,162,163
cv 122,164,165
cv 123,166,167
v_pk_mul_f32 v[168:169], v[168:169], v[10:11] op_sel_hi:[1,0]
v_pk_mul_f32 v[170:171], v[170:171], v[10:11] op_sel_hi:[1,0]
v_pk_mul_f32 v[172:173], v[172:173], v[10:11] op_sel_hi:[1,0]
v_pk_mul_f32 v[174:175], v[174:175], v[10:11] op_sel_hi:[1,0]
cv 124,168,169
cv 125,170,171
cv 126,172,173
cv 127,174,175
v_pk_mul_f32 v[176:177], v[176:177], v[8:9] op_sel_hi:[1,0]
v_pk_mul_f32 v[178:179], v[178:179], v[8:9] op_sel_hi:[1,0]
v_pk_mul_f32 v[180:181], v[180:181], v[8:9] op_sel_hi:[1,0]
v_pk_mul_f32 v[182:183], v[182:183], v[8:9] op_sel_hi:[1,0]
cv 128,176,177
cv 129,178,179
cv 130,180,181
cv 131,182,183
v_pk_mul_f32 v[184:185], v[184:185], v[10:11] op_sel_hi:[1,0]
v_pk_mul_f32 v[186:187], v[186:187], v[10:11] op_sel_hi:[1,0]
v_pk_mul_f32 v[188:189], v[188:189], v[10:11] op_sel_hi:[1,0]
v_pk_mul_f32 v[190:191], v[190:191], v[10:11] op_sel_hi:[1,0]
cv 132,184,185
cv 133,186,187
cv 134,188,189
cv 135,190,191
v_pk_mul_f32 v[192:193], v[192:193], v[8:9] op_sel_hi:[1,0]
v_pk_mul_f32 v[194:195], v[194:195], v[8:9] op_sel_hi:[1,0]
v_pk_mul_f32 v[196:197], v[196:197], v[8:9] op_sel_hi:[1,0]
v_pk_mul_f32 v[198:199], v[198:199], v[8:9] op_sel_hi:[1,0]
cv 136,192,193
cv 137,194,195
cv 138,196,197
cv 139,198,199
v_pk_mul_f32 v[200:201], v[200:201], v[10:11] op_sel_hi:[1,0]
v_pk_mul_f32 v[202:203], v[202:203], v[10:11] op_sel_hi:[1,0]
v_pk_mul_f32 v[204:205], v[204:205], v[10:11] op_sel_hi:[1,0]
v_pk_mul_f32 v[206:207], v[206:207], v[10:11] op_sel_hi:[1,0]
cv 140,200,201
cv 141,202,203
cv 142,204,205
cv 143,206,207
s_branch .Lk2_op
.Lk2_fin0_fB:
s_waitcnt lgkmcnt(0)
rqa 64,2,0
rqa 68,2,1024
rqa 72,2,2048
rqa 76,2,3072
rqa 80,2,4096
rqa 84,2,5120
rqa 88,2,6144
rqa 92,2,7168
rqa 96,2,8192
rqa 100,2,9216
rqa 104,2,10240
rqa 108,2,11264
rqa 112,2,12288
rqa 116,2,13312
rqa 120,2,14336
rqa 124,2,15360
ex 44
ex 45
ex 46
ex 47
ex 52
ex 53
ex 54
ex 55
ex 48
ex 49
ex 50
ex 51
ex 56
ex 57
ex 58
ex 59
cv 28,44,45
cv 29,46,47
cv 30,52,53
cv 31,54,55
cv 32,48,49
cv 33,50,51
cv 34,56,57
cv 35,58,59
s_waitcnt vmcnt(12) lgkmcnt(8)
mvav 80,64,28
s_add_u32 m0, s96, 0
v_sub_f32 v252, 1.0, v6
cv 252,252,252
mvav 88,64,32
dm 244,88,8
v_and_b32 v252, 0xffff0000, v252
v_sub_f32 v253, 1.0, v7
mvav 84,68,28
s_add_u32 m0, s96, 1024
cv 253,253,253
v_and_b32 v253, 0xffff0000, v253
mvav 92,68,32
mvvv 36,16,28
dm 245,88,8
v_max_f32 v60, v232, v234
v_sub_f32 v61, v232, v60
mvav 96,72,28
s_add_u32 m0, s96, 2048
v_sub_f32 v62, v234, v60
ex 61
mvav 104,72,32
dm 246,88,8
ex 62
s_nop 0
v_add_f32 v60, v61, v62
mvav 100,76,28
s_add_u32 m0, s96, 3072
v_rcp_f32 v60, v60
s_nop 0
v_mul_f32 v60, v60, v6
mvav 108,76,32
mvvv 40,16,32
dm 247,88,8
v_mul_f32 v61, v61, v60
v_mul_f32 v62, v62, v60
mvav 112,80,28
s_add_u32 m0, s96, 4096
v_lshlrev_b32 v64, 16, v216
v_and_b32 v65, 0xffff0000, v216
mvav 120,80,32
dm 248,88,8
v_lshlrev_b32 v66, 16, v224
v_and_b32 v67, 0xffff0000, v224
mvav 116,84,28
s_add_u32 m0, s96, 5120
v_mul_f32 v64, v61, v64
v_mul_f32 v65, v61, v65
mvav 124,84,32
dm 249,88,8
v_fmac_f32 v64, v62, v66
v_fmac_f32 v65, v62, v67
mvav 128,88,28
s_add_u32 m0, s96, 6144
cv 208,64,65
v_lshlrev_b32 v64, 16, v217
mvav 136,88,32
dm 250,88,8
v_and_b32 v65, 0xffff0000, v217
v_lshlrev_b32 v66, 16, v225
mvav 132,92,28
s_add_u32 m0, s96, 28672
v_and_b32 v67, 0xffff0000, v225
v_mul_f32 v64, v61, v64
mvav 140,92,32
dm 244,88,9
v_mul_f32 v65, v61, v65
v_fmac_f32 v64, v62, v66
s_waitcnt lgkmcnt(0)
mvav 144,96,28
s_add_u32 m0, s96, 29696
v_fmac_f32 v65, v62, v67
cv 209,64,65
mvav 152,96,32
dm 245,88,9
v_lshlrev_b32 v64, 16, v218
v_and_b32 v65, 0xffff0000, v218
mvav 148,100,28
s_add_u32 m0, s96, 30720
v_lshlrev_b32 v66, 16, v226
v_and_b32 v67, 0xffff0000, v226
mvav 156,100,32
dm 246,88,9
v_mul_f32 v64, v61, v64
v_mul_f32 v65, v61, v65
mvav 160,104,28
s_add_u32 m0, s96, 31744
v_fmac_f32 v64, v62, v66
v_fmac_f32 v65, v62, v67
mvav 168,104,32
dm 247,88,9
cv 210,64,65
v_lshlrev_b32 v64, 16, v219
mvav 164,108,28
s_add_u32 m0, s96, 32768
v_and_b32 v65, 0xffff0000, v219
v_lshlrev_b32 v66, 16, v227
mvav 172,108,32
dm 248,88,9
v_and_b32 v67, 0xffff0000, v227
v_mul_f32 v64, v61, v64
mvav 176,112,28
s_add_u32 m0, s96, 33792
v_mul_f32 v65, v61, v65
v_fmac_f32 v64, v62, v66
mvav 184,112,32
dm 249,88,9
v_fmac_f32 v65, v62, v67
cv 211,64,65
mvav 180,116,28
s_add_u32 m0, s96, 34816
s_mov_b64 exec, s[60:61]
v_mov_b32 v210, 0
v_mov_b32 v211, 0
s_mov_b64 exec, -1
s_nop 2
v_max_f32 v60, v233, v235
mvav 188,116,32
dm 250,88,9
v_sub_f32 v61, v233, v60
v_sub_f32 v62, v235, v60
mvav 192,120,28
ex 61
ex 62
s_nop 0
mvav 200,120,32
v_add_f32 v60, v61, v62
v_rcp_f32 v60, v60
s_nop 0
mvav 196,124,28
v_mul_f32 v60, v60, v7
v_mul_f32 v61, v61, v60
mvav 204,124,32
v_mul_f32 v62, v62, v60
v_lshlrev_b32 v64, 16, v220
v_and_b32 v65, 0xffff0000, v220
v_lshlrev_b32 v66, 16, v228
v_and_b32 v67, 0xffff0000, v228
v_mul_f32 v64, v61, v64
v_mul_f32 v65, v61, v65
v_fmac_f32 v64, v62, v66
v_fmac_f32 v65, v62, v67
cv 212,64,65
v_lshlrev_b32 v64, 16, v221
v_and_b32 v65, 0xffff0000, v221
v_lshlrev_b32 v66, 16, v229
v_and_b32 v67, 0xffff0000, v229
v_mul_f32 v64, v61, v64
v_mul_f32 v65, v61, v65
v_fmac_f32 v64, v62, v66
v_fmac_f32 v65, v62, v67
cv 213,64,65
v_lshlrev_b32 v64, 16, v222
v_and_b32 v65, 0xffff0000, v222
v_lshlrev_b32 v66, 16, v230
v_and_b32 v67, 0xffff0000, v230
v_mul_f32 v64, v61, v64
v_mul_f32 v65, v61, v65
v_fmac_f32 v64, v62, v66
v_fmac_f32 v65, v62, v67
cv 214,64,65
v_lshlrev_b32 v64, 16, v223
v_and_b32 v65, 0xffff0000, v223
v_lshlrev_b32 v66, 16, v231
v_and_b32 v67, 0xffff0000, v231
v_mul_f32 v64, v61, v64
v_mul_f32 v65, v61, v65
v_fmac_f32 v64, v62, v66
v_fmac_f32 v65, v62, v67
cv 215,64,65
s_mov_b64 exec, s[60:61]
v_mov_b32 v214, 0
v_mov_b32 v215, 0
s_mov_b64 exec, -1
s_nop 2
v_mov_b32 v24, v251
v_add_u32 v25, 57344, v251
v_add_u32 v26, 114688, v251
s_mov_b32 s99, s98
s_and_b32 s98, s98, 0xfc
s_mov_b32 s3, 0x20000
s_nop 3
v_rcp_f32 v8, v36
v_rcp_f32 v10, v40
s_nop 0
v_mul_f32 v8, v8, v252
v_mul_f32 v10, v10, v253
v_pk_mul_f32 v[80:81], v[80:81], v[8:9] op_sel_hi:[1,0]
v_pk_mul_f32 v[82:83], v[82:83], v[8:9] op_sel_hi:[1,0]
v_pk_mul_f32 v[84:85], v[84:85], v[8:9] op_sel_hi:[1,0]
v_pk_mul_f32 v[86:87], v[86:87], v[8:9] op_sel_hi:[1,0]
cv 80,80,81
cv 81,82,83
cv 82,84,85
cv 83,86,87
v_pk_mul_f32 v[88:89], v[88:89], v[10:11] op_sel_hi:[1,0]
v_pk_mul_f32 v[90:91], v[90:91], v[10:11] op_sel_hi:[1,0]
v_pk_mul_f32 v[92:93], v[92:93], v[10:11] op_sel_hi:[1,0]
v_pk_mul_f32 v[94:95], v[94:95], v[10:11] op_sel_hi:[1,0]
cv 84,88,89
cv 85,90,91
cv 86,92,93
cv 87,94,95
v_pk_mul_f32 v[96:97], v[96:97], v[8:9] op_sel_hi:[1,0]
v_pk_mul_f32 v[98:99], v[98:99], v[8:9] op_sel_hi:[1,0]
v_pk_mul_f32 v[100:101], v[100:101], v[8:9] op_sel_hi:[1,0]
v_pk_mul_f32 v[102:103], v[102:103], v[8:9] op_sel_hi:[1,0]
cv 88,96,97
cv 89,98,99
cv 90,100,101
cv 91,102,103
v_pk_mul_f32 v[104:105], v[104:105], v[10:11] op_sel_hi:[1,0]
v_pk_mul_f32 v[106:107], v[106:107], v[10:11] op_sel_hi:[1,0]
v_pk_mul_f32 v[108:109], v[108:109], v[10:11] op_sel_hi:[1,0]
v_pk_mul_f32 v[110:111], v[110:111], v[10:11] op_sel_hi:[1,0]
cv 92,104,105
cv 93,106,107
cv 94,108,109
cv 95,110,111
v_pk_mul_f32 v[112:113], v[112:113], v[8:9] op_sel_hi:[1,0]
v_pk_mul_f32 v[114:115], v[114:115], v[8:9] op_sel_hi:[1,0]
v_pk_mul_f32 v[116:117], v[116:117], v[8:9] op_sel_hi:[1,0]
v_pk_mul_f32 v[118:119], v[118:119], v[8:9] op_sel_hi:[1,0]
cv 96,112,113
cv 97,114,115
cv 98,116,117
cv 99,118,119
v_pk_mul_f32 v[120:121], v[120:121], v[10:11] op_sel_hi:[1,0]
v_pk_mul_f32 v[122:123], v[122:123], v[10:11] op_sel_hi:[1,0]
v_pk_mul_f32 v[124:125], v[124:125], v[10:11] op_sel_hi:[1,0]
v_pk_mul_f32 v[126:127], v[126:127], v[10:11] op_sel_hi:[1,0]
cv 100,120,121
cv 101,122,123
cv 102,124,125
cv 103,126,127
v_pk_mul_f32 v[128:129], v[128:129], v[8:9] op_sel_hi:[1,0]
v_pk_mul_f32 v[130:131], v[130:131], v[8:9] op_sel_hi:[1,0]
v_pk_mul_f32 v[132:133], v[132:133], v[8:9] op_sel_hi:[1,0]
v_pk_mul_f32 v[134:135], v[134:135], v[8:9] op_sel_hi:[1,0]
cv 104,128,129
cv 105,130,131
cv 106,132,133
cv 107,134,135
v_pk_mul_f32 v[136:137], v[136:137], v[10:11] op_sel_hi:[1,0]
v_pk_mul_f32 v[138:139], v[138:139], v[10:11] op_sel_hi:[1,0]
v_pk_mul_f32 v[140:141], v[140:141], v[10:11] op_sel_hi:[1,0]
v_pk_mul_f32 v[142:143], v[142:143], v[10:11] op_sel_hi:[1,0]
cv 108,136,137
cv 109,138,139
cv 110,140,141
cv 111,142,143
v_pk_mul_f32 v[144:145], v[144:145], v[8:9] op_sel_hi:[1,0]
v_pk_mul_f32 v[146:147], v[146:147], v[8:9] op_sel_hi:[1,0]
v_pk_mul_f32 v[148:149], v[148:149], v[8:9] op_sel_hi:[1,0]
v_pk_mul_f32 v[150:151], v[150:151], v[8:9] op_sel_hi:[1,0]
cv 112,144,145
cv 113,146,147
cv 114,148,149
cv 115,150,151
v_pk_mul_f32 v[152:153], v[152:153], v[10:11] op_sel_hi:[1,0]
v_pk_mul_f32 v[154:155], v[154:155], v[10:11] op_sel_hi:[1,0]
v_pk_mul_f32 v[156:157], v[156:157], v[10:11] op_sel_hi:[1,0]
v_pk_mul_f32 v[158:159], v[158:159], v[10:11] op_sel_hi:[1,0]
cv 116,152,153
cv 117,154,155
cv 118,156,157
cv 119,158,159
v_pk_mul_f32 v[160:161], v[160:161], v[8:9] op_sel_hi:[1,0]
v_pk_mul_f32 v[162:163], v[162:163], v[8:9] op_sel_hi:[1,0]
v_pk_mul_f32 v[164:165], v[164:165], v[8:9] op_sel_hi:[1,0]
v_pk_mul_f32 v[166:167], v[166:167], v[8:9] op_sel_hi:[1,0]
cv 120,160,161
cv 121,162,163
cv 122,164,165
cv 123,166,167
v_pk_mul_f32 v[168:169], v[168:169], v[10:11] op_sel_hi:[1,0]
v_pk_mul_f32 v[170:171], v[170:171], v[10:11] op_sel_hi:[1,0]
v_pk_mul_f32 v[172:173], v[172:173], v[10:11] op_sel_hi:[1,0]
v_pk_mul_f32 v[174:175], v[174:175], v[10:11] op_sel_hi:[1,0]
cv 124,168,169
cv 125,170,171
cv 126,172,173
cv 127,174,175
v_pk_mul_f32 v[176:177], v[176:177], v[8:9] op_sel_hi:[1,0]
v_pk_mul_f32 v[178:179], v[178:179], v[8:9] op_sel_hi:[1,0]
v_pk_mul_f32 v[180:181], v[180:181], v[8:9] op_sel_hi:[1,0]
v_pk_mul_f32 v[182:183], v[182:183], v[8:9] op_sel_hi:[1,0]
cv 128,176,177
cv 129,178,179
cv 130,180,181
cv 131,182,183
v_pk_mul_f32 v[184:185], v[184:185], v[10:11] op_sel_hi:[1,0]
v_pk_mul_f32 v[186:187], v[186:187], v[10:11] op_sel_hi:[1,0]
v_pk_mul_f32 v[188:189], v[188:189], v[10:11] op_sel_hi:[1,0]
v_pk_mul_f32 v[190:191], v[190:191], v[10:11] op_sel_hi:[1,0]
cv 132,184,185
cv 133,186,187
cv 134,188,189
cv 135,190,191
v_pk_mul_f32 v[192:193], v[192:193], v[8:9] op_sel_hi:[1,0]
v_pk_mul_f32 v[194:195], v[194:195], v[8:9] op_sel_hi:[1,0]
v_pk_mul_f32 v[196:197], v[196:197], v[8:9] op_sel_hi:[1,0]
v_pk_mul_f32 v[198:199], v[198:199], v[8:9] op_sel_hi:[1,0]
cv 136,192,193
cv 137,194,195
cv 138,196,197
cv 139,198,199
v_pk_mul_f32 v[200:201], v[200:201], v[10:11] op_sel_hi:[1,0]
v_pk_mul_f32 v[202:203], v[202:203], v[10:11] op_sel_hi:[1,0]
v_pk_mul_f32 v[204:205], v[204:205], v[10:11] op_sel_hi:[1,0]
v_pk_mul_f32 v[206:207], v[206:207], v[10:11] op_sel_hi:[1,0]
)ASM" ::: "memory");
  asm volatile(R"ASM(
cv 140,200,201
cv 141,202,203
cv 142,204,205
cv 143,206,207
s_branch .Lk2_op
.Lk2_fin1_fA:
s_waitcnt lgkmcnt(0)
rqa 64,2,0
rqa 68,2,1024
rqa 72,2,2048
rqa 76,2,3072
rqa 80,2,4096
rqa 84,2,5120
rqa 88,2,6144
rqa 92,2,7168
rqa 96,2,8192
rqa 100,2,9216
rqa 104,2,10240
rqa 108,2,11264
rqa 112,2,12288
rqa 116,2,13312
rqa 120,2,14336
rqa 124,2,15360
ex 60
ex 61
ex 62
ex 63
ex 68
ex 69
ex 70
ex 71
ex 64
ex 65
ex 66
ex 67
ex 72
ex 73
ex 74
ex 75
cv 28,60,61
cv 29,62,63
cv 30,68,69
cv 31,70,71
cv 32,64,65
cv 33,66,67
cv 34,72,73
cv 35,74,75
v_mov_b64 v[236:237], 0
v_mov_b64 v[240:241], 0
v_mov_b32 v238, v78
v_mov_b32 v239, v13
s_waitcnt vmcnt(12) lgkmcnt(8)
mvav 80,64,28
s_add_u32 m0, s96, 0
v_sub_f32 v8, v4, v242
v_min_f32 v8, 0x42a00000, v8
mvav 88,64,32
dm 244,88,8
v_max_f32 v8, 0xc2a00000, v8
ex 8
s_nop 0
mvav 84,68,28
s_add_u32 m0, s96, 1024
v_sub_f32 v10, v5, v243
v_min_f32 v10, 0x42a00000, v10
mvav 92,68,32
mvvv 36,16,28
dm 245,88,8
v_max_f32 v10, 0xc2a00000, v10
ex 10
s_nop 0
mvav 96,72,28
s_add_u32 m0, s96, 2048
v_sub_f32 v252, 1.0, v6
cv 252,252,252
mvav 104,72,32
dm 246,88,8
v_and_b32 v252, 0xffff0000, v252
v_sub_f32 v253, 1.0, v7
mvav 100,76,28
s_add_u32 m0, s96, 3072
cv 253,253,253
v_and_b32 v253, 0xffff0000, v253
mvav 108,76,32
mvvv 40,16,32
dm 247,88,8
v_max_f32 v44, v232, v234
v_sub_f32 v45, v232, v44
mvav 112,80,28
s_add_u32 m0, s96, 4096
v_pk_mul_f32 v[80:81], v[80:81], v[8:9] op_sel_hi:[1,0]
v_pk_mul_f32 v[82:83], v[82:83], v[8:9] op_sel_hi:[1,0]
v_sub_f32 v46, v234, v44
mvav 120,80,32
dm 248,88,8
v_pk_mul_f32 v[88:89], v[88:89], v[10:11] op_sel_hi:[1,0]
v_pk_mul_f32 v[90:91], v[90:91], v[10:11] op_sel_hi:[1,0]
ex 45
mvav 116,84,28
s_add_u32 m0, s96, 5120
v_pk_mul_f32 v[84:85], v[84:85], v[8:9] op_sel_hi:[1,0]
v_pk_mul_f32 v[86:87], v[86:87], v[8:9] op_sel_hi:[1,0]
ex 46
s_nop 0
mvav 124,84,32
dm 249,88,8
v_pk_mul_f32 v[92:93], v[92:93], v[10:11] op_sel_hi:[1,0]
v_pk_mul_f32 v[94:95], v[94:95], v[10:11] op_sel_hi:[1,0]
v_add_f32 v44, v45, v46
mvav 128,88,28
s_add_u32 m0, s96, 6144
v_pk_mul_f32 v[96:97], v[96:97], v[8:9] op_sel_hi:[1,0]
v_pk_mul_f32 v[98:99], v[98:99], v[8:9] op_sel_hi:[1,0]
mvva 80,238,192
v_rcp_f32 v44, v44
s_nop 0
mvav 136,88,32
dm 250,88,8
v_pk_mul_f32 v[104:105], v[104:105], v[10:11] op_sel_hi:[1,0]
v_pk_mul_f32 v[106:107], v[106:107], v[10:11] op_sel_hi:[1,0]
mvva 88,238,196
v_mul_f32 v44, v44, v6
mvav 132,92,28
s_add_u32 m0, s96, 28672
v_pk_mul_f32 v[100:101], v[100:101], v[8:9] op_sel_hi:[1,0]
v_pk_mul_f32 v[102:103], v[102:103], v[8:9] op_sel_hi:[1,0]
mvva 84,236,192
v_mul_f32 v45, v45, v44
mvav 140,92,32
dm 244,88,9
v_pk_mul_f32 v[108:109], v[108:109], v[10:11] op_sel_hi:[1,0]
v_pk_mul_f32 v[110:111], v[110:111], v[10:11] op_sel_hi:[1,0]
mvva 92,236,196
v_mul_f32 v46, v46, v44
s_waitcnt lgkmcnt(0)
mvav 144,96,28
s_add_u32 m0, s96, 29696
v_pk_mul_f32 v[112:113], v[112:113], v[8:9] op_sel_hi:[1,0]
v_pk_mul_f32 v[114:115], v[114:115], v[8:9] op_sel_hi:[1,0]
mvva 96,238,200
v_lshlrev_b32 v48, 16, v216
mvav 152,96,32
dm 245,88,9
v_pk_mul_f32 v[120:121], v[120:121], v[10:11] op_sel_hi:[1,0]
v_pk_mul_f32 v[122:123], v[122:123], v[10:11] op_sel_hi:[1,0]
mvva 104,238,204
v_and_b32 v49, 0xffff0000, v216
mvav 148,100,28
s_add_u32 m0, s96, 30720
v_pk_mul_f32 v[116:117], v[116:117], v[8:9] op_sel_hi:[1,0]
v_pk_mul_f32 v[118:119], v[118:119], v[8:9] op_sel_hi:[1,0]
mvva 100,236,200
v_lshlrev_b32 v50, 16, v224
mvav 156,100,32
dm 246,88,9
v_pk_mul_f32 v[124:125], v[124:125], v[10:11] op_sel_hi:[1,0]
v_pk_mul_f32 v[126:127], v[126:127], v[10:11] op_sel_hi:[1,0]
mvva 108,236,204
v_and_b32 v51, 0xffff0000, v224
mvav 160,104,28
s_add_u32 m0, s96, 31744
v_pk_mul_f32 v[128:129], v[128:129], v[8:9] op_sel_hi:[1,0]
v_pk_mul_f32 v[130:131], v[130:131], v[8:9] op_sel_hi:[1,0]
mvva 112,238,208
v_mul_f32 v48, v45, v48
mvav 168,104,32
dm 247,88,9
v_pk_mul_f32 v[136:137], v[136:137], v[10:11] op_sel_hi:[1,0]
v_pk_mul_f32 v[138:139], v[138:139], v[10:11] op_sel_hi:[1,0]
mvva 120,238,212
v_mul_f32 v49, v45, v49
mvav 164,108,28
s_add_u32 m0, s96, 32768
v_pk_mul_f32 v[132:133], v[132:133], v[8:9] op_sel_hi:[1,0]
v_pk_mul_f32 v[134:135], v[134:135], v[8:9] op_sel_hi:[1,0]
mvva 116,236,208
v_fmac_f32 v48, v46, v50
mvav 172,108,32
dm 248,88,9
v_pk_mul_f32 v[140:141], v[140:141], v[10:11] op_sel_hi:[1,0]
v_pk_mul_f32 v[142:143], v[142:143], v[10:11] op_sel_hi:[1,0]
mvva 124,236,212
v_fmac_f32 v49, v46, v51
mvav 176,112,28
s_add_u32 m0, s96, 33792
v_pk_mul_f32 v[144:145], v[144:145], v[8:9] op_sel_hi:[1,0]
v_pk_mul_f32 v[146:147], v[146:147], v[8:9] op_sel_hi:[1,0]
mvva 128,238,216
cv 208,48,49
mvav 184,112,32
dm 249,88,9
v_pk_mul_f32 v[152:153], v[152:153], v[10:11] op_sel_hi:[1,0]
v_pk_mul_f32 v[154:155], v[154:155], v[10:11] op_sel_hi:[1,0]
mvva 136,238,220
v_lshlrev_b32 v48, 16, v217
mvav 180,116,28
s_add_u32 m0, s96, 34816
v_pk_mul_f32 v[148:149], v[148:149], v[8:9] op_sel_hi:[1,0]
v_pk_mul_f32 v[150:151], v[150:151], v[8:9] op_sel_hi:[1,0]
mvva 132,236,216
v_and_b32 v49, 0xffff0000, v217
mvav 188,116,32
dm 250,88,9
v_pk_mul_f32 v[156:157], v[156:157], v[10:11] op_sel_hi:[1,0]
v_pk_mul_f32 v[158:159], v[158:159], v[10:11] op_sel_hi:[1,0]
mvva 140,236,220
v_lshlrev_b32 v50, 16, v225
mvav 192,120,28
v_pk_mul_f32 v[160:161], v[160:161], v[8:9] op_sel_hi:[1,0]
v_pk_mul_f32 v[162:163], v[162:163], v[8:9] op_sel_hi:[1,0]
mvva 144,238,224
v_and_b32 v51, 0xffff0000, v225
mvav 200,120,32
v_pk_mul_f32 v[168:169], v[168:169], v[10:11] op_sel_hi:[1,0]
v_pk_mul_f32 v[170:171], v[170:171], v[10:11] op_sel_hi:[1,0]
mvva 152,238,228
v_mul_f32 v48, v45, v48
mvav 196,124,28
v_pk_mul_f32 v[164:165], v[164:165], v[8:9] op_sel_hi:[1,0]
v_pk_mul_f32 v[166:167], v[166:167], v[8:9] op_sel_hi:[1,0]
mvva 148,236,224
v_mul_f32 v49, v45, v49
mvav 204,124,32
v_pk_mul_f32 v[172:173], v[172:173], v[10:11] op_sel_hi:[1,0]
v_pk_mul_f32 v[174:175], v[174:175], v[10:11] op_sel_hi:[1,0]
mvva 156,236,228
v_fmac_f32 v48, v46, v50
v_pk_mul_f32 v[176:177], v[176:177], v[8:9] op_sel_hi:[1,0]
v_pk_mul_f32 v[178:179], v[178:179], v[8:9] op_sel_hi:[1,0]
v_fmac_f32 v49, v46, v51
mvva 160,238,232
v_pk_mul_f32 v[184:185], v[184:185], v[10:11] op_sel_hi:[1,0]
v_pk_mul_f32 v[186:187], v[186:187], v[10:11] op_sel_hi:[1,0]
cv 209,48,49
mvva 168,238,236
v_pk_mul_f32 v[180:181], v[180:181], v[8:9] op_sel_hi:[1,0]
v_pk_mul_f32 v[182:183], v[182:183], v[8:9] op_sel_hi:[1,0]
v_lshlrev_b32 v48, 16, v218
mvva 164,236,232
v_pk_mul_f32 v[188:189], v[188:189], v[10:11] op_sel_hi:[1,0]
v_pk_mul_f32 v[190:191], v[190:191], v[10:11] op_sel_hi:[1,0]
v_and_b32 v49, 0xffff0000, v218
mvva 172,236,236
v_pk_mul_f32 v[192:193], v[192:193], v[8:9] op_sel_hi:[1,0]
v_pk_mul_f32 v[194:195], v[194:195], v[8:9] op_sel_hi:[1,0]
v_lshlrev_b32 v50, 16, v226
mvva 176,238,240
v_pk_mul_f32 v[200:201], v[200:201], v[10:11] op_sel_hi:[1,0]
v_pk_mul_f32 v[202:203], v[202:203], v[10:11] op_sel_hi:[1,0]
v_and_b32 v51, 0xffff0000, v226
mvva 184,238,244
v_pk_mul_f32 v[196:197], v[196:197], v[8:9] op_sel_hi:[1,0]
v_pk_mul_f32 v[198:199], v[198:199], v[8:9] op_sel_hi:[1,0]
v_mul_f32 v48, v45, v48
mvva 180,236,240
v_pk_mul_f32 v[204:205], v[204:205], v[10:11] op_sel_hi:[1,0]
v_pk_mul_f32 v[206:207], v[206:207], v[10:11] op_sel_hi:[1,0]
v_mul_f32 v49, v45, v49
mvva 188,236,244
v_fmac_f32 v48, v46, v50
mvva 192,238,248
v_fmac_f32 v49, v46, v51
mvva 200,238,252
cv 210,48,49
mvva 196,236,248
v_lshlrev_b32 v48, 16, v219
mvva 204,236,252
v_and_b32 v49, 0xffff0000, v219
v_lshlrev_b32 v50, 16, v227
v_and_b32 v51, 0xffff0000, v227
v_mul_f32 v48, v45, v48
v_mul_f32 v49, v45, v49
v_fmac_f32 v48, v46, v50
v_fmac_f32 v49, v46, v51
cv 211,48,49
s_mov_b64 exec, s[60:61]
v_mov_b32 v210, 0
v_mov_b32 v211, 0
s_mov_b64 exec, -1
s_nop 2
v_max_f32 v44, v233, v235
v_sub_f32 v45, v233, v44
v_sub_f32 v46, v235, v44
ex 45
ex 46
s_nop 0
v_add_f32 v44, v45, v46
v_rcp_f32 v44, v44
s_nop 0
v_mul_f32 v44, v44, v7
v_mul_f32 v45, v45, v44
v_mul_f32 v46, v46, v44
v_lshlrev_b32 v48, 16, v220
v_and_b32 v49, 0xffff0000, v220
v_lshlrev_b32 v50, 16, v228
v_and_b32 v51, 0xffff0000, v228
v_mul_f32 v48, v45, v48
v_mul_f32 v49, v45, v49
v_fmac_f32 v48, v46, v50
v_fmac_f32 v49, v46, v51
cv 212,48,49
v_lshlrev_b32 v48, 16, v221
v_and_b32 v49, 0xffff0000, v221
v_lshlrev_b32 v50, 16, v229
v_and_b32 v51, 0xffff0000, v229
v_mul_f32 v48, v45, v48
v_mul_f32 v49, v45, v49
v_fmac_f32 v48, v46, v50
v_fmac_f32 v49, v46, v51
cv 213,48,49
v_lshlrev_b32 v48, 16, v222
v_and_b32 v49, 0xffff0000, v222
v_lshlrev_b32 v50, 16, v230
v_and_b32 v51, 0xffff0000, v230
v_mul_f32 v48, v45, v48
v_mul_f32 v49, v45, v49
v_fmac_f32 v48, v46, v50
v_fmac_f32 v49, v46, v51
cv 214,48,49
v_lshlrev_b32 v48, 16, v223
v_and_b32 v49, 0xffff0000, v223
v_lshlrev_b32 v50, 16, v231
v_and_b32 v51, 0xffff0000, v231
v_mul_f32 v48, v45, v48
v_mul_f32 v49, v45, v49
v_fmac_f32 v48, v46, v50
v_fmac_f32 v49, v46, v51
cv 215,48,49
s_mov_b64 exec, s[60:61]
v_mov_b32 v214, 0
v_mov_b32 v215, 0
s_mov_b64 exec, -1
s_nop 2
v_fma_f32 v36, v36, v8, 1.0
v_fma_f32 v40, v40, v10, 1.0
v_mov_b32 v24, v251
v_add_u32 v25, 57344, v251
v_add_u32 v26, 114688, v251
s_mov_b32 s99, s98
s_and_b32 s98, s98, 0xfc
s_mov_b32 s3, 0x20000
s_nop 3
v_rcp_f32 v8, v36
v_rcp_f32 v10, v40
s_nop 0
v_mul_f32 v8, v8, v252
v_mul_f32 v10, v10, v253
v_pk_mul_f32 v[80:81], v[80:81], v[8:9] op_sel_hi:[1,0]
v_pk_mul_f32 v[82:83], v[82:83], v[8:9] op_sel_hi:[1,0]
v_pk_mul_f32 v[84:85], v[84:85], v[8:9] op_sel_hi:[1,0]
v_pk_mul_f32 v[86:87], v[86:87], v[8:9] op_sel_hi:[1,0]
cv 80,80,81
cv 81,82,83
cv 82,84,85
cv 83,86,87
v_pk_mul_f32 v[88:89], v[88:89], v[10:11] op_sel_hi:[1,0]
v_pk_mul_f32 v[90:91], v[90:91], v[10:11] op_sel_hi:[1,0]
v_pk_mul_f32 v[92:93], v[92:93], v[10:11] op_sel_hi:[1,0]
v_pk_mul_f32 v[94:95], v[94:95], v[10:11] op_sel_hi:[1,0]
cv 84,88,89
cv 85,90,91
cv 86,92,93
cv 87,94,95
v_pk_mul_f32 v[96:97], v[96:97], v[8:9] op_sel_hi:[1,0]
v_pk_mul_f32 v[98:99], v[98:99], v[8:9] op_sel_hi:[1,0]
v_pk_mul_f32 v[100:101], v[100:101], v[8:9] op_sel_hi:[1,0]
v_pk_mul_f32 v[102:103], v[102:103], v[8:9] op_sel_hi:[1,0]
cv 88,96,97
cv 89,98,99
cv 90,100,101
cv 91,102,103
v_pk_mul_f32 v[104:105], v[104:105], v[10:11] op_sel_hi:[1,0]
v_pk_mul_f32 v[106:107], v[106:107], v[10:11] op_sel_hi:[1,0]
v_pk_mul_f32 v[108:109], v[108:109], v[10:11] op_sel_hi:[1,0]
v_pk_mul_f32 v[110:111], v[110:111], v[10:11] op_sel_hi:[1,0]
cv 92,104,105
cv 93,106,107
cv 94,108,109
cv 95,110,111
v_pk_mul_f32 v[112:113], v[112:113], v[8:9] op_sel_hi:[1,0]
v_pk_mul_f32 v[114:115], v[114:115], v[8:9] op_sel_hi:[1,0]
v_pk_mul_f32 v[116:117], v[116:117], v[8:9] op_sel_hi:[1,0]
v_pk_mul_f32 v[118:119], v[118:119], v[8:9] op_sel_hi:[1,0]
cv 96,112,113
cv 97,114,115
cv 98,116,117
cv 99,118,119
v_pk_mul_f32 v[120:121], v[120:121], v[10:11] op_sel_hi:[1,0]
v_pk_mul_f32 v[122:123], v[122:123], v[10:11] op_sel_hi:[1,0]
v_pk_mul_f32 v[124:125], v[124:125], v[10:11] op_sel_hi:[1,0]
v_pk_mul_f32 v[126:127], v[126:127], v[10:11] op_sel_hi:[1,0]
cv 100,120,121
cv 101,122,123
cv 102,124,125
cv 103,126,127
v_pk_mul_f32 v[128:129], v[128:129], v[8:9] op_sel_hi:[1,0]
v_pk_mul_f32 v[130:131], v[130:131], v[8:9] op_sel_hi:[1,0]
v_pk_mul_f32 v[132:133], v[132:133], v[8:9] op_sel_hi:[1,0]
v_pk_mul_f32 v[134:135], v[134:135], v[8:9] op_sel_hi:[1,0]
cv 104,128,129
cv 105,130,131
cv 106,132,133
cv 107,134,135
v_pk_mul_f32 v[136:137], v[136:137], v[10:11] op_sel_hi:[1,0]
v_pk_mul_f32 v[138:139], v[138:139], v[10:11] op_sel_hi:[1,0]
v_pk_mul_f32 v[140:141], v[140:141], v[10:11] op_sel_hi:[1,0]
v_pk_mul_f32 v[142:143], v[142:143], v[10:11] op_sel_hi:[1,0]
cv 108,136,137
cv 109,138,139
cv 110,140,141
cv 111,142,143
v_pk_mul_f32 v[144:145], v[144:145], v[8:9] op_sel_hi:[1,0]
v_pk_mul_f32 v[146:147], v[146:147], v[8:9] op_sel_hi:[1,0]
v_pk_mul_f32 v[148:149], v[148:149], v[8:9] op_sel_hi:[1,0]
v_pk_mul_f32 v[150:151], v[150:151], v[8:9] op_sel_hi:[1,0]
cv 112,144,145
cv 113,146,147
cv 114,148,149
cv 115,150,151
v_pk_mul_f32 v[152:153], v[152:153], v[10:11] op_sel_hi:[1,0]
v_pk_mul_f32 v[154:155], v[154:155], v[10:11] op_sel_hi:[1,0]
v_pk_mul_f32 v[156:157], v[156:157], v[10:11] op_sel_hi:[1,0]
v_pk_mul_f32 v[158:159], v[158:159], v[10:11] op_sel_hi:[1,0]
cv 116,152,153
cv 117,154,155
cv 118,156,157
cv 119,158,159
v_pk_mul_f32 v[160:161], v[160:161], v[8:9] op_sel_hi:[1,0]
v_pk_mul_f32 v[162:163], v[162:163], v[8:9] op_sel_hi:[1,0]
v_pk_mul_f32 v[164:165], v[164:165], v[8:9] op_sel_hi:[1,0]
v_pk_mul_f32 v[166:167], v[166:167], v[8:9] op_sel_hi:[1,0]
cv 120,160,161
cv 121,162,163
cv 122,164,165
cv 123,166,167
v_pk_mul_f32 v[168:169], v[168:169], v[10:11] op_sel_hi:[1,0]
v_pk_mul_f32 v[170:171], v[170:171], v[10:11] op_sel_hi:[1,0]
v_pk_mul_f32 v[172:173], v[172:173], v[10:11] op_sel_hi:[1,0]
v_pk_mul_f32 v[174:175], v[174:175], v[10:11] op_sel_hi:[1,0]
cv 124,168,169
cv 125,170,171
cv 126,172,173
cv 127,174,175
v_pk_mul_f32 v[176:177], v[176:177], v[8:9] op_sel_hi:[1,0]
v_pk_mul_f32 v[178:179], v[178:179], v[8:9] op_sel_hi:[1,0]
v_pk_mul_f32 v[180:181], v[180:181], v[8:9] op_sel_hi:[1,0]
v_pk_mul_f32 v[182:183], v[182:183], v[8:9] op_sel_hi:[1,0]
cv 128,176,177
cv 129,178,179
cv 130,180,181
cv 131,182,183
v_pk_mul_f32 v[184:185], v[184:185], v[10:11] op_sel_hi:[1,0]
v_pk_mul_f32 v[186:187], v[186:187], v[10:11] op_sel_hi:[1,0]
v_pk_mul_f32 v[188:189], v[188:189], v[10:11] op_sel_hi:[1,0]
v_pk_mul_f32 v[190:191], v[190:191], v[10:11] op_sel_hi:[1,0]
cv 132,184,185
cv 133,186,187
cv 134,188,189
cv 135,190,191
v_pk_mul_f32 v[192:193], v[192:193], v[8:9] op_sel_hi:[1,0]
v_pk_mul_f32 v[194:195], v[194:195], v[8:9] op_sel_hi:[1,0]
v_pk_mul_f32 v[196:197], v[196:197], v[8:9] op_sel_hi:[1,0]
v_pk_mul_f32 v[198:199], v[198:199], v[8:9] op_sel_hi:[1,0]
cv 136,192,193
cv 137,194,195
cv 138,196,197
cv 139,198,199
v_pk_mul_f32 v[200:201], v[200:201], v[10:11] op_sel_hi:[1,0]
v_pk_mul_f32 v[202:203], v[202:203], v[10:11] op_sel_hi:[1,0]
v_pk_mul_f32 v[204:205], v[204:205], v[10:11] op_sel_hi:[1,0]
v_pk_mul_f32 v[206:207], v[206:207], v[10:11] op_sel_hi:[1,0]
cv 140,200,201
cv 141,202,203
cv 142,204,205
cv 143,206,207
s_branch .Lk2_op
.Lk2_fin1_fB:
s_waitcnt lgkmcnt(0)
rqa 64,2,0
rqa 68,2,1024
rqa 72,2,2048
rqa 76,2,3072
rqa 80,2,4096
rqa 84,2,5120
rqa 88,2,6144
rqa 92,2,7168
rqa 96,2,8192
rqa 100,2,9216
rqa 104,2,10240
rqa 108,2,11264
rqa 112,2,12288
rqa 116,2,13312
rqa 120,2,14336
rqa 124,2,15360
ex 60
ex 61
ex 62
ex 63
ex 68
ex 69
ex 70
ex 71
ex 64
ex 65
ex 66
ex 67
ex 72
ex 73
ex 74
ex 75
cv 28,60,61
cv 29,62,63
cv 30,68,69
cv 31,70,71
cv 32,64,65
cv 33,66,67
cv 34,72,73
cv 35,74,75
s_waitcnt vmcnt(12) lgkmcnt(8)
mvav 80,64,28
s_add_u32 m0, s96, 0
v_sub_f32 v252, 1.0, v6
cv 252,252,252
mvav 88,64,32
dm 244,88,8
v_and_b32 v252, 0xffff0000, v252
v_sub_f32 v253, 1.0, v7
mvav 84,68,28
s_add_u32 m0, s96, 1024
cv 253,253,253
v_and_b32 v253, 0xffff0000, v253
mvav 92,68,32
mvvv 36,16,28
dm 245,88,8
v_max_f32 v44, v232, v234
v_sub_f32 v45, v232, v44
mvav 96,72,28
s_add_u32 m0, s96, 2048
v_sub_f32 v46, v234, v44
ex 45
mvav 104,72,32
dm 246,88,8
ex 46
s_nop 0
v_add_f32 v44, v45, v46
mvav 100,76,28
s_add_u32 m0, s96, 3072
v_rcp_f32 v44, v44
s_nop 0
v_mul_f32 v44, v44, v6
mvav 108,76,32
mvvv 40,16,32
dm 247,88,8
v_mul_f32 v45, v45, v44
v_mul_f32 v46, v46, v44
mvav 112,80,28
s_add_u32 m0, s96, 4096
v_lshlrev_b32 v48, 16, v216
v_and_b32 v49, 0xffff0000, v216
mvav 120,80,32
dm 248,88,8
v_lshlrev_b32 v50, 16, v224
v_and_b32 v51, 0xffff0000, v224
mvav 116,84,28
s_add_u32 m0, s96, 5120
v_mul_f32 v48, v45, v48
v_mul_f32 v49, v45, v49
mvav 124,84,32
dm 249,88,8
v_fmac_f32 v48, v46, v50
v_fmac_f32 v49, v46, v51
mvav 128,88,28
s_add_u32 m0, s96, 6144
cv 208,48,49
v_lshlrev_b32 v48, 16, v217
mvav 136,88,32
dm 250,88,8
v_and_b32 v49, 0xffff0000, v217
v_lshlrev_b32 v50, 16, v225
mvav 132,92,28
s_add_u32 m0, s96, 28672
v_and_b32 v51, 0xffff0000, v225
v_mul_f32 v48, v45, v48
mvav 140,92,32
dm 244,88,9
v_mul_f32 v49, v45, v49
v_fmac_f32 v48, v46, v50
s_waitcnt lgkmcnt(0)
mvav 144,96,28
s_add_u32 m0, s96, 29696
v_fmac_f32 v49, v46, v51
cv 209,48,49
mvav 152,96,32
dm 245,88,9
v_lshlrev_b32 v48, 16, v218
v_and_b32 v49, 0xffff0000, v218
mvav 148,100,28
s_add_u32 m0, s96, 30720
v_lshlrev_b32 v50, 16, v226
v_and_b32 v51, 0xffff0000, v226
)ASM" ::: "memory");
  asm volatile(R"ASM(
mvav 156,100,32
dm 246,88,9
v_mul_f32 v48, v45, v48
v_mul_f32 v49, v45, v49
mvav 160,104,28
s_add_u32 m0, s96, 31744
v_fmac_f32 v48, v46, v50
v_fmac_f32 v49, v46, v51
mvav 168,104,32
dm 247,88,9
cv 210,48,49
v_lshlrev_b32 v48, 16, v219
mvav 164,108,28
s_add_u32 m0, s96, 32768
v_and_b32 v49, 0xffff0000, v219
v_lshlrev_b32 v50, 16, v227
mvav 172,108,32
dm 248,88,9
v_and_b32 v51, 0xffff0000, v227
v_mul_f32 v48, v45, v48
mvav 176,112,28
s_add_u32 m0, s96, 33792
v_mul_f32 v49, v45, v49
v_fmac_f32 v48, v46, v50
mvav 184,112,32
dm 249,88,9
v_fmac_f32 v49, v46, v51
cv 211,48,49
mvav 180,116,28
s_add_u32 m0, s96, 34816
s_mov_b64 exec, s[60:61]
v_mov_b32 v210, 0
v_mov_b32 v211, 0
s_mov_b64 exec, -1
s_nop 2
v_max_f32 v44, v233, v235
mvav 188,116,32
dm 250,88,9
v_sub_f32 v45, v233, v44
v_sub_f32 v46, v235, v44
mvav 192,120,28
ex 45
ex 46
s_nop 0
mvav 200,120,32
v_add_f32 v44, v45, v46
v_rcp_f32 v44, v44
s_nop 0
mvav 196,124,28
v_mul_f32 v44, v44, v7
v_mul_f32 v45, v45, v44
mvav 204,124,32
v_mul_f32 v46, v46, v44
v_lshlrev_b32 v48, 16, v220
v_and_b32 v49, 0xffff0000, v220
v_lshlrev_b32 v50, 16, v228
v_and_b32 v51, 0xffff0000, v228
v_mul_f32 v48, v45, v48
v_mul_f32 v49, v45, v49
v_fmac_f32 v48, v46, v50
v_fmac_f32 v49, v46, v51
cv 212,48,49
v_lshlrev_b32 v48, 16, v221
v_and_b32 v49, 0xffff0000, v221
v_lshlrev_b32 v50, 16, v229
v_and_b32 v51, 0xffff0000, v229
v_mul_f32 v48, v45, v48
v_mul_f32 v49, v45, v49
v_fmac_f32 v48, v46, v50
v_fmac_f32 v49, v46, v51
cv 213,48,49
v_lshlrev_b32 v48, 16, v222
v_and_b32 v49, 0xffff0000, v222
v_lshlrev_b32 v50, 16, v230
v_and_b32 v51, 0xffff0000, v230
v_mul_f32 v48, v45, v48
v_mul_f32 v49, v45, v49
v_fmac_f32 v48, v46, v50
v_fmac_f32 v49, v46, v51
cv 214,48,49
v_lshlrev_b32 v48, 16, v223
v_and_b32 v49, 0xffff0000, v223
v_lshlrev_b32 v50, 16, v231
v_and_b32 v51, 0xffff0000, v231
v_mul_f32 v48, v45, v48
v_mul_f32 v49, v45, v49
v_fmac_f32 v48, v46, v50
v_fmac_f32 v49, v46, v51
cv 215,48,49
s_mov_b64 exec, s[60:61]
v_mov_b32 v214, 0
v_mov_b32 v215, 0
s_mov_b64 exec, -1
s_nop 2
v_mov_b32 v24, v251
v_add_u32 v25, 57344, v251
v_add_u32 v26, 114688, v251
s_mov_b32 s99, s98
s_and_b32 s98, s98, 0xfc
s_mov_b32 s3, 0x20000
s_nop 3
v_rcp_f32 v8, v36
v_rcp_f32 v10, v40
s_nop 0
v_mul_f32 v8, v8, v252
v_mul_f32 v10, v10, v253
v_pk_mul_f32 v[80:81], v[80:81], v[8:9] op_sel_hi:[1,0]
v_pk_mul_f32 v[82:83], v[82:83], v[8:9] op_sel_hi:[1,0]
v_pk_mul_f32 v[84:85], v[84:85], v[8:9] op_sel_hi:[1,0]
v_pk_mul_f32 v[86:87], v[86:87], v[8:9] op_sel_hi:[1,0]
cv 80,80,81
cv 81,82,83
cv 82,84,85
cv 83,86,87
v_pk_mul_f32 v[88:89], v[88:89], v[10:11] op_sel_hi:[1,0]
v_pk_mul_f32 v[90:91], v[90:91], v[10:11] op_sel_hi:[1,0]
v_pk_mul_f32 v[92:93], v[92:93], v[10:11] op_sel_hi:[1,0]
v_pk_mul_f32 v[94:95], v[94:95], v[10:11] op_sel_hi:[1,0]
cv 84,88,89
cv 85,90,91
cv 86,92,93
cv 87,94,95
v_pk_mul_f32 v[96:97], v[96:97], v[8:9] op_sel_hi:[1,0]
v_pk_mul_f32 v[98:99], v[98:99], v[8:9] op_sel_hi:[1,0]
v_pk_mul_f32 v[100:101], v[100:101], v[8:9] op_sel_hi:[1,0]
v_pk_mul_f32 v[102:103], v[102:103], v[8:9] op_sel_hi:[1,0]
cv 88,96,97
cv 89,98,99
cv 90,100,101
cv 91,102,103
v_pk_mul_f32 v[104:105], v[104:105], v[10:11] op_sel_hi:[1,0]
v_pk_mul_f32 v[106:107], v[106:107], v[10:11] op_sel_hi:[1,0]
v_pk_mul_f32 v[108:109], v[108:109], v[10:11] op_sel_hi:[1,0]
v_pk_mul_f32 v[110:111], v[110:111], v[10:11] op_sel_hi:[1,0]
cv 92,104,105
cv 93,106,107
cv 94,108,109
cv 95,110,111
v_pk_mul_f32 v[112:113], v[112:113], v[8:9] op_sel_hi:[1,0]
v_pk_mul_f32 v[114:115], v[114:115], v[8:9] op_sel_hi:[1,0]
v_pk_mul_f32 v[116:117], v[116:117], v[8:9] op_sel_hi:[1,0]
v_pk_mul_f32 v[118:119], v[118:119], v[8:9] op_sel_hi:[1,0]
cv 96,112,113
cv 97,114,115
cv 98,116,117
cv 99,118,119
v_pk_mul_f32 v[120:121], v[120:121], v[10:11] op_sel_hi:[1,0]
v_pk_mul_f32 v[122:123], v[122:123], v[10:11] op_sel_hi:[1,0]
v_pk_mul_f32 v[124:125], v[124:125], v[10:11] op_sel_hi:[1,0]
v_pk_mul_f32 v[126:127], v[126:127], v[10:11] op_sel_hi:[1,0]
cv 100,120,121
cv 101,122,123
cv 102,124,125
cv 103,126,127
v_pk_mul_f32 v[128:129], v[128:129], v[8:9] op_sel_hi:[1,0]
v_pk_mul_f32 v[130:131], v[130:131], v[8:9] op_sel_hi:[1,0]
v_pk_mul_f32 v[132:133], v[132:133], v[8:9] op_sel_hi:[1,0]
v_pk_mul_f32 v[134:135], v[134:135], v[8:9] op_sel_hi:[1,0]
cv 104,128,129
cv 105,130,131
cv 106,132,133
cv 107,134,135
v_pk_mul_f32 v[136:137], v[136:137], v[10:11] op_sel_hi:[1,0]
v_pk_mul_f32 v[138:139], v[138:139], v[10:11] op_sel_hi:[1,0]
v_pk_mul_f32 v[140:141], v[140:141], v[10:11] op_sel_hi:[1,0]
v_pk_mul_f32 v[142:143], v[142:143], v[10:11] op_sel_hi:[1,0]
cv 108,136,137
cv 109,138,139
cv 110,140,141
cv 111,142,143
v_pk_mul_f32 v[144:145], v[144:145], v[8:9] op_sel_hi:[1,0]
v_pk_mul_f32 v[146:147], v[146:147], v[8:9] op_sel_hi:[1,0]
v_pk_mul_f32 v[148:149], v[148:149], v[8:9] op_sel_hi:[1,0]
v_pk_mul_f32 v[150:151], v[150:151], v[8:9] op_sel_hi:[1,0]
cv 112,144,145
cv 113,146,147
cv 114,148,149
cv 115,150,151
v_pk_mul_f32 v[152:153], v[152:153], v[10:11] op_sel_hi:[1,0]
v_pk_mul_f32 v[154:155], v[154:155], v[10:11] op_sel_hi:[1,0]
v_pk_mul_f32 v[156:157], v[156:157], v[10:11] op_sel_hi:[1,0]
v_pk_mul_f32 v[158:159], v[158:159], v[10:11] op_sel_hi:[1,0]
cv 116,152,153
cv 117,154,155
cv 118,156,157
cv 119,158,159
v_pk_mul_f32 v[160:161], v[160:161], v[8:9] op_sel_hi:[1,0]
v_pk_mul_f32 v[162:163], v[162:163], v[8:9] op_sel_hi:[1,0]
v_pk_mul_f32 v[164:165], v[164:165], v[8:9] op_sel_hi:[1,0]
v_pk_mul_f32 v[166:167], v[166:167], v[8:9] op_sel_hi:[1,0]
cv 120,160,161
cv 121,162,163
cv 122,164,165
cv 123,166,167
v_pk_mul_f32 v[168:169], v[168:169], v[10:11] op_sel_hi:[1,0]
v_pk_mul_f32 v[170:171], v[170:171], v[10:11] op_sel_hi:[1,0]
v_pk_mul_f32 v[172:173], v[172:173], v[10:11] op_sel_hi:[1,0]
v_pk_mul_f32 v[174:175], v[174:175], v[10:11] op_sel_hi:[1,0]
cv 124,168,169
cv 125,170,171
cv 126,172,173
cv 127,174,175
v_pk_mul_f32 v[176:177], v[176:177], v[8:9] op_sel_hi:[1,0]
v_pk_mul_f32 v[178:179], v[178:179], v[8:9] op_sel_hi:[1,0]
v_pk_mul_f32 v[180:181], v[180:181], v[8:9] op_sel_hi:[1,0]
v_pk_mul_f32 v[182:183], v[182:183], v[8:9] op_sel_hi:[1,0]
cv 128,176,177
cv 129,178,179
cv 130,180,181
cv 131,182,183
v_pk_mul_f32 v[184:185], v[184:185], v[10:11] op_sel_hi:[1,0]
v_pk_mul_f32 v[186:187], v[186:187], v[10:11] op_sel_hi:[1,0]
v_pk_mul_f32 v[188:189], v[188:189], v[10:11] op_sel_hi:[1,0]
v_pk_mul_f32 v[190:191], v[190:191], v[10:11] op_sel_hi:[1,0]
cv 132,184,185
cv 133,186,187
cv 134,188,189
cv 135,190,191
v_pk_mul_f32 v[192:193], v[192:193], v[8:9] op_sel_hi:[1,0]
v_pk_mul_f32 v[194:195], v[194:195], v[8:9] op_sel_hi:[1,0]
v_pk_mul_f32 v[196:197], v[196:197], v[8:9] op_sel_hi:[1,0]
v_pk_mul_f32 v[198:199], v[198:199], v[8:9] op_sel_hi:[1,0]
cv 136,192,193
cv 137,194,195
cv 138,196,197
cv 139,198,199
v_pk_mul_f32 v[200:201], v[200:201], v[10:11] op_sel_hi:[1,0]
v_pk_mul_f32 v[202:203], v[202:203], v[10:11] op_sel_hi:[1,0]
v_pk_mul_f32 v[204:205], v[204:205], v[10:11] op_sel_hi:[1,0]
v_pk_mul_f32 v[206:207], v[206:207], v[10:11] op_sel_hi:[1,0]
cv 140,200,201
cv 141,202,203
cv 142,204,205
cv 143,206,207
s_branch .Lk2_op
.Lk2_ev10:
v_cmp_lt_f32 vcc, s71, v8
s_mov_b32 s67, 0
s_nop 3
s_cbranch_vccnz .Lk2_rare0
.Lk2_ret10:
s_cmp_eq_u32 s66, s49
s_cbranch_scc1 .Lk2_seam11
s_branch .Lk2_reg11
.Lk2_ev11:
v_cmp_lt_f32 vcc, s71, v8
s_mov_b32 s67, 0
s_nop 3
s_cbranch_vccnz .Lk2_rare1
.Lk2_ret11:
s_cmp_eq_u32 s66, s49
s_cbranch_scc1 .Lk2_seam12
s_cmp_eq_u32 s66, s63
s_cbranch_scc1 .Lk2_spc12
s_branch .Lk2_reg12
.Lk2_ev12:
v_cmp_lt_f32 vcc, s71, v8
s_mov_b32 s67, 1
s_nop 3
s_cbranch_vccnz .Lk2_rare0
.Lk2_ret12:
s_cmp_eq_u32 s66, s49
s_cbranch_scc1 .Lk2_seam13
s_branch .Lk2_reg13
.Lk2_ev13:
v_cmp_lt_f32 vcc, s71, v8
s_mov_b32 s67, 1
s_nop 3
s_cbranch_vccnz .Lk2_rare1
.Lk2_ret13:
s_cmp_eq_u32 s66, s49
s_cbranch_scc1 .Lk2_seam10
s_branch .Lk2_reg10
.Lk2_ev20:
v_cmp_lt_f32 vcc, s71, v8
s_mov_b32 s67, 2
s_nop 3
s_cbranch_vccnz .Lk2_rare0
.Lk2_ret20:
s_cmp_eq_u32 s66, s50
s_cbranch_scc1 .Lk2_fin21
s_cmp_eq_u32 s66, s63
s_cbranch_scc1 .Lk2_spc21
s_branch .Lk2_reg21
.Lk2_ev21:
v_cmp_lt_f32 vcc, s71, v8
s_mov_b32 s67, 2
s_nop 3
s_cbranch_vccnz .Lk2_rare1
.Lk2_ret21:
s_cmp_eq_u32 s66, s50
s_cbranch_scc1 .Lk2_fin22
s_cmp_eq_u32 s66, s63
s_cbranch_scc1 .Lk2_spc22
s_branch .Lk2_reg22
.Lk2_ev22:
v_cmp_lt_f32 vcc, s71, v8
s_mov_b32 s67, 3
s_nop 3
s_cbranch_vccnz .Lk2_rare0
.Lk2_ret22:
s_cmp_eq_u32 s66, s50
s_cbranch_scc1 .Lk2_fin23
s_cmp_eq_u32 s66, s63
s_cbranch_scc1 .Lk2_spc23
s_branch .Lk2_reg23
.Lk2_ev23:
v_cmp_lt_f32 vcc, s71, v8
s_mov_b32 s67, 3
s_nop 3
s_cbranch_vccnz .Lk2_rare1
.Lk2_ret23:
s_cmp_eq_u32 s66, s50
s_cbranch_scc1 .Lk2_fin20
s_cmp_eq_u32 s66, s63
s_cbranch_scc1 .Lk2_spc20
s_branch .Lk2_reg20
.Lk2_rare0:
s_nop 7
m3 208,60,61,62
m3 209,63,68,69
m3 208,208,70,71
v_max_f32 v208, v208, v209
v_mov_b32 v209, v208
s_nop 1
v_permlane32_swap_b32 v208, v209
s_nop 0
v_max_f32 v208, v208, v209
v_mov_b32 v209, v208
s_nop 1
v_permlane16_swap_b32 v208, v209
s_nop 0
v_max_f32 v208, v208, v209
v_max_f32 v208, 0, v208
v_add_f32 v4, v4, v208
v_sub_f32 v210, 0, v208
ex 210
v_sub_f32 v60, v60, v208
v_sub_f32 v61, v61, v208
v_sub_f32 v62, v62, v208
v_sub_f32 v63, v63, v208
v_sub_f32 v68, v68, v208
v_sub_f32 v69, v69, v208
v_sub_f32 v70, v70, v208
v_sub_f32 v71, v71, v208
v_sub_f32 v20, 0, v4
v_sub_f32 v21, 0, v4
v_sub_f32 v22, 0, v4
v_sub_f32 v23, 0, v4
v_pk_mul_f32 v[36:37], v[36:37], v[210:211] op_sel_hi:[1,0]
v_pk_mul_f32 v[38:39], v[38:39], v[210:211] op_sel_hi:[1,0]
v_pk_mul_f32 v[80:81], v[80:81], v[210:211] op_sel_hi:[1,0]
v_pk_mul_f32 v[82:83], v[82:83], v[210:211] op_sel_hi:[1,0]
v_pk_mul_f32 v[84:85], v[84:85], v[210:211] op_sel_hi:[1,0]
v_pk_mul_f32 v[86:87], v[86:87], v[210:211] op_sel_hi:[1,0]
v_pk_mul_f32 v[96:97], v[96:97], v[210:211] op_sel_hi:[1,0]
v_pk_mul_f32 v[98:99], v[98:99], v[210:211] op_sel_hi:[1,0]
v_pk_mul_f32 v[100:101], v[100:101], v[210:211] op_sel_hi:[1,0]
v_pk_mul_f32 v[102:103], v[102:103], v[210:211] op_sel_hi:[1,0]
v_pk_mul_f32 v[112:113], v[112:113], v[210:211] op_sel_hi:[1,0]
v_pk_mul_f32 v[114:115], v[114:115], v[210:211] op_sel_hi:[1,0]
v_pk_mul_f32 v[116:117], v[116:117], v[210:211] op_sel_hi:[1,0]
v_pk_mul_f32 v[118:119], v[118:119], v[210:211] op_sel_hi:[1,0]
v_pk_mul_f32 v[128:129], v[128:129], v[210:211] op_sel_hi:[1,0]
v_pk_mul_f32 v[130:131], v[130:131], v[210:211] op_sel_hi:[1,0]
v_pk_mul_f32 v[132:133], v[132:133], v[210:211] op_sel_hi:[1,0]
v_pk_mul_f32 v[134:135], v[134:135], v[210:211] op_sel_hi:[1,0]
v_pk_mul_f32 v[144:145], v[144:145], v[210:211] op_sel_hi:[1,0]
v_pk_mul_f32 v[146:147], v[146:147], v[210:211] op_sel_hi:[1,0]
v_pk_mul_f32 v[148:149], v[148:149], v[210:211] op_sel_hi:[1,0]
v_pk_mul_f32 v[150:151], v[150:151], v[210:211] op_sel_hi:[1,0]
v_pk_mul_f32 v[160:161], v[160:161], v[210:211] op_sel_hi:[1,0]
v_pk_mul_f32 v[162:163], v[162:163], v[210:211] op_sel_hi:[1,0]
v_pk_mul_f32 v[164:165], v[164:165], v[210:211] op_sel_hi:[1,0]
v_pk_mul_f32 v[166:167], v[166:167], v[210:211] op_sel_hi:[1,0]
v_pk_mul_f32 v[176:177], v[176:177], v[210:211] op_sel_hi:[1,0]
v_pk_mul_f32 v[178:179], v[178:179], v[210:211] op_sel_hi:[1,0]
v_pk_mul_f32 v[180:181], v[180:181], v[210:211] op_sel_hi:[1,0]
v_pk_mul_f32 v[182:183], v[182:183], v[210:211] op_sel_hi:[1,0]
v_pk_mul_f32 v[192:193], v[192:193], v[210:211] op_sel_hi:[1,0]
v_pk_mul_f32 v[194:195], v[194:195], v[210:211] op_sel_hi:[1,0]
v_pk_mul_f32 v[196:197], v[196:197], v[210:211] op_sel_hi:[1,0]
v_pk_mul_f32 v[198:199], v[198:199], v[210:211] op_sel_hi:[1,0]
m3 212,64,65,66
m3 213,67,72,73
m3 212,212,74,75
v_max_f32 v212, v212, v213
v_mov_b32 v213, v212
s_nop 1
v_permlane32_swap_b32 v212, v213
s_nop 0
v_max_f32 v212, v212, v213
v_mov_b32 v213, v212
s_nop 1
v_permlane16_swap_b32 v212, v213
s_nop 0
v_max_f32 v212, v212, v213
v_max_f32 v212, 0, v212
v_add_f32 v5, v5, v212
v_sub_f32 v214, 0, v212
ex 214
v_sub_f32 v64, v64, v212
v_sub_f32 v65, v65, v212
v_sub_f32 v66, v66, v212
v_sub_f32 v67, v67, v212
v_sub_f32 v72, v72, v212
v_sub_f32 v73, v73, v212
v_sub_f32 v74, v74, v212
v_sub_f32 v75, v75, v212
v_sub_f32 v24, 0, v5
v_sub_f32 v25, 0, v5
v_sub_f32 v26, 0, v5
v_sub_f32 v27, 0, v5
v_pk_mul_f32 v[40:41], v[40:41], v[214:215] op_sel_hi:[1,0]
v_pk_mul_f32 v[42:43], v[42:43], v[214:215] op_sel_hi:[1,0]
v_pk_mul_f32 v[88:89], v[88:89], v[214:215] op_sel_hi:[1,0]
v_pk_mul_f32 v[90:91], v[90:91], v[214:215] op_sel_hi:[1,0]
v_pk_mul_f32 v[92:93], v[92:93], v[214:215] op_sel_hi:[1,0]
v_pk_mul_f32 v[94:95], v[94:95], v[214:215] op_sel_hi:[1,0]
v_pk_mul_f32 v[104:105], v[104:105], v[214:215] op_sel_hi:[1,0]
v_pk_mul_f32 v[106:107], v[106:107], v[214:215] op_sel_hi:[1,0]
v_pk_mul_f32 v[108:109], v[108:109], v[214:215] op_sel_hi:[1,0]
v_pk_mul_f32 v[110:111], v[110:111], v[214:215] op_sel_hi:[1,0]
v_pk_mul_f32 v[120:121], v[120:121], v[214:215] op_sel_hi:[1,0]
v_pk_mul_f32 v[122:123], v[122:123], v[214:215] op_sel_hi:[1,0]
v_pk_mul_f32 v[124:125], v[124:125], v[214:215] op_sel_hi:[1,0]
v_pk_mul_f32 v[126:127], v[126:127], v[214:215] op_sel_hi:[1,0]
v_pk_mul_f32 v[136:137], v[136:137], v[214:215] op_sel_hi:[1,0]
v_pk_mul_f32 v[138:139], v[138:139], v[214:215] op_sel_hi:[1,0]
v_pk_mul_f32 v[140:141], v[140:141], v[214:215] op_sel_hi:[1,0]
v_pk_mul_f32 v[142:143], v[142:143], v[214:215] op_sel_hi:[1,0]
v_pk_mul_f32 v[152:153], v[152:153], v[214:215] op_sel_hi:[1,0]
v_pk_mul_f32 v[154:155], v[154:155], v[214:215] op_sel_hi:[1,0]
v_pk_mul_f32 v[156:157], v[156:157], v[214:215] op_sel_hi:[1,0]
v_pk_mul_f32 v[158:159], v[158:159], v[214:215] op_sel_hi:[1,0]
v_pk_mul_f32 v[168:169], v[168:169], v[214:215] op_sel_hi:[1,0]
v_pk_mul_f32 v[170:171], v[170:171], v[214:215] op_sel_hi:[1,0]
v_pk_mul_f32 v[172:173], v[172:173], v[214:215] op_sel_hi:[1,0]
v_pk_mul_f32 v[174:175], v[174:175], v[214:215] op_sel_hi:[1,0]
v_pk_mul_f32 v[184:185], v[184:185], v[214:215] op_sel_hi:[1,0]
v_pk_mul_f32 v[186:187], v[186:187], v[214:215] op_sel_hi:[1,0]
v_pk_mul_f32 v[188:189], v[188:189], v[214:215] op_sel_hi:[1,0]
v_pk_mul_f32 v[190:191], v[190:191], v[214:215] op_sel_hi:[1,0]
v_pk_mul_f32 v[200:201], v[200:201], v[214:215] op_sel_hi:[1,0]
v_pk_mul_f32 v[202:203], v[202:203], v[214:215] op_sel_hi:[1,0]
v_pk_mul_f32 v[204:205], v[204:205], v[214:215] op_sel_hi:[1,0]
v_pk_mul_f32 v[206:207], v[206:207], v[214:215] op_sel_hi:[1,0]
s_nop 1
s_cmp_eq_u32 s67, 0
s_cbranch_scc1 .Lk2_ret10
s_cmp_eq_u32 s67, 1
s_cbranch_scc1 .Lk2_ret12
s_cmp_eq_u32 s67, 2
s_cbranch_scc1 .Lk2_ret20
s_branch .Lk2_ret22
.Lk2_rare1:
s_nop 7
m3 208,44,45,46
m3 209,47,52,53
m3 208,208,54,55
v_max_f32 v208, v208, v209
v_mov_b32 v209, v208
s_nop 1
v_permlane32_swap_b32 v208, v209
s_nop 0
v_max_f32 v208, v208, v209
v_mov_b32 v209, v208
s_nop 1
v_permlane16_swap_b32 v208, v209
s_nop 0
v_max_f32 v208, v208, v209
v_max_f32 v208, 0, v208
v_add_f32 v4, v4, v208
v_sub_f32 v210, 0, v208
ex 210
v_sub_f32 v44, v44, v208
v_sub_f32 v45, v45, v208
v_sub_f32 v46, v46, v208
v_sub_f32 v47, v47, v208
v_sub_f32 v52, v52, v208
v_sub_f32 v53, v53, v208
v_sub_f32 v54, v54, v208
v_sub_f32 v55, v55, v208
v_sub_f32 v20, 0, v4
v_sub_f32 v21, 0, v4
v_sub_f32 v22, 0, v4
v_sub_f32 v23, 0, v4
v_pk_mul_f32 v[36:37], v[36:37], v[210:211] op_sel_hi:[1,0]
v_pk_mul_f32 v[38:39], v[38:39], v[210:211] op_sel_hi:[1,0]
v_pk_mul_f32 v[80:81], v[80:81], v[210:211] op_sel_hi:[1,0]
v_pk_mul_f32 v[82:83], v[82:83], v[210:211] op_sel_hi:[1,0]
v_pk_mul_f32 v[84:85], v[84:85], v[210:211] op_sel_hi:[1,0]
v_pk_mul_f32 v[86:87], v[86:87], v[210:211] op_sel_hi:[1,0]
v_pk_mul_f32 v[96:97], v[96:97], v[210:211] op_sel_hi:[1,0]
v_pk_mul_f32 v[98:99], v[98:99], v[210:211] op_sel_hi:[1,0]
v_pk_mul_f32 v[100:101], v[100:101], v[210:211] op_sel_hi:[1,0]
v_pk_mul_f32 v[102:103], v[102:103], v[210:211] op_sel_hi:[1,0]
v_pk_mul_f32 v[112:113], v[112:113], v[210:211] op_sel_hi:[1,0]
v_pk_mul_f32 v[114:115], v[114:115], v[210:211] op_sel_hi:[1,0]
v_pk_mul_f32 v[116:117], v[116:117], v[210:211] op_sel_hi:[1,0]
v_pk_mul_f32 v[118:119], v[118:119], v[210:211] op_sel_hi:[1,0]
v_pk_mul_f32 v[128:129], v[128:129], v[210:211] op_sel_hi:[1,0]
v_pk_mul_f32 v[130:131], v[130:131], v[210:211] op_sel_hi:[1,0]
v_pk_mul_f32 v[132:133], v[132:133], v[210:211] op_sel_hi:[1,0]
v_pk_mul_f32 v[134:135], v[134:135], v[210:211] op_sel_hi:[1,0]
v_pk_mul_f32 v[144:145], v[144:145], v[210:211] op_sel_hi:[1,0]
v_pk_mul_f32 v[146:147], v[146:147], v[210:211] op_sel_hi:[1,0]
v_pk_mul_f32 v[148:149], v[148:149], v[210:211] op_sel_hi:[1,0]
v_pk_mul_f32 v[150:151], v[150:151], v[210:211] op_sel_hi:[1,0]
v_pk_mul_f32 v[160:161], v[160:161], v[210:211] op_sel_hi:[1,0]
v_pk_mul_f32 v[162:163], v[162:163], v[210:211] op_sel_hi:[1,0]
v_pk_mul_f32 v[164:165], v[164:165], v[210:211] op_sel_hi:[1,0]
v_pk_mul_f32 v[166:167], v[166:167], v[210:211] op_sel_hi:[1,0]
v_pk_mul_f32 v[176:177], v[176:177], v[210:211] op_sel_hi:[1,0]
v_pk_mul_f32 v[178:179], v[178:179], v[210:211] op_sel_hi:[1,0]
v_pk_mul_f32 v[180:181], v[180:181], v[210:211] op_sel_hi:[1,0]
v_pk_mul_f32 v[182:183], v[182:183], v[210:211] op_sel_hi:[1,0]
v_pk_mul_f32 v[192:193], v[192:193], v[210:211] op_sel_hi:[1,0]
v_pk_mul_f32 v[194:195], v[194:195], v[210:211] op_sel_hi:[1,0]
v_pk_mul_f32 v[196:197], v[196:197], v[210:211] op_sel_hi:[1,0]
v_pk_mul_f32 v[198:199], v[198:199], v[210:211] op_sel_hi:[1,0]
m3 212,48,49,50
m3 213,51,56,57
m3 212,212,58,59
v_max_f32 v212, v212, v213
v_mov_b32 v213, v212
s_nop 1
v_permlane32_swap_b32 v212, v213
s_nop 0
v_max_f32 v212, v212, v213
v_mov_b32 v213, v212
s_nop 1
v_permlane16_swap_b32 v212, v213
s_nop 0
v_max_f32 v212, v212, v213
v_max_f32 v212, 0, v212
v_add_f32 v5, v5, v212
v_sub_f32 v214, 0, v212
ex 214
v_sub_f32 v48, v48, v212
v_sub_f32 v49, v49, v212
v_sub_f32 v50, v50, v212
v_sub_f32 v51, v51, v212
v_sub_f32 v56, v56, v212
v_sub_f32 v57, v57, v212
v_sub_f32 v58, v58, v212
v_sub_f32 v59, v59, v212
v_sub_f32 v24, 0, v5
v_sub_f32 v25, 0, v5
v_sub_f32 v26, 0, v5
v_sub_f32 v27, 0, v5
v_pk_mul_f32 v[40:41], v[40:41], v[214:215] op_sel_hi:[1,0]
v_pk_mul_f32 v[42:43], v[42:43], v[214:215] op_sel_hi:[1,0]
v_pk_mul_f32 v[88:89], v[88:89], v[214:215] op_sel_hi:[1,0]
v_pk_mul_f32 v[90:91], v[90:91], v[214:215] op_sel_hi:[1,0]
v_pk_mul_f32 v[92:93], v[92:93], v[214:215] op_sel_hi:[1,0]
v_pk_mul_f32 v[94:95], v[94:95], v[214:215] op_sel_hi:[1,0]
v_pk_mul_f32 v[104:105], v[104:105], v[214:215] op_sel_hi:[1,0]
v_pk_mul_f32 v[106:107], v[106:107], v[214:215] op_sel_hi:[1,0]
v_pk_mul_f32 v[108:109], v[108:109], v[214:215] op_sel_hi:[1,0]
v_pk_mul_f32 v[110:111], v[110:111], v[214:215] op_sel_hi:[1,0]
v_pk_mul_f32 v[120:121], v[120:121], v[214:215] op_sel_hi:[1,0]
v_pk_mul_f32 v[122:123], v[122:123], v[214:215] op_sel_hi:[1,0]
v_pk_mul_f32 v[124:125], v[124:125], v[214:215] op_sel_hi:[1,0]
v_pk_mul_f32 v[126:127], v[126:127], v[214:215] op_sel_hi:[1,0]
v_pk_mul_f32 v[136:137], v[136:137], v[214:215] op_sel_hi:[1,0]
v_pk_mul_f32 v[138:139], v[138:139], v[214:215] op_sel_hi:[1,0]
v_pk_mul_f32 v[140:141], v[140:141], v[214:215] op_sel_hi:[1,0]
v_pk_mul_f32 v[142:143], v[142:143], v[214:215] op_sel_hi:[1,0]
v_pk_mul_f32 v[152:153], v[152:153], v[214:215] op_sel_hi:[1,0]
v_pk_mul_f32 v[154:155], v[154:155], v[214:215] op_sel_hi:[1,0]
v_pk_mul_f32 v[156:157], v[156:157], v[214:215] op_sel_hi:[1,0]
v_pk_mul_f32 v[158:159], v[158:159], v[214:215] op_sel_hi:[1,0]
v_pk_mul_f32 v[168:169], v[168:169], v[214:215] op_sel_hi:[1,0]
v_pk_mul_f32 v[170:171], v[170:171], v[214:215] op_sel_hi:[1,0]
v_pk_mul_f32 v[172:173], v[172:173], v[214:215] op_sel_hi:[1,0]
v_pk_mul_f32 v[174:175], v[174:175], v[214:215] op_sel_hi:[1,0]
v_pk_mul_f32 v[184:185], v[184:185], v[214:215] op_sel_hi:[1,0]
v_pk_mul_f32 v[186:187], v[186:187], v[214:215] op_sel_hi:[1,0]
v_pk_mul_f32 v[188:189], v[188:189], v[214:215] op_sel_hi:[1,0]
v_pk_mul_f32 v[190:191], v[190:191], v[214:215] op_sel_hi:[1,0]
v_pk_mul_f32 v[200:201], v[200:201], v[214:215] op_sel_hi:[1,0]
v_pk_mul_f32 v[202:203], v[202:203], v[214:215] op_sel_hi:[1,0]
v_pk_mul_f32 v[204:205], v[204:205], v[214:215] op_sel_hi:[1,0]
v_pk_mul_f32 v[206:207], v[206:207], v[214:215] op_sel_hi:[1,0]
s_nop 1
s_cmp_eq_u32 s67, 0
s_cbranch_scc1 .Lk2_ret11
s_cmp_eq_u32 s67, 1
s_cbranch_scc1 .Lk2_ret13
s_cmp_eq_u32 s67, 2
s_cbranch_scc1 .Lk2_ret21
s_branch .Lk2_ret23
.Lk2_epi0:
s_add_u32 m0, s96, 0
s_nop 0
dm 244,88,8
s_add_u32 m0, s96, 1024
s_nop 0
dm 245,88,8
)ASM" ::: "memory");
  asm volatile(R"ASM(
s_add_u32 m0, s96, 2048
s_nop 0
dm 246,88,8
s_add_u32 m0, s96, 3072
s_nop 0
dm 247,88,8
s_add_u32 m0, s96, 4096
s_nop 0
dm 248,88,8
s_add_u32 m0, s96, 5120
s_nop 0
dm 249,88,8
s_add_u32 m0, s96, 6144
s_nop 0
dm 250,88,8
s_add_u32 m0, s96, 28672
s_nop 0
dm 244,88,9
s_add_u32 m0, s96, 29696
s_nop 0
dm 245,88,9
s_add_u32 m0, s96, 30720
s_nop 0
dm 246,88,9
s_add_u32 m0, s96, 31744
s_nop 0
dm 247,88,9
s_add_u32 m0, s96, 32768
s_nop 0
dm 248,88,9
s_add_u32 m0, s96, 33792
s_nop 0
dm 249,88,9
s_add_u32 m0, s96, 34816
s_nop 0
dm 250,88,9
.Lk2_epi:
v_mov_b32 v24, v251
v_add_u32 v25, 57344, v251
v_add_u32 v26, 114688, v251
s_mov_b32 s99, s98
s_and_b32 s98, s98, 0xfc
s_mov_b32 s3, 0x20000
v_sub_f32 v252, 1.0, v6
cv 252,252,252
v_and_b32 v252, 0xffff0000, v252
v_sub_f32 v253, 1.0, v7
cv 253,253,253
v_and_b32 v253, 0xffff0000, v253
s_cmp_eq_u32 s53, 0
s_cbranch_scc1 .Lk2_out
s_cmp_lg_u32 s62, 0
s_cbranch_scc1 .Lk2_have
s_mov_b32 s63, 0x400000
.Lk2_poll:
global_load_dword v77, v79, s[30:31] sc1
s_waitcnt vmcnt(0)
v_readfirstlane_b32 s62, v77
s_nop 1
s_cmp_eq_u32 s62, s82
s_cbranch_scc1 .Lk2_got
s_sleep 8
s_sub_u32 s63, s63, 1
s_cmp_lg_u32 s63, 0
s_cbranch_scc1 .Lk2_poll
v_mov_b32 v36, 0x7fc00000
v_mov_b32 v40, 0x7fc00000
.Lk2_got:
s_mov_b64 s[68:69], s[28:29]
global_load_dwordx4 a[192:195], v0, s[68:69] offset:0 sc1
global_load_dwordx4 a[196:199], v0, s[68:69] offset:1024 sc1
global_load_dwordx4 a[200:203], v0, s[68:69] offset:2048 sc1
global_load_dwordx4 a[204:207], v0, s[68:69] offset:3072 sc1
s_add_u32 s68, s68, 0x1000
s_addc_u32 s69, s69, 0
global_load_dwordx4 a[208:211], v0, s[68:69] offset:0 sc1
global_load_dwordx4 a[212:215], v0, s[68:69] offset:1024 sc1
global_load_dwordx4 a[216:219], v0, s[68:69] offset:2048 sc1
global_load_dwordx4 a[220:223], v0, s[68:69] offset:3072 sc1
s_add_u32 s68, s68, 0x1000
s_addc_u32 s69, s69, 0
global_load_dwordx4 a[224:227], v0, s[68:69] offset:0 sc1
global_load_dwordx4 a[228:231], v0, s[68:69] offset:1024 sc1
global_load_dwordx4 a[232:235], v0, s[68:69] offset:2048 sc1
global_load_dwordx4 a[236:239], v0, s[68:69] offset:3072 sc1
s_add_u32 s68, s68, 0x1000
s_addc_u32 s69, s69, 0
global_load_dwordx4 a[240:243], v0, s[68:69] offset:0 sc1
global_load_dwordx4 a[244:247], v0, s[68:69] offset:1024 sc1
global_load_dwordx4 a[248:251], v0, s[68:69] offset:2048 sc1
global_load_dwordx4 a[252:255], v0, s[68:69] offset:3072 sc1
s_add_u32 s68, s68, 0x1000
s_addc_u32 s69, s69, 0
v_lshrrev_b32 v9, 1, v0
global_load_dwordx2 v[242:243], v9, s[68:69] sc1
s_waitcnt vmcnt(0)
.Lk2_have:
v_mov_b64 v[236:237], 0
v_mov_b64 v[240:241], 0
v_mov_b32 v238, v78
v_mov_b32 v239, v13
s_waitcnt vmcnt(14)
v_sub_f32 v8, v4, v242
v_min_f32 v8, 0x42a00000, v8
v_max_f32 v8, 0xc2a00000, v8
ex 8
s_nop 0
v_sub_f32 v10, v5, v243
v_min_f32 v10, 0x42a00000, v10
v_max_f32 v10, 0xc2a00000, v10
ex 10
s_nop 0
v_fma_f32 v36, v36, v8, 1.0
v_fma_f32 v40, v40, v10, 1.0
v_pk_mul_f32 v[80:81], v[80:81], v[8:9] op_sel_hi:[1,0]
v_pk_mul_f32 v[82:83], v[82:83], v[8:9] op_sel_hi:[1,0]
v_pk_mul_f32 v[84:85], v[84:85], v[8:9] op_sel_hi:[1,0]
v_pk_mul_f32 v[86:87], v[86:87], v[8:9] op_sel_hi:[1,0]
v_pk_mul_f32 v[88:89], v[88:89], v[10:11] op_sel_hi:[1,0]
v_pk_mul_f32 v[90:91], v[90:91], v[10:11] op_sel_hi:[1,0]
v_pk_mul_f32 v[92:93], v[92:93], v[10:11] op_sel_hi:[1,0]
v_pk_mul_f32 v[94:95], v[94:95], v[10:11] op_sel_hi:[1,0]
v_pk_mul_f32 v[96:97], v[96:97], v[8:9] op_sel_hi:[1,0]
v_pk_mul_f32 v[98:99], v[98:99], v[8:9] op_sel_hi:[1,0]
v_pk_mul_f32 v[100:101], v[100:101], v[8:9] op_sel_hi:[1,0]
v_pk_mul_f32 v[102:103], v[102:103], v[8:9] op_sel_hi:[1,0]
v_pk_mul_f32 v[104:105], v[104:105], v[10:11] op_sel_hi:[1,0]
v_pk_mul_f32 v[106:107], v[106:107], v[10:11] op_sel_hi:[1,0]
v_pk_mul_f32 v[108:109], v[108:109], v[10:11] op_sel_hi:[1,0]
v_pk_mul_f32 v[110:111], v[110:111], v[10:11] op_sel_hi:[1,0]
v_pk_mul_f32 v[112:113], v[112:113], v[8:9] op_sel_hi:[1,0]
v_pk_mul_f32 v[114:115], v[114:115], v[8:9] op_sel_hi:[1,0]
v_pk_mul_f32 v[116:117], v[116:117], v[8:9] op_sel_hi:[1,0]
v_pk_mul_f32 v[118:119], v[118:119], v[8:9] op_sel_hi:[1,0]
v_pk_mul_f32 v[120:121], v[120:121], v[10:11] op_sel_hi:[1,0]
v_pk_mul_f32 v[122:123], v[122:123], v[10:11] op_sel_hi:[1,0]
v_pk_mul_f32 v[124:125], v[124:125], v[10:11] op_sel_hi:[1,0]
v_pk_mul_f32 v[126:127], v[126:127], v[10:11] op_sel_hi:[1,0]
v_pk_mul_f32 v[128:129], v[128:129], v[8:9] op_sel_hi:[1,0]
v_pk_mul_f32 v[130:131], v[130:131], v[8:9] op_sel_hi:[1,0]
v_pk_mul_f32 v[132:133], v[132:133], v[8:9] op_sel_hi:[1,0]
v_pk_mul_f32 v[134:135], v[134:135], v[8:9] op_sel_hi:[1,0]
v_pk_mul_f32 v[136:137], v[136:137], v[10:11] op_sel_hi:[1,0]
v_pk_mul_f32 v[138:139], v[138:139], v[10:11] op_sel_hi:[1,0]
v_pk_mul_f32 v[140:141], v[140:141], v[10:11] op_sel_hi:[1,0]
v_pk_mul_f32 v[142:143], v[142:143], v[10:11] op_sel_hi:[1,0]
v_pk_mul_f32 v[144:145], v[144:145], v[8:9] op_sel_hi:[1,0]
v_pk_mul_f32 v[146:147], v[146:147], v[8:9] op_sel_hi:[1,0]
v_pk_mul_f32 v[148:149], v[148:149], v[8:9] op_sel_hi:[1,0]
v_pk_mul_f32 v[150:151], v[150:151], v[8:9] op_sel_hi:[1,0]
v_pk_mul_f32 v[152:153], v[152:153], v[10:11] op_sel_hi:[1,0]
v_pk_mul_f32 v[154:155], v[154:155], v[10:11] op_sel_hi:[1,0]
v_pk_mul_f32 v[156:157], v[156:157], v[10:11] op_sel_hi:[1,0]
v_pk_mul_f32 v[158:159], v[158:159], v[10:11] op_sel_hi:[1,0]
v_pk_mul_f32 v[160:161], v[160:161], v[8:9] op_sel_hi:[1,0]
v_pk_mul_f32 v[162:163], v[162:163], v[8:9] op_sel_hi:[1,0]
v_pk_mul_f32 v[164:165], v[164:165], v[8:9] op_sel_hi:[1,0]
v_pk_mul_f32 v[166:167], v[166:167], v[8:9] op_sel_hi:[1,0]
v_pk_mul_f32 v[168:169], v[168:169], v[10:11] op_sel_hi:[1,0]
v_pk_mul_f32 v[170:171], v[170:171], v[10:11] op_sel_hi:[1,0]
v_pk_mul_f32 v[172:173], v[172:173], v[10:11] op_sel_hi:[1,0]
v_pk_mul_f32 v[174:175], v[174:175], v[10:11] op_sel_hi:[1,0]
v_pk_mul_f32 v[176:177], v[176:177], v[8:9] op_sel_hi:[1,0]
v_pk_mul_f32 v[178:179], v[178:179], v[8:9] op_sel_hi:[1,0]
v_pk_mul_f32 v[180:181], v[180:181], v[8:9] op_sel_hi:[1,0]
v_pk_mul_f32 v[182:183], v[182:183], v[8:9] op_sel_hi:[1,0]
v_pk_mul_f32 v[184:185], v[184:185], v[10:11] op_sel_hi:[1,0]
v_pk_mul_f32 v[186:187], v[186:187], v[10:11] op_sel_hi:[1,0]
v_pk_mul_f32 v[188:189], v[188:189], v[10:11] op_sel_hi:[1,0]
v_pk_mul_f32 v[190:191], v[190:191], v[10:11] op_sel_hi:[1,0]
v_pk_mul_f32 v[192:193], v[192:193], v[8:9] op_sel_hi:[1,0]
v_pk_mul_f32 v[194:195], v[194:195], v[8:9] op_sel_hi:[1,0]
v_pk_mul_f32 v[196:197], v[196:197], v[8:9] op_sel_hi:[1,0]
v_pk_mul_f32 v[198:199], v[198:199], v[8:9] op_sel_hi:[1,0]
v_pk_mul_f32 v[200:201], v[200:201], v[10:11] op_sel_hi:[1,0]
v_pk_mul_f32 v[202:203], v[202:203], v[10:11] op_sel_hi:[1,0]
v_pk_mul_f32 v[204:205], v[204:205], v[10:11] op_sel_hi:[1,0]
v_pk_mul_f32 v[206:207], v[206:207], v[10:11] op_sel_hi:[1,0]
mvva 80,238,192
mvva 84,236,192
mvva 88,238,196
mvva 92,236,196
mvva 96,238,200
mvva 100,236,200
mvva 104,238,204
mvva 108,236,204
mvva 112,238,208
mvva 116,236,208
mvva 120,238,212
mvva 124,236,212
mvva 128,238,216
mvva 132,236,216
mvva 136,238,220
mvva 140,236,220
mvva 144,238,224
mvva 148,236,224
mvva 152,238,228
mvva 156,236,228
mvva 160,238,232
mvva 164,236,232
mvva 168,238,236
mvva 172,236,236
mvva 176,238,240
mvva 180,236,240
mvva 184,238,244
mvva 188,236,244
mvva 192,238,248
mvva 196,236,248
mvva 200,238,252
mvva 204,236,252
s_nop 3
.Lk2_out:
v_rcp_f32 v8, v36
v_rcp_f32 v10, v40
s_nop 0
v_mul_f32 v8, v8, v252
v_mul_f32 v10, v10, v253
v_pk_mul_f32 v[80:81], v[80:81], v[8:9] op_sel_hi:[1,0]
v_pk_mul_f32 v[82:83], v[82:83], v[8:9] op_sel_hi:[1,0]
v_pk_mul_f32 v[84:85], v[84:85], v[8:9] op_sel_hi:[1,0]
v_pk_mul_f32 v[86:87], v[86:87], v[8:9] op_sel_hi:[1,0]
cv 80,80,81
cv 81,82,83
cv 82,84,85
cv 83,86,87
v_pk_mul_f32 v[88:89], v[88:89], v[10:11] op_sel_hi:[1,0]
v_pk_mul_f32 v[90:91], v[90:91], v[10:11] op_sel_hi:[1,0]
v_pk_mul_f32 v[92:93], v[92:93], v[10:11] op_sel_hi:[1,0]
v_pk_mul_f32 v[94:95], v[94:95], v[10:11] op_sel_hi:[1,0]
cv 84,88,89
cv 85,90,91
cv 86,92,93
cv 87,94,95
v_pk_mul_f32 v[96:97], v[96:97], v[8:9] op_sel_hi:[1,0]
v_pk_mul_f32 v[98:99], v[98:99], v[8:9] op_sel_hi:[1,0]
v_pk_mul_f32 v[100:101], v[100:101], v[8:9] op_sel_hi:[1,0]
v_pk_mul_f32 v[102:103], v[102:103], v[8:9] op_sel_hi:[1,0]
cv 88,96,97
cv 89,98,99
cv 90,100,101
cv 91,102,103
v_pk_mul_f32 v[104:105], v[104:105], v[10:11] op_sel_hi:[1,0]
v_pk_mul_f32 v[106:107], v[106:107], v[10:11] op_sel_hi:[1,0]
v_pk_mul_f32 v[108:109], v[108:109], v[10:11] op_sel_hi:[1,0]
v_pk_mul_f32 v[110:111], v[110:111], v[10:11] op_sel_hi:[1,0]
cv 92,104,105
cv 93,106,107
cv 94,108,109
cv 95,110,111
v_pk_mul_f32 v[112:113], v[112:113], v[8:9] op_sel_hi:[1,0]
v_pk_mul_f32 v[114:115], v[114:115], v[8:9] op_sel_hi:[1,0]
v_pk_mul_f32 v[116:117], v[116:117], v[8:9] op_sel_hi:[1,0]
v_pk_mul_f32 v[118:119], v[118:119], v[8:9] op_sel_hi:[1,0]
cv 96,112,113
cv 97,114,115
cv 98,116,117
cv 99,118,119
v_pk_mul_f32 v[120:121], v[120:121], v[10:11] op_sel_hi:[1,0]
v_pk_mul_f32 v[122:123], v[122:123], v[10:11] op_sel_hi:[1,0]
v_pk_mul_f32 v[124:125], v[124:125], v[10:11] op_sel_hi:[1,0]
v_pk_mul_f32 v[126:127], v[126:127], v[10:11] op_sel_hi:[1,0]
cv 100,120,121
cv 101,122,123
cv 102,124,125
cv 103,126,127
v_pk_mul_f32 v[128:129], v[128:129], v[8:9] op_sel_hi:[1,0]
v_pk_mul_f32 v[130:131], v[130:131], v[8:9] op_sel_hi:[1,0]
v_pk_mul_f32 v[132:133], v[132:133], v[8:9] op_sel_hi:[1,0]
v_pk_mul_f32 v[134:135], v[134:135], v[8:9] op_sel_hi:[1,0]
cv 104,128,129
cv 105,130,131
cv 106,132,133
cv 107,134,135
v_pk_mul_f32 v[136:137], v[136:137], v[10:11] op_sel_hi:[1,0]
v_pk_mul_f32 v[138:139], v[138:139], v[10:11] op_sel_hi:[1,0]
v_pk_mul_f32 v[140:141], v[140:141], v[10:11] op_sel_hi:[1,0]
v_pk_mul_f32 v[142:143], v[142:143], v[10:11] op_sel_hi:[1,0]
cv 108,136,137
cv 109,138,139
cv 110,140,141
cv 111,142,143
v_pk_mul_f32 v[144:145], v[144:145], v[8:9] op_sel_hi:[1,0]
v_pk_mul_f32 v[146:147], v[146:147], v[8:9] op_sel_hi:[1,0]
v_pk_mul_f32 v[148:149], v[148:149], v[8:9] op_sel_hi:[1,0]
v_pk_mul_f32 v[150:151], v[150:151], v[8:9] op_sel_hi:[1,0]
cv 112,144,145
cv 113,146,147
cv 114,148,149
cv 115,150,151
v_pk_mul_f32 v[152:153], v[152:153], v[10:11] op_sel_hi:[1,0]
v_pk_mul_f32 v[154:155], v[154:155], v[10:11] op_sel_hi:[1,0]
v_pk_mul_f32 v[156:157], v[156:157], v[10:11] op_sel_hi:[1,0]
v_pk_mul_f32 v[158:159], v[158:159], v[10:11] op_sel_hi:[1,0]
cv 116,152,153
cv 117,154,155
cv 118,156,157
cv 119,158,159
v_pk_mul_f32 v[160:161], v[160:161], v[8:9] op_sel_hi:[1,0]
v_pk_mul_f32 v[162:163], v[162:163], v[8:9] op_sel_hi:[1,0]
v_pk_mul_f32 v[164:165], v[164:165], v[8:9] op_sel_hi:[1,0]
v_pk_mul_f32 v[166:167], v[166:167], v[8:9] op_sel_hi:[1,0]
cv 120,160,161
cv 121,162,163
cv 122,164,165
cv 123,166,167
v_pk_mul_f32 v[168:169], v[168:169], v[10:11] op_sel_hi:[1,0]
v_pk_mul_f32 v[170:171], v[170:171], v[10:11] op_sel_hi:[1,0]
v_pk_mul_f32 v[172:173], v[172:173], v[10:11] op_sel_hi:[1,0]
v_pk_mul_f32 v[174:175], v[174:175], v[10:11] op_sel_hi:[1,0]
cv 124,168,169
cv 125,170,171
cv 126,172,173
cv 127,174,175
v_pk_mul_f32 v[176:177], v[176:177], v[8:9] op_sel_hi:[1,0]
v_pk_mul_f32 v[178:179], v[178:179], v[8:9] op_sel_hi:[1,0]
v_pk_mul_f32 v[180:181], v[180:181], v[8:9] op_sel_hi:[1,0]
v_pk_mul_f32 v[182:183], v[182:183], v[8:9] op_sel_hi:[1,0]
cv 128,176,177
cv 129,178,179
cv 130,180,181
cv 131,182,183
v_pk_mul_f32 v[184:185], v[184:185], v[10:11] op_sel_hi:[1,0]
v_pk_mul_f32 v[186:187], v[186:187], v[10:11] op_sel_hi:[1,0]
v_pk_mul_f32 v[188:189], v[188:189], v[10:11] op_sel_hi:[1,0]
v_pk_mul_f32 v[190:191], v[190:191], v[10:11] op_sel_hi:[1,0]
cv 132,184,185
cv 133,186,187
cv 134,188,189
cv 135,190,191
v_pk_mul_f32 v[192:193], v[192:193], v[8:9] op_sel_hi:[1,0]
v_pk_mul_f32 v[194:195], v[194:195], v[8:9] op_sel_hi:[1,0]
v_pk_mul_f32 v[196:197], v[196:197], v[8:9] op_sel_hi:[1,0]
v_pk_mul_f32 v[198:199], v[198:199], v[8:9] op_sel_hi:[1,0]
cv 136,192,193
cv 137,194,195
cv 138,196,197
cv 139,198,199
v_pk_mul_f32 v[200:201], v[200:201], v[10:11] op_sel_hi:[1,0]
v_pk_mul_f32 v[202:203], v[202:203], v[10:11] op_sel_hi:[1,0]
v_pk_mul_f32 v[204:205], v[204:205], v[10:11] op_sel_hi:[1,0]
v_pk_mul_f32 v[206:207], v[206:207], v[10:11] op_sel_hi:[1,0]
cv 140,200,201
cv 141,202,203
cv 142,204,205
cv 143,206,207
s_waitcnt vmcnt(14)
v_max_f32 v44, v232, v234
v_sub_f32 v45, v232, v44
v_sub_f32 v46, v234, v44
ex 45
ex 46
s_nop 0
v_add_f32 v44, v45, v46
v_rcp_f32 v44, v44
s_nop 0
v_mul_f32 v44, v44, v6
v_mul_f32 v45, v45, v44
v_mul_f32 v46, v46, v44
v_lshlrev_b32 v48, 16, v216
v_and_b32 v49, 0xffff0000, v216
v_lshlrev_b32 v50, 16, v224
v_and_b32 v51, 0xffff0000, v224
v_mul_f32 v48, v45, v48
v_mul_f32 v49, v45, v49
v_fmac_f32 v48, v46, v50
v_fmac_f32 v49, v46, v51
cv 208,48,49
v_lshlrev_b32 v48, 16, v217
v_and_b32 v49, 0xffff0000, v217
v_lshlrev_b32 v50, 16, v225
v_and_b32 v51, 0xffff0000, v225
v_mul_f32 v48, v45, v48
v_mul_f32 v49, v45, v49
v_fmac_f32 v48, v46, v50
v_fmac_f32 v49, v46, v51
cv 209,48,49
v_lshlrev_b32 v48, 16, v218
v_and_b32 v49, 0xffff0000, v218
v_lshlrev_b32 v50, 16, v226
v_and_b32 v51, 0xffff0000, v226
v_mul_f32 v48, v45, v48
v_mul_f32 v49, v45, v49
v_fmac_f32 v48, v46, v50
v_fmac_f32 v49, v46, v51
cv 210,48,49
v_lshlrev_b32 v48, 16, v219
v_and_b32 v49, 0xffff0000, v219
v_lshlrev_b32 v50, 16, v227
v_and_b32 v51, 0xffff0000, v227
v_mul_f32 v48, v45, v48
v_mul_f32 v49, v45, v49
v_fmac_f32 v48, v46, v50
v_fmac_f32 v49, v46, v51
cv 211,48,49
s_mov_b64 exec, s[60:61]
v_mov_b32 v210, 0
v_mov_b32 v211, 0
s_mov_b64 exec, -1
s_nop 2
v_max_f32 v44, v233, v235
v_sub_f32 v45, v233, v44
v_sub_f32 v46, v235, v44
ex 45
ex 46
s_nop 0
v_add_f32 v44, v45, v46
v_rcp_f32 v44, v44
s_nop 0
v_mul_f32 v44, v44, v7
v_mul_f32 v45, v45, v44
v_mul_f32 v46, v46, v44
v_lshlrev_b32 v48, 16, v220
v_and_b32 v49, 0xffff0000, v220
v_lshlrev_b32 v50, 16, v228
v_and_b32 v51, 0xffff0000, v228
v_mul_f32 v48, v45, v48
v_mul_f32 v49, v45, v49
v_fmac_f32 v48, v46, v50
v_fmac_f32 v49, v46, v51
cv 212,48,49
v_lshlrev_b32 v48, 16, v221
v_and_b32 v49, 0xffff0000, v221
v_lshlrev_b32 v50, 16, v229
v_and_b32 v51, 0xffff0000, v229
v_mul_f32 v48, v45, v48
v_mul_f32 v49, v45, v49
v_fmac_f32 v48, v46, v50
v_fmac_f32 v49, v46, v51
cv 213,48,49
v_lshlrev_b32 v48, 16, v222
v_and_b32 v49, 0xffff0000, v222
v_lshlrev_b32 v50, 16, v230
v_and_b32 v51, 0xffff0000, v230
v_mul_f32 v48, v45, v48
v_mul_f32 v49, v45, v49
v_fmac_f32 v48, v46, v50
v_fmac_f32 v49, v46, v51
cv 214,48,49
v_lshlrev_b32 v48, 16, v223
v_and_b32 v49, 0xffff0000, v223
v_lshlrev_b32 v50, 16, v231
v_and_b32 v51, 0xffff0000, v231
v_mul_f32 v48, v45, v48
v_mul_f32 v49, v45, v49
v_fmac_f32 v48, v46, v50
v_fmac_f32 v49, v46, v51
cv 215,48,49
s_mov_b64 exec, s[60:61]
v_mov_b32 v214, 0
v_mov_b32 v215, 0
s_mov_b64 exec, -1
s_nop 2
.Lk2_op:
s_waitcnt vmcnt(0)
s_barrier
rt 160,24,0
rt 162,24,3584
rt 164,24,32
rt 166,24,3616
rt 168,24,64
rt 170,24,3648
rt 172,24,96
rt 174,24,3680
rt 176,24,128
rt 178,24,3712
rt 180,24,160
rt 182,24,3744
rt 184,24,192
rt 186,24,3776
s_waitcnt lgkmcnt(10)
zavv 0,160,80
rt 188,24,7168
s_add_u32 m0, s96, 57344
zavv 4,160,84
rt 190,24,10752
zavv 8,164,80
rt 160,24,7200
dm 244,88,10
zavv 12,164,84
rt 162,24,10784
s_waitcnt lgkmcnt(10)
zavv 16,168,80
rt 164,24,7232
s_add_u32 m0, s96, 58368
zavv 20,168,84
rt 166,24,10816
zavv 24,172,80
rt 168,24,7264
dm 245,88,10
zavv 28,172,84
rt 170,24,10848
s_waitcnt lgkmcnt(10)
zavv 32,176,80
rt 172,24,7296
s_add_u32 m0, s96, 59392
zavv 36,176,84
rt 174,24,10880
zavv 40,180,80
rt 176,24,7328
dm 246,88,10
zavv 44,180,84
rt 178,24,10912
s_waitcnt lgkmcnt(10)
zavv 48,184,80
rt 180,24,7360
s_add_u32 m0, s96, 60416
zavv 52,184,84
rt 182,24,10944
zavv 56,188,80
rt 184,24,14336
dm 247,88,10
zavv 60,188,84
rt 186,24,17920
s_waitcnt lgkmcnt(10)
zavv 64,160,80
rt 188,24,14368
s_add_u32 m0, s96, 61440
zavv 68,160,84
rt 190,24,17952
zavv 72,164,80
rt 160,24,14400
dm 248,88,10
zavv 76,164,84
rt 162,24,17984
s_waitcnt lgkmcnt(10)
zavv 80,168,80
rt 164,24,14432
s_add_u32 m0, s96, 62464
zavv 84,168,84
rt 166,24,18016
zavv 88,172,80
rt 168,24,14464
dm 249,88,10
zavv 92,172,84
rt 170,24,18048
s_waitcnt lgkmcnt(10)
zavv 96,176,80
rt 172,24,14496
s_add_u32 m0, s96, 63488
zavv 100,176,84
rt 174,24,18080
zavv 104,180,80
rt 176,24,14528
dm 250,88,10
zavv 108,180,84
rt 178,24,18112
s_waitcnt lgkmcnt(10)
mavv 0,184,88
rt 180,24,21504
s_add_u32 m0, s96, 86016
mavv 4,184,92
rt 182,24,25088
mavv 8,188,88
rt 184,24,21536
dm 244,88,11
mavv 12,188,92
rt 186,24,25120
s_waitcnt lgkmcnt(10)
mavv 16,160,88
rt 188,24,21568
s_add_u32 m0, s96, 87040
mavv 20,160,92
rt 190,24,25152
mavv 24,164,88
rt 160,24,21600
dm 245,88,11
mavv 28,164,92
rt 162,24,25184
s_waitcnt lgkmcnt(10)
mavv 32,168,88
rt 164,24,21632
s_add_u32 m0, s96, 88064
mavv 36,168,92
rt 166,24,25216
mavv 40,172,88
rt 168,24,21664
dm 246,88,11
mavv 44,172,92
rt 170,24,25248
s_waitcnt lgkmcnt(10)
mavv 48,176,88
rt 172,24,21696
s_add_u32 m0, s96, 89088
mavv 52,176,92
rt 174,24,25280
mavv 56,180,88
rt 176,24,28672
dm 247,88,11
mavv 60,180,92
rt 178,24,32256
s_waitcnt lgkmcnt(10)
mavv 64,184,88
rt 180,24,28704
s_add_u32 m0, s96, 90112
mavv 68,184,92
rt 182,24,32288
mavv 72,188,88
rt 184,24,28736
dm 248,88,11
mavv 76,188,92
)ASM" ::: "memory");
  asm volatile(R"ASM(
rt 186,24,32320
s_waitcnt lgkmcnt(10)
mavv 80,160,88
rt 188,24,28768
s_add_u32 m0, s96, 91136
mavv 84,160,92
rt 190,24,32352
mavv 88,164,88
rt 160,24,28800
dm 249,88,11
mavv 92,164,92
rt 162,24,32384
s_waitcnt lgkmcnt(10)
mavv 96,168,88
rt 164,24,28832
s_add_u32 m0, s96, 92160
mavv 100,168,92
rt 166,24,32416
mavv 104,172,88
rt 168,24,28864
dm 250,88,11
mavv 108,172,92
rt 170,24,32448
s_waitcnt lgkmcnt(10)
mavv 0,176,96
rt 172,24,35840
s_cmp_eq_u32 s17, 4
s_cselect_b64 s[0:1], s[92:93], s[88:89]
s_cselect_b32 s2, s94, s90
mavv 4,176,100
rt 174,24,39424
mavv 8,180,96
rt 176,24,35872
s_add_u32 m0, s96, 114688
mavv 12,180,100
rt 178,24,39456
s_waitcnt lgkmcnt(10)
mavv 16,184,96
rt 180,24,35904
dm 244,0,12
mavv 20,184,100
rt 182,24,39488
mavv 24,188,96
rt 184,24,35936
s_add_u32 m0, s96, 115712
mavv 28,188,100
rt 186,24,39520
s_waitcnt lgkmcnt(10)
mavv 32,160,96
rt 188,24,35968
dm 245,0,12
mavv 36,160,100
rt 190,24,39552
mavv 40,164,96
rt 160,24,36000
s_add_u32 m0, s96, 116736
mavv 44,164,100
rt 162,24,39584
s_waitcnt lgkmcnt(10)
mavv 48,168,96
rt 164,24,36032
dm 246,0,12
mavv 52,168,100
rt 166,24,39616
mavv 56,172,96
rt 168,24,43008
s_add_u32 m0, s96, 117760
mavv 60,172,100
rt 170,24,46592
s_waitcnt lgkmcnt(10)
mavv 64,176,96
rt 172,24,43040
dm 247,0,12
mavv 68,176,100
rt 174,24,46624
mavv 72,180,96
rt 176,24,43072
s_add_u32 m0, s96, 118784
mavv 76,180,100
rt 178,24,46656
s_waitcnt lgkmcnt(10)
mavv 80,184,96
rt 180,24,43104
dm 248,0,12
mavv 84,184,100
rt 182,24,46688
mavv 88,188,96
rt 184,24,43136
s_add_u32 m0, s96, 119808
mavv 92,188,100
rt 186,24,46720
s_waitcnt lgkmcnt(10)
mavv 96,160,96
rt 188,24,43168
dm 249,0,12
mavv 100,160,100
rt 190,24,46752
mavv 104,164,96
rt 160,24,43200
s_add_u32 m0, s96, 120832
mavv 108,164,100
rt 162,24,46784
s_waitcnt lgkmcnt(10)
mavv 0,168,104
rt 164,24,50176
dm 250,0,12
mavv 4,168,108
rt 166,24,53760
mavv 8,172,104
rt 168,24,50208
mavv 12,172,108
rt 170,24,53792
s_waitcnt lgkmcnt(10)
mavv 16,176,104
rt 172,24,50240
mavv 20,176,108
rt 174,24,53824
mavv 24,180,104
rt 176,24,50272
mavv 28,180,108
rt 178,24,53856
s_waitcnt lgkmcnt(10)
mavv 32,184,104
rt 180,24,50304
mavv 36,184,108
rt 182,24,53888
mavv 40,188,104
rt 184,24,50336
mavv 44,188,108
rt 186,24,53920
s_waitcnt lgkmcnt(10)
mavv 48,160,104
rt 188,24,50368
mavv 52,160,108
rt 190,24,53952
s_waitcnt vmcnt(14)
s_barrier
mavv 56,164,104
rt 160,25,0
mavv 60,164,108
rt 162,25,3584
s_waitcnt lgkmcnt(10)
mavv 64,168,104
rt 164,25,32
s_add_u32 m0, s96, 0
mavv 68,168,108
rt 166,25,3616
mavv 72,172,104
rt 168,25,64
mavv 76,172,108
rt 170,25,3648
s_waitcnt lgkmcnt(10)
mavv 80,176,104
rt 172,25,96
dm 244,88,13
mavv 84,176,108
rt 174,25,3680
mavv 88,180,104
rt 176,25,128
mavv 92,180,108
rt 178,25,3712
s_waitcnt lgkmcnt(10)
mavv 96,184,104
rt 180,25,160
s_add_u32 m0, s96, 1024
mavv 100,184,108
rt 182,25,3744
mavv 104,188,104
rt 184,25,192
mavv 108,188,108
rt 186,25,3776
s_waitcnt lgkmcnt(10)
mavv 0,160,112
rt 188,25,7168
dm 245,88,13
mavv 4,160,116
rt 190,25,10752
mavv 8,164,112
rt 160,25,7200
mavv 12,164,116
rt 162,25,10784
s_waitcnt lgkmcnt(10)
mavv 16,168,112
rt 164,25,7232
s_add_u32 m0, s96, 2048
mavv 20,168,116
rt 166,25,10816
mavv 24,172,112
rt 168,25,7264
mavv 28,172,116
rt 170,25,10848
s_waitcnt lgkmcnt(10)
mavv 32,176,112
rt 172,25,7296
dm 246,88,13
mavv 36,176,116
rt 174,25,10880
mavv 40,180,112
rt 176,25,7328
mavv 44,180,116
rt 178,25,10912
s_waitcnt lgkmcnt(10)
mavv 48,184,112
rt 180,25,7360
s_add_u32 m0, s96, 3072
mavv 52,184,116
rt 182,25,10944
mavv 56,188,112
rt 184,25,14336
mavv 60,188,116
rt 186,25,17920
s_waitcnt lgkmcnt(10)
mavv 64,160,112
rt 188,25,14368
dm 247,88,13
mavv 68,160,116
rt 190,25,17952
mavv 72,164,112
rt 160,25,14400
mavv 76,164,116
rt 162,25,17984
s_waitcnt lgkmcnt(10)
mavv 80,168,112
rt 164,25,14432
s_add_u32 m0, s96, 4096
mavv 84,168,116
rt 166,25,18016
mavv 88,172,112
rt 168,25,14464
mavv 92,172,116
rt 170,25,18048
s_waitcnt lgkmcnt(10)
mavv 96,176,112
rt 172,25,14496
dm 248,88,13
mavv 100,176,116
rt 174,25,18080
mavv 104,180,112
rt 176,25,14528
mavv 108,180,116
rt 178,25,18112
s_waitcnt lgkmcnt(10)
mavv 0,184,120
rt 180,25,21504
s_add_u32 m0, s96, 5120
mavv 4,184,124
rt 182,25,25088
mavv 8,188,120
rt 184,25,21536
mavv 12,188,124
rt 186,25,25120
s_waitcnt lgkmcnt(10)
mavv 16,160,120
rt 188,25,21568
dm 249,88,13
mavv 20,160,124
rt 190,25,25152
mavv 24,164,120
rt 160,25,21600
mavv 28,164,124
rt 162,25,25184
s_waitcnt lgkmcnt(10)
mavv 32,168,120
rt 164,25,21632
s_add_u32 m0, s96, 6144
mavv 36,168,124
rt 166,25,25216
mavv 40,172,120
rt 168,25,21664
mavv 44,172,124
rt 170,25,25248
s_waitcnt lgkmcnt(10)
mavv 48,176,120
rt 172,25,21696
dm 250,88,13
mavv 52,176,124
rt 174,25,25280
s_waitcnt vmcnt(14)
s_barrier
mavv 56,180,120
rt 176,25,28672
mavv 60,180,124
rt 178,25,32256
s_waitcnt lgkmcnt(10)
mavv 64,184,120
rt 180,25,28704
s_cmp_eq_u32 s17, 6
s_cselect_b64 s[0:1], s[92:93], s[88:89]
s_cselect_b32 s2, s94, s90
mavv 68,184,124
rt 182,25,32288
mavv 72,188,120
rt 184,25,28736
mavv 76,188,124
rt 186,25,32320
s_waitcnt lgkmcnt(10)
mavv 80,160,120
rt 188,25,28768
s_add_u32 m0, s96, 28672
mavv 84,160,124
rt 190,25,32352
mavv 88,164,120
rt 160,25,28800
mavv 92,164,124
rt 162,25,32384
s_waitcnt lgkmcnt(10)
mavv 96,168,120
rt 164,25,28832
dm 244,0,14
mavv 100,168,124
rt 166,25,32416
mavv 104,172,120
rt 168,25,28864
mavv 108,172,124
rt 170,25,32448
s_waitcnt lgkmcnt(10)
mavv 0,176,128
rt 172,25,35840
s_add_u32 m0, s96, 29696
mavv 4,176,132
rt 174,25,39424
mavv 8,180,128
rt 176,25,35872
mavv 12,180,132
rt 178,25,39456
s_waitcnt lgkmcnt(10)
mavv 16,184,128
rt 180,25,35904
dm 245,0,14
mavv 20,184,132
rt 182,25,39488
mavv 24,188,128
rt 184,25,35936
mavv 28,188,132
rt 186,25,39520
s_waitcnt lgkmcnt(10)
mavv 32,160,128
rt 188,25,35968
s_add_u32 m0, s96, 30720
mavv 36,160,132
rt 190,25,39552
mavv 40,164,128
rt 160,25,36000
mavv 44,164,132
rt 162,25,39584
s_waitcnt lgkmcnt(10)
mavv 48,168,128
rt 164,25,36032
dm 246,0,14
mavv 52,168,132
rt 166,25,39616
mavv 56,172,128
rt 168,25,43008
mavv 60,172,132
rt 170,25,46592
s_waitcnt lgkmcnt(10)
mavv 64,176,128
rt 172,25,43040
s_add_u32 m0, s96, 31744
mavv 68,176,132
rt 174,25,46624
mavv 72,180,128
rt 176,25,43072
mavv 76,180,132
rt 178,25,46656
s_waitcnt lgkmcnt(10)
mavv 80,184,128
rt 180,25,43104
dm 247,0,14
mavv 84,184,132
rt 182,25,46688
mavv 88,188,128
rt 184,25,43136
mavv 92,188,132
rt 186,25,46720
s_waitcnt lgkmcnt(10)
mavv 96,160,128
rt 188,25,43168
s_add_u32 m0, s96, 32768
mavv 100,160,132
rt 190,25,46752
mavv 104,164,128
rt 160,25,43200
mavv 108,164,132
rt 162,25,46784
s_waitcnt lgkmcnt(10)
mavv 0,168,136
rt 164,25,50176
dm 248,0,14
mavv 4,168,140
rt 166,25,53760
mavv 8,172,136
rt 168,25,50208
mavv 12,172,140
rt 170,25,53792
s_waitcnt lgkmcnt(10)
mavv 16,176,136
rt 172,25,50240
s_add_u32 m0, s96, 33792
mavv 20,176,140
rt 174,25,53824
mavv 24,180,136
rt 176,25,50272
mavv 28,180,140
rt 178,25,53856
s_waitcnt lgkmcnt(10)
mavv 32,184,136
rt 180,25,50304
dm 249,0,14
mavv 36,184,140
rt 182,25,53888
mavv 40,188,136
rt 184,25,50336
mavv 44,188,140
rt 186,25,53920
s_waitcnt lgkmcnt(10)
mavv 48,160,136
rt 188,25,50368
s_add_u32 m0, s96, 34816
mavv 52,160,140
rt 190,25,53952
s_waitcnt vmcnt(13)
s_barrier
mavv 56,164,136
rt 160,26,0
mavv 60,164,140
rt 162,26,3584
s_waitcnt lgkmcnt(10)
mavv 64,168,136
rt 164,26,32
dm 250,0,14
mavv 68,168,140
rt 166,26,3616
mavv 72,172,136
rt 168,26,64
mavv 76,172,140
rt 170,26,3648
s_waitcnt lgkmcnt(10)
mavv 80,176,136
rt 172,26,96
s_add_u32 m0, s96, 57344
mavv 84,176,140
rt 174,26,3680
mavv 88,180,136
rt 176,26,128
mavv 92,180,140
rt 178,26,3712
s_waitcnt lgkmcnt(10)
mavv 96,184,136
rt 180,26,160
dm 244,88,15
mavv 100,184,140
rt 182,26,3744
mavv 104,188,136
rt 184,26,192
mavv 108,188,140
rt 186,26,3776
s_waitcnt lgkmcnt(10)
mavv 0,160,208
rt 188,26,7168
s_add_u32 m0, s96, 58368
mavv 4,160,212
rt 190,26,10752
mavv 8,164,208
rt 160,26,7200
mavv 12,164,212
rt 162,26,10784
s_waitcnt lgkmcnt(10)
mavv 16,168,208
rt 164,26,7232
dm 245,88,15
mavv 20,168,212
rt 166,26,10816
mavv 24,172,208
rt 168,26,7264
mavv 28,172,212
rt 170,26,10848
s_waitcnt lgkmcnt(10)
mavv 32,176,208
rt 172,26,7296
s_add_u32 m0, s96, 59392
mavv 36,176,212
rt 174,26,10880
mavv 40,180,208
rt 176,26,7328
mavv 44,180,212
rt 178,26,10912
s_waitcnt lgkmcnt(10)
mavv 48,184,208
rt 180,26,7360
dm 246,88,15
mavv 52,184,212
rt 182,26,10944
mavv 56,188,208
rt 184,26,14336
mavv 60,188,212
rt 186,26,17920
s_waitcnt lgkmcnt(10)
mavv 64,160,208
rt 188,26,14368
s_add_u32 m0, s96, 60416
mavv 68,160,212
rt 190,26,17952
mavv 72,164,208
rt 160,26,14400
mavv 76,164,212
rt 162,26,17984
s_waitcnt lgkmcnt(10)
mavv 80,168,208
rt 164,26,14432
dm 247,88,15
mavv 84,168,212
rt 166,26,18016
mavv 88,172,208
rt 168,26,14464
mavv 92,172,212
rt 170,26,18048
s_waitcnt lgkmcnt(10)
mavv 96,176,208
rt 172,26,14496
s_add_u32 m0, s96, 61440
mavv 100,176,212
rt 174,26,18080
mavv 104,180,208
rt 176,26,14528
mavv 108,180,212
rt 178,26,18112
s_waitcnt lgkmcnt(10)
zavv 112,184,80
rt 180,26,21504
dm 248,88,15
zavv 116,184,84
rt 182,26,25088
zavv 120,188,80
rt 184,26,21536
zavv 124,188,84
rt 186,26,25120
s_waitcnt lgkmcnt(10)
zavv 128,160,80
rt 188,26,21568
s_add_u32 m0, s96, 62464
zavv 132,160,84
rt 190,26,25152
zavv 136,164,80
rt 160,26,21600
zavv 140,164,84
rt 162,26,25184
s_waitcnt lgkmcnt(10)
zavv 144,168,80
rt 164,26,21632
dm 249,88,15
zavv 148,168,84
rt 166,26,25216
zavv 152,172,80
rt 168,26,21664
zavv 156,172,84
rt 170,26,25248
s_waitcnt lgkmcnt(10)
zavv 160,176,80
rt 172,26,21696
s_add_u32 m0, s96, 63488
zavv 164,176,84
rt 174,26,25280
s_waitcnt vmcnt(13)
s_barrier
mavv 112,180,88
rt 176,24,0
dm 250,88,15
mavv 116,180,92
rt 178,24,3584
s_cmp_eq_u32 s17, 8
s_cselect_b64 s[0:1], s[92:93], s[88:89]
s_cselect_b32 s2, s94, s90
s_waitcnt lgkmcnt(10)
mavv 120,184,88
rt 180,24,32
s_add_u32 m0, s96, 86016
mavv 124,184,92
rt 182,24,3616
dm 244,0,16
mavv 128,188,88
rt 184,24,64
s_add_u32 m0, s96, 87040
mavv 132,188,92
rt 186,24,3648
dm 245,0,16
s_waitcnt lgkmcnt(10)
mavv 136,160,88
rt 188,24,96
s_add_u32 m0, s96, 88064
mavv 140,160,92
rt 190,24,3680
dm 246,0,16
mavv 144,164,88
rt 160,24,128
s_add_u32 m0, s96, 89088
mavv 148,164,92
rt 162,24,3712
dm 247,0,16
s_waitcnt lgkmcnt(10)
)ASM" ::: "memory");
  asm volatile(R"ASM(
mavv 152,168,88
rt 164,24,160
s_add_u32 m0, s96, 90112
mavv 156,168,92
rt 166,24,3744
dm 248,0,16
mavv 160,172,88
rt 168,24,192
s_add_u32 m0, s96, 91136
mavv 164,172,92
rt 170,24,3776
dm 249,0,16
s_waitcnt lgkmcnt(10)
mavv 112,176,96
rt 172,24,7168
s_add_u32 m0, s96, 92160
mavv 116,176,100
rt 174,24,10752
dm 250,0,16
mavv 120,180,96
rt 176,24,7200
ar 216,0
ar 217,1
mavv 124,180,100
rt 178,24,10784
ar 218,2
ar 219,3
s_waitcnt lgkmcnt(10)
mavv 128,184,96
rt 180,24,7232
ar 220,4
ar 221,5
mavv 132,184,100
rt 182,24,10816
ar 222,6
ar 223,7
mavv 136,188,96
rt 184,24,7264
cv 224,216,217
cv 225,218,219
mavv 140,188,100
rt 186,24,10848
cv 226,220,221
cv 227,222,223
s_waitcnt lgkmcnt(10)
mavv 144,160,96
rt 188,24,7296
s_add_u32 s68, s34, 0
s_addc_u32 s69, s35, 0
global_store_dwordx4 v0, v[224:227], s[68:69] offset:0 sc1
mavv 148,160,100
rt 190,24,10880
ar 228,8
ar 229,9
mavv 152,164,96
rt 160,24,7328
ar 230,10
ar 231,11
mavv 156,164,100
rt 162,24,10912
ar 232,12
ar 233,13
s_waitcnt lgkmcnt(10)
mavv 160,168,96
rt 164,24,7360
ar 234,14
ar 235,15
mavv 164,168,100
rt 166,24,10944
cv 236,228,229
cv 237,230,231
mavv 112,172,104
rt 168,24,14336
cv 238,232,233
cv 239,234,235
mavv 116,172,108
rt 170,24,17920
global_store_dwordx4 v0, v[236:239], s[68:69] offset:1024 sc1
ar 216,16
s_waitcnt lgkmcnt(10)
mavv 120,176,104
rt 172,24,14368
ar 217,17
ar 218,18
mavv 124,176,108
rt 174,24,17952
ar 219,19
ar 220,20
mavv 128,180,104
rt 176,24,14400
ar 221,21
ar 222,22
mavv 132,180,108
rt 178,24,17984
ar 223,23
cv 224,216,217
s_waitcnt lgkmcnt(10)
mavv 136,184,104
rt 180,24,14432
cv 225,218,219
cv 226,220,221
mavv 140,184,108
rt 182,24,18016
cv 227,222,223
global_store_dwordx4 v0, v[224:227], s[68:69] offset:2048 sc1
mavv 144,188,104
rt 184,24,14464
ar 228,24
ar 229,25
mavv 148,188,108
rt 186,24,18048
ar 230,26
ar 231,27
s_waitcnt lgkmcnt(10)
mavv 152,160,104
rt 188,24,14496
ar 232,28
ar 233,29
mavv 156,160,108
rt 190,24,18080
ar 234,30
ar 235,31
mavv 160,164,104
rt 160,24,14528
cv 236,228,229
cv 237,230,231
mavv 164,164,108
rt 162,24,18112
cv 238,232,233
cv 239,234,235
s_waitcnt lgkmcnt(10)
mavv 112,168,112
rt 164,24,21504
global_store_dwordx4 v0, v[236:239], s[68:69] offset:3072 sc1
ar 216,32
mavv 116,168,116
rt 166,24,25088
ar 217,33
ar 218,34
mavv 120,172,112
rt 168,24,21536
ar 219,35
ar 220,36
mavv 124,172,116
rt 170,24,25120
ar 221,37
ar 222,38
s_waitcnt lgkmcnt(10)
mavv 128,176,112
rt 172,24,21568
ar 223,39
cv 224,216,217
mavv 132,176,116
rt 174,24,25152
cv 225,218,219
cv 226,220,221
mavv 136,180,112
rt 176,24,21600
cv 227,222,223
s_add_u32 s68, s34, 4096
s_addc_u32 s69, s35, 0
mavv 140,180,116
rt 178,24,25184
global_store_dwordx4 v0, v[224:227], s[68:69] offset:0 sc1
ar 228,40
s_waitcnt lgkmcnt(10)
mavv 144,184,112
rt 180,24,21632
ar 229,41
ar 230,42
mavv 148,184,116
rt 182,24,25216
ar 231,43
ar 232,44
mavv 152,188,112
rt 184,24,21664
ar 233,45
ar 234,46
mavv 156,188,116
rt 186,24,25248
ar 235,47
cv 236,228,229
s_waitcnt lgkmcnt(10)
mavv 160,160,112
rt 188,24,21696
cv 237,230,231
cv 238,232,233
mavv 164,160,116
rt 190,24,25280
cv 239,234,235
global_store_dwordx4 v0, v[236:239], s[68:69] offset:1024 sc1
s_waitcnt vmcnt(20)
s_barrier
mavv 112,164,120
rt 160,24,28672
ar 216,48
ar 217,49
mavv 116,164,124
rt 162,24,32256
ar 218,50
ar 219,51
s_waitcnt lgkmcnt(10)
mavv 120,168,120
rt 164,24,28704
ar 220,52
ar 221,53
mavv 124,168,124
rt 166,24,32288
ar 222,54
ar 223,55
mavv 128,172,120
rt 168,24,28736
cv 224,216,217
cv 225,218,219
mavv 132,172,124
rt 170,24,32320
cv 226,220,221
cv 227,222,223
s_waitcnt lgkmcnt(10)
mavv 136,176,120
rt 172,24,28768
global_store_dwordx4 v0, v[224:227], s[68:69] offset:2048 sc1
ar 228,56
mavv 140,176,124
rt 174,24,32352
ar 229,57
ar 230,58
mavv 144,180,120
rt 176,24,28800
ar 231,59
ar 232,60
mavv 148,180,124
rt 178,24,32384
ar 233,61
ar 234,62
s_waitcnt lgkmcnt(10)
mavv 152,184,120
rt 180,24,28832
ar 235,63
cv 236,228,229
mavv 156,184,124
rt 182,24,32416
cv 237,230,231
cv 238,232,233
mavv 160,188,120
rt 184,24,28864
cv 239,234,235
s_add_u32 s68, s34, 28672
s_addc_u32 s69, s35, 0
mavv 164,188,124
rt 186,24,32448
global_store_dwordx4 v0, v[236:239], s[68:69] offset:0 sc1
ar 216,64
s_waitcnt lgkmcnt(10)
mavv 112,160,128
rt 188,24,35840
ar 217,65
ar 218,66
mavv 116,160,132
rt 190,24,39424
ar 219,67
ar 220,68
mavv 120,164,128
rt 160,24,35872
ar 221,69
ar 222,70
mavv 124,164,132
rt 162,24,39456
ar 223,71
cv 224,216,217
s_waitcnt lgkmcnt(10)
mavv 128,168,128
rt 164,24,35904
cv 225,218,219
cv 226,220,221
mavv 132,168,132
rt 166,24,39488
cv 227,222,223
global_store_dwordx4 v0, v[224:227], s[68:69] offset:1024 sc1
mavv 136,172,128
rt 168,24,35936
ar 228,72
ar 229,73
mavv 140,172,132
rt 170,24,39520
ar 230,74
ar 231,75
s_waitcnt lgkmcnt(10)
mavv 144,176,128
rt 172,24,35968
ar 232,76
ar 233,77
mavv 148,176,132
rt 174,24,39552
ar 234,78
ar 235,79
mavv 152,180,128
rt 176,24,36000
cv 236,228,229
cv 237,230,231
mavv 156,180,132
rt 178,24,39584
cv 238,232,233
cv 239,234,235
s_waitcnt lgkmcnt(10)
mavv 160,184,128
rt 180,24,36032
global_store_dwordx4 v0, v[236:239], s[68:69] offset:2048 sc1
ar 216,80
mavv 164,184,132
rt 182,24,39616
ar 217,81
ar 218,82
mavv 112,188,136
rt 184,24,43008
ar 219,83
ar 220,84
mavv 116,188,140
rt 186,24,46592
ar 221,85
ar 222,86
s_waitcnt lgkmcnt(10)
mavv 120,160,136
rt 188,24,43040
ar 223,87
cv 224,216,217
mavv 124,160,140
rt 190,24,46624
cv 225,218,219
cv 226,220,221
mavv 128,164,136
rt 160,24,43072
cv 227,222,223
global_store_dwordx4 v0, v[224:227], s[68:69] offset:3072 sc1
mavv 132,164,140
rt 162,24,46656
ar 228,88
ar 229,89
s_waitcnt lgkmcnt(10)
mavv 136,168,136
rt 164,24,43104
ar 230,90
ar 231,91
mavv 140,168,140
rt 166,24,46688
ar 232,92
ar 233,93
mavv 144,172,136
rt 168,24,43136
ar 234,94
ar 235,95
mavv 148,172,140
rt 170,24,46720
cv 236,228,229
cv 237,230,231
s_waitcnt lgkmcnt(10)
mavv 152,176,136
rt 172,24,43168
cv 238,232,233
cv 239,234,235
mavv 156,176,140
rt 174,24,46752
s_add_u32 s68, s34, 32768
s_addc_u32 s69, s35, 0
global_store_dwordx4 v0, v[236:239], s[68:69] offset:0 sc1
mavv 160,180,136
rt 176,24,43200
ar 216,96
ar 217,97
mavv 164,180,140
rt 178,24,46784
ar 218,98
ar 219,99
s_waitcnt vmcnt(12)
s_barrier
s_waitcnt lgkmcnt(10)
mavv 112,184,208
rt 180,24,50176
ar 220,100
ar 221,101
mavv 116,184,212
rt 182,24,53760
ar 222,102
ar 223,103
mavv 120,188,208
rt 184,25,0
cv 224,216,217
cv 225,218,219
mavv 124,188,212
rt 186,25,3584
cv 226,220,221
cv 227,222,223
s_waitcnt lgkmcnt(10)
mavv 128,160,208
rt 188,25,7168
global_store_dwordx4 v0, v[224:227], s[68:69] offset:1024 sc1
ar 228,104
mavv 132,160,212
rt 190,25,10752
ar 229,105
ar 230,106
mavv 136,164,208
rt 160,25,14336
ar 231,107
ar 232,108
mavv 140,164,212
rt 162,25,17920
ar 233,109
ar 234,110
s_waitcnt lgkmcnt(10)
mavv 144,168,208
rt 164,25,21504
ar 235,111
cv 236,228,229
mavv 148,168,212
rt 166,25,25088
cv 237,230,231
cv 238,232,233
mavv 152,172,208
rt 168,25,28672
cv 239,234,235
global_store_dwordx4 v0, v[236:239], s[68:69] offset:2048 sc1
mavv 156,172,212
rt 170,25,32256
s_waitcnt lgkmcnt(10)
mavv 160,176,208
rt 172,25,35840
mavv 164,176,212
rt 174,25,39424
zavv 168,180,80
rt 176,25,43008
zavv 172,180,84
rt 178,25,46592
s_waitcnt lgkmcnt(10)
mavv 168,184,88
rt 180,25,50176
mavv 172,184,92
rt 182,25,53760
mavv 168,188,96
rt 184,24,50208
mavv 172,188,100
rt 186,24,53792
s_waitcnt lgkmcnt(10)
mavv 168,160,104
rt 188,25,32
ar 216,112
ar 217,113
mavv 172,160,108
rt 190,25,3616
ar 218,114
ar 219,115
mavv 168,164,112
rt 160,25,7200
ar 220,116
ar 221,117
mavv 172,164,116
rt 162,25,10784
ar 222,118
ar 223,119
s_waitcnt lgkmcnt(10)
mavv 168,168,120
rt 164,25,14368
cv 224,216,217
cv 225,218,219
mavv 172,168,124
rt 166,25,17952
cv 226,220,221
cv 227,222,223
mavv 168,172,128
rt 168,25,21536
s_add_u32 s68, s34, 57344
s_addc_u32 s69, s35, 0
global_store_dwordx4 v0, v[224:227], s[68:69] offset:0 sc1
mavv 172,172,132
rt 170,25,25120
ar 228,120
ar 229,121
s_waitcnt lgkmcnt(10)
mavv 168,176,136
rt 172,25,28704
ar 230,122
ar 231,123
mavv 172,176,140
rt 174,25,32288
ar 232,124
ar 233,125
mavv 168,180,208
rt 176,25,35872
ar 234,126
ar 235,127
mavv 172,180,212
rt 178,25,39456
cv 236,228,229
cv 237,230,231
s_waitcnt lgkmcnt(10)
zavv 176,184,80
rt 180,25,43040
cv 238,232,233
cv 239,234,235
zavv 180,184,84
rt 182,25,46624
global_store_dwordx4 v0, v[236:239], s[68:69] offset:1024 sc1
ar 216,128
mavv 176,188,88
rt 184,25,50208
ar 217,129
ar 218,130
mavv 180,188,92
rt 186,25,53792
ar 219,131
ar 220,132
s_waitcnt lgkmcnt(10)
mavv 176,160,96
rt 188,24,50240
ar 221,133
ar 222,134
mavv 180,160,100
rt 190,24,53824
ar 223,135
cv 224,216,217
mavv 176,164,104
rt 160,25,64
cv 225,218,219
cv 226,220,221
mavv 180,164,108
rt 162,25,3648
cv 227,222,223
global_store_dwordx4 v0, v[224:227], s[68:69] offset:2048 sc1
s_waitcnt lgkmcnt(10)
mavv 176,168,112
rt 164,25,7232
ar 228,136
ar 229,137
mavv 180,168,116
rt 166,25,10816
ar 230,138
ar 231,139
mavv 176,172,120
rt 168,25,14400
ar 232,140
ar 233,141
mavv 180,172,124
rt 170,25,17984
ar 234,142
ar 235,143
s_waitcnt lgkmcnt(10)
mavv 176,176,128
rt 172,25,21568
cv 236,228,229
cv 237,230,231
mavv 180,176,132
rt 174,25,25152
cv 238,232,233
cv 239,234,235
mavv 176,180,136
rt 176,25,28736
global_store_dwordx4 v0, v[236:239], s[68:69] offset:3072 sc1
ar 216,144
mavv 180,180,140
rt 178,25,32320
ar 217,145
ar 218,146
s_waitcnt lgkmcnt(10)
mavv 176,184,208
rt 180,25,35904
ar 219,147
ar 220,148
mavv 180,184,212
rt 182,25,39488
ar 221,149
ar 222,150
zavv 184,188,80
rt 184,25,43072
ar 223,151
cv 224,216,217
zavv 188,188,84
rt 186,25,46656
cv 225,218,219
cv 226,220,221
s_waitcnt lgkmcnt(10)
mavv 184,160,88
rt 188,25,50240
cv 227,222,223
s_add_u32 s68, s34, 61440
s_addc_u32 s69, s35, 0
mavv 188,160,92
rt 190,25,53824
global_store_dwordx4 v0, v[224:227], s[68:69] offset:0 sc1
ar 228,152
mavv 184,164,96
rt 160,24,50272
ar 229,153
ar 230,154
mavv 188,164,100
rt 162,24,53856
ar 231,155
ar 232,156
s_waitcnt lgkmcnt(10)
mavv 184,168,104
rt 164,25,96
)ASM" ::: "memory");
  asm volatile(R"ASM(
ar 233,157
ar 234,158
mavv 188,168,108
rt 166,25,3680
ar 235,159
cv 236,228,229
mavv 184,172,112
rt 168,25,7264
cv 237,230,231
cv 238,232,233
mavv 188,172,116
rt 170,25,10848
cv 239,234,235
global_store_dwordx4 v0, v[236:239], s[68:69] offset:1024 sc1
s_waitcnt lgkmcnt(10)
mavv 184,176,120
rt 172,25,14432
ar 216,160
ar 217,161
mavv 188,176,124
rt 174,25,18016
ar 218,162
ar 219,163
mavv 184,180,128
rt 176,25,21600
ar 220,164
ar 221,165
mavv 188,180,132
rt 178,25,25184
ar 222,166
ar 223,167
s_waitcnt lgkmcnt(10)
mavv 184,184,136
rt 180,25,28768
cv 224,216,217
cv 225,218,219
mavv 188,184,140
rt 182,25,32352
cv 226,220,221
cv 227,222,223
mavv 184,188,208
rt 184,25,35936
global_store_dwordx4 v0, v[224:227], s[68:69] offset:2048 sc1
ar 228,168
mavv 188,188,212
rt 186,25,39520
ar 229,169
ar 230,170
s_waitcnt lgkmcnt(10)
zavv 192,160,80
rt 188,25,43104
ar 231,171
ar 232,172
zavv 196,160,84
rt 190,25,46688
ar 233,173
ar 234,174
mavv 192,164,88
rt 160,25,50272
ar 235,175
cv 236,228,229
mavv 196,164,92
rt 162,25,53856
cv 237,230,231
cv 238,232,233
s_waitcnt lgkmcnt(10)
mavv 192,168,96
rt 164,24,50304
cv 239,234,235
s_add_u32 s68, s34, 86016
s_addc_u32 s69, s35, 0
mavv 196,168,100
rt 166,24,53888
global_store_dwordx4 v0, v[236:239], s[68:69] offset:0 sc1
ar 216,176
mavv 192,172,104
rt 168,25,128
ar 217,177
ar 218,178
mavv 196,172,108
rt 170,25,3712
ar 219,179
ar 220,180
s_waitcnt lgkmcnt(10)
mavv 192,176,112
rt 172,25,7296
ar 221,181
ar 222,182
mavv 196,176,116
rt 174,25,10880
ar 223,183
cv 224,216,217
mavv 192,180,120
rt 176,25,14464
cv 225,218,219
cv 226,220,221
mavv 196,180,124
rt 178,25,18048
cv 227,222,223
global_store_dwordx4 v0, v[224:227], s[68:69] offset:1024 sc1
s_waitcnt lgkmcnt(10)
mavv 192,184,128
rt 180,25,21632
ar 228,184
ar 229,185
mavv 196,184,132
rt 182,25,25216
ar 230,186
ar 231,187
mavv 192,188,136
rt 184,25,28800
ar 232,188
ar 233,189
mavv 196,188,140
rt 186,25,32384
ar 234,190
ar 235,191
s_waitcnt vmcnt(9)
s_barrier
s_cmp_lg_u32 s51, 3
s_cbranch_scc1 .Lk2_nohalf
v_mov_b32 v8, s98
v_mov_b32 v10, s82
s_mov_b64 exec, 1
global_store_dword v8, v10, s[58:59] sc1
s_mov_b64 exec, -1
.Lk2_nohalf:
s_waitcnt lgkmcnt(10)
mavv 192,160,208
rt 188,25,35968
cv 236,228,229
cv 237,230,231
mavv 196,160,212
rt 190,25,39552
cv 238,232,233
cv 239,234,235
zavv 200,164,80
rt 160,25,43136
global_store_dwordx4 v0, v[236:239], s[68:69] offset:2048 sc1
zavv 204,164,84
rt 162,25,46720
s_waitcnt lgkmcnt(10)
mavv 200,168,88
rt 164,25,50304
mavv 204,168,92
rt 166,25,53888
mavv 200,172,96
rt 168,24,50336
ar 216,192
ar 217,193
mavv 204,172,100
rt 170,24,53920
ar 218,194
ar 219,195
s_waitcnt lgkmcnt(10)
mavv 200,176,104
rt 172,25,160
ar 220,196
ar 221,197
mavv 204,176,108
rt 174,25,3744
ar 222,198
ar 223,199
mavv 200,180,112
rt 176,25,7328
cv 224,216,217
cv 225,218,219
mavv 204,180,116
rt 178,25,10912
cv 226,220,221
cv 227,222,223
s_waitcnt lgkmcnt(10)
mavv 200,184,120
rt 180,25,14496
global_store_dwordx4 v0, v[224:227], s[68:69] offset:3072 sc1
mavv 204,184,124
rt 182,25,18080
mavv 200,188,128
rt 184,25,21664
mavv 204,188,132
rt 186,25,25248
s_waitcnt lgkmcnt(10)
mavv 200,160,136
rt 188,25,28832
mavv 204,160,140
rt 190,25,32416
mavv 200,164,208
rt 160,25,36000
mavv 204,164,212
rt 162,25,39584
s_waitcnt lgkmcnt(10)
zavv 208,168,80
rt 164,25,43168
zavv 212,168,84
rt 166,25,46752
mavv 208,172,88
rt 168,25,50336
mavv 212,172,92
rt 170,25,53920
s_waitcnt lgkmcnt(10)
mavv 208,176,96
rt 172,24,50368
ar 228,200
ar 229,201
mavv 212,176,100
rt 174,24,53952
ar 230,202
ar 231,203
mavv 208,180,104
rt 176,25,192
ar 232,204
ar 233,205
mavv 212,180,108
rt 178,25,3776
ar 234,206
ar 235,207
s_waitcnt lgkmcnt(10)
mavv 208,184,112
rt 180,25,7360
cv 236,228,229
cv 237,230,231
mavv 212,184,116
rt 182,25,10944
cv 238,232,233
cv 239,234,235
mavv 208,188,120
rt 184,25,14528
s_add_u32 s68, s34, 90112
s_addc_u32 s69, s35, 0
global_store_dwordx4 v0, v[236:239], s[68:69] offset:0 sc1
mavv 212,188,124
rt 186,25,18112
s_waitcnt lgkmcnt(10)
mavv 208,160,128
rt 188,25,21696
mavv 212,160,132
rt 190,25,25280
mavv 208,164,136
rt 160,25,28864
mavv 212,164,140
rt 162,25,32448
s_waitcnt lgkmcnt(10)
mavv 208,168,208
rt 164,25,36032
mavv 212,168,212
rt 166,25,39616
zavv 216,172,80
rt 168,25,43200
zavv 220,172,84
rt 170,25,46784
s_waitcnt lgkmcnt(10)
mavv 216,176,88
rt 172,25,50368
mavv 220,176,92
rt 174,25,53952
mavv 216,180,96
ar 216,208
ar 217,209
mavv 220,180,100
ar 218,210
ar 219,211
s_waitcnt lgkmcnt(8)
mavv 216,184,104
ar 220,212
ar 221,213
mavv 220,184,108
ar 222,214
ar 223,215
mavv 216,188,112
cv 224,216,217
cv 225,218,219
mavv 220,188,116
cv 226,220,221
cv 227,222,223
s_waitcnt lgkmcnt(4)
mavv 216,160,120
global_store_dwordx4 v0, v[224:227], s[68:69] offset:1024 sc1
mavv 220,160,124
mavv 216,164,128
mavv 220,164,132
s_waitcnt lgkmcnt(0)
mavv 216,168,136
mavv 220,168,140
mavv 216,172,208
mavv 220,172,212
s_nop 7
ar 228,216
ar 229,217
ar 230,218
ar 231,219
ar 232,220
ar 233,221
ar 234,222
ar 235,223
cv 236,228,229
cv 237,230,231
cv 238,232,233
cv 239,234,235
global_store_dwordx4 v0, v[236:239], s[68:69] offset:2048 sc1
s_nop 1
v_mov_b64 v[236:237], 0
v_mov_b64 v[240:241], 0
v_mov_b32 v238, v78
v_mov_b32 v239, v13
s_bitcmp1_b32 s99, 0
s_cselect_b32 s62, 0, 0xffff
s_bitcmp1_b32 s99, 1
s_cselect_b32 s63, 0, 0xffff0000
s_or_b32 s62, s62, s63
s_andn2_b32 s66, 0x11111111, s62
v_lshrrev_b32 v11, 2, v0
v_and_b32 v11, 0x7c, v11
s_mov_b32 s63, 0x400000
s_waitcnt vmcnt(0)
s_barrier
s_cmp_lg_u32 s51, 3
s_cbranch_scc1 .Lk2_gl
v_add_u32 v8, 64, v8
v_mov_b32 v10, s82
s_mov_b64 exec, 1
global_store_dword v8, v10, s[58:59] sc1
s_mov_b64 exec, -1
.Lk2_gl:
global_load_dword v9, v11, s[58:59] sc1
s_waitcnt vmcnt(0)
v_cmp_eq_u32 vcc, s82, v9
s_lshr_b32 s70, s62, 1
s_and_b32 s70, s70, s62
s_lshr_b32 s65, s70, 2
s_and_b32 s70, s70, s65
s_and_b32 s70, s70, s66
s_andn2_b32 s66, s66, s70
s_andn2_b32 s67, vcc_lo, s62
s_cbranch_scc0 .Lk2_gnone
s_or_b32 s62, s62, s67
s_bitcmp1_b32 s67, 0
s_cbranch_scc0 .Lk2_gs0
v_add_u32 v10, 0, v0
global_load_dwordx4 a[0:3], v10, s[86:87] sc1
.Lk2_gs0:
s_bitcmp1_b32 s67, 1
s_cbranch_scc0 .Lk2_gs1
v_add_u32 v10, 114688, v0
global_load_dwordx4 a[4:7], v10, s[86:87] sc1
.Lk2_gs1:
s_bitcmp1_b32 s67, 2
s_cbranch_scc0 .Lk2_gs2
v_add_u32 v10, 229376, v0
global_load_dwordx4 a[8:11], v10, s[86:87] sc1
.Lk2_gs2:
s_bitcmp1_b32 s67, 3
s_cbranch_scc0 .Lk2_gs3
v_add_u32 v10, 344064, v0
global_load_dwordx4 a[12:15], v10, s[86:87] sc1
.Lk2_gs3:
s_bitcmp1_b32 s67, 4
s_cbranch_scc0 .Lk2_gs4
v_add_u32 v10, 458752, v0
global_load_dwordx4 a[16:19], v10, s[86:87] sc1
.Lk2_gs4:
s_bitcmp1_b32 s67, 5
s_cbranch_scc0 .Lk2_gs5
v_add_u32 v10, 573440, v0
global_load_dwordx4 a[20:23], v10, s[86:87] sc1
.Lk2_gs5:
s_bitcmp1_b32 s67, 6
s_cbranch_scc0 .Lk2_gs6
v_add_u32 v10, 688128, v0
global_load_dwordx4 a[24:27], v10, s[86:87] sc1
.Lk2_gs6:
s_bitcmp1_b32 s67, 7
s_cbranch_scc0 .Lk2_gs7
v_add_u32 v10, 802816, v0
global_load_dwordx4 a[28:31], v10, s[86:87] sc1
.Lk2_gs7:
s_bitcmp1_b32 s67, 8
s_cbranch_scc0 .Lk2_gs8
v_add_u32 v10, 917504, v0
global_load_dwordx4 a[32:35], v10, s[86:87] sc1
.Lk2_gs8:
s_bitcmp1_b32 s67, 9
s_cbranch_scc0 .Lk2_gs9
v_add_u32 v10, 1032192, v0
global_load_dwordx4 a[36:39], v10, s[86:87] sc1
.Lk2_gs9:
s_bitcmp1_b32 s67, 10
s_cbranch_scc0 .Lk2_gs10
v_add_u32 v10, 1146880, v0
global_load_dwordx4 a[40:43], v10, s[86:87] sc1
.Lk2_gs10:
s_bitcmp1_b32 s67, 11
s_cbranch_scc0 .Lk2_gs11
v_add_u32 v10, 1261568, v0
global_load_dwordx4 a[44:47], v10, s[86:87] sc1
.Lk2_gs11:
s_bitcmp1_b32 s67, 12
s_cbranch_scc0 .Lk2_gs12
v_add_u32 v10, 1376256, v0
global_load_dwordx4 a[48:51], v10, s[86:87] sc1
.Lk2_gs12:
s_bitcmp1_b32 s67, 13
s_cbranch_scc0 .Lk2_gs13
v_add_u32 v10, 1490944, v0
global_load_dwordx4 a[52:55], v10, s[86:87] sc1
.Lk2_gs13:
s_bitcmp1_b32 s67, 14
s_cbranch_scc0 .Lk2_gs14
v_add_u32 v10, 1605632, v0
global_load_dwordx4 a[56:59], v10, s[86:87] sc1
.Lk2_gs14:
s_bitcmp1_b32 s67, 15
s_cbranch_scc0 .Lk2_gs15
v_add_u32 v10, 1720320, v0
global_load_dwordx4 a[60:63], v10, s[86:87] sc1
.Lk2_gs15:
s_bitcmp1_b32 s67, 16
s_cbranch_scc0 .Lk2_gs16
v_add_u32 v10, 0, v0
global_load_dwordx4 a[64:67], v10, s[18:19] sc1
.Lk2_gs16:
s_bitcmp1_b32 s67, 17
s_cbranch_scc0 .Lk2_gs17
v_add_u32 v10, 114688, v0
global_load_dwordx4 a[68:71], v10, s[18:19] sc1
.Lk2_gs17:
s_bitcmp1_b32 s67, 18
s_cbranch_scc0 .Lk2_gs18
v_add_u32 v10, 229376, v0
global_load_dwordx4 a[72:75], v10, s[18:19] sc1
.Lk2_gs18:
s_bitcmp1_b32 s67, 19
s_cbranch_scc0 .Lk2_gs19
v_add_u32 v10, 344064, v0
global_load_dwordx4 a[76:79], v10, s[18:19] sc1
.Lk2_gs19:
s_bitcmp1_b32 s67, 20
s_cbranch_scc0 .Lk2_gs20
v_add_u32 v10, 458752, v0
global_load_dwordx4 a[80:83], v10, s[18:19] sc1
.Lk2_gs20:
s_bitcmp1_b32 s67, 21
s_cbranch_scc0 .Lk2_gs21
v_add_u32 v10, 573440, v0
global_load_dwordx4 a[84:87], v10, s[18:19] sc1
.Lk2_gs21:
s_bitcmp1_b32 s67, 22
s_cbranch_scc0 .Lk2_gs22
v_add_u32 v10, 688128, v0
global_load_dwordx4 a[88:91], v10, s[18:19] sc1
.Lk2_gs22:
s_bitcmp1_b32 s67, 23
s_cbranch_scc0 .Lk2_gs23
v_add_u32 v10, 802816, v0
global_load_dwordx4 a[92:95], v10, s[18:19] sc1
.Lk2_gs23:
s_bitcmp1_b32 s67, 24
s_cbranch_scc0 .Lk2_gs24
v_add_u32 v10, 917504, v0
global_load_dwordx4 a[96:99], v10, s[18:19] sc1
.Lk2_gs24:
s_bitcmp1_b32 s67, 25
s_cbranch_scc0 .Lk2_gs25
v_add_u32 v10, 1032192, v0
global_load_dwordx4 a[100:103], v10, s[18:19] sc1
.Lk2_gs25:
s_bitcmp1_b32 s67, 26
s_cbranch_scc0 .Lk2_gs26
v_add_u32 v10, 1146880, v0
global_load_dwordx4 a[104:107], v10, s[18:19] sc1
.Lk2_gs26:
s_bitcmp1_b32 s67, 27
s_cbranch_scc0 .Lk2_gs27
v_add_u32 v10, 1261568, v0
global_load_dwordx4 a[108:111], v10, s[18:19] sc1
.Lk2_gs27:
s_bitcmp1_b32 s67, 28
s_cbranch_scc0 .Lk2_gs28
v_add_u32 v10, 1376256, v0
global_load_dwordx4 a[112:115], v10, s[18:19] sc1
.Lk2_gs28:
s_bitcmp1_b32 s67, 29
s_cbranch_scc0 .Lk2_gs29
v_add_u32 v10, 1490944, v0
global_load_dwordx4 a[116:119], v10, s[18:19] sc1
.Lk2_gs29:
s_bitcmp1_b32 s67, 30
s_cbranch_scc0 .Lk2_gs30
v_add_u32 v10, 1605632, v0
global_load_dwordx4 a[120:123], v10, s[18:19] sc1
.Lk2_gs30:
s_bitcmp1_b32 s67, 31
s_cbranch_scc0 .Lk2_gs31
v_add_u32 v10, 1720320, v0
global_load_dwordx4 a[124:127], v10, s[18:19] sc1
.Lk2_gs31:
.Lk2_gnone:
s_bitcmp1_b32 s70, 0
s_cbranch_scc0 .Lk2_ga0
zvva 80,238,0
zvva 84,236,0
mvva 80,238,4
mvva 84,236,4
mvva 80,238,8
mvva 84,236,8
mvva 80,238,12
mvva 84,236,12
.Lk2_ga0:
s_bitcmp1_b32 s70, 4
s_cbranch_scc0 .Lk2_ga1
zvva 88,238,16
zvva 92,236,16
mvva 88,238,20
mvva 92,236,20
mvva 88,238,24
mvva 92,236,24
mvva 88,238,28
mvva 92,236,28
.Lk2_ga1:
s_bitcmp1_b32 s70, 8
s_cbranch_scc0 .Lk2_ga2
zvva 96,238,32
zvva 100,236,32
mvva 96,238,36
mvva 100,236,36
mvva 96,238,40
mvva 100,236,40
mvva 96,238,44
mvva 100,236,44
.Lk2_ga2:
s_bitcmp1_b32 s70, 12
s_cbranch_scc0 .Lk2_ga3
zvva 104,238,48
zvva 108,236,48
mvva 104,238,52
mvva 108,236,52
mvva 104,238,56
mvva 108,236,56
mvva 104,238,60
mvva 108,236,60
.Lk2_ga3:
s_bitcmp1_b32 s70, 16
s_cbranch_scc0 .Lk2_ga4
zvva 112,238,64
zvva 116,236,64
mvva 112,238,68
mvva 116,236,68
mvva 112,238,72
mvva 116,236,72
mvva 112,238,76
mvva 116,236,76
.Lk2_ga4:
s_bitcmp1_b32 s70, 20
s_cbranch_scc0 .Lk2_ga5
zvva 120,238,80
zvva 124,236,80
mvva 120,238,84
mvva 124,236,84
mvva 120,238,88
mvva 124,236,88
mvva 120,238,92
mvva 124,236,92
.Lk2_ga5:
s_bitcmp1_b32 s70, 24
s_cbranch_scc0 .Lk2_ga6
zvva 128,238,96
zvva 132,236,96
mvva 128,238,100
mvva 132,236,100
mvva 128,238,104
mvva 132,236,104
mvva 128,238,108
mvva 132,236,108
.Lk2_ga6:
s_bitcmp1_b32 s70, 28
s_cbranch_scc0 .Lk2_ga7
zvva 136,238,112
zvva 140,236,112
mvva 136,238,116
mvva 140,236,116
mvva 136,238,120
mvva 140,236,120
mvva 136,238,124
mvva 140,236,124
.Lk2_ga7:
s_cmp_eq_u32 s62, -1
s_cbranch_scc1 .Lk2_gdone
s_sub_u32 s63, s63, 1
s_cmp_lg_u32 s63, 0
s_cbranch_scc1 .Lk2_gl
v_mov_b32 v238, 0x7fc07fc0
s_mov_b32 s62, -1
.Lk2_gdone:
s_waitcnt vmcnt(0)
s_lshr_b32 s70, s62, 1
s_and_b32 s70, s70, s62
s_lshr_b32 s65, s70, 2
s_and_b32 s70, s70, s65
s_and_b32 s70, s70, s66
s_andn2_b32 s66, s66, s70
s_bitcmp1_b32 s70, 0
)ASM" ::: "memory");
  asm volatile(R"ASM(
s_cbranch_scc0 .Lk2_gb0
zvva 80,238,0
zvva 84,236,0
mvva 80,238,4
mvva 84,236,4
mvva 80,238,8
mvva 84,236,8
mvva 80,238,12
mvva 84,236,12
.Lk2_gb0:
s_bitcmp1_b32 s70, 4
s_cbranch_scc0 .Lk2_gb1
zvva 88,238,16
zvva 92,236,16
mvva 88,238,20
mvva 92,236,20
mvva 88,238,24
mvva 92,236,24
mvva 88,238,28
mvva 92,236,28
.Lk2_gb1:
s_bitcmp1_b32 s70, 8
s_cbranch_scc0 .Lk2_gb2
zvva 96,238,32
zvva 100,236,32
mvva 96,238,36
mvva 100,236,36
mvva 96,238,40
mvva 100,236,40
mvva 96,238,44
mvva 100,236,44
.Lk2_gb2:
s_bitcmp1_b32 s70, 12
s_cbranch_scc0 .Lk2_gb3
zvva 104,238,48
zvva 108,236,48
mvva 104,238,52
mvva 108,236,52
mvva 104,238,56
mvva 108,236,56
mvva 104,238,60
mvva 108,236,60
.Lk2_gb3:
s_bitcmp1_b32 s70, 16
s_cbranch_scc0 .Lk2_gb4
zvva 112,238,64
zvva 116,236,64
mvva 112,238,68
mvva 116,236,68
mvva 112,238,72
mvva 116,236,72
mvva 112,238,76
mvva 116,236,76
.Lk2_gb4:
s_bitcmp1_b32 s70, 20
s_cbranch_scc0 .Lk2_gb5
zvva 120,238,80
zvva 124,236,80
mvva 120,238,84
mvva 124,236,84
mvva 120,238,88
mvva 124,236,88
mvva 120,238,92
mvva 124,236,92
.Lk2_gb5:
s_bitcmp1_b32 s70, 24
s_cbranch_scc0 .Lk2_gb6
zvva 128,238,96
zvva 132,236,96
mvva 128,238,100
mvva 132,236,100
mvva 128,238,104
mvva 132,236,104
mvva 128,238,108
mvva 132,236,108
.Lk2_gb6:
s_bitcmp1_b32 s70, 28
s_cbranch_scc0 .Lk2_gb7
zvva 136,238,112
zvva 140,236,112
mvva 136,238,116
mvva 140,236,116
mvva 136,238,120
mvva 140,236,120
mvva 136,238,124
mvva 140,236,124
.Lk2_gb7:
s_nop 7
s_bitcmp1_b32 s99, 0
s_cbranch_scc0 .Lk2_r0skip
v_pk_add_f32 v[80:81], v[80:81], v[88:89]
v_pk_add_f32 v[82:83], v[82:83], v[90:91]
v_pk_add_f32 v[84:85], v[84:85], v[92:93]
v_pk_add_f32 v[86:87], v[86:87], v[94:95]
v_pk_add_f32 v[96:97], v[96:97], v[104:105]
v_pk_add_f32 v[98:99], v[98:99], v[106:107]
v_pk_add_f32 v[100:101], v[100:101], v[108:109]
v_pk_add_f32 v[102:103], v[102:103], v[110:111]
v_pk_add_f32 v[80:81], v[80:81], v[96:97]
v_pk_add_f32 v[82:83], v[82:83], v[98:99]
v_pk_add_f32 v[84:85], v[84:85], v[100:101]
v_pk_add_f32 v[86:87], v[86:87], v[102:103]
cv 88,80,81
cv 89,82,83
cv 90,84,85
cv 91,86,87
v_add_u32 v10, 14336, v255
global_store_dwordx2 v255, v[88:89], s[4:5]
global_store_dwordx2 v10, v[90:91], s[4:5]
.Lk2_r0skip:
s_bitcmp1_b32 s99, 1
s_cbranch_scc0 .Lk2_end
v_pk_add_f32 v[112:113], v[112:113], v[120:121]
v_pk_add_f32 v[114:115], v[114:115], v[122:123]
v_pk_add_f32 v[116:117], v[116:117], v[124:125]
v_pk_add_f32 v[118:119], v[118:119], v[126:127]
v_pk_add_f32 v[128:129], v[128:129], v[136:137]
v_pk_add_f32 v[130:131], v[130:131], v[138:139]
v_pk_add_f32 v[132:133], v[132:133], v[140:141]
v_pk_add_f32 v[134:135], v[134:135], v[142:143]
v_pk_add_f32 v[112:113], v[112:113], v[128:129]
v_pk_add_f32 v[114:115], v[114:115], v[130:131]
v_pk_add_f32 v[116:117], v[116:117], v[132:133]
v_pk_add_f32 v[118:119], v[118:119], v[134:135]
cv 120,112,113
cv 121,114,115
cv 122,116,117
cv 123,118,119
v_add_u32 v10, 14336, v255
global_store_dwordx2 v255, v[120:121], s[6:7]
global_store_dwordx2 v10, v[122:123], s[6:7]
.Lk2_end:
s_waitcnt vmcnt(0)
.purgem rt
.purgem rqv
.purgem zavv
.purgem dm
.purgem mavv
.purgem ar
.purgem cv
.purgem ex
.purgem m3
.purgem rqa
.purgem zvaa
.purgem mvaa
.purgem mvav
.purgem mvvv
.purgem mvva
.purgem zvva
)ASM" ::: "memory");
}

extern "C" void kernel_launch(void* const* d_in, const int* in_sizes, int n_in, void* d_out, int out_size, void* d_ws, size_t ws_size, hipStream_t stream) {
  (void)in_sizes; (void)n_in; (void)out_size; (void)ws_size;
  (void)hipFuncSetAttribute((const void*)fused_kernel, hipFuncAttributeMaxDynamicSharedMemorySize, (int)F_LDS);
  typedef const unsigned short* P;
  char* ws = (char*)d_ws;
  fused_kernel<<<256, 256, F_LDS, stream>>>((P)d_in[0], (P)d_in[1], (P)d_in[4], (P)d_in[5], (P)d_in[6], (P)d_in[7], (P)d_in[9], (P)d_in[2], (P)d_in[8], ws, (unsigned short*)d_out);
}
```
